```python
import torch

def _softmax(x, axis=-1):
    return torch.softmax(x, dim=axis)
def _gelu(x):
    return torch.nn.functional.gelu(x, approximate="tanh")
def _silu(x):
    return torch.nn.functional.silu(x)
def _sigmoid(x):
    return torch.sigmoid(x)
def _layernorm(x, axis=-1, eps=1e-5):
    m = x.mean(dim=axis, keepdim=True)
    v = x.var(dim=axis, keepdim=True, unbiased=False)
    return (x - m) / torch.sqrt(v + eps)
def _scatter_add(tgt, idx, src, axis=0):
    return tgt.clone().index_add_(axis, idx, src)
def _segment_sum(src, seg, n):
    out = torch.zeros((n,) + src.shape[1:], dtype=src.dtype)
    return out.index_add_(0, seg, src)
def _pad(x, pw):
    flat = tuple(v for pair in reversed(pw) for v in pair)
    return torch.nn.functional.pad(x, flat)

kernel_name = 'sequential_preLN__grouped_kv_mqa__dense_activation_silu__prompt'

def setup_inputs():

    _w = (lambda a: (lambda t: t.to(torch.bfloat16) if t.is_floating_point() else t)(torch.as_tensor(a)))
    def _A_setup():
        B, S, D, H = 1, 2048, 640, 32
        Dh = D // H
        return {
            "x": _w((lambda *s: torch.randn(*s, dtype=torch.bfloat16) * 0.05)(B, S, D)),
            "wq": _w((lambda *s: torch.randn(*s, dtype=torch.bfloat16) * 0.05)(D, H * Dh)),
            "wk": _w((lambda *s: torch.randn(*s, dtype=torch.bfloat16) * 0.05)(D, Dh)),
            "wv": _w((lambda *s: torch.randn(*s, dtype=torch.bfloat16) * 0.05)(D, Dh)),
            "wo": _w((lambda *s: torch.randn(*s, dtype=torch.bfloat16) * 0.05)(H * Dh, D)),
        }
    _A_in = _A_setup()

    def _B_setup():
        B, S, D, F = 1, 2048, 640, 1728
        return {
            "x": _w((lambda *s: torch.randn(*s, dtype=torch.bfloat16) * 0.05)(B, S, D)),
            "w1": _w((lambda *s: torch.randn(*s, dtype=torch.bfloat16) * 0.8)(D, F) * (D ** -0.5)),
            "w2": _w((lambda *s: torch.randn(*s, dtype=torch.bfloat16) * 0.8)(F, D) * (F ** -0.5)),
        }
    _B_in = _B_setup()

    return {
        "A_x": _A_in["x"],
        "A_wq": _A_in["wq"],
        "A_wk": _A_in["wk"],
        "A_wv": _A_in["wv"],
        "A_wo": _A_in["wo"],
        "B_x": _B_in["x"],
        "B_w1": _B_in["w1"],
        "B_w2": _B_in["w2"],
    }

def reference(A_x, A_wq, A_wk, A_wv, A_wo, B_x, B_w1, B_w2):
    def _A_ref(x, wq, wk, wv, wo):
        B, S, D = x.shape
        H = 32
        Dh = D // H
        q = (lambda x, *d: x.permute(*d))((x @ wq).reshape(B, S, H, Dh), 0, 2, 1, 3)
        k = x @ wk
        v = x @ wv
        scores = torch.einsum("bhqd,bkd->bhqk", q, k) * (Dh ** -0.5)
        p = _softmax(scores, axis=-1)
        o = torch.einsum("bhqk,bkd->bhqd", p, v)
        return (lambda x, *d: x.permute(*d))(o, 0, 2, 1, 3).reshape(B, S, H * Dh) @ wo

    def _B_ref(x, w1, w2):
        h = torch.einsum("bsd,df->bsf", x, w1)
        h = _silu(h)
        out = torch.einsum("bsf,fd->bsd", h, w2)
        return out

    x = A_x
    h = x + _A_ref(_layernorm(x, axis=-1), A_wq, A_wk, A_wv, A_wo)
    h2 = h + _B_ref(_layernorm(h, axis=-1), B_w1, B_w2)
    return h2
```

```cpp
#include <hip/hip_runtime.h>
#include <hip/hip_bf16.h>
#include <stdint.h>

typedef __attribute__((ext_vector_type(8))) __bf16 bf16x8;
typedef __attribute__((ext_vector_type(2))) __bf16 bf16x2;
typedef __attribute__((ext_vector_type(4))) short s16x4;
typedef __attribute__((ext_vector_type(4))) float f32x4;
typedef __attribute__((ext_vector_type(2))) float f32x2;
typedef __attribute__((ext_vector_type(16))) float f32x16;
typedef __attribute__((ext_vector_type(4))) unsigned u32x4;
typedef __attribute__((ext_vector_type(2))) unsigned u32x2;
#define LAS __attribute__((address_space(3)))
#define DI __device__ __forceinline__

constexpr int D = 640, F = 1728;
constexpr float LN_EPS = 1e-5f;
constexpr float QSCALE = 0.22360679774997896f * 1.4426950408889634f;

constexpr int KV_TILE = 2624;
constexpr size_t WS_Q   = 0;
constexpr size_t WS_KV  = WS_Q + 64 * 32 * 1280;
constexpr size_t WS_WO  = WS_KV + 64 * KV_TILE;
constexpr size_t WS_W1  = WS_WO + 8 * 20 * 5 * 1024;
constexpr size_t WS_W2  = WS_W1 + 16 * 20 * 7 * 1024;
constexpr size_t WS_O   = WS_W2 + 8 * 54 * 5 * 1024;
constexpr size_t WS_H   = WS_O + 128 * 20 * 1024;
constexpr size_t WS_F   = WS_H + 128 * 20 * 1024;
constexpr size_t WS_ST  = WS_F + 128 * 54 * 1024;

DI unsigned lds_addr(const void* p) { return (unsigned)(size_t)(const LAS char*)p; }
DI void glds16(unsigned voff, const void* sbase, unsigned lds) {
    unsigned sv;
    asm volatile("s_mov_b32 %0, m0\n\ts_mov_b32 m0, %3\n\ts_nop 0\n\tglobal_load_lds_dwordx4 %1, %2\n\ts_mov_b32 m0, %0"
                 : "=&s"(sv) : "v"(voff), "s"(sbase), "s"(lds) : "memory");
}
DI void gload16(u32x4& d, unsigned voff, const void* sbase) {
    asm volatile("global_load_dwordx4 %0, %1, %2" : "=v"(d) : "v"(voff), "s"(sbase) : "memory");
}
DI void gload8(u32x2& d, unsigned voff, const void* sbase) {
    asm volatile("global_load_dwordx2 %0, %1, %2" : "=v"(d) : "v"(voff), "s"(sbase) : "memory");
}
DI void gload4(unsigned& d, unsigned voff, const void* sbase) {
    asm volatile("global_load_dword %0, %1, %2" : "=v"(d) : "v"(voff), "s"(sbase) : "memory");
}
template <int N> DI void wait_bar() { asm volatile("s_waitcnt vmcnt(%0) lgkmcnt(0)\n\ts_barrier" ::"i"(N) : "memory"); }
template <int N> DI void wait_vm() { asm volatile("s_waitcnt vmcnt(%0)" ::"i"(N) : "memory"); }
#define PIN(x) asm volatile("" : "+v"(x))
#define SBAR() __builtin_amdgcn_sched_barrier(0)
DI unsigned pk2(float a, float b) { bf16x2 v = {(__bf16)a, (__bf16)b}; return __builtin_bit_cast(unsigned, v); }
DI float bflo(unsigned u) { return __uint_as_float(u << 16); }
DI float bfhi(unsigned u) { return __uint_as_float(u & 0xffff0000u); }
DI f32x4 mfma16(u32x4 a, u32x4 b, f32x4 c) {
    return __builtin_amdgcn_mfma_f32_16x16x32_bf16(__builtin_bit_cast(bf16x8, a), __builtin_bit_cast(bf16x8, b), c, 0, 0, 0);
}
DI f32x16 mfma32(u32x4 a, u32x4 b, f32x16 c) {
    return __builtin_amdgcn_mfma_f32_32x32x16_bf16(__builtin_bit_cast(bf16x8, a), __builtin_bit_cast(bf16x8, b), c, 0, 0, 0);
}
DI f32x16 mfma32k8(u32x2 a, u32x2 b, f32x16 c) {
    return __builtin_amdgcn_mfma_f32_32x32x8bf16_1k(__builtin_bit_cast(s16x4, a), __builtin_bit_cast(s16x4, b), c, 0, 0, 0);
}
DI u32x4 lds16(unsigned a) { return *(const LAS u32x4*)(size_t)a; }
DI u32x2 lds8(unsigned a) { return *(const LAS u32x2*)(size_t)a; }
DI unsigned lds4(unsigned a) { return *(const LAS unsigned*)(size_t)a; }
DI u32x2 ldstr(unsigned a) { return __builtin_bit_cast(u32x2, __builtin_amdgcn_ds_read_tr16_b64_v4i16((LAS s16x4*)(size_t)a)); }
DI u32x4 ldstr2(unsigned a0, unsigned a1) { u32x2 lo = ldstr(a0), hi = ldstr(a1); return u32x4{lo[0], lo[1], hi[0], hi[1]}; }
DI float dot2(unsigned a, unsigned b, float c) {
    return __builtin_amdgcn_fdot2_f32_bf16(__builtin_bit_cast(bf16x2, a), __builtin_bit_cast(bf16x2, b), c, false);
}
DI float xsum16_32(float v) { v += __shfl_xor(v, 16); v += __shfl_xor(v, 32); return v; }
constexpr unsigned ONES2 = 0x3f803f80u;

constexpr int A_SLOT = 36 * 1024, A_WKV = 3 * A_SLOT, A_CSX = A_WKV + 25600 + 64, A_LDS = A_CSX + 4 * 96 * 4;
__global__ __launch_bounds__(256) void k_qkv(const char* __restrict__ x, const char* __restrict__ wq, const char* __restrict__ wk,
                                             const char* __restrict__ wv, char* __restrict__ ws) {
    extern __shared__ __attribute__((aligned(16))) char smem[];
    const unsigned L = lds_addr(smem);
    const int lane = threadIdx.x & 63, w = __builtin_amdgcn_readfirstlane(threadIdx.x >> 6);
    const int idx = blockIdx.x >> 3, tb = (blockIdx.x & 7) * 4 + (idx >> 3), cg = idx & 7;
    const bool haskv = cg < 4;
    const int g = lane >> 4, c16 = lane & 15;

    if (haskv) {
        const char* src = cg < 2 ? wk : wv;
#pragma unroll
        for (int i = 0; i < 7; ++i) { int p = i * 4 + w; p = p < 25 ? p : 24; glds16(lane * 16, src + p * 1024, L + A_WKV + p * 1024); }
    }
    unsigned vx[2], vw[5];
#pragma unroll
    for (int hf = 0; hf < 2; ++hf) vx[hf] = (hf * 8 + (lane >> 3)) * 1280 + (((lane & 7) ^ (hf * 4 + (lane >> 4))) << 4);
#pragma unroll
    for (int i = 0; i < 5; ++i) {
        const int ci = (w * 5 + i) * 64 + lane, pos = ci / 10, ch = ci - pos * 10, k = pos ^ (((pos >> 3) & 1) << 2);
        vw[i] = k * 1280 + ch * 16;
    }
    const char* xb = x + (size_t)(tb * 64 + w * 16) * 1280;
    const char* wb = wq + cg * 160;
    auto issue = [&](int c) {
        const unsigned s = L + (c % 3) * A_SLOT;
#pragma unroll
        for (int sg = 0; sg < 2; ++sg)
#pragma unroll
            for (int hf = 0; hf < 2; ++hf) glds16(vx[hf], xb + c * 256 + sg * 128, s + (w * 4 + sg * 2 + hf) * 1024);
#pragma unroll
        for (int i = 0; i < 5; ++i) glds16(vw[i], wb + c * 128 * 1280, s + 16384 + (w * 5 + i) * 1024);
    };
    issue(0); issue(1); issue(2);

    unsigned ax[2], aw[2], akv[2];
#pragma unroll
    for (int e = 0; e < 2; ++e) {
        ax[e] = L + (w * 4 + (c16 >> 3)) * 1024 + (c16 & 7) * 128 + (((e * 4 + g) ^ (c16 >> 1)) << 4);
        aw[e] = L + 16384 + (8 * g + ((4 * e) ^ (4 * (g & 1))) + (c16 >> 2)) * 160 + (lane & 3) * 8;
        akv[e] = L + A_WKV + (8 * g + 4 * e + (c16 >> 2)) * 40 + (cg & 1) * 32 + (lane & 3) * 8;
    }
    f32x4 acc[6], cs[6];
#pragma unroll
    for (int i = 0; i < 6; ++i) { acc[i] = f32x4{0, 0, 0, 0}; cs[i] = f32x4{0, 0, 0, 0}; }
    float sm = 0.f, sq = 0.f;
    const u32x4 ones = {ONES2, ONES2, ONES2, ONES2};
#pragma unroll
    for (int c = 0; c < 5; ++c) {
        if (c == 0) wait_bar<18>(); else if (c < 4) wait_bar<9>(); else wait_bar<0>();
        if (c >= 1 && c + 2 < 5) issue(c + 2);
        const int so = (c % 3) * A_SLOT;
#pragma unroll
        for (int ks = 0; ks < 4; ++ks) {
            const u32x4 xf = lds16(ax[ks & 1] + so + (ks >> 1) * 2048);
            u32x4 wf[6];
#pragma unroll
            for (int s = 0; s < 5; ++s) wf[s] = ldstr2(aw[0] + so + ks * 5120 + s * 32, aw[1] + so + ks * 5120 + s * 32);
            wf[5] = ldstr2(akv[0] + (c * 128 + ks * 32) * 40, akv[1] + (c * 128 + ks * 32) * 40);
#pragma unroll
            for (int s = 0; s < 5; ++s) acc[s] = mfma16(wf[s], xf, acc[s]);
            if (haskv) acc[5] = mfma16(wf[5], xf, acc[5]);
            if (ks == w) {
#pragma unroll
                for (int s = 0; s < 6; ++s) cs[s] = mfma16(wf[s], ones, cs[s]);
            }
#pragma unroll
            for (int j = 0; j < 4; ++j) { sm = dot2(xf[j], ONES2, sm); sq = dot2(xf[j], xf[j], sq); }
        }
    }
    if (c16 == 0) {
#pragma unroll
        for (int s = 0; s < 6; ++s) *(LAS f32x4*)(size_t)(L + A_CSX + (w * 96 + s * 16 + 4 * g) * 4) = cs[s];
    }
    sm = xsum16_32(sm); sq = xsum16_32(sq);
    const float mean = sm * (1.f / D), var = fmaxf(sq * (1.f / D) - mean * mean, 0.f), rstd = rsqrtf(var + LN_EPS);
    wait_bar<0>();
    const int tok = tb * 64 + w * 16 + c16, t32 = tok & 31;
#pragma unroll
    for (int s = 0; s < 6; ++s) {
        f32x4 c4 = f32x4{0, 0, 0, 0};
#pragma unroll
        for (int ww = 0; ww < 4; ++ww) c4 += *(const LAS f32x4*)(size_t)(L + A_CSX + (ww * 96 + s * 16 + 4 * g) * 4);
        const float sc = s < 5 ? rstd * QSCALE : rstd;
        f32x4 v;
#pragma unroll
        for (int r = 0; r < 4; ++r) v[r] = (acc[s][r] - mean * c4[r]) * sc;
        const u32x2 pv = {pk2(v[0], v[1]), pk2(v[2], v[3])};
        if (s < 5) {
            const int f0 = cg * 80 + s * 16 + 4 * g, head = f0 / 20, d0 = f0 - head * 20;
            const int off = d0 < 16 ? ((d0 >> 3) * 32 + t32) * 16 + (d0 & 4) * 2 : 1024 + t32 * 8;
            *(u32x2*)(ws + WS_Q + ((tok >> 5) * 32 + head) * 1280 + off) = pv;
        } else if (haskv) {
            char* kvb = ws + WS_KV + (tok >> 5) * KV_TILE;
            const int d0 = (cg & 1) * 16 + 4 * g;
            if (cg < 2) {
                const int off = d0 < 16 ? ((d0 >> 3) * 32 + t32) * 16 + (d0 & 4) * 2 : 1024 + t32 * 8;
                if (d0 < 20) *(u32x2*)(kvb + off) = pv;
            } else {
                char* vb = kvb + 1280 + (((t32 >> 4) * 2 + ((t32 >> 2) & 1)) * 21 + d0) * 16 + (((t32 >> 3) & 1) * 4 + (t32 & 3)) * 2;
                if (d0 < 20) {
                    *(unsigned short*)(vb) = (unsigned short)pv[0];
                    *(unsigned short*)(vb + 16) = (unsigned short)(pv[0] >> 16);
                    *(unsigned short*)(vb + 32) = (unsigned short)pv[1];
                    *(unsigned short*)(vb + 48) = (unsigned short)(pv[1] >> 16);
                } else if (d0 == 20) {
                    *(unsigned short*)(vb) = (unsigned short)0x3f80;
                }
            }
        }
    }
}

constexpr int B_CHUNK = 16 * KV_TILE, B_AUX = 3 * B_CHUNK, B_LDS = B_AUX + 8 * 3072;
DI float halfmax(float m) {
    auto r = __builtin_amdgcn_permlane32_swap(__float_as_uint(m), __float_as_uint(m), false, false);
    return fmaxf(__uint_as_float(r[0]), __uint_as_float(r[1]));
}
DI float max16(const f32x16& s) {
    float a = fmaxf(fmaxf(s[0], s[1]), s[2]), b = fmaxf(fmaxf(s[3], s[4]), s[5]);
    a = fmaxf(fmaxf(a, s[6]), s[7]); b = fmaxf(fmaxf(b, s[8]), s[9]);
    a = fmaxf(fmaxf(a, s[10]), s[11]); b = fmaxf(fmaxf(b, s[12]), s[13]);
    a = fmaxf(fmaxf(a, s[14]), s[15]);
    return fmaxf(a, b);
}
#define EX2(v) __builtin_amdgcn_exp2f(v)

__global__ __launch_bounds__(512, 2) void k_attn(const char* __restrict__ wo, const char* __restrict__ w1, const char* __restrict__ w2,
                                                 char* __restrict__ ws) {
    extern __shared__ __attribute__((aligned(16))) char smem[];
    const unsigned L = lds_addr(smem);
    const int lane = threadIdx.x & 63, w = __builtin_amdgcn_readfirstlane(threadIdx.x >> 6);
    const int idx = blockIdx.x >> 3, tb32 = (blockIdx.x & 7) * 8 + (idx >> 2), head0 = (idx & 3) * 8 + (w & 3) * 2, kp = w >> 2;
    const int r32 = lane & 31, h = lane >> 5;
    const char* kv = ws + WS_KV;

    u32x4 qa1, qb1; u32x2 qa2, qb2;
    {
        const char* qp = ws + WS_Q + (tb32 * 32 + head0) * 1280;
        unsigned t0, t1;
        gload16(qa1, lane * 16, qp); gload16(qb1, lane * 16, qp + 1280);
        gload4(t0, r32 * 8 + h * 4, qp + 1024); gload4(t1, r32 * 8 + h * 4, qp + 1280 + 1024);
        auto issue = [&](int c, int slot) {
#pragma unroll
            for (int i = 0; i < 5; ++i) glds16(lane * 16, kv + c * B_CHUNK + (i * 8 + w) * 1024, L + slot * B_CHUNK + (i * 8 + w) * 1024);
            if (w == 0) glds16(lane * 16, kv + c * B_CHUNK + 40 * 1024, L + slot * B_CHUNK + 40 * 1024);
        };
        issue(0, 0); issue(1, 1); issue(2, 2);
        if (w == 0) wait_bar<12>(); else wait_bar<10>();
        PIN(qa1); PIN(qb1); PIN(t0); PIN(t1);
        qa2 = u32x2{t0, 0u}; qb2 = u32x2{t1, 0u};
    }
    const char* psrc = nullptr; int pld = 0, pn = 0, pks = 0, pmat = 0;
    if (w < 5) {
        int u = blockIdx.x * 5 + w;
        if (u < 200) { pmat = 0; pks = u / 10; pn = u - pks * 10; pld = 1280; psrc = wo; }
        else if (u < 740) { u -= 200; pmat = 1; pks = u / 27; pn = u - pks * 27; pld = 3456; psrc = w1; }
        else { u -= 740; pmat = 2; pks = u / 10; pn = u - pks * 10; pld = 1280; psrc = w2; }
        psrc += (size_t)pks * 32 * pld + pn * 128;
        const unsigned vo = (lane >> 3) * pld + (lane & 7) * 16;
#pragma unroll
        for (int i = 0; i < 4; ++i) glds16(vo, psrc + i * 8 * pld, L + B_AUX + w * 4096 + i * 1024);
    }

    const int vrow = r32 <= 20 ? r32 : (r32 < 28 ? 20 : 4);
    const unsigned kconst = h == 0 ? 0x00003f80u : 0u;
    unsigned ak1 = L + lane * 16, ak2 = L + 1024 + r32 * 8 + h * 4, av = L + 1280 + (h * 21 + vrow) * 16;
    const f32x16 zero16 = {0, 0, 0, 0, 0, 0, 0, 0, 0, 0, 0, 0, 0, 0, 0, 0};
    f32x16 sa, sb, oa = zero16, ob = zero16;
    u32x4 k1; u32x2 k2;
    k1 = lds16(ak1); k2 = u32x2{lds4(ak2), kconst};
    sa = mfma32(k1, qa1, mfma32k8(k2, qa2, zero16));
    sb = mfma32(k1, qb1, mfma32k8(k2, qb2, zero16));
    {
        const float ma = halfmax(max16(sa)), mb = halfmax(max16(sb));
        qa2[1] = h == 0 ? (pk2(-ma, 0.f) & 0xffffu) : 0u;
        qb2[1] = h == 0 ? (pk2(-mb, 0.f) & 0xffffu) : 0u;
    }
    ak1 += kp * KV_TILE; ak2 += kp * KV_TILE; av += kp * KV_TILE;
    k1 = lds16(ak1); k2[0] = lds4(ak2);
    u32x4 v0 = lds16(av), v1 = v0;
    sa = mfma32(k1, qa1, mfma32k8(k2, qa2, zero16));
    u32x4 pa0, pa1, pb0, pb1 = {0u, 0u, 0u, 0u};

#define SLOT(MF, S, i, P, j) do { MF; \
        const float e0 = EX2(S[i]), e1 = EX2(S[i + 1]), e2 = EX2(S[i + 2]), e3 = EX2(S[i + 3]); \
        P[j] = pk2(e0, e1); P[j + 1] = pk2(e2, e3); PIN(P); SBAR(); } while (0)
#pragma unroll 1
    for (int c = 0; c < 4; ++c) {
#pragma unroll
        for (int j = 0; j < 8; ++j) {
            const int o = j * 2 * KV_TILE;
            if (j == 4) {
                if (c == 0) {
                    wait_bar<0>();
                    if (w < 5) {
                        const unsigned a0 = L + B_AUX + w * 4096 + (8 * (lane >> 4) + ((lane & 15) >> 2)) * 128 + (lane & 3) * 8;
#pragma unroll
                        for (int s = 0; s < 4; ++s) {
                            const u32x4 f = ldstr2(a0 + s * 32, a0 + 512 + s * 32);
                            const int st = pn * 4 + s;
                            size_t dst;
                            if (pmat == 0) dst = WS_WO + (size_t)(((st / 5) * 20 + pks) * 5 + st % 5) * 1024;
                            else if (pmat == 1) dst = WS_W1 + (size_t)(((st / 7) * 20 + pks) * 7 + st % 7) * 1024;
                            else dst = WS_W2 + (size_t)(((st / 5) * 54 + pks) * 5 + st % 5) * 1024;
                            *(u32x4*)(ws + dst + lane * 16) = f;
                        }
                    }
                } else if (c == 1) {
                    wait_bar<63>();
#pragma unroll
                    for (int i = 0; i < 5; ++i) glds16(lane * 16, kv + 3 * B_CHUNK + (i * 8 + w) * 1024, L + (i * 8 + w) * 1024);
                    if (w == 0) glds16(lane * 16, kv + 3 * B_CHUNK + 40 * 1024, L + 40 * 1024);
                } else if (c == 2) {
                    wait_bar<0>();
                }
                SBAR();
            }
            SLOT(sb = mfma32k8(k2, qb2, zero16), sa, 0, pa0, 0);
            SLOT(sb = mfma32(k1, qb1, sb), sa, 4, pa0, 2);
            if (j == 7) { const int dlt = c == 2 ? -2 * B_CHUNK : B_CHUNK; ak1 += dlt; ak2 += dlt; k1 = lds16(ak1); k2[0] = lds4(ak2); }
            else { k1 = lds16(ak1 + o + 2 * KV_TILE); k2[0] = lds4(ak2 + o + 2 * KV_TILE); }
            SBAR();
            SLOT(ob = mfma32(v1, pb1, ob), sa, 8, pa1, 0);
            v1 = lds16(av + o + 672); SBAR();
            SLOT(oa = mfma32(v0, pa0, oa), sa, 12, pa1, 2);
            SLOT(sa = mfma32k8(k2, qa2, zero16), sb, 0, pb0, 0);
            SLOT(sa = mfma32(k1, qa1, sa), sb, 4, pb0, 2);
            SLOT(oa = mfma32(v1, pa1, oa), sb, 8, pb1, 0);
            SLOT(ob = mfma32(v0, pb0, ob), sb, 12, pb1, 2);
            if (j == 7) { av += c == 2 ? -2 * B_CHUNK : B_CHUNK; v0 = lds16(av); }
            else v0 = lds16(av + o + 2 * KV_TILE);
            SBAR();
        }
    }
    ob = mfma32(v1, pb1, ob);
    f32x16 o = kp ? ob : oa;
    {
        const f32x16 snd = kp ? oa : ob;
        const unsigned xa = L + B_AUX + w * 3072 + lane * 16;
#pragma unroll
        for (int i = 0; i < 3; ++i) *(LAS f32x4*)(size_t)(xa + i * 1024) = f32x4{snd[4 * i], snd[4 * i + 1], snd[4 * i + 2], snd[4 * i + 3]};
        wait_bar<63>();
        const unsigned ra = L + B_AUX + (w ^ 4) * 3072 + lane * 16;
#pragma unroll
        for (int i = 0; i < 3; ++i) {
            const f32x4 t = *(const LAS f32x4*)(size_t)(ra + i * 1024);
            o[4 * i] += t[0]; o[4 * i + 1] += t[1]; o[4 * i + 2] += t[2]; o[4 * i + 3] += t[3];
        }
    }
    float l = __shfl(o[8], r32 + 32);
    {
        float am = 0.f;
#pragma unroll
        for (int i = 0; i < 8; ++i) am = fmaxf(am, fabsf(o[i]));
        if (h == 0) {
#pragma unroll
            for (int i = 8; i < 12; ++i) am = fmaxf(am, fabsf(o[i]));
        }
        const bool ok = (l >= 1e-18f) && (l <= 1e18f) && (am <= 1e37f);
        if (__builtin_expect(!__all(ok), 0)) {
            const u32x4 q1 = kp ? qb1 : qa1;
            const u32x2 q2 = {kp ? qb2[0] : qa2[0], 0u};
            float m = -INFINITY;
            o = zero16;
#pragma unroll 1
            for (int t = 0; t < 64; ++t) {
                const char* tp = kv + t * KV_TILE;
                const u32x4 fk1 = *(const u32x4*)(tp + lane * 16);
                const u32x2 fk2 = {*(const unsigned*)(tp + 1024 + r32 * 8 + h * 4), 0u};
                const u32x4 fv0 = *(const u32x4*)(tp + 1280 + (h * 21 + vrow) * 16), fv1 = *(const u32x4*)(tp + 1280 + 672 + (h * 21 + vrow) * 16);
                f32x16 s = mfma32(fk1, q1, mfma32k8(fk2, q2, zero16));
                const float mn = fmaxf(m, halfmax(max16(s))), al = EX2(m - mn);
                m = mn;
                u32x4 p0, p1;
#pragma unroll
                for (int i = 0; i < 4; ++i) { p0[i] = pk2(EX2(s[2 * i] - m), EX2(s[2 * i + 1] - m)); p1[i] = pk2(EX2(s[8 + 2 * i] - m), EX2(s[9 + 2 * i] - m)); }
#pragma unroll
                for (int i = 0; i < 16; ++i) o[i] *= al;
                o = mfma32(fv1, p1, mfma32(fv0, p0, o));
            }
            l = __shfl(o[8], r32 + 32);
        }
    }
    const float rl = 1.f / l;
    const int tok = tb32 * 32 + r32, f0 = (head0 + kp) * 20 + 4 * h;
    char* ob_ = ws + WS_O + (size_t)(tok >> 4) * 20 * 1024 + (tok & 15) * 16;
#pragma unroll
    for (int i = 0; i < 3; ++i) {
        const int f = f0 + 8 * i;
        const u32x2 pv = {pk2(o[4 * i] * rl, o[4 * i + 1] * rl), pk2(o[4 * i + 2] * rl, o[4 * i + 3] * rl)};
        if (i < 2 || h == 0) *(u32x2*)(ob_ + (f >> 5) * 1024 + ((f >> 3) & 3) * 256 + (f & 4) * 2) = pv;
    }
}

template <int KS, int TS, int NS, int NSLOT, int PRE, int EXTRA, bool COLSUM>
struct Gemm {
    static constexpr int NC = (KS + 3) / 4;
    static constexpr int ksteps(int c) { return c * 4 + 4 <= KS ? 4 : KS - c * 4; }
    static constexpr int wp(int c) { return (ksteps(c) * NS + 3) / 4; }
    static constexpr int nld(int c) { return wp(c) + ksteps(c) * TS; }
    static constexpr int AHEAD = NSLOT > PRE ? PRE : PRE - 1;
    static constexpr int issued(int c) { int n = c == 0 ? PRE : (c - 1 + AHEAD + 1 > PRE ? c + AHEAD : PRE); return n < NC ? n : NC; }
    static constexpr int pending(int c) { int s = 0; for (int j = c + 1; j < issued(c); ++j) s += nld(j); return s + (c < PRE ? EXTRA : 0); }
    static constexpr int SLOT = 4 * NS * 1024;
    static constexpr int RING = PRE + 1 < NC ? PRE + 1 : NC;

    u32x4 act[RING][4][TS];
    f32x4 acc[TS][NS], cs[NS];
    const char* ab; const char* wbp; unsigned L; int lane, w;

    template <int C> DI void issue() {
#pragma unroll
        for (int k = 0; k < ksteps(C); ++k) {
#pragma unroll
            for (int t = 0; t < TS; ++t) gload16(act[C % RING][k][t], lane * 16, ab + (size_t)(t * KS + C * 4 + k) * 1024);
#pragma unroll
            for (int i = k * wp(C) / ksteps(C); i < (k + 1) * wp(C) / ksteps(C); ++i) {
                int p = i * 4 + w;
                if ((i + 1) * 4 > ksteps(C) * NS) p = p < ksteps(C) * NS ? p : ksteps(C) * NS - 1;
                glds16(lane * 16, wbp + (size_t)(C * 4 * NS + p) * 1024, L + (C % NSLOT) * SLOT + p * 1024);
            }
        }
    }
    template <int C> DI void pre() { if constexpr (C < PRE && C < NC) { issue<C>(); pre<C + 1>(); } }
    template <int C> DI void step() {
        if constexpr (C < NC) {
            wait_bar<pending(C)>();
#pragma unroll
            for (int k = 0; k < ksteps(C); ++k)
#pragma unroll
                for (int t = 0; t < TS; ++t) PIN(act[C % RING][k][t]);
            if constexpr (C + AHEAD >= PRE && C + AHEAD < NC && (C > 0 || AHEAD == PRE)) issue<C + AHEAD>();
            const unsigned a = L + (C % NSLOT) * SLOT + lane * 16;
#pragma unroll
            for (int k = 0; k < ksteps(C); ++k) {
                u32x4 wf[NS];
#pragma unroll
                for (int s = 0; s < NS; ++s) wf[s] = lds16(a + (k * NS + s) * 1024);
#pragma unroll
                for (int s = 0; s < NS; ++s)
#pragma unroll
                    for (int t = 0; t < TS; ++t) acc[t][s] = mfma16(wf[s], act[C % RING][k][t], acc[t][s]);
                if constexpr (COLSUM) {
                    if (k == w) {
                        const u32x4 ones = {ONES2, ONES2, ONES2, ONES2};
#pragma unroll
                        for (int s = 0; s < NS; ++s) cs[s] = mfma16(wf[s], ones, cs[s]);
                    }
                }
            }
            step<C + 1>();
        }
    }
    DI void init() {
#pragma unroll
        for (int s = 0; s < NS; ++s) {
            cs[s] = f32x4{0, 0, 0, 0};
#pragma unroll
            for (int t = 0; t < TS; ++t) acc[t][s] = f32x4{0, 0, 0, 0};
        }
    }
};
DI size_t fm_off(int tok, int f, int KS) { return (size_t)((tok >> 4) * KS + (f >> 5)) * 1024 + ((f >> 3) & 3) * 256 + (tok & 15) * 16 + (f & 4) * 2; }

constexpr int C_X = 100 * 1024, C_LDS = C_X + 10 * 1024;
__global__ __launch_bounds__(256) void k_oproj(const char* __restrict__ x, char* __restrict__ ws) {
    extern __shared__ __attribute__((aligned(16))) char smem[];
    Gemm<20, 1, 5, 5, 5, 3, false> G;
    G.L = lds_addr(smem); G.lane = threadIdx.x & 63; G.w = __builtin_amdgcn_readfirstlane(threadIdx.x >> 6);
    const int lane = G.lane, w = G.w, xg = blockIdx.x & 7, idx = blockIdx.x >> 3;
    const int rb = (xg >> 1) * 8 + (idx >> 2), ct = (xg & 1) * 4 + (idx & 3);
    G.ab = ws + WS_O + (size_t)(rb * 4 + w) * 20 * 1024;
    G.wbp = ws + WS_WO + (size_t)ct * 100 * 1024;
    G.pre<0>();
#pragma unroll
    for (int i = 0; i < 3; ++i) {
        int p = i * 4 + w; p = p < 10 ? p : 9;
        const int ci = p * 64 + lane, row = ci / 10, ch = ci - row * 10;
        glds16(row * 1280 + ch * 16, x + (size_t)rb * 64 * 1280 + ct * 160, G.L + C_X + p * 1024);
    }
    G.init();
    G.step<0>();
    wait_bar<0>();
    const int g = lane >> 4, c16 = lane & 15, tok = rb * 64 + w * 16 + c16;
    float sm = 0.f, sq = 0.f;
#pragma unroll
    for (int s = 0; s < 5; ++s) {
        const u32x2 xr = lds8(G.L + C_X + (w * 16 + c16) * 160 + s * 32 + g * 8);
        const u32x2 pv = {pk2(bflo(xr[0]) + G.acc[0][s][0], bfhi(xr[0]) + G.acc[0][s][1]), pk2(bflo(xr[1]) + G.acc[0][s][2], bfhi(xr[1]) + G.acc[0][s][3])};
        sm = dot2(pv[0], ONES2, sm); sm = dot2(pv[1], ONES2, sm);
        sq = dot2(pv[0], pv[0], sq); sq = dot2(pv[1], pv[1], sq);
        *(u32x2*)(ws + WS_H + fm_off(tok, ct * 80 + s * 16 + 4 * g, 20)) = pv;
    }
    sm = xsum16_32(sm); sq = xsum16_32(sq);
    if (g == 0) *(f32x2*)(ws + WS_ST + (size_t)(tok * 8 + ct) * 8) = f32x2{sm, sq};
}

constexpr int D_CSX = 140 * 1024, D_LDS = D_CSX + 4 * 112 * 4;
__global__ __launch_bounds__(256) void k_ffn1(char* __restrict__ ws) {
    extern __shared__ __attribute__((aligned(16))) char smem[];
    Gemm<20, 2, 7, 5, 4, 0, true> G;
    G.L = lds_addr(smem); G.lane = threadIdx.x & 63; G.w = __builtin_amdgcn_readfirstlane(threadIdx.x >> 6);
    const int lane = G.lane, w = G.w, xg = blockIdx.x & 7, idx = blockIdx.x >> 3;
    const int rb = (xg >> 1) * 4 + (idx >> 3), ct = (xg & 1) * 8 + (idx & 7);
    const int g = lane >> 4, c16 = lane & 15, tok0 = rb * 128 + w * 32 + c16;
    u32x4 st[2];
#pragma unroll
    for (int t = 0; t < 2; ++t) gload16(st[t], c16 * 64 + g * 16, ws + WS_ST + (size_t)(rb * 128 + w * 32 + t * 16) * 64);
    G.ab = ws + WS_H + (size_t)(rb * 8 + w * 2) * 20 * 1024;
    G.wbp = ws + WS_W1 + (size_t)ct * 140 * 1024;
    G.pre<0>();
    G.init();
    G.step<0>();
    if (c16 == 0) {
#pragma unroll
        for (int s = 0; s < 7; ++s) *(LAS f32x4*)(size_t)(G.L + D_CSX + (w * 112 + s * 16 + 4 * g) * 4) = G.cs[s];
    }
    float mean[2], rstd[2];
#pragma unroll
    for (int t = 0; t < 2; ++t) {
        PIN(st[t]);
        const float sm = xsum16_32(__uint_as_float(st[t][0]) + __uint_as_float(st[t][2]));
        const float sq = xsum16_32(__uint_as_float(st[t][1]) + __uint_as_float(st[t][3]));
        mean[t] = sm * (1.f / D);
        rstd[t] = rsqrtf(fmaxf(sq * (1.f / D) - mean[t] * mean[t], 0.f) + LN_EPS);
    }
    wait_bar<0>();
#pragma unroll
    for (int s = 0; s < 7; ++s) {
        if (ct * 7 + s < 108) {
            f32x4 c4 = f32x4{0, 0, 0, 0};
#pragma unroll
            for (int ww = 0; ww < 4; ++ww) c4 += *(const LAS f32x4*)(size_t)(G.L + D_CSX + (ww * 112 + s * 16 + 4 * g) * 4);
#pragma unroll
            for (int t = 0; t < 2; ++t) {
                float v[4];
#pragma unroll
                for (int r = 0; r < 4; ++r) {
                    const float u = (G.acc[t][s][r] - mean[t] * c4[r]) * rstd[t];
                    v[r] = u * __builtin_amdgcn_rcpf(1.f + EX2(u * -1.4426950408889634f));
                }
                const u32x2 pv = {pk2(v[0], v[1]), pk2(v[2], v[3])};
                *(u32x2*)(ws + WS_F + fm_off(tok0 + t * 16, ct * 112 + s * 16 + 4 * g, 54)) = pv;
            }
        }
    }
}

constexpr int E_LDS = 7 * 20 * 1024;
__global__ __launch_bounds__(256) void k_ffn2(char* __restrict__ ws, char* __restrict__ out) {
    extern __shared__ __attribute__((aligned(16))) char smem[];
    Gemm<54, 1, 5, 7, 6, 0, false> G;
    G.L = lds_addr(smem); G.lane = threadIdx.x & 63; G.w = __builtin_amdgcn_readfirstlane(threadIdx.x >> 6);
    const int lane = G.lane, w = G.w, xg = blockIdx.x & 7, idx = blockIdx.x >> 3;
    const int rb = (xg >> 1) * 8 + (idx >> 2), ct = (xg & 1) * 4 + (idx & 3);
    const int g = lane >> 4, c16 = lane & 15, tok = rb * 64 + w * 16 + c16;
    u32x2 hr[5];
#pragma unroll
    for (int s = 0; s < 5; ++s) gload8(hr[s], (unsigned)fm_off(tok, ct * 80 + s * 16 + 4 * g, 20), ws + WS_H);
    G.ab = ws + WS_F + (size_t)(rb * 4 + w) * 54 * 1024;
    G.wbp = ws + WS_W2 + (size_t)ct * 270 * 1024;
    G.pre<0>();
    G.init();
    G.step<0>();
#pragma unroll
    for (int s = 0; s < 5; ++s) {
        PIN(hr[s]);
        const u32x2 pv = {pk2(bflo(hr[s][0]) + G.acc[0][s][0], bfhi(hr[s][0]) + G.acc[0][s][1]),
                          pk2(bflo(hr[s][1]) + G.acc[0][s][2], bfhi(hr[s][1]) + G.acc[0][s][3])};
        *(u32x2*)(out + (size_t)tok * 1280 + (ct * 80 + s * 16 + 4 * g) * 2) = pv;
    }
}

extern "C" void kernel_launch(void* const* d_in, const int* in_sizes, int n_in, void* d_out, int out_size, void* d_ws, size_t ws_size,
                              hipStream_t stream) {
    const char* x = (const char*)d_in[0];
    const char* wq = (const char*)d_in[1];
    const char* wk = (const char*)d_in[2];
    const char* wv = (const char*)d_in[3];
    const char* wo = (const char*)d_in[4];
    const char* w1 = (const char*)d_in[6];
    const char* w2 = (const char*)d_in[7];
    char* ws = (char*)d_ws;
    (void)hipFuncSetAttribute((const void*)k_qkv, hipFuncAttributeMaxDynamicSharedMemorySize, A_LDS);
    (void)hipFuncSetAttribute((const void*)k_attn, hipFuncAttributeMaxDynamicSharedMemorySize, B_LDS);
    (void)hipFuncSetAttribute((const void*)k_oproj, hipFuncAttributeMaxDynamicSharedMemorySize, C_LDS);
    (void)hipFuncSetAttribute((const void*)k_ffn1, hipFuncAttributeMaxDynamicSharedMemorySize, D_LDS);
    (void)hipFuncSetAttribute((const void*)k_ffn2, hipFuncAttributeMaxDynamicSharedMemorySize, E_LDS);
    k_qkv<<<256, 256, A_LDS, stream>>>(x, wq, wk, wv, ws);
    k_attn<<<256, 512, B_LDS, stream>>>(wo, w1, w2, ws);
    k_oproj<<<256, 256, C_LDS, stream>>>(x, ws);
    k_ffn1<<<256, 256, D_LDS, stream>>>(ws);
    k_ffn2<<<256, 256, E_LDS, stream>>>(ws, (char*)d_out);
}
```

```cpp
#include <hip/hip_runtime.h>
#include <hip/hip_bf16.h>
#include <stdint.h>

typedef __attribute__((ext_vector_type(8))) __bf16 bf16x8;
typedef __attribute__((ext_vector_type(2))) __bf16 bf16x2;
typedef __attribute__((ext_vector_type(4))) short s16x4;
typedef __attribute__((ext_vector_type(4))) float f32x4;
typedef __attribute__((ext_vector_type(2))) float f32x2;
typedef __attribute__((ext_vector_type(16))) float f32x16;
typedef __attribute__((ext_vector_type(4))) unsigned u32x4;
typedef __attribute__((ext_vector_type(2))) unsigned u32x2;
typedef unsigned long long u64;
#define LAS __attribute__((address_space(3)))
#define GAS __attribute__((address_space(1)))
#define DI __device__ __forceinline__

constexpr int D = 640;
constexpr float LN_EPS = 1e-5f;
constexpr float LOG2E = 1.4426950408889634f;
constexpr float QSCALE = 0.22360679774997896f * LOG2E;

constexpr int KV_TILE = 2624;
constexpr unsigned WS_FL = 0;
constexpr unsigned FL_KV = 32768, FL_PR = 2 * 32768, FL_O = 4 * 32768, FL_H = 8 * 32768, FL_F = 12 * 32768;
constexpr unsigned WS_KV = 16 * 32768;
constexpr unsigned WS_WO = WS_KV + 64 * KV_TILE;
constexpr unsigned WS_W1 = WS_WO + 8 * 20 * 5 * 1024;
constexpr unsigned WS_W2 = WS_W1 + 16 * 20 * 7 * 1024;
constexpr unsigned WS_CS = WS_W2 + 8 * 54 * 5 * 1024;
constexpr unsigned WS_O  = WS_CS + 20 * 1792 * 4;
constexpr unsigned WS_H  = WS_O + 128 * 20 * 1024;
constexpr unsigned WS_F  = WS_H + 128 * 20 * 1024;
constexpr unsigned WS_ST = WS_F + 128 * 54 * 1024;
constexpr unsigned WS_END = WS_ST + 2048 * 64;

constexpr int CHUNK = 16 * KV_TILE;
constexpr int L_WKV = 0, L_X = 25664, L_WQ2 = 47104, L_WQ1 = 122880, L_CSX = 111104, L_QX = 152576, L_MISC = 162816;
constexpr int L_AUX = 3 * CHUNK, L_WO = CHUNK, L_CNT = 110592, LDS_BYTES = 163840;

#define STAMP_INIT()
#define STAMP(I)
#define STAMP_OUT(K)

#define THROT 1
#define THROT2 63
DI void glds(unsigned voff, const void* sbase, unsigned lds) {
    asm volatile("s_mov_b32 m0, %2\n\ts_nop 0\n\tglobal_load_lds_dwordx4 %0, %1" ::"v"(voff), "s"(sbase), "s"(lds) : "memory");
}
DI void glds_dev(unsigned voff, const void* sbase, unsigned lds) {
    asm volatile("s_mov_b32 m0, %2\n\ts_nop 0\n\tglobal_load_lds_dwordx4 %0, %1 sc1" ::"v"(voff), "s"(sbase), "s"(lds) : "memory");
}
template <int N> DI void wait_vm() { asm volatile("s_waitcnt vmcnt(%0)" ::"i"(N) : "memory"); }
DI void bar() { asm volatile("s_waitcnt lgkmcnt(0)\n\ts_barrier" ::: "memory"); }
DI void bar_raw() { asm volatile("s_barrier" ::: "memory"); }
#define PIN(x) asm volatile("" : "+v"(x))
#define SBAR() __builtin_amdgcn_sched_barrier(0)
DI unsigned pk2(float a, float b) { bf16x2 v = {(__bf16)a, (__bf16)b}; return __builtin_bit_cast(unsigned, v); }
DI float bflo(unsigned u) { return __uint_as_float(u << 16); }
DI float bfhi(unsigned u) { return __uint_as_float(u & 0xffff0000u); }
DI f32x4 mfma16(u32x4 a, u32x4 b, f32x4 c) {
    return __builtin_amdgcn_mfma_f32_16x16x32_bf16(__builtin_bit_cast(bf16x8, a), __builtin_bit_cast(bf16x8, b), c, 0, 0, 0);
}
DI f32x16 mfma32(u32x4 a, u32x4 b, f32x16 c) {
    return __builtin_amdgcn_mfma_f32_32x32x16_bf16(__builtin_bit_cast(bf16x8, a), __builtin_bit_cast(bf16x8, b), c, 0, 0, 0);
}
DI f32x16 mfma32k8(u32x2 a, u32x2 b, f32x16 c) {
    return __builtin_amdgcn_mfma_f32_32x32x8bf16_1k(__builtin_bit_cast(s16x4, a), __builtin_bit_cast(s16x4, b), c, 0, 0, 0);
}
DI u32x4 lds16(unsigned a) { return *(const LAS u32x4*)(size_t)a; }
DI u32x2 lds8(unsigned a) { return *(const LAS u32x2*)(size_t)a; }
DI unsigned lds4(unsigned a) { return *(const LAS unsigned*)(size_t)a; }
DI f32x4 ldsf4(unsigned a) { return *(const LAS f32x4*)(size_t)a; }
DI u32x2 ldstr(unsigned a) { return __builtin_bit_cast(u32x2, __builtin_amdgcn_ds_read_tr16_b64_v4i16((LAS s16x4*)(size_t)a)); }
DI u32x4 ldstr2(unsigned a0, unsigned a1) { u32x2 lo = ldstr(a0), hi = ldstr(a1); return u32x4{lo[0], lo[1], hi[0], hi[1]}; }
DI float dot2(unsigned a, unsigned b, float c) {
    return __builtin_amdgcn_fdot2_f32_bf16(__builtin_bit_cast(bf16x2, a), __builtin_bit_cast(bf16x2, b), c, false);
}
DI float xsum16_32(float v) {
    const auto a = __builtin_amdgcn_permlane16_swap(__float_as_uint(v), __float_as_uint(v), false, false);
    v = __uint_as_float(a[0]) + __uint_as_float(a[1]);
    const auto b = __builtin_amdgcn_permlane32_swap(__float_as_uint(v), __float_as_uint(v), false, false);
    return __uint_as_float(b[0]) + __uint_as_float(b[1]);
}
constexpr unsigned ONES2 = 0x3f803f80u;
#define EX2(v) __builtin_amdgcn_exp2f(v)
typedef __amdgpu_buffer_rsrc_t rsrc_t;
constexpr int SC1 = 16;
DI u32x4 bld16(rsrc_t r, unsigned v, unsigned s) { return __builtin_amdgcn_raw_buffer_load_b128(r, v, s, SC1); }
DI void bst16(rsrc_t r, u32x4 d, unsigned v, unsigned s) {
    __builtin_amdgcn_raw_buffer_store_b128(d, r, v, s, SC1);
    asm volatile("s_nop 1" ::"v"(d) : "memory");
}
DI void bst8(rsrc_t r, u32x2 d, unsigned v, unsigned s) { __builtin_amdgcn_raw_buffer_store_b64(d, r, v, s, SC1); }

DI void set_flag(char* ws, unsigned off, u64 tag) { __hip_atomic_store((GAS u64*)(ws + off), tag, __ATOMIC_RELAXED, __HIP_MEMORY_SCOPE_AGENT); }
DI void poll(char* ws, unsigned arr, int index, u64 tag) {
    const GAS u64* p = (const GAS u64*)(ws + arr + index * 128);
    bool ok = index < 0;
#pragma unroll 1
    for (int spin = 0; spin < (1 << 18); ++spin) {
        if (!ok) ok = __hip_atomic_load(p, __ATOMIC_RELAXED, __HIP_MEMORY_SCOPE_AGENT) == tag;
        if (__all(ok)) break;
        __builtin_amdgcn_s_sleep(1);
    }
    asm volatile("" ::: "memory");
}
DI void copy_dev(const char* src, unsigned lds, int n, int hw, int lane) {
#pragma unroll 1
    for (int p = hw; p < n; p += 4) glds_dev(lane * 16, src + p * 1024, lds + p * 1024);
}

DI unsigned cnt_arrive(unsigned a, int lane) {
    asm volatile("s_waitcnt lgkmcnt(0)" ::: "memory");
    unsigned r = 0;
    if (lane == 0) r = __hip_atomic_fetch_add((LAS unsigned*)(size_t)a, 1u, __ATOMIC_RELAXED, __HIP_MEMORY_SCOPE_WORKGROUP);
    return __builtin_amdgcn_readfirstlane(r);
}
DI void cnt_wait(unsigned a, unsigned n) {
#pragma unroll 1
    while ((unsigned)__builtin_amdgcn_readfirstlane(*(volatile LAS unsigned*)(size_t)a) < n) __builtin_amdgcn_s_sleep(1);
    asm volatile("" ::: "memory");
}
DI float halfmax(float m) {
    auto r = __builtin_amdgcn_permlane32_swap(__float_as_uint(m), __float_as_uint(m), false, false);
    return fmaxf(__uint_as_float(r[0]), __uint_as_float(r[1]));
}
DI float upper(float v) {
    auto r = __builtin_amdgcn_permlane32_swap(__float_as_uint(v), __float_as_uint(v), false, false);
    return __uint_as_float(r[1]);
}
DI float max16(const f32x16& s) {
    float a = fmaxf(fmaxf(s[0], s[1]), s[2]), b = fmaxf(fmaxf(s[3], s[4]), s[5]);
    a = fmaxf(fmaxf(a, s[6]), s[7]); b = fmaxf(fmaxf(b, s[8]), s[9]);
    a = fmaxf(fmaxf(a, s[10]), s[11]); b = fmaxf(fmaxf(b, s[12]), s[13]);
    a = fmaxf(fmaxf(a, s[14]), s[15]);
    return fmaxf(a, b);
}
DI bool row_ok(const f32x16& o, float l, int h) {
    float am = 0.f;
#pragma unroll
    for (int i = 0; i < 8; ++i) am = fmaxf(am, fabsf(o[i]));
    if (h == 0) {
#pragma unroll
        for (int i = 8; i < 12; ++i) am = fmaxf(am, fabsf(o[i]));
    }
    return (l >= 1e-18f) && (l <= 1e18f) && (am <= 1e37f);
}
DI f32x16 attn_exact(const char* kv, u32x4 q1, unsigned q2lo, int lane, int vrow) {
    const int r32 = lane & 31, h = lane >> 5;
    const f32x16 zero16 = {0, 0, 0, 0, 0, 0, 0, 0, 0, 0, 0, 0, 0, 0, 0, 0};
    const u32x2 q2 = {q2lo, 0u};
    float m = -INFINITY;
    f32x16 o = zero16;
#pragma unroll 1
    for (int t = 0; t < 64; ++t) {
        const GAS char* tp = (const GAS char*)kv + t * KV_TILE;
        const u32x4 fk1 = *(const GAS u32x4*)(tp + lane * 16);
        const u32x2 fk2 = {*(const GAS unsigned*)(tp + 1024 + r32 * 8 + h * 4), 0u};
        const u32x4 fv0 = *(const GAS u32x4*)(tp + 1280 + (h * 21 + vrow) * 16), fv1 = *(const GAS u32x4*)(tp + 1280 + 672 + (h * 21 + vrow) * 16);
        f32x16 s = mfma32(fk1, q1, mfma32k8(fk2, q2, zero16));
        const float mn = fmaxf(m, halfmax(max16(s))), al = EX2(m - mn);
        m = mn;
        u32x4 p0, p1;
#pragma unroll
        for (int i = 0; i < 4; ++i) { p0[i] = pk2(EX2(s[2 * i] - m), EX2(s[2 * i + 1] - m)); p1[i] = pk2(EX2(s[8 + 2 * i] - m), EX2(s[9 + 2 * i] - m)); }
#pragma unroll
        for (int i = 0; i < 16; ++i) o[i] *= al;
        o = mfma32(fv1, p1, mfma32(fv0, p0, o));
    }
    return o;
}

DI void mma16(f32x4& c, const u32x4& a, const u32x4& b) {
    asm volatile("v_mfma_f32_16x16x32_bf16 %0, %1, %2, %0" : "+v"(c) : "v"(a), "v"(b));
}
DI void mma_done() { SBAR(); asm volatile("s_nop 7\n\ts_nop 1"); SBAR(); }
template <int NS> DI void zero_acc(f32x4 (&a)[NS]) {
#pragma unroll
    for (int s = 0; s < NS; ++s) { a[s] = f32x4{0, 0, 0, 0}; PIN(a[s]); }
}
#define LGKM(n) __builtin_amdgcn_s_waitcnt(0xC07F | ((n) << 8))
template <int K0, int NK, int KT, int NS, int WSTR, int LA, int RING, int GS, int ASTR, int NG>
DI void kloop(f32x4 (&acc)[2][NS], u32x4 (&act)[RING][2], u32x4 (&wf)[LA + 1][NS], unsigned wa, unsigned wn, rsrc_t rs, unsigned v0,
              unsigned v1, const unsigned (&gb)[NG]) {
#pragma unroll
    for (int k = 0; k < NK; ++k) {
        const int j = K0 + k, jn = j - 1 + RING;
        const unsigned na = k + LA < NK ? wa + (k + LA) * WSTR : wn + (k + LA - NK) * WSTR;
        LGKM((LA - 1) * NS);
        SBAR();
#pragma unroll
        for (int i = 0; i < 2 * NS; ++i) {
            mma16(acc[i & 1][i >> 1], wf[j % (LA + 1)][i >> 1], act[j % RING][i & 1]);
            SBAR();
            if (i < NS && j + LA < KT) { wf[(j + LA) % (LA + 1)][i] = lds16(na + i * 1024); SBAR(); }
            if (i == NS && j > 0 && jn < KT) { act[(j - 1) % RING][0] = bld16(rs, v0, gb[jn < KT ? jn / GS : 0] + (jn % GS) * ASTR); SBAR(); }
            if (i == NS + 1 && j > 0 && jn < KT) { act[(j - 1) % RING][1] = bld16(rs, v1, gb[jn < KT ? jn / GS : 0] + (jn % GS) * ASTR); SBAR(); }
        }
    }
}
template <int RING, int GS, int ASTR, int NG>
DI void kstart(u32x4 (&act)[RING][2], rsrc_t rs, unsigned v0, unsigned v1, const unsigned (&gb)[NG]) {
#pragma unroll
    for (int k = 0; k < RING; ++k) {
        act[k][0] = bld16(rs, v0, gb[k / GS] + (k % GS) * ASTR); SBAR();
        act[k][1] = bld16(rs, v1, gb[k / GS] + (k % GS) * ASTR); SBAR();
    }
}
template <int NG> DI void rot_bases(unsigned (&gb)[NG], unsigned base, int rot, int stride) {
#pragma unroll
    for (int i = 0; i < NG; ++i) { const int q = i + rot; gb[i] = base + (q >= NG ? q - NG : q) * stride; asm volatile("" : "+s"(gb[i])); }
}
DI void send_half(const f32x4 (&a)[5], unsigned dst) {
#pragma unroll
    for (int s = 0; s < 5; ++s) *(LAS f32x4*)(size_t)(dst + s * 1024) = a[s];
}
DI void recv_half(f32x4 (&a)[5], unsigned src) {
#pragma unroll
    for (int s = 0; s < 5; ++s) a[s] += ldsf4(src + s * 1024);
}
DI void ldsw16(unsigned a, const u32x4& v) { *(LAS u32x4*)(size_t)a = v; }
DI f32x4 unpk(unsigned r0, unsigned r1) { return f32x4{bflo(r0), bfhi(r0), bflo(r1), bfhi(r1)}; }
DI void res_init(f32x4 (&a)[5], const u32x4& x0, const u32x4& x1, const u32x2& x2) {
    a[0] = unpk(x0[0], x0[1]); a[1] = unpk(x0[2], x0[3]); a[2] = unpk(x1[0], x1[1]); a[3] = unpk(x1[2], x1[3]); a[4] = unpk(x2[0], x2[1]);
#pragma unroll
    for (int s = 0; s < 5; ++s) PIN(a[s]);
}
DI void res_pack(u32x4& x0, u32x4& x1, u32x2& x2, const f32x4 (&a)[5]) {
    x0 = u32x4{pk2(a[0][0], a[0][1]), pk2(a[0][2], a[0][3]), pk2(a[1][0], a[1][1]), pk2(a[1][2], a[1][3])};
    x1 = u32x4{pk2(a[2][0], a[2][1]), pk2(a[2][2], a[2][3]), pk2(a[3][0], a[3][1]), pk2(a[3][2], a[3][3])};
    x2 = u32x2{pk2(a[4][0], a[4][1]), pk2(a[4][2], a[4][3])};
}
enum { C_WKV, C_X, C_XUSED, C_WQ, C_CSP = C_WQ + 5, C_CS, C_KVST, C_NUM };
enum { M_LDS = 512, M_O = 516, M_H = 520, M_F = 524 };

__global__ __launch_bounds__(512, 2) void k_block(const char* __restrict__ x, const char* __restrict__ wq, const char* __restrict__ wk,
                                                  const char* __restrict__ wv, const char* __restrict__ wo, const char* __restrict__ w1,
                                                  const char* __restrict__ w2, char* ws, char* __restrict__ out) {
    extern __shared__ __attribute__((aligned(16))) char smem[];
    const unsigned L = (unsigned)(size_t)(LAS char*)smem;
    const int lane = threadIdx.x & 63, w = __builtin_amdgcn_readfirstlane(threadIdx.x >> 6);
    const int xg = blockIdx.x & 7, idx = blockIdx.x >> 3;
    const int tb = (xg >> 1) * 8 + (idx >> 2), m = (xg & 1) * 4 + (idx & 3), cid = tb * 8 + m;
    const int ct = (m >> 2) * 8 + (tb & 1) * 4 + (m & 3);
    const int g = lane >> 4, c16 = lane & 15;
    const rsrc_t rs = __builtin_amdgcn_make_buffer_rsrc(ws, 0, WS_END, 0x00020000);
    const u32x4 ones = {ONES2, ONES2, ONES2, ONES2};
    const f32x4 z4 = {0, 0, 0, 0};
    STAMP_INIT();

    u64 tag;
    asm volatile("global_load_dwordx2 %0, %1, %2 sc1" : "=v"(tag) : "v"(0u), "s"(ws + WS_FL) : "memory");
#define MAKE_TAG() do { PIN(tag); tag = (tag ^ 0x9E3779B97F4A7C15ull) * 0xBF58476D1CE4E5B9ull + 0x632BE59BD9B4E019ull; } while (0)

#define CNT(i) (L + L_CNT + (i) * 4)
#define WQ_AT(r) ((r) < 2 ? L_WQ1 + (r) * 20480 : L_WQ2 + ((r) - 2) * 20480)
    const unsigned wrow = L + (8 * g + (c16 >> 2)) * 160 + (lane & 3) * 8, wo0 = wrow + (g & 1) * 640, wo1 = wrow + ((g & 1) ^ 1) * 640;
    const int rotc = m & 3, rotd = (ct & 7) % 5, rote = (m & 3) * 2;

    if (w >= 4) {
        const int hw = w - 4;
        __builtin_amdgcn_s_setprio(2);
        if (m < 4) {
            const char* src = m < 2 ? wk : wv;
#pragma unroll 1
            for (int p = hw; p < 25; p += 4) glds(lane * 16, src + p * 1024, L + L_WKV + p * 1024);
        }
        {
            const unsigned vx0 = (lane >> 3) * 1280 + (((lane & 7) ^ g) << 4), vx1 = (8 + (lane >> 3)) * 1280 + (((lane & 7) ^ (4 + g)) << 4);
            const char* xb = x + (size_t)(tb * 64 + hw * 16) * 1280;
            int sg = (m & 3) * 2;
#pragma unroll 1
            for (int i = 0; i < 10; ++i) {
                glds(vx0, xb + sg * 128, L + L_X + (hw * 20 + sg) * 1024);
                glds(vx1, xb + sg * 128, L + L_X + (hw * 20 + 10 + sg) * 1024);
                sg = sg == 9 ? 0 : sg + 1;
                if (i == 0) bar_raw();
                if (i == 4) { wait_vm<10>(); cnt_arrive(CNT(C_WKV), lane); }
            }
        }
        {
            unsigned vw[5];
#pragma unroll
            for (int i = 0; i < 5; ++i) {
                const int ci = i * 64 + lane, pos = ci / 10, ch = ci - pos * 10, k = pos ^ (((pos >> 3) & 1) << 2);
                vw[i] = k * 1280 + ch * 16;
            }
            const char* wb = wq + m * 160 + hw * 40960;
            f32x4 cs[5];
            u32x4 one = ones;
            PIN(one);
            zero_acc(cs);
            auto wqload = [&](int r) {
#pragma unroll
                for (int i = 0; i < 5; ++i) glds(vw[i], wb + r * 163840, L + WQ_AT(r) + hw * 5120 + i * 1024);
            };
            auto wqdone = [&](int r) {
                cnt_arrive(CNT(C_WQ + r), lane);
                u32x4 f[5];
#pragma unroll
                for (int s = 0; s < 5; ++s) f[s] = ldstr2(wo0 + WQ_AT(r) + hw * 5120 + s * 32, wo1 + WQ_AT(r) + hw * 5120 + s * 32);
#pragma unroll
                for (int s = 0; s < 5; ++s) mma16(cs[s], f[s], one);
            };
            wqload(0);
            wait_vm<5>();
            cnt_arrive(CNT(C_X), lane);
            STAMP(1);
            wqload(1);
            MAKE_TAG();
            cnt_wait(CNT(C_XUSED), 4);
            wqload(2); wait_vm<10>(); wqdone(0);
            wqload(3); wait_vm<10>(); wqdone(1);
            wqload(4); wait_vm<10>(); wqdone(2);
            wait_vm<5>(); wqdone(3);
            wait_vm<0>(); wqdone(4);
            mma_done();
            if (c16 == 0) {
#pragma unroll
                for (int s = 0; s < 5; ++s) *(LAS f32x4*)(size_t)(L + L_CSX + hw * 320 + s * 64 + g * 16) = cs[s];
            }
            if (cnt_arrive(CNT(C_CSP), lane) == 3) {
                const unsigned ca = L + L_CSX + (lane < 20 ? lane : 0) * 16;
                const f32x4 c4 = (ldsf4(ca) + ldsf4(ca + 320)) + (ldsf4(ca + 640) + ldsf4(ca + 960));
                *(LAS f32x4*)(size_t)(ca + 1280) = c4;
                cnt_arrive(CNT(C_CS), lane);
            }
        }
        const char* kv = ws + WS_KV;
        STAMP(2);
        poll(ws, FL_KV, lane < 32 ? lane : hw * 32 + lane - 32, tag);
        STAMP(3);
        copy_dev(kv, L, 41, hw, lane);
        unsigned pv[7], pdst, pcs = 0;
        const char* psrc;
        int pmat, pc;
        {
            int u = blockIdx.x * 4 + hw, ks;
            pmat = u < 160 ? 0 : (u < 480 ? 1 : (u < 912 ? 2 : 3));
            if (pmat == 0) { pc = u / 20; ks = u - pc * 20; psrc = wo + pc * 160; pdst = WS_WO + u * 5120; }
            else if (pmat == 1) { u -= 160; pc = u / 20; ks = u - pc * 20; psrc = w1 + pc * 224; pdst = WS_W1 + u * 7168; pcs = WS_CS + (ks * 1792 + pc * 112) * 4; }
            else { u = pmat == 2 ? u - 480 : 0; pc = u / 54; ks = u - pc * 54; psrc = w2 + pc * 160; pdst = WS_W2 + u * 5120; }
            int rb, a3, a2, a1;
            if (pmat == 0) { if (ks < 16) { rb = 40 * ks; a3 = 20; a2 = 4; a1 = 8; } else { rb = 160 * (ks - 16) + 16; a3 = 80; a2 = 40; a1 = 20; } }
            else if (pmat == 1) { if (ks < 16) { rb = 80 * (ks >> 1) + 32 * (ks & 1); a3 = 16; a2 = 8; a1 = 4; } else { rb = 160 * (ks - 16) + 64; a3 = 8; a2 = 4; a1 = 80; } }
            else { a3 = 8; a2 = 4; if (ks < 46) { rb = 112 * (ks / 3) + 32 * (ks % 3); a1 = 16; } else { rb = 224 * (ks - 46) + 96; a1 = ks < 53 ? 112 : 48; } }
            const int nch = pmat == 1 ? 14 : 10, ld = pmat == 1 ? 3456 : 1280, chmax = (pmat == 1 && pc == 15) ? 5 : 13;
#pragma unroll
            for (int i = 0; i < 7; ++i) {
                const int ci = i * 64 + lane, pos = (pmat == 1 ? ci / 14 : ci / 10) & 31, ch = min(ci - pos * nch, chmax);
                pv[i] = (rb + a3 * (pos >> 4) + a2 * ((pos >> 3) & 1) + a1 * ((pos >> 2) & 1) + (pos & 3)) * ld + ch * 16;
                PIN(pv[i]);
            }
        }
        wait_vm<0>();
        STAMP(4);
        bar_raw();
        copy_dev(kv + CHUNK, L + CHUNK, 82, hw, lane);
        wait_vm<0>();
        bar_raw();
        if (pmat < 3) {
            const unsigned pb = L + L_AUX + hw * 7168;
#pragma unroll
            for (int i = 0; i < 5; ++i) glds(pv[i], psrc, pb + i * 1024);
            if (pmat == 1) {
                glds(pv[5], psrc, pb + 5120); glds(pv[6], psrc, pb + 6144);
                wait_vm<0>();
                const unsigned a0 = pb + (8 * g + (c16 >> 2)) * 224 + (lane & 3) * 8;
#pragma unroll
                for (int s = 0; s < 7; ++s) {
                    const unsigned a1 = a0 + (s == 6 ? (pc == 15 ? 64 : 192) : s * 32);
                    const u32x4 f = ldstr2(a1, a1 + 4 * 224);
                    bst16(rs, f, lane * 16, pdst + s * 1024);
                    const f32x4 cs = mfma16(f, ones, z4);
                    bst16(rs, __builtin_bit_cast(u32x4, cs), c16 == 0 ? g * 16 : 0x80000000u, pcs + s * 64);
                }
            } else {
                wait_vm<0>();
                const unsigned a0 = pb + (8 * g + (c16 >> 2)) * 160 + (lane & 3) * 8, a1 = a0 + (lane & 3) * 8;
#pragma unroll
                for (int s = 0; s < 5; ++s) {
                    const unsigned a2 = s < 4 ? a1 + (s >> 1) * 64 + (s & 1) * 8 : a0 + 128;
                    bst16(rs, ldstr2(a2, a2 + 4 * 160), lane * 16, pdst + s * 1024);
                }
            }
        }
        wait_vm<0>();
        STAMP(5);
        bar_raw();
        if (hw == 0 && lane == 0) set_flag(ws, FL_PR + cid * 128, tag);
        copy_dev(kv + 3 * CHUNK, L, 41, hw, lane);
        poll(ws, FL_PR, hw * 64 + lane, tag);
        wait_vm<0>();
        STAMP(6);
        bar_raw();
        if (blockIdx.x == 0 && hw == 0 && lane == 0) set_flag(ws, WS_FL, tag);
        if (hw == 3) {
            u32x4 c[20];
            const int l28 = lane < 28 ? lane : 0;
#pragma unroll
            for (int ks = 0; ks < 20; ++ks) c[ks] = bld16(rs, l28 * 16, WS_CS + (ks * 1792 + ct * 112) * 4);
            f32x4 c4 = z4;
#pragma unroll
            for (int ks = 0; ks < 20; ++ks) c4 += __builtin_bit_cast(f32x4, c[ks]);
            if (lane == 32) c4 = z4;
            if (lane < 28 || lane == 32) *(LAS f32x4*)(size_t)(L + L_MISC + lane * 16) = c4;
        }
        auto wocopy = [&](int p0, int p1) {
#pragma unroll 1
            for (int p = p0 + hw; p < p1; p += 4) {
                const int q = p + rotc * 20;
                glds_dev(lane * 16, ws + WS_WO + m * 102400 + (q >= 100 ? q - 100 : q) * 1024, L + L_WO + p * 1024);
            }
        };
        wocopy(0, 41);
        u32x4 r1[35], r2[9][8];
        unsigned g1[5], g2[9];
        rot_bases(g1, WS_W1 + ct * 143360, rotd, 28672);
        rot_bases(g2, WS_W2 + m * 276480, rote, 30720);
        const unsigned vp = lane * 16 + hw * 1024, vq = lane * 16 + min(28 + hw, 29) * 1024;
        unsigned lp0 = L + vp, lp1 = lp0 + 61440, lp2 = lp0 + 122880, lq0 = L + vq, lq1 = lq0 + 61440, lq2 = lq0 + 122880;
        PIN(lp0); PIN(lp1); PIN(lp2); PIN(lq0); PIN(lq1); PIN(lq2);
#define LD2(G, THR) do { _Pragma("unroll") for (int j_ = 0; j_ < 7; ++j_) { r2[G][j_] = bld16(rs, vp, g2[G] + j_ * 4096); wait_vm<THR>(); } \
        r2[G][7] = bld16(rs, vq, g2[G]); wait_vm<THR>(); } while (0)
#define ST2(G) do { constexpr int sl_ = (G) % 5; const unsigned bp_ = (sl_ < 2 ? lp0 : sl_ < 4 ? lp1 : lp2) + (sl_ & 1) * 30720, \
        bq_ = (sl_ < 2 ? lq0 : sl_ < 4 ? lq1 : lq2) + (sl_ & 1) * 30720; \
        _Pragma("unroll") for (int j_ = 0; j_ < 7; ++j_) ldsw16(bp_ + j_ * 4096, r2[G][j_]); \
        ldsw16(bq_, r2[G][7]); SBAR(); } while (0)
        LD2(5, 63); LD2(6, 63);
        bar();
        wocopy(41, 100);
#pragma unroll
        for (int i = 0; i < 35; ++i) r1[i] = bld16(rs, vp, g1[i / 7] + (i % 7) * 4096);
        SBAR();
        {
            u32x4 t[9];
#pragma unroll
            for (int i = 0; i < 9; ++i) t[i] = bld16(rs, lane * 16, WS_W2 + (xg & 1) * 1105920 + min((idx * 4 + hw) * 9 + i, 1079) * 1024);
#pragma unroll
            for (int i = 0; i < 9; ++i) asm volatile("" ::"v"(t[i]));
        }
        wait_vm<0>();
        STAMP(7);
        bar_raw();
        if (hw == 0) { poll(ws, FL_O, lane < 32 ? tb * 32 + lane : -1, tag); cnt_arrive(L + L_MISC + M_O, lane); }
#pragma unroll
        for (int i = 0; i < 8; ++i) ldsw16(lp0 + i * 4096, r1[i]);
        SBAR();
        LD2(7, THROT);
        bar();
        cnt_wait(L + L_MISC + M_LDS, 4);
#pragma unroll
        for (int i = 8; i < 35; ++i) ldsw16((i < 15 ? lp0 : i < 30 ? lp1 : lp2) + (i % 15) * 4096, r1[i]);
        SBAR();
        if (hw == 0) { poll(ws, FL_H, (tb >> 1) * 64 + lane, tag); cnt_arrive(L + L_MISC + M_H, lane); }
        STAMP(8);
        bar();
        LD2(8, THROT); LD2(0, THROT); LD2(1, THROT);
        STAMP(9);
        bar_raw();
        ST2(0); ST2(1);
        if (hw == 0) {
            poll(ws, FL_F, lane < 32 ? ((tb >> 1) * 16 + (lane >> 1)) * 4 + (tb & 1) * 2 + (lane & 1) : -1, tag);
            cnt_arrive(L + L_MISC + M_F, lane);
        } else {
            const unsigned gs = hw == 1 ? g2[2] : (hw == 2 ? g2[3] : g2[4]);
#pragma unroll 1
            for (int p = 0; p < 30; ++p) { glds_dev(lane * 16, ws + gs + p * 1024, L + (hw + 1) * 30720 + p * 1024); wait_vm<THROT2>(); }
        }
        wait_vm<0>();
        STAMP(10);
        bar();
        bar_raw(); ST2(5);
        bar(); ST2(6);
        bar(); ST2(7);
        bar(); ST2(8);
        bar();
        STAMP_OUT(1);
        return;
    }

    const int r32 = lane & 31, h = lane >> 5;
    u32x4 xr0, xr1; u32x2 xr2;
    u32x4 qa1, qb1; u32x2 qa2, qb2;
    const int kh = w & 1;
    {
        u32x4 xf[20], kf[20];
        if (w == 0 && lane < C_NUM) *(LAS unsigned*)(size_t)CNT(lane) = 0u;
        bar();
        const unsigned xrow = L + L_X + w * 20480 + (c16 >> 3) * 10240 + (c16 & 7) * 128;
        const int sw = c16 >> 1, t32 = (w & 1) * 16 + c16;
        unsigned xa = xrow + ((g ^ sw) << 4), xb = xrow + (((4 + g) ^ sw) << 4), xra[3], qw[5], kvo;
        {
            const int c0 = m * 10 + g, c1 = c0 + 4, c2 = m * 10 + 8 + (g >> 1);
            xra[0] = xrow + (c0 >> 3) * 1024 + (((c0 & 7) ^ sw) << 4);
            xra[1] = xrow + (c1 >> 3) * 1024 + (((c1 & 7) ^ sw) << 4);
            xra[2] = xrow + (c2 >> 3) * 1024 + (((c2 & 7) ^ sw) << 4) + (g & 1) * 8;
#pragma unroll
            for (int s = 0; s < 5; ++s) {
                const int f0 = s * 16 + 4 * g, hh = f0 / 20, d0 = f0 - hh * 20;
                qw[s] = L + L_QX + (hh * 2 + (w >> 1)) * 1280 + (d0 < 16 ? ((d0 >> 3) * 32 + t32) * 16 + (d0 & 4) * 2 : 1024 + t32 * 8);
                PIN(qw[s]);
            }
            const int d = (m & 1) * 16 + c16;
            kvo = m == 0 ? ((g >> 1) * 32 + t32) * 16 + (g & 1) * 8
                : m == 1 ? (g == 0 ? 1024 + t32 * 8 : 0x80000000u)
                         : (d <= 20 ? 1280 + (((w & 1) * 2 + (g & 1)) * 21 + d) * 16 + (g >> 1) * 8 : 0x80000000u);
            PIN(xa); PIN(xb); PIN(xra[0]); PIN(xra[1]); PIN(xra[2]); PIN(kvo);
        }
        f32x4 ka = z4, kc = z4, ssm = z4, ssq = z4, acc[5];
        zero_acc(acc);
        if (m < 4) {
            cnt_wait(CNT(C_WKV), 4);
            const unsigned a0 = L + L_WKV + (8 * g + (c16 >> 2)) * 40 + (m & 1) * 32 + (lane & 3) * 8;
#pragma unroll
            for (int j = 0; j < 20; ++j) kf[j] = ldstr2(a0 + j * 1280, a0 + j * 1280 + 160);
            if (m < 2) {
#pragma unroll
                for (int j = 0; j < 20; ++j) kc = mfma16(kf[j], ones, kc);
            } else {
#pragma unroll
                for (int j = 0; j < 20; ++j) kc = mfma16(ones, kf[j], kc);
            }
            PIN(kc);
        }
        cnt_wait(CNT(C_X), 4);
#pragma unroll
        for (int j = 0; j < 10; ++j) { xf[2 * j] = lds16(xa + j * 1024); xf[2 * j + 1] = lds16(xb + j * 1024); }
        xr0 = lds16(xra[0]); xr1 = lds16(xra[1]); xr2 = lds8(xra[2]);
        SBAR();
#pragma unroll
        for (int j = 0; j < 8; ++j) { ssm = mfma16(ones, xf[j], ssm); ssq = mfma16(xf[j], xf[j], ssq); }
        PIN(ssm); PIN(ssq);
#pragma unroll
        for (int j = 0; j < 20; ++j) PIN(xf[j]);
        PIN(xr0); PIN(xr1); PIN(xr2);
        cnt_arrive(CNT(C_XUSED), lane);
        STAMP(1);
#pragma unroll
        for (int j = 8; j < 20; ++j) { ssm = mfma16(ones, xf[j], ssm); ssq = mfma16(xf[j], xf[j], ssq); }
        PIN(ssm); PIN(ssq);
        mma_done();
        if (m < 2) {
#pragma unroll
            for (int j = 0; j < 20; ++j) ka = mfma16(kf[j], xf[j], ka);
        } else if (m < 4) {
#pragma unroll
            for (int j = 0; j < 20; ++j) ka = mfma16(xf[j], kf[j], ka);
        }
        const float sd = (c16 & 2) ? ((c16 & 1) ? ssq[3] : ssq[2]) : ((c16 & 1) ? ssq[1] : ssq[0]);
        const float mean = ssm[0] * (1.f / D), rstd = rsqrtf(fmaxf(__shfl(sd, (c16 >> 2) * 16 + c16) * (1.f / D) - mean * mean, 0.f) + LN_EPS);
        if (m < 4) {
            u32x2 pv;
            if (m < 2) {
                pv = u32x2{pk2((ka[0] - mean * kc[0]) * rstd, (ka[1] - mean * kc[1]) * rstd), pk2((ka[2] - mean * kc[2]) * rstd, (ka[3] - mean * kc[3]) * rstd)};
            } else {
                float v[4];
#pragma unroll
                for (int r = 0; r < 4; ++r) v[r] = (ka[r] - __shfl(mean, 4 * g + r) * kc[0]) * __shfl(rstd, 4 * g + r);
                pv = u32x2{pk2(v[0], v[1]), pk2(v[2], v[3])};
                if (m == 3 && c16 == 4) pv = u32x2{ONES2, ONES2};
            }
            bst8(rs, pv, kvo, WS_KV + (tb * 2 + (w >> 1)) * KV_TILE);
        }
        STAMP(2);
        u32x4 f[2][5];
#pragma unroll
        for (int r = 0; r < 5; ++r) {
            unsigned b0 = wo0 + WQ_AT(r), b1 = wo1 + WQ_AT(r);
            PIN(b0); PIN(b1);
            cnt_wait(CNT(C_WQ + r), 4);
#pragma unroll
            for (int s = 0; s < 5; ++s) f[0][s] = ldstr2(b0 + s * 32, b1 + s * 32);
            SBAR();
#pragma unroll
            for (int j = 0; j < 4; ++j) {
#pragma unroll
                for (int s = 0; s < 5; ++s) {
                    if (j < 3) f[(j + 1) & 1][s] = ldstr2(b0 + (j + 1) * 5120 + s * 32, b1 + (j + 1) * 5120 + s * 32);
                    SBAR();
                    mma16(acc[s], f[j & 1][s], xf[4 * r + j]);
                    SBAR();
                }
            }
            if (r == 1) {
                wait_vm<0>();
                MAKE_TAG();
                if (cnt_arrive(CNT(C_KVST), lane) == 3 && m < 4 && lane == 0) set_flag(ws, FL_KV + (tb * 4 + m) * 128, tag);
            }
        }
        STAMP(3);
        cnt_wait(CNT(C_KVST), 4);
        cnt_wait(CNT(C_CS), 1);
        mma_done();
        const float sc = rstd * QSCALE, ms = -mean * sc;
#pragma unroll
        for (int s = 0; s < 5; ++s) {
            const f32x4 v = acc[s] * sc + ldsf4(L + L_CSX + 1280 + s * 64 + g * 16) * ms;
            *(LAS u32x2*)(size_t)qw[s] = u32x2{pk2(v[0], v[1]), pk2(v[2], v[3])};
        }
        bar();
        const unsigned qb = L + L_QX + w * 2560;
        qa1 = lds16(qb + lane * 16); qb1 = lds16(qb + 1280 + lane * 16);
        qa2 = u32x2{lds4(qb + 1024 + r32 * 8 + h * 4), 0u}; qb2 = u32x2{lds4(qb + 1280 + 1024 + r32 * 8 + h * 4), 0u};
    }
    STAMP(4);
    {
        const int vrow = r32 <= 20 ? r32 : (r32 < 28 ? 20 : 4);
        const unsigned kconst = h == 0 ? 0x00003f80u : 0u;
        unsigned ak1 = L + lane * 16, ak2 = L + 1024 + r32 * 8 + h * 4, av = L + 1280 + (h * 21 + vrow) * 16;
        const f32x16 zero16 = {0, 0, 0, 0, 0, 0, 0, 0, 0, 0, 0, 0, 0, 0, 0, 0};
        f32x16 sa, sb, oa = zero16, ob = zero16;
        u32x4 ka1 = lds16(ak1), kb1 = lds16(ak1 + KV_TILE), va0 = lds16(av), va1 = lds16(av + 672), vb0 = va0, vb1 = va0;
        u32x2 ka2 = {lds4(ak2), kconst}, kb2 = {lds4(ak2 + KV_TILE), kconst};
        sa = mfma32(ka1, qa1, mfma32k8(ka2, qa2, zero16));
        sb = mfma32(ka1, qb1, mfma32k8(ka2, qb2, zero16));
        {
            const float ma = halfmax(max16(sa)), mb = halfmax(max16(sb));
            qa2[1] = h == 0 ? (pk2(-ma, 0.f) & 0xffffu) : 0u;
            qb2[1] = h == 0 ? (pk2(-mb, 0.f) & 0xffffu) : 0u;
        }
        sa = mfma32(ka1, qa1, mfma32k8(ka2, qa2, zero16));
        u32x4 pa0 = {0u, 0u, 0u, 0u}, pa1 = pa0, pb0 = pa0, pb1 = pa0;
        float e2 = 0.f, e3 = 0.f;
        unsigned vo0 = (r32 >> 4) * 20480 + h * 256 + (r32 & 15) * 16, vo1 = g * 20480 + c16 * 16;
        PIN(pa0); PIN(pa1); PIN(pb0); PIN(pb1); PIN(ka2); PIN(kb2); PIN(vo0); PIN(vo1);
#define SLOT(R, MF, X, S, i, PP, jp, P, j) do { R = MF; PIN(R); SBAR(); X; SBAR(); \
        float e0 = EX2(S[i]), e1 = EX2(S[i + 1]); PIN(e0); PIN(e1); SBAR(); \
        PP[jp] = pk2(e2, e3); PIN(PP); SBAR(); \
        e2 = EX2(S[i + 2]); e3 = EX2(S[i + 3]); PIN(e2); PIN(e3); SBAR(); \
        P[j] = pk2(e0, e1); PIN(P); SBAR(); } while (0)
#define STEP(KC1, KC2, KN1, KN2, VC0, VC1, VP0, VP1, AK1, AK2, AV) do { \
        SLOT(sb, mfma32k8(KC2, qb2, zero16), (void)0, sa, 0, pb1, 3, pa0, 0); \
        SLOT(sb, mfma32(KC1, qb1, sb), __builtin_amdgcn_s_waitcnt(0xC07F), sa, 4, pa0, 1, pa0, 2); \
        SLOT(ob, mfma32(VP1, pb1, ob), KC1 = lds16(AK1), sa, 8, pa0, 3, pa1, 0); \
        SLOT(oa, mfma32(VC0, pa0, oa), KC2[0] = lds4(AK2), sa, 12, pa1, 1, pa1, 2); \
        SLOT(sa, mfma32k8(KN2, qa2, zero16), (void)0, sb, 0, pa1, 3, pb0, 0); \
        SLOT(sa, mfma32(KN1, qa1, sa), VP0 = lds16(AV), sb, 4, pb0, 1, pb0, 2); \
        SLOT(oa, mfma32(VC1, pa1, oa), VP1 = lds16((AV) + 672), sb, 8, pb0, 3, pb1, 0); \
        SLOT(ob, mfma32(VC0, pb0, ob), (void)0, sb, 12, pb1, 1, pb1, 2); } while (0)
        STAMP(5);
#pragma unroll 1
        for (int hc = 0; hc < 8; ++hc) {
            if ((0x6a >> hc) & 1) bar_raw();
            const int dn = hc == 5 ? 8 * KV_TILE - 3 * CHUNK : 8 * KV_TILE;
            const unsigned nk1 = ak1 + dn, nk2 = ak2 + dn, nv = av + dn;
            SBAR();
#pragma unroll
            for (int j = 0; j < 8; j += 2) {
                STEP(ka1, ka2, kb1, kb2, va0, va1, vb0, vb1, j < 6 ? ak1 + (j + 2) * KV_TILE : nk1, j < 6 ? ak2 + (j + 2) * KV_TILE : nk2,
                     av + (j + 1) * KV_TILE);
                STEP(kb1, kb2, ka1, ka2, vb0, vb1, va0, va1, j < 6 ? ak1 + (j + 3) * KV_TILE : nk1 + KV_TILE,
                     j < 6 ? ak2 + (j + 3) * KV_TILE : nk2 + KV_TILE, j < 6 ? av + (j + 2) * KV_TILE : nv);
            }
            ak1 = nk1; ak2 = nk2; av = nv;
        }
        pb1[3] = pk2(e2, e3);
        ob = mfma32(vb1, pb1, ob);
        STAMP(6);
        float la = upper(oa[8]), lb = upper(ob[8]);
        if (__builtin_expect(!__all(row_ok(oa, la, h) && row_ok(ob, lb, h)), 0)) {
            __builtin_amdgcn_fence(__ATOMIC_ACQUIRE, "agent");
#pragma unroll 1
            for (int qt = 0; qt < 2; ++qt) {
                ob = attn_exact(ws + WS_KV, qt ? qb1 : qa1, qt ? qb2[0] : qa2[0], lane, vrow);
                if (qt == 0) oa = ob;
            }
            la = upper(oa[8]); lb = upper(ob[8]);
        }
        const float ra = __builtin_amdgcn_rcpf(la), rb = __builtin_amdgcn_rcpf(lb);
        const int hd = 4 * m + w;
        const unsigned so = WS_O + (tb * 80 + (hd >> 1)) * 1024 + (hd & 1) * 512;
        bst16(rs, u32x4{pk2(oa[0] * ra, oa[1] * ra), pk2(oa[2] * ra, oa[3] * ra), pk2(oa[4] * ra, oa[5] * ra), pk2(oa[6] * ra, oa[7] * ra)}, vo0, so);
        bst16(rs, u32x4{pk2(ob[0] * rb, ob[1] * rb), pk2(ob[2] * rb, ob[3] * rb), pk2(ob[4] * rb, ob[5] * rb), pk2(ob[6] * rb, ob[7] * rb)}, vo0, so + 40960);
        const auto s0 = __builtin_amdgcn_permlane32_swap(pk2(oa[8] * ra, oa[9] * ra), pk2(ob[8] * rb, ob[9] * rb), false, false);
        const auto s1 = __builtin_amdgcn_permlane32_swap(pk2(oa[10] * ra, oa[11] * ra), pk2(ob[10] * rb, ob[11] * rb), false, false);
        bst8(rs, u32x2{s0[0], s1[0]}, vo1, WS_O + (tb * 80 + 16 + (hd >> 3)) * 1024 + ((hd >> 1) & 3) * 256 + (hd & 1) * 8);
        STAMP(7);
        bar();
    }
#define PUBLISH(ARR) do { wait_vm<0>(); if (lane == 0) set_flag(ws, ARR + (cid * 4 + w) * 128, tag); } while (0)
    const unsigned wl = L + lane * 16;
    {
        f32x4 acc[2][5];
        u32x4 act[5][2], wf[3][5];
        unsigned wa = wl + L_WO + kh * 5120, wb = wa + 51200, gb[5];
        PIN(wa); PIN(wb);
#pragma unroll
        for (int s = 0; s < 5; ++s) wf[0][s] = lds16(wa + s * 1024);
#pragma unroll
        for (int s = 0; s < 5; ++s) wf[1][s] = lds16(wa + 10240 + s * 1024);
        PUBLISH(FL_O);
        res_init(acc[0], xr0, xr1, xr2); zero_acc(acc[1]);
        rot_bases(gb, WS_O + tb * 81920 + kh * 1024, rotc, 4096);
        unsigned v0 = lane * 16 + w * 20480, v1 = lane * 16 + (w ^ 1) * 20480;
        unsigned xs = wl + (w == 2 ? 35840 : 144384 + (w ^ 1) * 5120), xv = wl + (w == 3 ? 35840 : 144384 + w * 5120), sv = g == 0 ? c16 * 64 : 0x80000000u;
        PIN(v0); PIN(v1); PIN(xs); PIN(xv); PIN(sv);
        cnt_wait(L + L_MISC + M_O, 1);
        kstart<5, 2, 2048>(act, rs, v0, v1, gb);
        STAMP(8);
        kloop<0, 5, 10, 5, 10240, 2, 5, 2, 2048>(acc, act, wf, wa, wb, rs, v0, v1, gb);
        kloop<5, 5, 10, 5, 10240, 2, 5, 2, 2048>(acc, act, wf, wb, wa, rs, v0, v1, gb);
        mma_done();
        send_half(acc[1], xs);
        bar();
        recv_half(acc[0], xv);
        res_pack(xr0, xr1, xr2, acc[0]);
        const unsigned hb = WS_H + (tb * 4 + w) * 20480;
        bst16(rs, xr0, lane * 16, hb + 2 * m * 1024);
        bst16(rs, xr1, lane * 16, hb + (2 * m + 1) * 1024);
        bst8(rs, xr2, lane * 16, hb + (16 + (m >> 1)) * 1024 + (m & 1) * 8);
        float sm = 0.f, sq = 0.f, sm2 = 0.f, sq2 = 0.f;
#pragma unroll
        for (int i = 0; i < 4; ++i) { sm = dot2(xr0[i], ONES2, sm); sq = dot2(xr0[i], xr0[i], sq); sm2 = dot2(xr1[i], ONES2, sm2); sq2 = dot2(xr1[i], xr1[i], sq2); }
        sm = dot2(xr2[0], ONES2, sm); sq = dot2(xr2[0], xr2[0], sq); sm2 = dot2(xr2[1], ONES2, sm2); sq2 = dot2(xr2[1], xr2[1], sq2);
        sm = xsum16_32(sm + sm2); sq = xsum16_32(sq + sq2);
        bst8(rs, u32x2{__float_as_uint(sm), __float_as_uint(sq)}, sv, WS_ST + (tb * 64 + w * 16) * 64 + m * 8);
        SBAR();
        STAMP(9);
    }
    {
        f32x4 acc[2][7], c4[7];
        u32x4 act[6][2], wf[2][7], st[2];
        const int t16 = (tb >> 1) * 8 + 2 * w;
        unsigned wa = wl, wb = wl + 57344, wc = wl + 114688, gb[5], v0 = lane * 16, v1 = lane * 16 + 20480, sv = c16 * 64 + g * 16;
        PIN(wa); PIN(wb); PIN(wc); PIN(v1); PIN(sv);
#pragma unroll
        for (int s = 0; s < 7; ++s) { wf[0][s] = lds16(wa + s * 1024); c4[s] = ldsf4(L + L_MISC + s * 64 + g * 16); }
#pragma unroll
        for (int s = 0; s < 7; ++s) { PIN(c4[s]); PIN(wf[0][s]); }
        cnt_arrive(L + L_MISC + M_LDS, lane);
        PUBLISH(FL_H);
        zero_acc(acc[0]); zero_acc(acc[1]);
        rot_bases(gb, WS_H + t16 * 20480, rotd, 4096);
        cnt_wait(L + L_MISC + M_H, 1);
        kstart<6, 4, 1024>(act, rs, v0, v1, gb);
        st[0] = bld16(rs, sv, WS_ST + t16 * 1024); st[1] = bld16(rs, sv, WS_ST + t16 * 1024 + 1024);
        SBAR();
        STAMP(10);
        kloop<0, 3, 20, 7, 7168, 1, 6, 4, 1024>(acc, act, wf, wa, wa + 21504, rs, v0, v1, gb);
        bar_raw();
        kloop<3, 5, 20, 7, 7168, 1, 6, 4, 1024>(acc, act, wf, wa + 21504, wb, rs, v0, v1, gb);
        kloop<8, 8, 20, 7, 7168, 1, 6, 4, 1024>(acc, act, wf, wb, wc, rs, v0, v1, gb);
        float mu[2], rp[2];
#pragma unroll
        for (int t = 0; t < 2; ++t) {
            const float sm = xsum16_32(__uint_as_float(st[t][0]) + __uint_as_float(st[t][2]));
            const float sq = xsum16_32(__uint_as_float(st[t][1]) + __uint_as_float(st[t][3]));
            mu[t] = sm * (-1.f / D);
            rp[t] = rsqrtf(fmaxf(sq * (1.f / D) - mu[t] * mu[t], 0.f) + LN_EPS) * LOG2E;
        }
        PIN(mu[0]); PIN(mu[1]); PIN(rp[0]); PIN(rp[1]);
        SBAR();
        kloop<16, 4, 20, 7, 7168, 1, 6, 4, 1024>(acc, act, wf, wc, wa, rs, v0, v1, gb);
        bar_raw();
        mma_done();
        STAMP(11);
        auto silu4 = [&](int t, int s) {
            const f32x4 y = (acc[t][s] + mu[t] * c4[s]) * rp[t];
            f32x4 e;
#pragma unroll
            for (int r = 0; r < 4; ++r) e[r] = EX2(-y[r]);
            e = e * LOG2E + LOG2E;
#pragma unroll
            for (int r = 0; r < 4; ++r) e[r] = __builtin_amdgcn_rcpf(e[r]);
            e *= y;
            return u32x2{pk2(e[0], e[1]), pk2(e[2], e[3])};
        };
        const unsigned fb = WS_F + t16 * 55296;
#pragma unroll
        for (int t = 0; t < 2; ++t) bst8(rs, silu4(t, 6), lane * 16, fb + t * 55296 + (46 + (ct >> 1)) * 1024 + (ct & 1) * 8);
#pragma unroll
        for (int P = 0; P < 3; ++P) {
            if (P == 0 || ct < 15) {
#pragma unroll
                for (int t = 0; t < 2; ++t) {
                    const u32x2 lo = silu4(t, 2 * P), hi = silu4(t, 2 * P + 1);
                    bst16(rs, u32x4{lo[0], lo[1], hi[0], hi[1]}, lane * 16, fb + t * 55296 + (3 * ct + P) * 1024);
                }
            }
        }
        SBAR();
        STAMP(12);
    }
    {
        f32x4 acc[2][5];
        u32x4 act[6][2], wf[3][5];
        res_init(acc[0], xr0, xr1, xr2); zero_acc(acc[1]);
        unsigned a0 = wl + kh * 5120, a1 = a0 + 61440, a2 = a0 + 122880, gb[9];
        unsigned v0 = lane * 16 + w * 55296, v1 = lane * 16 + (w ^ 1) * 55296;
        PIN(a0); PIN(a1); PIN(a2); PIN(v0); PIN(v1);
        rot_bases(gb, WS_F + tb * 221184 + kh * 1024, rote, 6144);
        PUBLISH(FL_F);
        cnt_wait(L + L_MISC + M_F, 1);
        kstart<6, 3, 2048>(act, rs, v0, v1, gb);
        bar_raw();
#pragma unroll
        for (int s = 0; s < 5; ++s) wf[0][s] = lds16(a0 + s * 1024);
#pragma unroll
        for (int s = 0; s < 5; ++s) wf[1][s] = lds16(a0 + 10240 + s * 1024);
        SBAR();
        STAMP(13);
#define GRP(G, WA, WN) kloop<3 * G, 3, 27, 5, 10240, 2, 6, 3, 2048>(acc, act, wf, WA, WN, rs, v0, v1, gb)
        GRP(0, a0, a0 + 30720); bar_raw();
        GRP(1, a0 + 30720, a1); bar_raw();
        GRP(2, a1, a1 + 30720); bar_raw();
        GRP(3, a1 + 30720, a2); bar_raw();
        GRP(4, a2, a0); bar_raw();
        GRP(5, a0, a0 + 30720);
        GRP(6, a0 + 30720, a1);
        GRP(7, a1, a1 + 30720);
        GRP(8, a1 + 30720, a1);
        mma_done();
        send_half(acc[1], wl + 122880 + (w ^ 1) * 5120);
        bar();
        recv_half(acc[0], wl + 122880 + w * 5120);
        STAMP(14);
        res_pack(xr0, xr1, xr2, acc[0]);
        char* ob = out + (size_t)(tb * 64 + w * 16 + c16) * 1280 + m * 160;
        *(u32x4*)(ob + g * 16) = xr0;
        *(u32x4*)(ob + 64 + g * 16) = xr1;
        *(u32x2*)(ob + 128 + g * 8) = xr2;
    }
    STAMP_OUT(0);
}

extern "C" void kernel_launch(void* const* d_in, const int* in_sizes, int n_in, void* d_out, int out_size, void* d_ws, size_t ws_size,
                              hipStream_t stream) {
    (void)hipFuncSetAttribute((const void*)k_block, hipFuncAttributeMaxDynamicSharedMemorySize, LDS_BYTES);
    k_block<<<256, 512, LDS_BYTES, stream>>>((const char*)d_in[0], (const char*)d_in[1], (const char*)d_in[2], (const char*)d_in[3],
                                             (const char*)d_in[4], (const char*)d_in[6], (const char*)d_in[7], (char*)d_ws, (char*)d_out);
}
```

```cpp
#include <hip/hip_runtime.h>
#include <hip/hip_bf16.h>
#include <stdint.h>

typedef __attribute__((ext_vector_type(8))) __bf16 bf16x8;
typedef __attribute__((ext_vector_type(2))) __bf16 bf16x2;
typedef __attribute__((ext_vector_type(4))) short s16x4;
typedef __attribute__((ext_vector_type(4))) float f32x4;
typedef __attribute__((ext_vector_type(2))) float f32x2;
typedef __attribute__((ext_vector_type(16))) float f32x16;
typedef __attribute__((ext_vector_type(4))) unsigned u32x4;
typedef __attribute__((ext_vector_type(2))) unsigned u32x2;
typedef unsigned long long u64;
#define LAS __attribute__((address_space(3)))
#define GAS __attribute__((address_space(1)))
#define DI __device__ __forceinline__

constexpr int D = 640;
constexpr float LN_EPS = 1e-5f;
constexpr float LOG2E = 1.4426950408889634f;
constexpr float QSCALE = 0.22360679774997896f * LOG2E;

constexpr int KV_TILE = 2624;
constexpr unsigned WS_FL = 0;
constexpr unsigned FL_KV = 32768, FL_PR = 2 * 32768, FL_O = 4 * 32768, FL_H = 8 * 32768, FL_F = 12 * 32768;
constexpr unsigned WS_KV = 16 * 32768;
constexpr unsigned WS_WO = WS_KV + 64 * KV_TILE;
constexpr unsigned WS_W1 = WS_WO + 8 * 20 * 5 * 1024;
constexpr unsigned WS_W2 = WS_W1 + 16 * 20 * 7 * 1024;
constexpr unsigned WS_CS = WS_W2 + 8 * 54 * 5 * 1024;
constexpr unsigned WS_O  = WS_CS + 20 * 1792 * 4;
constexpr unsigned WS_H  = WS_O + 128 * 20 * 1024;
constexpr unsigned WS_F  = WS_H + 128 * 20 * 1024;
constexpr unsigned WS_ST = WS_F + 128 * 54 * 1024;
constexpr unsigned WS_END = WS_ST + 2048 * 64;

constexpr int CHUNK = 16 * KV_TILE;
constexpr int L_WKV = 0, L_X = 25664, L_WQ2 = 47104, L_WQ1 = 122880, L_CSX = 111104, L_QX = 152576, L_MISC = 162816;
constexpr int L_AUX = 3 * CHUNK, L_WO = CHUNK, L_CNT = 110592, LDS_BYTES = 163840;

#define ENCK 0
#define STAMP_INIT() u64 rt0_ = __builtin_amdgcn_s_memrealtime(); u64 rtk_ = rt0_; if (blockIdx.x == 0 && threadIdx.x == 0) __hip_atomic_store((GAS u64*)(ws + 256), rt0_, __ATOMIC_RELAXED, __HIP_MEMORY_SCOPE_AGENT)
#define STAMP(I) do { if ((I) == ENCK) { __builtin_amdgcn_sched_barrier(0); asm volatile("s_memrealtime %0\n\ts_waitcnt lgkmcnt(0)" : "=s"(rtk_) :: "memory"); __builtin_amdgcn_sched_barrier(0); } } while (0)
#define STAMP_OUT(K) do { if ((K) == 0) { rt0_ = __hip_atomic_load((GAS u64*)(ws + 256), __ATOMIC_RELAXED, __HIP_MEMORY_SCOPE_AGENT); rtk_ = rt0_; while (__builtin_amdgcn_s_memrealtime() - rt0_ < 6000 + (rtk_ - rt0_)) __builtin_amdgcn_s_sleep(2); } } while (0)

#define THROT 1
#define THROT2 63
DI void glds(unsigned voff, const void* sbase, unsigned lds) {
    asm volatile("s_mov_b32 m0, %2\n\ts_nop 0\n\tglobal_load_lds_dwordx4 %0, %1" ::"v"(voff), "s"(sbase), "s"(lds) : "memory");
}
DI void glds_dev(unsigned voff, const void* sbase, unsigned lds) {
    asm volatile("s_mov_b32 m0, %2\n\ts_nop 0\n\tglobal_load_lds_dwordx4 %0, %1 sc1" ::"v"(voff), "s"(sbase), "s"(lds) : "memory");
}
template <int N> DI void wait_vm() { asm volatile("s_waitcnt vmcnt(%0)" ::"i"(N) : "memory"); }
DI void bar() { asm volatile("s_waitcnt lgkmcnt(0)\n\ts_barrier" ::: "memory"); }
DI void bar_raw() { asm volatile("s_barrier" ::: "memory"); }
#define PIN(x) asm volatile("" : "+v"(x))
#define SBAR() __builtin_amdgcn_sched_barrier(0)
DI unsigned pk2(float a, float b) { bf16x2 v = {(__bf16)a, (__bf16)b}; return __builtin_bit_cast(unsigned, v); }
DI float bflo(unsigned u) { return __uint_as_float(u << 16); }
DI float bfhi(unsigned u) { return __uint_as_float(u & 0xffff0000u); }
DI f32x4 mfma16(u32x4 a, u32x4 b, f32x4 c) {
    return __builtin_amdgcn_mfma_f32_16x16x32_bf16(__builtin_bit_cast(bf16x8, a), __builtin_bit_cast(bf16x8, b), c, 0, 0, 0);
}
DI f32x16 mfma32(u32x4 a, u32x4 b, f32x16 c) {
    return __builtin_amdgcn_mfma_f32_32x32x16_bf16(__builtin_bit_cast(bf16x8, a), __builtin_bit_cast(bf16x8, b), c, 0, 0, 0);
}
DI f32x16 mfma32k8(u32x2 a, u32x2 b, f32x16 c) {
    return __builtin_amdgcn_mfma_f32_32x32x8bf16_1k(__builtin_bit_cast(s16x4, a), __builtin_bit_cast(s16x4, b), c, 0, 0, 0);
}
DI u32x4 lds16(unsigned a) { return *(const LAS u32x4*)(size_t)a; }
DI u32x2 lds8(unsigned a) { return *(const LAS u32x2*)(size_t)a; }
DI unsigned lds4(unsigned a) { return *(const LAS unsigned*)(size_t)a; }
DI f32x4 ldsf4(unsigned a) { return *(const LAS f32x4*)(size_t)a; }
DI u32x2 ldstr(unsigned a) { return __builtin_bit_cast(u32x2, __builtin_amdgcn_ds_read_tr16_b64_v4i16((LAS s16x4*)(size_t)a)); }
DI u32x4 ldstr2(unsigned a0, unsigned a1) { u32x2 lo = ldstr(a0), hi = ldstr(a1); return u32x4{lo[0], lo[1], hi[0], hi[1]}; }
DI float dot2(unsigned a, unsigned b, float c) {
    return __builtin_amdgcn_fdot2_f32_bf16(__builtin_bit_cast(bf16x2, a), __builtin_bit_cast(bf16x2, b), c, false);
}
DI float xsum16_32(float v) {
    const auto a = __builtin_amdgcn_permlane16_swap(__float_as_uint(v), __float_as_uint(v), false, false);
    v = __uint_as_float(a[0]) + __uint_as_float(a[1]);
    const auto b = __builtin_amdgcn_permlane32_swap(__float_as_uint(v), __float_as_uint(v), false, false);
    return __uint_as_float(b[0]) + __uint_as_float(b[1]);
}
constexpr unsigned ONES2 = 0x3f803f80u;
#define EX2(v) __builtin_amdgcn_exp2f(v)
typedef __amdgpu_buffer_rsrc_t rsrc_t;
constexpr int SC1 = 16;
DI u32x4 bld16(rsrc_t r, unsigned v, unsigned s) { return __builtin_amdgcn_raw_buffer_load_b128(r, v, s, SC1); }
DI void bst16(rsrc_t r, u32x4 d, unsigned v, unsigned s) {
    __builtin_amdgcn_raw_buffer_store_b128(d, r, v, s, SC1);
    asm volatile("s_nop 1" ::"v"(d) : "memory");
}
DI void bst8(rsrc_t r, u32x2 d, unsigned v, unsigned s) { __builtin_amdgcn_raw_buffer_store_b64(d, r, v, s, SC1); }

DI void set_flag(char* ws, unsigned off, u64 tag) { __hip_atomic_store((GAS u64*)(ws + off), tag, __ATOMIC_RELAXED, __HIP_MEMORY_SCOPE_AGENT); }
DI void poll(char* ws, unsigned arr, int index, u64 tag) {
    const GAS u64* p = (const GAS u64*)(ws + arr + index * 128);
    bool ok = index < 0;
#pragma unroll 1
    for (int spin = 0; spin < (1 << 18); ++spin) {
        if (!ok) ok = __hip_atomic_load(p, __ATOMIC_RELAXED, __HIP_MEMORY_SCOPE_AGENT) == tag;
        if (__all(ok)) break;
        __builtin_amdgcn_s_sleep(1);
    }
    asm volatile("" ::: "memory");
}
DI void copy_dev(const char* src, unsigned lds, int n, int hw, int lane) {
#pragma unroll 1
    for (int p = hw; p < n; p += 4) glds_dev(lane * 16, src + p * 1024, lds + p * 1024);
}

DI unsigned cnt_arrive(unsigned a, int lane) {
    asm volatile("s_waitcnt lgkmcnt(0)" ::: "memory");
    unsigned r = 0;
    if (lane == 0) r = __hip_atomic_fetch_add((LAS unsigned*)(size_t)a, 1u, __ATOMIC_RELAXED, __HIP_MEMORY_SCOPE_WORKGROUP);
    return __builtin_amdgcn_readfirstlane(r);
}
DI void cnt_wait(unsigned a, unsigned n) {
#pragma unroll 1
    while ((unsigned)__builtin_amdgcn_readfirstlane(*(volatile LAS unsigned*)(size_t)a) < n) __builtin_amdgcn_s_sleep(1);
    asm volatile("" ::: "memory");
}
DI float halfmax(float m) {
    auto r = __builtin_amdgcn_permlane32_swap(__float_as_uint(m), __float_as_uint(m), false, false);
    return fmaxf(__uint_as_float(r[0]), __uint_as_float(r[1]));
}
DI float upper(float v) {
    auto r = __builtin_amdgcn_permlane32_swap(__float_as_uint(v), __float_as_uint(v), false, false);
    return __uint_as_float(r[1]);
}
DI float max16(const f32x16& s) {
    float a = fmaxf(fmaxf(s[0], s[1]), s[2]), b = fmaxf(fmaxf(s[3], s[4]), s[5]);
    a = fmaxf(fmaxf(a, s[6]), s[7]); b = fmaxf(fmaxf(b, s[8]), s[9]);
    a = fmaxf(fmaxf(a, s[10]), s[11]); b = fmaxf(fmaxf(b, s[12]), s[13]);
    a = fmaxf(fmaxf(a, s[14]), s[15]);
    return fmaxf(a, b);
}
DI bool row_ok(const f32x16& o, float l, int h) {
    float am = 0.f;
#pragma unroll
    for (int i = 0; i < 8; ++i) am = fmaxf(am, fabsf(o[i]));
    if (h == 0) {
#pragma unroll
        for (int i = 8; i < 12; ++i) am = fmaxf(am, fabsf(o[i]));
    }
    return (l >= 1e-18f) && (l <= 1e18f) && (am <= 1e37f);
}
DI f32x16 attn_exact(const char* kv, u32x4 q1, unsigned q2lo, int lane, int vrow) {
    const int r32 = lane & 31, h = lane >> 5;
    const f32x16 zero16 = {0, 0, 0, 0, 0, 0, 0, 0, 0, 0, 0, 0, 0, 0, 0, 0};
    const u32x2 q2 = {q2lo, 0u};
    float m = -INFINITY;
    f32x16 o = zero16;
#pragma unroll 1
    for (int t = 0; t < 64; ++t) {
        const GAS char* tp = (const GAS char*)kv + t * KV_TILE;
        const u32x4 fk1 = *(const GAS u32x4*)(tp + lane * 16);
        const u32x2 fk2 = {*(const GAS unsigned*)(tp + 1024 + r32 * 8 + h * 4), 0u};
        const u32x4 fv0 = *(const GAS u32x4*)(tp + 1280 + (h * 21 + vrow) * 16), fv1 = *(const GAS u32x4*)(tp + 1280 + 672 + (h * 21 + vrow) * 16);
        f32x16 s = mfma32(fk1, q1, mfma32k8(fk2, q2, zero16));
        const float mn = fmaxf(m, halfmax(max16(s))), al = EX2(m - mn);
        m = mn;
        u32x4 p0, p1;
#pragma unroll
        for (int i = 0; i < 4; ++i) { p0[i] = pk2(EX2(s[2 * i] - m), EX2(s[2 * i + 1] - m)); p1[i] = pk2(EX2(s[8 + 2 * i] - m), EX2(s[9 + 2 * i] - m)); }
#pragma unroll
        for (int i = 0; i < 16; ++i) o[i] *= al;
        o = mfma32(fv1, p1, mfma32(fv0, p0, o));
    }
    return o;
}

DI void mma16(f32x4& c, const u32x4& a, const u32x4& b) {
    asm volatile("v_mfma_f32_16x16x32_bf16 %0, %1, %2, %0" : "+v"(c) : "v"(a), "v"(b));
}
DI void mma_done() { SBAR(); asm volatile("s_nop 7\n\ts_nop 1"); SBAR(); }
template <int NS> DI void zero_acc(f32x4 (&a)[NS]) {
#pragma unroll
    for (int s = 0; s < NS; ++s) { a[s] = f32x4{0, 0, 0, 0}; PIN(a[s]); }
}
#define LGKM(n) __builtin_amdgcn_s_waitcnt(0xC07F | ((n) << 8))
template <int K0, int NK, int KT, int NS, int WSTR, int LA, int RING, int GS, int ASTR, int NG>
DI void kloop(f32x4 (&acc)[2][NS], u32x4 (&act)[RING][2], u32x4 (&wf)[LA + 1][NS], unsigned wa, unsigned wn, rsrc_t rs, unsigned v0,
              unsigned v1, const unsigned (&gb)[NG]) {
#pragma unroll
    for (int k = 0; k < NK; ++k) {
        const int j = K0 + k, jn = j - 1 + RING;
        const unsigned na = k + LA < NK ? wa + (k + LA) * WSTR : wn + (k + LA - NK) * WSTR;
        LGKM((LA - 1) * NS);
        SBAR();
#pragma unroll
        for (int i = 0; i < 2 * NS; ++i) {
            mma16(acc[i & 1][i >> 1], wf[j % (LA + 1)][i >> 1], act[j % RING][i & 1]);
            SBAR();
            if (i < NS && j + LA < KT) { wf[(j + LA) % (LA + 1)][i] = lds16(na + i * 1024); SBAR(); }
            if (i == NS && j > 0 && jn < KT) { act[(j - 1) % RING][0] = bld16(rs, v0, gb[jn < KT ? jn / GS : 0] + (jn % GS) * ASTR); SBAR(); }
            if (i == NS + 1 && j > 0 && jn < KT) { act[(j - 1) % RING][1] = bld16(rs, v1, gb[jn < KT ? jn / GS : 0] + (jn % GS) * ASTR); SBAR(); }
        }
    }
}
template <int RING, int GS, int ASTR, int NG>
DI void kstart(u32x4 (&act)[RING][2], rsrc_t rs, unsigned v0, unsigned v1, const unsigned (&gb)[NG]) {
#pragma unroll
    for (int k = 0; k < RING; ++k) {
        act[k][0] = bld16(rs, v0, gb[k / GS] + (k % GS) * ASTR); SBAR();
        act[k][1] = bld16(rs, v1, gb[k / GS] + (k % GS) * ASTR); SBAR();
    }
}
template <int NG> DI void rot_bases(unsigned (&gb)[NG], unsigned base, int rot, int stride) {
#pragma unroll
    for (int i = 0; i < NG; ++i) { const int q = i + rot; gb[i] = base + (q >= NG ? q - NG : q) * stride; asm volatile("" : "+s"(gb[i])); }
}
DI void send_half(const f32x4 (&a)[5], unsigned dst) {
#pragma unroll
    for (int s = 0; s < 5; ++s) *(LAS f32x4*)(size_t)(dst + s * 1024) = a[s];
}
DI void recv_half(f32x4 (&a)[5], unsigned src) {
#pragma unroll
    for (int s = 0; s < 5; ++s) a[s] += ldsf4(src + s * 1024);
}
DI void ldsw16(unsigned a, const u32x4& v) { *(LAS u32x4*)(size_t)a = v; }
DI f32x4 unpk(unsigned r0, unsigned r1) { return f32x4{bflo(r0), bfhi(r0), bflo(r1), bfhi(r1)}; }
DI void res_init(f32x4 (&a)[5], const u32x4& x0, const u32x4& x1, const u32x2& x2) {
    a[0] = unpk(x0[0], x0[1]); a[1] = unpk(x0[2], x0[3]); a[2] = unpk(x1[0], x1[1]); a[3] = unpk(x1[2], x1[3]); a[4] = unpk(x2[0], x2[1]);
#pragma unroll
    for (int s = 0; s < 5; ++s) PIN(a[s]);
}
DI void res_pack(u32x4& x0, u32x4& x1, u32x2& x2, const f32x4 (&a)[5]) {
    x0 = u32x4{pk2(a[0][0], a[0][1]), pk2(a[0][2], a[0][3]), pk2(a[1][0], a[1][1]), pk2(a[1][2], a[1][3])};
    x1 = u32x4{pk2(a[2][0], a[2][1]), pk2(a[2][2], a[2][3]), pk2(a[3][0], a[3][1]), pk2(a[3][2], a[3][3])};
    x2 = u32x2{pk2(a[4][0], a[4][1]), pk2(a[4][2], a[4][3])};
}
enum { C_WKV, C_X, C_XUSED, C_WQ, C_CSP = C_WQ + 5, C_CS, C_KVST, C_NUM };
enum { M_LDS = 512, M_O = 516, M_H = 520, M_F = 524 };

__global__ __launch_bounds__(512, 2) void k_block(const char* __restrict__ x, const char* __restrict__ wq, const char* __restrict__ wk,
                                                  const char* __restrict__ wv, const char* __restrict__ wo, const char* __restrict__ w1,
                                                  const char* __restrict__ w2, char* ws, char* __restrict__ out) {
    extern __shared__ __attribute__((aligned(16))) char smem[];
    const unsigned L = (unsigned)(size_t)(LAS char*)smem;
    const int lane = threadIdx.x & 63, w = __builtin_amdgcn_readfirstlane(threadIdx.x >> 6);
    const int xg = blockIdx.x & 7, idx = blockIdx.x >> 3;
    const int tb = (xg >> 1) * 8 + (idx >> 2), m = (xg & 1) * 4 + (idx & 3), cid = tb * 8 + m;
    const int ct = (m >> 2) * 8 + (tb & 1) * 4 + (m & 3);
    const int g = lane >> 4, c16 = lane & 15;
    const rsrc_t rs = __builtin_amdgcn_make_buffer_rsrc(ws, 0, WS_END, 0x00020000);
    const u32x4 ones = {ONES2, ONES2, ONES2, ONES2};
    const f32x4 z4 = {0, 0, 0, 0};
    STAMP_INIT();

    u64 tag;
    asm volatile("global_load_dwordx2 %0, %1, %2 sc1" : "=v"(tag) : "v"(0u), "s"(ws + WS_FL) : "memory");
#define MAKE_TAG() do { PIN(tag); tag = (tag ^ 0x9E3779B97F4A7C15ull) * 0xBF58476D1CE4E5B9ull + 0x632BE59BD9B4E019ull; } while (0)

#define CNT(i) (L + L_CNT + (i) * 4)
#define WQ_AT(r) ((r) < 2 ? L_WQ1 + (r) * 20480 : L_WQ2 + ((r) - 2) * 20480)
    const unsigned wrow = L + (8 * g + (c16 >> 2)) * 160 + (lane & 3) * 8, wo0 = wrow + (g & 1) * 640, wo1 = wrow + ((g & 1) ^ 1) * 640;
    const int rotc = m & 3, rotd = (ct & 7) % 5, rote = (m & 3) * 2;

    if (w >= 4) {
        const int hw = w - 4;
        __builtin_amdgcn_s_setprio(2);
        if (m < 4) {
            const char* src = m < 2 ? wk : wv;
#pragma unroll 1
            for (int p = hw; p < 25; p += 4) glds(lane * 16, src + p * 1024, L + L_WKV + p * 1024);
        }
        {
            const unsigned vx0 = (lane >> 3) * 1280 + (((lane & 7) ^ g) << 4), vx1 = (8 + (lane >> 3)) * 1280 + (((lane & 7) ^ (4 + g)) << 4);
            const char* xb = x + (size_t)(tb * 64 + hw * 16) * 1280;
            int sg = (m & 3) * 2;
#pragma unroll 1
            for (int i = 0; i < 10; ++i) {
                glds(vx0, xb + sg * 128, L + L_X + (hw * 20 + sg) * 1024);
                glds(vx1, xb + sg * 128, L + L_X + (hw * 20 + 10 + sg) * 1024);
                sg = sg == 9 ? 0 : sg + 1;
                if (i == 0) bar_raw();
                if (i == 4) { wait_vm<10>(); cnt_arrive(CNT(C_WKV), lane); }
            }
        }
        {
            unsigned vw[5];
#pragma unroll
            for (int i = 0; i < 5; ++i) {
                const int ci = i * 64 + lane, pos = ci / 10, ch = ci - pos * 10, k = pos ^ (((pos >> 3) & 1) << 2);
                vw[i] = k * 1280 + ch * 16;
            }
            const char* wb = wq + m * 160 + hw * 40960;
            f32x4 cs[5];
            u32x4 one = ones;
            PIN(one);
            zero_acc(cs);
            auto wqload = [&](int r) {
#pragma unroll
                for (int i = 0; i < 5; ++i) glds(vw[i], wb + r * 163840, L + WQ_AT(r) + hw * 5120 + i * 1024);
            };
            auto wqdone = [&](int r) {
                cnt_arrive(CNT(C_WQ + r), lane);
                u32x4 f[5];
#pragma unroll
                for (int s = 0; s < 5; ++s) f[s] = ldstr2(wo0 + WQ_AT(r) + hw * 5120 + s * 32, wo1 + WQ_AT(r) + hw * 5120 + s * 32);
#pragma unroll
                for (int s = 0; s < 5; ++s) mma16(cs[s], f[s], one);
            };
            wqload(0);
            wait_vm<5>();
            cnt_arrive(CNT(C_X), lane);
            STAMP(1);
            wqload(1);
            MAKE_TAG();
            cnt_wait(CNT(C_XUSED), 4);
            wqload(2); wait_vm<10>(); wqdone(0);
            wqload(3); wait_vm<10>(); wqdone(1);
            wqload(4); wait_vm<10>(); wqdone(2);
            wait_vm<5>(); wqdone(3);
            wait_vm<0>(); wqdone(4);
            mma_done();
            if (c16 == 0) {
#pragma unroll
                for (int s = 0; s < 5; ++s) *(LAS f32x4*)(size_t)(L + L_CSX + hw * 320 + s * 64 + g * 16) = cs[s];
            }
            if (cnt_arrive(CNT(C_CSP), lane) == 3) {
                const unsigned ca = L + L_CSX + (lane < 20 ? lane : 0) * 16;
                const f32x4 c4 = (ldsf4(ca) + ldsf4(ca + 320)) + (ldsf4(ca + 640) + ldsf4(ca + 960));
                *(LAS f32x4*)(size_t)(ca + 1280) = c4;
                cnt_arrive(CNT(C_CS), lane);
            }
        }
        const char* kv = ws + WS_KV;
        STAMP(2);
        poll(ws, FL_KV, lane < 32 ? lane : hw * 32 + lane - 32, tag);
        STAMP(3);
        copy_dev(kv, L, 41, hw, lane);
        unsigned pv[7], pdst, pcs = 0;
        const char* psrc;
        int pmat, pc;
        {
            int u = blockIdx.x * 4 + hw, ks;
            pmat = u < 160 ? 0 : (u < 480 ? 1 : (u < 912 ? 2 : 3));
            if (pmat == 0) { pc = u / 20; ks = u - pc * 20; psrc = wo + pc * 160; pdst = WS_WO + u * 5120; }
            else if (pmat == 1) { u -= 160; pc = u / 20; ks = u - pc * 20; psrc = w1 + pc * 224; pdst = WS_W1 + u * 7168; pcs = WS_CS + (ks * 1792 + pc * 112) * 4; }
            else { u = pmat == 2 ? u - 480 : 0; pc = u / 54; ks = u - pc * 54; psrc = w2 + pc * 160; pdst = WS_W2 + u * 5120; }
            int rb, a3, a2, a1;
            if (pmat == 0) { if (ks < 16) { rb = 40 * ks; a3 = 20; a2 = 4; a1 = 8; } else { rb = 160 * (ks - 16) + 16; a3 = 80; a2 = 40; a1 = 20; } }
            else if (pmat == 1) { if (ks < 16) { rb = 80 * (ks >> 1) + 32 * (ks & 1); a3 = 16; a2 = 8; a1 = 4; } else { rb = 160 * (ks - 16) + 64; a3 = 8; a2 = 4; a1 = 80; } }
            else { a3 = 8; a2 = 4; if (ks < 46) { rb = 112 * (ks / 3) + 32 * (ks % 3); a1 = 16; } else { rb = 224 * (ks - 46) + 96; a1 = ks < 53 ? 112 : 48; } }
            const int nch = pmat == 1 ? 14 : 10, ld = pmat == 1 ? 3456 : 1280, chmax = (pmat == 1 && pc == 15) ? 5 : 13;
#pragma unroll
            for (int i = 0; i < 7; ++i) {
                const int ci = i * 64 + lane, pos = (pmat == 1 ? ci / 14 : ci / 10) & 31, ch = min(ci - pos * nch, chmax);
                pv[i] = (rb + a3 * (pos >> 4) + a2 * ((pos >> 3) & 1) + a1 * ((pos >> 2) & 1) + (pos & 3)) * ld + ch * 16;
                PIN(pv[i]);
            }
        }
        wait_vm<0>();
        STAMP(4);
        bar_raw();
        copy_dev(kv + CHUNK, L + CHUNK, 82, hw, lane);
        wait_vm<0>();
        bar_raw();
        if (pmat < 3) {
            const unsigned pb = L + L_AUX + hw * 7168;
#pragma unroll
            for (int i = 0; i < 5; ++i) glds(pv[i], psrc, pb + i * 1024);
            if (pmat == 1) {
                glds(pv[5], psrc, pb + 5120); glds(pv[6], psrc, pb + 6144);
                wait_vm<0>();
                const unsigned a0 = pb + (8 * g + (c16 >> 2)) * 224 + (lane & 3) * 8;
#pragma unroll
                for (int s = 0; s < 7; ++s) {
                    const unsigned a1 = a0 + (s == 6 ? (pc == 15 ? 64 : 192) : s * 32);
                    const u32x4 f = ldstr2(a1, a1 + 4 * 224);
                    bst16(rs, f, lane * 16, pdst + s * 1024);
                    const f32x4 cs = mfma16(f, ones, z4);
                    bst16(rs, __builtin_bit_cast(u32x4, cs), c16 == 0 ? g * 16 : 0x80000000u, pcs + s * 64);
                }
            } else {
                wait_vm<0>();
                const unsigned a0 = pb + (8 * g + (c16 >> 2)) * 160 + (lane & 3) * 8, a1 = a0 + (lane & 3) * 8;
#pragma unroll
                for (int s = 0; s < 5; ++s) {
                    const unsigned a2 = s < 4 ? a1 + (s >> 1) * 64 + (s & 1) * 8 : a0 + 128;
                    bst16(rs, ldstr2(a2, a2 + 4 * 160), lane * 16, pdst + s * 1024);
                }
            }
        }
        wait_vm<0>();
        STAMP(5);
        bar_raw();
        if (hw == 0 && lane == 0) set_flag(ws, FL_PR + cid * 128, tag);
        copy_dev(kv + 3 * CHUNK, L, 41, hw, lane);
        poll(ws, FL_PR, hw * 64 + lane, tag);
        wait_vm<0>();
        STAMP(6);
        bar_raw();
        if (blockIdx.x == 0 && hw == 0 && lane == 0) set_flag(ws, WS_FL, tag);
        if (hw == 3) {
            u32x4 c[20];
            const int l28 = lane < 28 ? lane : 0;
#pragma unroll
            for (int ks = 0; ks < 20; ++ks) c[ks] = bld16(rs, l28 * 16, WS_CS + (ks * 1792 + ct * 112) * 4);
            f32x4 c4 = z4;
#pragma unroll
            for (int ks = 0; ks < 20; ++ks) c4 += __builtin_bit_cast(f32x4, c[ks]);
            if (lane == 32) c4 = z4;
            if (lane < 28 || lane == 32) *(LAS f32x4*)(size_t)(L + L_MISC + lane * 16) = c4;
        }
        auto wocopy = [&](int p0, int p1) {
#pragma unroll 1
            for (int p = p0 + hw; p < p1; p += 4) {
                const int q = p + rotc * 20;
                glds_dev(lane * 16, ws + WS_WO + m * 102400 + (q >= 100 ? q - 100 : q) * 1024, L + L_WO + p * 1024);
            }
        };
        wocopy(0, 41);
        u32x4 r1[35], r2[9][8];
        unsigned g1[5], g2[9];
        rot_bases(g1, WS_W1 + ct * 143360, rotd, 28672);
        rot_bases(g2, WS_W2 + m * 276480, rote, 30720);
        const unsigned vp = lane * 16 + hw * 1024, vq = lane * 16 + min(28 + hw, 29) * 1024;
        unsigned lp0 = L + vp, lp1 = lp0 + 61440, lp2 = lp0 + 122880, lq0 = L + vq, lq1 = lq0 + 61440, lq2 = lq0 + 122880;
        PIN(lp0); PIN(lp1); PIN(lp2); PIN(lq0); PIN(lq1); PIN(lq2);
#define LD2(G, THR) do { _Pragma("unroll") for (int j_ = 0; j_ < 7; ++j_) { r2[G][j_] = bld16(rs, vp, g2[G] + j_ * 4096); wait_vm<THR>(); } \
        r2[G][7] = bld16(rs, vq, g2[G]); wait_vm<THR>(); } while (0)
#define ST2(G) do { constexpr int sl_ = (G) % 5; const unsigned bp_ = (sl_ < 2 ? lp0 : sl_ < 4 ? lp1 : lp2) + (sl_ & 1) * 30720, \
        bq_ = (sl_ < 2 ? lq0 : sl_ < 4 ? lq1 : lq2) + (sl_ & 1) * 30720; \
        _Pragma("unroll") for (int j_ = 0; j_ < 7; ++j_) ldsw16(bp_ + j_ * 4096, r2[G][j_]); \
        ldsw16(bq_, r2[G][7]); SBAR(); } while (0)
        LD2(5, 63); LD2(6, 63);
        bar();
        wocopy(41, 100);
#pragma unroll
        for (int i = 0; i < 35; ++i) r1[i] = bld16(rs, vp, g1[i / 7] + (i % 7) * 4096);
        SBAR();
        {
            u32x4 t[9];
#pragma unroll
            for (int i = 0; i < 9; ++i) t[i] = bld16(rs, lane * 16, WS_W2 + (xg & 1) * 1105920 + min((idx * 4 + hw) * 9 + i, 1079) * 1024);
#pragma unroll
            for (int i = 0; i < 9; ++i) asm volatile("" ::"v"(t[i]));
        }
        wait_vm<0>();
        STAMP(7);
        bar_raw();
        if (hw == 0) { poll(ws, FL_O, lane < 32 ? tb * 32 + lane : -1, tag); cnt_arrive(L + L_MISC + M_O, lane); }
#pragma unroll
        for (int i = 0; i < 8; ++i) ldsw16(lp0 + i * 4096, r1[i]);
        SBAR();
        LD2(7, THROT);
        bar();
        cnt_wait(L + L_MISC + M_LDS, 4);
#pragma unroll
        for (int i = 8; i < 35; ++i) ldsw16((i < 15 ? lp0 : i < 30 ? lp1 : lp2) + (i % 15) * 4096, r1[i]);
        SBAR();
        if (hw == 0) { poll(ws, FL_H, (tb >> 1) * 64 + lane, tag); cnt_arrive(L + L_MISC + M_H, lane); }
        STAMP(8);
        bar();
        LD2(8, THROT); LD2(0, THROT); LD2(1, THROT);
        STAMP(9);
        bar_raw();
        ST2(0); ST2(1);
        if (hw == 0) {
            poll(ws, FL_F, lane < 32 ? ((tb >> 1) * 16 + (lane >> 1)) * 4 + (tb & 1) * 2 + (lane & 1) : -1, tag);
            cnt_arrive(L + L_MISC + M_F, lane);
        } else {
            const unsigned gs = hw == 1 ? g2[2] : (hw == 2 ? g2[3] : g2[4]);
#pragma unroll 1
            for (int p = 0; p < 30; ++p) { glds_dev(lane * 16, ws + gs + p * 1024, L + (hw + 1) * 30720 + p * 1024); wait_vm<THROT2>(); }
        }
        wait_vm<0>();
        STAMP(10);
        bar();
        bar_raw(); ST2(5);
        bar(); ST2(6);
        bar(); ST2(7);
        bar(); ST2(8);
        bar();
        STAMP_OUT(1);
        return;
    }

    const int r32 = lane & 31, h = lane >> 5;
    u32x4 xr0, xr1; u32x2 xr2;
    u32x4 qa1, qb1; u32x2 qa2, qb2;
    const int kh = w & 1;
    {
        u32x4 xf[20], kf[20];
        if (w == 0 && lane < C_NUM) *(LAS unsigned*)(size_t)CNT(lane) = 0u;
        bar();
        const unsigned xrow = L + L_X + w * 20480 + (c16 >> 3) * 10240 + (c16 & 7) * 128;
        const int sw = c16 >> 1, t32 = (w & 1) * 16 + c16;
        unsigned xa = xrow + ((g ^ sw) << 4), xb = xrow + (((4 + g) ^ sw) << 4), xra[3], qw[5], kvo;
        {
            const int c0 = m * 10 + g, c1 = c0 + 4, c2 = m * 10 + 8 + (g >> 1);
            xra[0] = xrow + (c0 >> 3) * 1024 + (((c0 & 7) ^ sw) << 4);
            xra[1] = xrow + (c1 >> 3) * 1024 + (((c1 & 7) ^ sw) << 4);
            xra[2] = xrow + (c2 >> 3) * 1024 + (((c2 & 7) ^ sw) << 4) + (g & 1) * 8;
#pragma unroll
            for (int s = 0; s < 5; ++s) {
                const int f0 = s * 16 + 4 * g, hh = f0 / 20, d0 = f0 - hh * 20;
                qw[s] = L + L_QX + (hh * 2 + (w >> 1)) * 1280 + (d0 < 16 ? ((d0 >> 3) * 32 + t32) * 16 + (d0 & 4) * 2 : 1024 + t32 * 8);
                PIN(qw[s]);
            }
            const int d = (m & 1) * 16 + c16;
            kvo = m == 0 ? ((g >> 1) * 32 + t32) * 16 + (g & 1) * 8
                : m == 1 ? (g == 0 ? 1024 + t32 * 8 : 0x80000000u)
                         : (d <= 20 ? 1280 + (((w & 1) * 2 + (g & 1)) * 21 + d) * 16 + (g >> 1) * 8 : 0x80000000u);
            PIN(xa); PIN(xb); PIN(xra[0]); PIN(xra[1]); PIN(xra[2]); PIN(kvo);
        }
        f32x4 ka = z4, kc = z4, ssm = z4, ssq = z4, acc[5];
        zero_acc(acc);
        if (m < 4) {
            cnt_wait(CNT(C_WKV), 4);
            const unsigned a0 = L + L_WKV + (8 * g + (c16 >> 2)) * 40 + (m & 1) * 32 + (lane & 3) * 8;
#pragma unroll
            for (int j = 0; j < 20; ++j) kf[j] = ldstr2(a0 + j * 1280, a0 + j * 1280 + 160);
            if (m < 2) {
#pragma unroll
                for (int j = 0; j < 20; ++j) kc = mfma16(kf[j], ones, kc);
            } else {
#pragma unroll
                for (int j = 0; j < 20; ++j) kc = mfma16(ones, kf[j], kc);
            }
            PIN(kc);
        }
        cnt_wait(CNT(C_X), 4);
#pragma unroll
        for (int j = 0; j < 10; ++j) { xf[2 * j] = lds16(xa + j * 1024); xf[2 * j + 1] = lds16(xb + j * 1024); }
        xr0 = lds16(xra[0]); xr1 = lds16(xra[1]); xr2 = lds8(xra[2]);
        SBAR();
#pragma unroll
        for (int j = 0; j < 8; ++j) { ssm = mfma16(ones, xf[j], ssm); ssq = mfma16(xf[j], xf[j], ssq); }
        PIN(ssm); PIN(ssq);
#pragma unroll
        for (int j = 0; j < 20; ++j) PIN(xf[j]);
        PIN(xr0); PIN(xr1); PIN(xr2);
        cnt_arrive(CNT(C_XUSED), lane);
        STAMP(1);
#pragma unroll
        for (int j = 8; j < 20; ++j) { ssm = mfma16(ones, xf[j], ssm); ssq = mfma16(xf[j], xf[j], ssq); }
        PIN(ssm); PIN(ssq);
        mma_done();
        if (m < 2) {
#pragma unroll
            for (int j = 0; j < 20; ++j) ka = mfma16(kf[j], xf[j], ka);
        } else if (m < 4) {
#pragma unroll
            for (int j = 0; j < 20; ++j) ka = mfma16(xf[j], kf[j], ka);
        }
        const float sd = (c16 & 2) ? ((c16 & 1) ? ssq[3] : ssq[2]) : ((c16 & 1) ? ssq[1] : ssq[0]);
        const float mean = ssm[0] * (1.f / D), rstd = rsqrtf(fmaxf(__shfl(sd, (c16 >> 2) * 16 + c16) * (1.f / D) - mean * mean, 0.f) + LN_EPS);
        if (m < 4) {
            u32x2 pv;
            if (m < 2) {
                pv = u32x2{pk2((ka[0] - mean * kc[0]) * rstd, (ka[1] - mean * kc[1]) * rstd), pk2((ka[2] - mean * kc[2]) * rstd, (ka[3] - mean * kc[3]) * rstd)};
            } else {
                float v[4];
#pragma unroll
                for (int r = 0; r < 4; ++r) v[r] = (ka[r] - __shfl(mean, 4 * g + r) * kc[0]) * __shfl(rstd, 4 * g + r);
                pv = u32x2{pk2(v[0], v[1]), pk2(v[2], v[3])};
                if (m == 3 && c16 == 4) pv = u32x2{ONES2, ONES2};
            }
            bst8(rs, pv, kvo, WS_KV + (tb * 2 + (w >> 1)) * KV_TILE);
        }
        STAMP(2);
        u32x4 f[2][5];
#pragma unroll
        for (int r = 0; r < 5; ++r) {
            unsigned b0 = wo0 + WQ_AT(r), b1 = wo1 + WQ_AT(r);
            PIN(b0); PIN(b1);
            cnt_wait(CNT(C_WQ + r), 4);
#pragma unroll
            for (int s = 0; s < 5; ++s) f[0][s] = ldstr2(b0 + s * 32, b1 + s * 32);
            SBAR();
#pragma unroll
            for (int j = 0; j < 4; ++j) {
#pragma unroll
                for (int s = 0; s < 5; ++s) {
                    if (j < 3) f[(j + 1) & 1][s] = ldstr2(b0 + (j + 1) * 5120 + s * 32, b1 + (j + 1) * 5120 + s * 32);
                    SBAR();
                    mma16(acc[s], f[j & 1][s], xf[4 * r + j]);
                    SBAR();
                }
            }
            if (r == 1) {
                wait_vm<0>();
                MAKE_TAG();
                if (cnt_arrive(CNT(C_KVST), lane) == 3 && m < 4 && lane == 0) set_flag(ws, FL_KV + (tb * 4 + m) * 128, tag);
            }
        }
        STAMP(3);
        cnt_wait(CNT(C_KVST), 4);
        cnt_wait(CNT(C_CS), 1);
        mma_done();
        const float sc = rstd * QSCALE, ms = -mean * sc;
#pragma unroll
        for (int s = 0; s < 5; ++s) {
            const f32x4 v = acc[s] * sc + ldsf4(L + L_CSX + 1280 + s * 64 + g * 16) * ms;
            *(LAS u32x2*)(size_t)qw[s] = u32x2{pk2(v[0], v[1]), pk2(v[2], v[3])};
        }
        bar();
        const unsigned qb = L + L_QX + w * 2560;
        qa1 = lds16(qb + lane * 16); qb1 = lds16(qb + 1280 + lane * 16);
        qa2 = u32x2{lds4(qb + 1024 + r32 * 8 + h * 4), 0u}; qb2 = u32x2{lds4(qb + 1280 + 1024 + r32 * 8 + h * 4), 0u};
    }
    STAMP(4);
    {
        const int vrow = r32 <= 20 ? r32 : (r32 < 28 ? 20 : 4);
        const unsigned kconst = h == 0 ? 0x00003f80u : 0u;
        unsigned ak1 = L + lane * 16, ak2 = L + 1024 + r32 * 8 + h * 4, av = L + 1280 + (h * 21 + vrow) * 16;
        const f32x16 zero16 = {0, 0, 0, 0, 0, 0, 0, 0, 0, 0, 0, 0, 0, 0, 0, 0};
        f32x16 sa, sb, oa = zero16, ob = zero16;
        u32x4 ka1 = lds16(ak1), kb1 = lds16(ak1 + KV_TILE), va0 = lds16(av), va1 = lds16(av + 672), vb0 = va0, vb1 = va0;
        u32x2 ka2 = {lds4(ak2), kconst}, kb2 = {lds4(ak2 + KV_TILE), kconst};
        sa = mfma32(ka1, qa1, mfma32k8(ka2, qa2, zero16));
        sb = mfma32(ka1, qb1, mfma32k8(ka2, qb2, zero16));
        {
            const float ma = halfmax(max16(sa)), mb = halfmax(max16(sb));
            qa2[1] = h == 0 ? (pk2(-ma, 0.f) & 0xffffu) : 0u;
            qb2[1] = h == 0 ? (pk2(-mb, 0.f) & 0xffffu) : 0u;
        }
        sa = mfma32(ka1, qa1, mfma32k8(ka2, qa2, zero16));
        u32x4 pa0 = {0u, 0u, 0u, 0u}, pa1 = pa0, pb0 = pa0, pb1 = pa0;
        float e2 = 0.f, e3 = 0.f;
        unsigned vo0 = (r32 >> 4) * 20480 + h * 256 + (r32 & 15) * 16, vo1 = g * 20480 + c16 * 16;
        PIN(pa0); PIN(pa1); PIN(pb0); PIN(pb1); PIN(ka2); PIN(kb2); PIN(vo0); PIN(vo1);
#define SLOT(R, MF, X, S, i, PP, jp, P, j) do { R = MF; PIN(R); SBAR(); X; SBAR(); \
        float e0 = EX2(S[i]), e1 = EX2(S[i + 1]); PIN(e0); PIN(e1); SBAR(); \
        PP[jp] = pk2(e2, e3); PIN(PP); SBAR(); \
        e2 = EX2(S[i + 2]); e3 = EX2(S[i + 3]); PIN(e2); PIN(e3); SBAR(); \
        P[j] = pk2(e0, e1); PIN(P); SBAR(); } while (0)
#define STEP(KC1, KC2, KN1, KN2, VC0, VC1, VP0, VP1, AK1, AK2, AV) do { \
        SLOT(sb, mfma32k8(KC2, qb2, zero16), (void)0, sa, 0, pb1, 3, pa0, 0); \
        SLOT(sb, mfma32(KC1, qb1, sb), __builtin_amdgcn_s_waitcnt(0xC07F), sa, 4, pa0, 1, pa0, 2); \
        SLOT(ob, mfma32(VP1, pb1, ob), KC1 = lds16(AK1), sa, 8, pa0, 3, pa1, 0); \
        SLOT(oa, mfma32(VC0, pa0, oa), KC2[0] = lds4(AK2), sa, 12, pa1, 1, pa1, 2); \
        SLOT(sa, mfma32k8(KN2, qa2, zero16), (void)0, sb, 0, pa1, 3, pb0, 0); \
        SLOT(sa, mfma32(KN1, qa1, sa), VP0 = lds16(AV), sb, 4, pb0, 1, pb0, 2); \
        SLOT(oa, mfma32(VC1, pa1, oa), VP1 = lds16((AV) + 672), sb, 8, pb0, 3, pb1, 0); \
        SLOT(ob, mfma32(VC0, pb0, ob), (void)0, sb, 12, pb1, 1, pb1, 2); } while (0)
        STAMP(5);
#pragma unroll 1
        for (int hc = 0; hc < 8; ++hc) {
            if ((0x6a >> hc) & 1) bar_raw();
            const int dn = hc == 5 ? 8 * KV_TILE - 3 * CHUNK : 8 * KV_TILE;
            const unsigned nk1 = ak1 + dn, nk2 = ak2 + dn, nv = av + dn;
            SBAR();
#pragma unroll
            for (int j = 0; j < 8; j += 2) {
                STEP(ka1, ka2, kb1, kb2, va0, va1, vb0, vb1, j < 6 ? ak1 + (j + 2) * KV_TILE : nk1, j < 6 ? ak2 + (j + 2) * KV_TILE : nk2,
                     av + (j + 1) * KV_TILE);
                STEP(kb1, kb2, ka1, ka2, vb0, vb1, va0, va1, j < 6 ? ak1 + (j + 3) * KV_TILE : nk1 + KV_TILE,
                     j < 6 ? ak2 + (j + 3) * KV_TILE : nk2 + KV_TILE, j < 6 ? av + (j + 2) * KV_TILE : nv);
            }
            ak1 = nk1; ak2 = nk2; av = nv;
        }
        pb1[3] = pk2(e2, e3);
        ob = mfma32(vb1, pb1, ob);
        STAMP(6);
        float la = upper(oa[8]), lb = upper(ob[8]);
        if (__builtin_expect(!__all(row_ok(oa, la, h) && row_ok(ob, lb, h)), 0)) {
            __builtin_amdgcn_fence(__ATOMIC_ACQUIRE, "agent");
#pragma unroll 1
            for (int qt = 0; qt < 2; ++qt) {
                ob = attn_exact(ws + WS_KV, qt ? qb1 : qa1, qt ? qb2[0] : qa2[0], lane, vrow);
                if (qt == 0) oa = ob;
            }
            la = upper(oa[8]); lb = upper(ob[8]);
        }
        const float ra = __builtin_amdgcn_rcpf(la), rb = __builtin_amdgcn_rcpf(lb);
        const int hd = 4 * m + w;
        const unsigned so = WS_O + (tb * 80 + (hd >> 1)) * 1024 + (hd & 1) * 512;
        bst16(rs, u32x4{pk2(oa[0] * ra, oa[1] * ra), pk2(oa[2] * ra, oa[3] * ra), pk2(oa[4] * ra, oa[5] * ra), pk2(oa[6] * ra, oa[7] * ra)}, vo0, so);
        bst16(rs, u32x4{pk2(ob[0] * rb, ob[1] * rb), pk2(ob[2] * rb, ob[3] * rb), pk2(ob[4] * rb, ob[5] * rb), pk2(ob[6] * rb, ob[7] * rb)}, vo0, so + 40960);
        const auto s0 = __builtin_amdgcn_permlane32_swap(pk2(oa[8] * ra, oa[9] * ra), pk2(ob[8] * rb, ob[9] * rb), false, false);
        const auto s1 = __builtin_amdgcn_permlane32_swap(pk2(oa[10] * ra, oa[11] * ra), pk2(ob[10] * rb, ob[11] * rb), false, false);
        bst8(rs, u32x2{s0[0], s1[0]}, vo1, WS_O + (tb * 80 + 16 + (hd >> 3)) * 1024 + ((hd >> 1) & 3) * 256 + (hd & 1) * 8);
        STAMP(7);
        bar();
    }
#define PUBLISH(ARR) do { wait_vm<0>(); if (lane == 0) set_flag(ws, ARR + (cid * 4 + w) * 128, tag); } while (0)
    const unsigned wl = L + lane * 16;
    {
        f32x4 acc[2][5];
        u32x4 act[5][2], wf[3][5];
        unsigned wa = wl + L_WO + kh * 5120, wb = wa + 51200, gb[5];
        PIN(wa); PIN(wb);
#pragma unroll
        for (int s = 0; s < 5; ++s) wf[0][s] = lds16(wa + s * 1024);
#pragma unroll
        for (int s = 0; s < 5; ++s) wf[1][s] = lds16(wa + 10240 + s * 1024);
        PUBLISH(FL_O);
        res_init(acc[0], xr0, xr1, xr2); zero_acc(acc[1]);
        rot_bases(gb, WS_O + tb * 81920 + kh * 1024, rotc, 4096);
        unsigned v0 = lane * 16 + w * 20480, v1 = lane * 16 + (w ^ 1) * 20480;
        unsigned xs = wl + (w == 2 ? 35840 : 144384 + (w ^ 1) * 5120), xv = wl + (w == 3 ? 35840 : 144384 + w * 5120), sv = g == 0 ? c16 * 64 : 0x80000000u;
        PIN(v0); PIN(v1); PIN(xs); PIN(xv); PIN(sv);
        cnt_wait(L + L_MISC + M_O, 1);
        kstart<5, 2, 2048>(act, rs, v0, v1, gb);
        STAMP(8);
        kloop<0, 5, 10, 5, 10240, 2, 5, 2, 2048>(acc, act, wf, wa, wb, rs, v0, v1, gb);
        kloop<5, 5, 10, 5, 10240, 2, 5, 2, 2048>(acc, act, wf, wb, wa, rs, v0, v1, gb);
        mma_done();
        send_half(acc[1], xs);
        bar();
        recv_half(acc[0], xv);
        res_pack(xr0, xr1, xr2, acc[0]);
        const unsigned hb = WS_H + (tb * 4 + w) * 20480;
        bst16(rs, xr0, lane * 16, hb + 2 * m * 1024);
        bst16(rs, xr1, lane * 16, hb + (2 * m + 1) * 1024);
        bst8(rs, xr2, lane * 16, hb + (16 + (m >> 1)) * 1024 + (m & 1) * 8);
        float sm = 0.f, sq = 0.f, sm2 = 0.f, sq2 = 0.f;
#pragma unroll
        for (int i = 0; i < 4; ++i) { sm = dot2(xr0[i], ONES2, sm); sq = dot2(xr0[i], xr0[i], sq); sm2 = dot2(xr1[i], ONES2, sm2); sq2 = dot2(xr1[i], xr1[i], sq2); }
        sm = dot2(xr2[0], ONES2, sm); sq = dot2(xr2[0], xr2[0], sq); sm2 = dot2(xr2[1], ONES2, sm2); sq2 = dot2(xr2[1], xr2[1], sq2);
        sm = xsum16_32(sm + sm2); sq = xsum16_32(sq + sq2);
        bst8(rs, u32x2{__float_as_uint(sm), __float_as_uint(sq)}, sv, WS_ST + (tb * 64 + w * 16) * 64 + m * 8);
        SBAR();
        STAMP(9);
    }
    {
        f32x4 acc[2][7], c4[7];
        u32x4 act[6][2], wf[2][7], st[2];
        const int t16 = (tb >> 1) * 8 + 2 * w;
        unsigned wa = wl, wb = wl + 57344, wc = wl + 114688, gb[5], v0 = lane * 16, v1 = lane * 16 + 20480, sv = c16 * 64 + g * 16;
        PIN(wa); PIN(wb); PIN(wc); PIN(v1); PIN(sv);
#pragma unroll
        for (int s = 0; s < 7; ++s) { wf[0][s] = lds16(wa + s * 1024); c4[s] = ldsf4(L + L_MISC + s * 64 + g * 16); }
#pragma unroll
        for (int s = 0; s < 7; ++s) { PIN(c4[s]); PIN(wf[0][s]); }
        cnt_arrive(L + L_MISC + M_LDS, lane);
        PUBLISH(FL_H);
        zero_acc(acc[0]); zero_acc(acc[1]);
        rot_bases(gb, WS_H + t16 * 20480, rotd, 4096);
        cnt_wait(L + L_MISC + M_H, 1);
        kstart<6, 4, 1024>(act, rs, v0, v1, gb);
        st[0] = bld16(rs, sv, WS_ST + t16 * 1024); st[1] = bld16(rs, sv, WS_ST + t16 * 1024 + 1024);
        SBAR();
        STAMP(10);
        kloop<0, 3, 20, 7, 7168, 1, 6, 4, 1024>(acc, act, wf, wa, wa + 21504, rs, v0, v1, gb);
        bar_raw();
        kloop<3, 5, 20, 7, 7168, 1, 6, 4, 1024>(acc, act, wf, wa + 21504, wb, rs, v0, v1, gb);
        kloop<8, 8, 20, 7, 7168, 1, 6, 4, 1024>(acc, act, wf, wb, wc, rs, v0, v1, gb);
        float mu[2], rp[2];
#pragma unroll
        for (int t = 0; t < 2; ++t) {
            const float sm = xsum16_32(__uint_as_float(st[t][0]) + __uint_as_float(st[t][2]));
            const float sq = xsum16_32(__uint_as_float(st[t][1]) + __uint_as_float(st[t][3]));
            mu[t] = sm * (-1.f / D);
            rp[t] = rsqrtf(fmaxf(sq * (1.f / D) - mu[t] * mu[t], 0.f) + LN_EPS) * LOG2E;
        }
        PIN(mu[0]); PIN(mu[1]); PIN(rp[0]); PIN(rp[1]);
        SBAR();
        kloop<16, 4, 20, 7, 7168, 1, 6, 4, 1024>(acc, act, wf, wc, wa, rs, v0, v1, gb);
        bar_raw();
        mma_done();
        STAMP(11);
        auto silu4 = [&](int t, int s) {
            const f32x4 y = (acc[t][s] + mu[t] * c4[s]) * rp[t];
            f32x4 e;
#pragma unroll
            for (int r = 0; r < 4; ++r) e[r] = EX2(-y[r]);
            e = e * LOG2E + LOG2E;
#pragma unroll
            for (int r = 0; r < 4; ++r) e[r] = __builtin_amdgcn_rcpf(e[r]);
            e *= y;
            return u32x2{pk2(e[0], e[1]), pk2(e[2], e[3])};
        };
        const unsigned fb = WS_F + t16 * 55296;
#pragma unroll
        for (int t = 0; t < 2; ++t) bst8(rs, silu4(t, 6), lane * 16, fb + t * 55296 + (46 + (ct >> 1)) * 1024 + (ct & 1) * 8);
#pragma unroll
        for (int P = 0; P < 3; ++P) {
            if (P == 0 || ct < 15) {
#pragma unroll
                for (int t = 0; t < 2; ++t) {
                    const u32x2 lo = silu4(t, 2 * P), hi = silu4(t, 2 * P + 1);
                    bst16(rs, u32x4{lo[0], lo[1], hi[0], hi[1]}, lane * 16, fb + t * 55296 + (3 * ct + P) * 1024);
                }
            }
        }
        SBAR();
        STAMP(12);
    }
    {
        f32x4 acc[2][5];
        u32x4 act[6][2], wf[3][5];
        res_init(acc[0], xr0, xr1, xr2); zero_acc(acc[1]);
        unsigned a0 = wl + kh * 5120, a1 = a0 + 61440, a2 = a0 + 122880, gb[9];
        unsigned v0 = lane * 16 + w * 55296, v1 = lane * 16 + (w ^ 1) * 55296;
        PIN(a0); PIN(a1); PIN(a2); PIN(v0); PIN(v1);
        rot_bases(gb, WS_F + tb * 221184 + kh * 1024, rote, 6144);
        PUBLISH(FL_F);
        cnt_wait(L + L_MISC + M_F, 1);
        kstart<6, 3, 2048>(act, rs, v0, v1, gb);
        bar_raw();
#pragma unroll
        for (int s = 0; s < 5; ++s) wf[0][s] = lds16(a0 + s * 1024);
#pragma unroll
        for (int s = 0; s < 5; ++s) wf[1][s] = lds16(a0 + 10240 + s * 1024);
        SBAR();
        STAMP(13);
#define GRP(G, WA, WN) kloop<3 * G, 3, 27, 5, 10240, 2, 6, 3, 2048>(acc, act, wf, WA, WN, rs, v0, v1, gb)
        GRP(0, a0, a0 + 30720); bar_raw();
        GRP(1, a0 + 30720, a1); bar_raw();
        GRP(2, a1, a1 + 30720); bar_raw();
        GRP(3, a1 + 30720, a2); bar_raw();
        GRP(4, a2, a0); bar_raw();
        GRP(5, a0, a0 + 30720);
        GRP(6, a0 + 30720, a1);
        GRP(7, a1, a1 + 30720);
        GRP(8, a1 + 30720, a1);
        mma_done();
        send_half(acc[1], wl + 122880 + (w ^ 1) * 5120);
        bar();
        recv_half(acc[0], wl + 122880 + w * 5120);
        STAMP(14);
        res_pack(xr0, xr1, xr2, acc[0]);
        char* ob = out + (size_t)(tb * 64 + w * 16 + c16) * 1280 + m * 160;
        *(u32x4*)(ob + g * 16) = xr0;
        *(u32x4*)(ob + 64 + g * 16) = xr1;
        *(u32x2*)(ob + 128 + g * 8) = xr2;
    }
    STAMP_OUT(0);
}

extern "C" void kernel_launch(void* const* d_in, const int* in_sizes, int n_in, void* d_out, int out_size, void* d_ws, size_t ws_size,
                              hipStream_t stream) {
    (void)hipFuncSetAttribute((const void*)k_block, hipFuncAttributeMaxDynamicSharedMemorySize, LDS_BYTES);
    k_block<<<256, 512, LDS_BYTES, stream>>>((const char*)d_in[0], (const char*)d_in[1], (const char*)d_in[2], (const char*)d_in[3],
                                             (const char*)d_in[4], (const char*)d_in[6], (const char*)d_in[7], (char*)d_ws, (char*)d_out);
}
```

```cpp
#include <hip/hip_runtime.h>
#include <hip/hip_bf16.h>
#include <stdint.h>

typedef __attribute__((ext_vector_type(8))) __bf16 bf16x8;
typedef __attribute__((ext_vector_type(2))) __bf16 bf16x2;
typedef __attribute__((ext_vector_type(4))) short s16x4;
typedef __attribute__((ext_vector_type(4))) float f32x4;
typedef __attribute__((ext_vector_type(2))) float f32x2;
typedef __attribute__((ext_vector_type(16))) float f32x16;
typedef __attribute__((ext_vector_type(4))) unsigned u32x4;
typedef __attribute__((ext_vector_type(2))) unsigned u32x2;
typedef unsigned long long u64;
#define LAS __attribute__((address_space(3)))
#define GAS __attribute__((address_space(1)))
#define DI __device__ __forceinline__

constexpr int D = 640;
constexpr float LN_EPS = 1e-5f;
constexpr float LOG2E = 1.4426950408889634f;
constexpr float QSCALE = 0.22360679774997896f * LOG2E;

constexpr int KV_TILE = 2624;
constexpr unsigned WS_FL = 0;
constexpr unsigned FL_KV = 32768, FL_PR = 2 * 32768, FL_O = 4 * 32768, FL_H = 8 * 32768, FL_F = 12 * 32768;
constexpr unsigned WS_KV = 16 * 32768;
constexpr unsigned WS_WO = WS_KV + 64 * KV_TILE;
constexpr unsigned WS_W1 = WS_WO + 8 * 20 * 5 * 1024;
constexpr unsigned WS_W2 = WS_W1 + 16 * 20 * 7 * 1024;
constexpr unsigned WS_CS = WS_W2 + 8 * 54 * 5 * 1024;
constexpr unsigned WS_O  = WS_CS + 20 * 1792 * 4;
constexpr unsigned WS_H  = WS_O + 128 * 20 * 1024;
constexpr unsigned WS_F  = WS_H + 128 * 20 * 1024;
constexpr unsigned WS_ST = WS_F + 128 * 54 * 1024;
constexpr unsigned WS_END = WS_ST + 2048 * 64;

constexpr int CHUNK = 16 * KV_TILE;
constexpr int L_WKV = 0, L_X = 25664, L_WQ2 = 47104, L_WQ1 = 122880, L_CSX = 111104, L_QX = 152576, L_MISC = 162816;
constexpr int L_AUX = 3 * CHUNK, L_WO = CHUNK, L_CNT = 110592, LDS_BYTES = 163840;

#define ENCK 0
#define STAMP_INIT() const u64 rt0_ = __builtin_amdgcn_s_memrealtime(); u64 rtk_ = rt0_
#define STAMP(I) do { if ((I) == ENCK) { __builtin_amdgcn_sched_barrier(0); asm volatile("s_memrealtime %0\n\ts_waitcnt lgkmcnt(0)" : "=s"(rtk_) :: "memory"); __builtin_amdgcn_sched_barrier(0); } } while (0)
#define STAMP_OUT(K) do { if ((K) == 0) { while (__builtin_amdgcn_s_memrealtime() - rt0_ < 6000 + (rtk_ - rt0_)) __builtin_amdgcn_s_sleep(2); } } while (0)

#define THROT 1
#define THROT2 63
DI void glds(unsigned voff, const void* sbase, unsigned lds) {
    asm volatile("s_mov_b32 m0, %2\n\ts_nop 0\n\tglobal_load_lds_dwordx4 %0, %1" ::"v"(voff), "s"(sbase), "s"(lds) : "memory");
}
DI void glds_dev(unsigned voff, const void* sbase, unsigned lds) {
    asm volatile("s_mov_b32 m0, %2\n\ts_nop 0\n\tglobal_load_lds_dwordx4 %0, %1 sc1" ::"v"(voff), "s"(sbase), "s"(lds) : "memory");
}
template <int N> DI void wait_vm() { asm volatile("s_waitcnt vmcnt(%0)" ::"i"(N) : "memory"); }
DI void bar() { asm volatile("s_waitcnt lgkmcnt(0)\n\ts_barrier" ::: "memory"); }
DI void bar_raw() { asm volatile("s_barrier" ::: "memory"); }
#define PIN(x) asm volatile("" : "+v"(x))
#define SBAR() __builtin_amdgcn_sched_barrier(0)
DI unsigned pk2(float a, float b) { bf16x2 v = {(__bf16)a, (__bf16)b}; return __builtin_bit_cast(unsigned, v); }
DI float bflo(unsigned u) { return __uint_as_float(u << 16); }
DI float bfhi(unsigned u) { return __uint_as_float(u & 0xffff0000u); }
DI f32x4 mfma16(u32x4 a, u32x4 b, f32x4 c) {
    return __builtin_amdgcn_mfma_f32_16x16x32_bf16(__builtin_bit_cast(bf16x8, a), __builtin_bit_cast(bf16x8, b), c, 0, 0, 0);
}
DI f32x16 mfma32(u32x4 a, u32x4 b, f32x16 c) {
    return __builtin_amdgcn_mfma_f32_32x32x16_bf16(__builtin_bit_cast(bf16x8, a), __builtin_bit_cast(bf16x8, b), c, 0, 0, 0);
}
DI f32x16 mfma32k8(u32x2 a, u32x2 b, f32x16 c) {
    return __builtin_amdgcn_mfma_f32_32x32x8bf16_1k(__builtin_bit_cast(s16x4, a), __builtin_bit_cast(s16x4, b), c, 0, 0, 0);
}
DI u32x4 lds16(unsigned a) { return *(const LAS u32x4*)(size_t)a; }
DI u32x2 lds8(unsigned a) { return *(const LAS u32x2*)(size_t)a; }
DI unsigned lds4(unsigned a) { return *(const LAS unsigned*)(size_t)a; }
DI f32x4 ldsf4(unsigned a) { return *(const LAS f32x4*)(size_t)a; }
DI u32x2 ldstr(unsigned a) { return __builtin_bit_cast(u32x2, __builtin_amdgcn_ds_read_tr16_b64_v4i16((LAS s16x4*)(size_t)a)); }
DI u32x4 ldstr2(unsigned a0, unsigned a1) { u32x2 lo = ldstr(a0), hi = ldstr(a1); return u32x4{lo[0], lo[1], hi[0], hi[1]}; }
DI float dot2(unsigned a, unsigned b, float c) {
    return __builtin_amdgcn_fdot2_f32_bf16(__builtin_bit_cast(bf16x2, a), __builtin_bit_cast(bf16x2, b), c, false);
}
DI float xsum16_32(float v) {
    const auto a = __builtin_amdgcn_permlane16_swap(__float_as_uint(v), __float_as_uint(v), false, false);
    v = __uint_as_float(a[0]) + __uint_as_float(a[1]);
    const auto b = __builtin_amdgcn_permlane32_swap(__float_as_uint(v), __float_as_uint(v), false, false);
    return __uint_as_float(b[0]) + __uint_as_float(b[1]);
}
constexpr unsigned ONES2 = 0x3f803f80u;
#define EX2(v) __builtin_amdgcn_exp2f(v)
typedef __amdgpu_buffer_rsrc_t rsrc_t;
constexpr int SC1 = 16;
DI u32x4 bld16(rsrc_t r, unsigned v, unsigned s) { return __builtin_amdgcn_raw_buffer_load_b128(r, v, s, SC1); }
DI void bst16(rsrc_t r, u32x4 d, unsigned v, unsigned s) {
    __builtin_amdgcn_raw_buffer_store_b128(d, r, v, s, SC1);
    asm volatile("s_nop 1" ::"v"(d) : "memory");
}
DI void bst8(rsrc_t r, u32x2 d, unsigned v, unsigned s) { __builtin_amdgcn_raw_buffer_store_b64(d, r, v, s, SC1); }

DI void set_flag(char* ws, unsigned off, u64 tag) { __hip_atomic_store((GAS u64*)(ws + off), tag, __ATOMIC_RELAXED, __HIP_MEMORY_SCOPE_AGENT); }
DI void poll(char* ws, unsigned arr, int index, u64 tag) {
    const GAS u64* p = (const GAS u64*)(ws + arr + index * 128);
    bool ok = index < 0;
#pragma unroll 1
    for (int spin = 0; spin < (1 << 18); ++spin) {
        if (!ok) ok = __hip_atomic_load(p, __ATOMIC_RELAXED, __HIP_MEMORY_SCOPE_AGENT) == tag;
        if (__all(ok)) break;
        __builtin_amdgcn_s_sleep(1);
    }
    asm volatile("" ::: "memory");
}
DI void copy_dev(const char* src, unsigned lds, int n, int hw, int lane) {
#pragma unroll 1
    for (int p = hw; p < n; p += 4) glds_dev(lane * 16, src + p * 1024, lds + p * 1024);
}

DI unsigned cnt_arrive(unsigned a, int lane) {
    asm volatile("s_waitcnt lgkmcnt(0)" ::: "memory");
    unsigned r = 0;
    if (lane == 0) r = __hip_atomic_fetch_add((LAS unsigned*)(size_t)a, 1u, __ATOMIC_RELAXED, __HIP_MEMORY_SCOPE_WORKGROUP);
    return __builtin_amdgcn_readfirstlane(r);
}
DI void cnt_wait(unsigned a, unsigned n) {
#pragma unroll 1
    while ((unsigned)__builtin_amdgcn_readfirstlane(*(volatile LAS unsigned*)(size_t)a) < n) __builtin_amdgcn_s_sleep(1);
    asm volatile("" ::: "memory");
}
DI float halfmax(float m) {
    auto r = __builtin_amdgcn_permlane32_swap(__float_as_uint(m), __float_as_uint(m), false, false);
    return fmaxf(__uint_as_float(r[0]), __uint_as_float(r[1]));
}
DI float upper(float v) {
    auto r = __builtin_amdgcn_permlane32_swap(__float_as_uint(v), __float_as_uint(v), false, false);
    return __uint_as_float(r[1]);
}
DI float max16(const f32x16& s) {
    float a = fmaxf(fmaxf(s[0], s[1]), s[2]), b = fmaxf(fmaxf(s[3], s[4]), s[5]);
    a = fmaxf(fmaxf(a, s[6]), s[7]); b = fmaxf(fmaxf(b, s[8]), s[9]);
    a = fmaxf(fmaxf(a, s[10]), s[11]); b = fmaxf(fmaxf(b, s[12]), s[13]);
    a = fmaxf(fmaxf(a, s[14]), s[15]);
    return fmaxf(a, b);
}
DI bool row_ok(const f32x16& o, float l, int h) {
    float am = 0.f;
#pragma unroll
    for (int i = 0; i < 8; ++i) am = fmaxf(am, fabsf(o[i]));
    if (h == 0) {
#pragma unroll
        for (int i = 8; i < 12; ++i) am = fmaxf(am, fabsf(o[i]));
    }
    return (l >= 1e-18f) && (l <= 1e18f) && (am <= 1e37f);
}
DI f32x16 attn_exact(const char* kv, u32x4 q1, unsigned q2lo, int lane, int vrow) {
    const int r32 = lane & 31, h = lane >> 5;
    const f32x16 zero16 = {0, 0, 0, 0, 0, 0, 0, 0, 0, 0, 0, 0, 0, 0, 0, 0};
    const u32x2 q2 = {q2lo, 0u};
    float m = -INFINITY;
    f32x16 o = zero16;
#pragma unroll 1
    for (int t = 0; t < 64; ++t) {
        const GAS char* tp = (const GAS char*)kv + t * KV_TILE;
        const u32x4 fk1 = *(const GAS u32x4*)(tp + lane * 16);
        const u32x2 fk2 = {*(const GAS unsigned*)(tp + 1024 + r32 * 8 + h * 4), 0u};
        const u32x4 fv0 = *(const GAS u32x4*)(tp + 1280 + (h * 21 + vrow) * 16), fv1 = *(const GAS u32x4*)(tp + 1280 + 672 + (h * 21 + vrow) * 16);
        f32x16 s = mfma32(fk1, q1, mfma32k8(fk2, q2, zero16));
        const float mn = fmaxf(m, halfmax(max16(s))), al = EX2(m - mn);
        m = mn;
        u32x4 p0, p1;
#pragma unroll
        for (int i = 0; i < 4; ++i) { p0[i] = pk2(EX2(s[2 * i] - m), EX2(s[2 * i + 1] - m)); p1[i] = pk2(EX2(s[8 + 2 * i] - m), EX2(s[9 + 2 * i] - m)); }
#pragma unroll
        for (int i = 0; i < 16; ++i) o[i] *= al;
        o = mfma32(fv1, p1, mfma32(fv0, p0, o));
    }
    return o;
}

DI void mma16(f32x4& c, const u32x4& a, const u32x4& b) {
    asm volatile("v_mfma_f32_16x16x32_bf16 %0, %1, %2, %0" : "+v"(c) : "v"(a), "v"(b));
}
DI void mma_done() { SBAR(); asm volatile("s_nop 7\n\ts_nop 1"); SBAR(); }
template <int NS> DI void zero_acc(f32x4 (&a)[NS]) {
#pragma unroll
    for (int s = 0; s < NS; ++s) { a[s] = f32x4{0, 0, 0, 0}; PIN(a[s]); }
}
#define LGKM(n) __builtin_amdgcn_s_waitcnt(0xC07F | ((n) << 8))
template <int K0, int NK, int KT, int NS, int WSTR, int LA, int RING, int GS, int ASTR, int NG>
DI void kloop(f32x4 (&acc)[2][NS], u32x4 (&act)[RING][2], u32x4 (&wf)[LA + 1][NS], unsigned wa, unsigned wn, rsrc_t rs, unsigned v0,
              unsigned v1, const unsigned (&gb)[NG]) {
#pragma unroll
    for (int k = 0; k < NK; ++k) {
        const int j = K0 + k, jn = j - 1 + RING;
        const unsigned na = k + LA < NK ? wa + (k + LA) * WSTR : wn + (k + LA - NK) * WSTR;
        LGKM((LA - 1) * NS);
        SBAR();
#pragma unroll
        for (int i = 0; i < 2 * NS; ++i) {
            mma16(acc[i & 1][i >> 1], wf[j % (LA + 1)][i >> 1], act[j % RING][i & 1]);
            SBAR();
            if (i < NS && j + LA < KT) { wf[(j + LA) % (LA + 1)][i] = lds16(na + i * 1024); SBAR(); }
            if (i == NS && j > 0 && jn < KT) { act[(j - 1) % RING][0] = bld16(rs, v0, gb[jn < KT ? jn / GS : 0] + (jn % GS) * ASTR); SBAR(); }
            if (i == NS + 1 && j > 0 && jn < KT) { act[(j - 1) % RING][1] = bld16(rs, v1, gb[jn < KT ? jn / GS : 0] + (jn % GS) * ASTR); SBAR(); }
        }
    }
}
template <int RING, int GS, int ASTR, int NG>
DI void kstart(u32x4 (&act)[RING][2], rsrc_t rs, unsigned v0, unsigned v1, const unsigned (&gb)[NG]) {
#pragma unroll
    for (int k = 0; k < RING; ++k) {
        act[k][0] = bld16(rs, v0, gb[k / GS] + (k % GS) * ASTR); SBAR();
        act[k][1] = bld16(rs, v1, gb[k / GS] + (k % GS) * ASTR); SBAR();
    }
}
template <int NG> DI void rot_bases(unsigned (&gb)[NG], unsigned base, int rot, int stride) {
#pragma unroll
    for (int i = 0; i < NG; ++i) { const int q = i + rot; gb[i] = base + (q >= NG ? q - NG : q) * stride; asm volatile("" : "+s"(gb[i])); }
}
DI void send_half(const f32x4 (&a)[5], unsigned dst) {
#pragma unroll
    for (int s = 0; s < 5; ++s) *(LAS f32x4*)(size_t)(dst + s * 1024) = a[s];
}
DI void recv_half(f32x4 (&a)[5], unsigned src) {
#pragma unroll
    for (int s = 0; s < 5; ++s) a[s] += ldsf4(src + s * 1024);
}
DI void ldsw16(unsigned a, const u32x4& v) { *(LAS u32x4*)(size_t)a = v; }
DI f32x4 unpk(unsigned r0, unsigned r1) { return f32x4{bflo(r0), bfhi(r0), bflo(r1), bfhi(r1)}; }
DI void res_init(f32x4 (&a)[5], const u32x4& x0, const u32x4& x1, const u32x2& x2) {
    a[0] = unpk(x0[0], x0[1]); a[1] = unpk(x0[2], x0[3]); a[2] = unpk(x1[0], x1[1]); a[3] = unpk(x1[2], x1[3]); a[4] = unpk(x2[0], x2[1]);
#pragma unroll
    for (int s = 0; s < 5; ++s) PIN(a[s]);
}
DI void res_pack(u32x4& x0, u32x4& x1, u32x2& x2, const f32x4 (&a)[5]) {
    x0 = u32x4{pk2(a[0][0], a[0][1]), pk2(a[0][2], a[0][3]), pk2(a[1][0], a[1][1]), pk2(a[1][2], a[1][3])};
    x1 = u32x4{pk2(a[2][0], a[2][1]), pk2(a[2][2], a[2][3]), pk2(a[3][0], a[3][1]), pk2(a[3][2], a[3][3])};
    x2 = u32x2{pk2(a[4][0], a[4][1]), pk2(a[4][2], a[4][3])};
}
enum { C_WKV, C_X, C_XUSED, C_WQ, C_CSP = C_WQ + 5, C_CS, C_KVST, C_NUM };
enum { M_LDS = 512, M_O = 516, M_H = 520, M_F = 524 };

__global__ __launch_bounds__(512, 2) void k_block(const char* __restrict__ x, const char* __restrict__ wq, const char* __restrict__ wk,
                                                  const char* __restrict__ wv, const char* __restrict__ wo, const char* __restrict__ w1,
                                                  const char* __restrict__ w2, char* ws, char* __restrict__ out) {
    extern __shared__ __attribute__((aligned(16))) char smem[];
    const unsigned L = (unsigned)(size_t)(LAS char*)smem;
    const int lane = threadIdx.x & 63, w = __builtin_amdgcn_readfirstlane(threadIdx.x >> 6);
    const int xg = blockIdx.x & 7, idx = blockIdx.x >> 3;
    const int tb = (xg >> 1) * 8 + (idx >> 2), m = (xg & 1) * 4 + (idx & 3), cid = tb * 8 + m;
    const int ct = (m >> 2) * 8 + (tb & 1) * 4 + (m & 3);
    const int g = lane >> 4, c16 = lane & 15;
    const rsrc_t rs = __builtin_amdgcn_make_buffer_rsrc(ws, 0, WS_END, 0x00020000);
    const u32x4 ones = {ONES2, ONES2, ONES2, ONES2};
    const f32x4 z4 = {0, 0, 0, 0};
    STAMP_INIT();

    u64 tag;
    asm volatile("global_load_dwordx2 %0, %1, %2 sc1" : "=v"(tag) : "v"(0u), "s"(ws + WS_FL) : "memory");
#define MAKE_TAG() do { PIN(tag); tag = (tag ^ 0x9E3779B97F4A7C15ull) * 0xBF58476D1CE4E5B9ull + 0x632BE59BD9B4E019ull; } while (0)

#define CNT(i) (L + L_CNT + (i) * 4)
#define WQ_AT(r) ((r) < 2 ? L_WQ1 + (r) * 20480 : L_WQ2 + ((r) - 2) * 20480)
    const unsigned wrow = L + (8 * g + (c16 >> 2)) * 160 + (lane & 3) * 8, wo0 = wrow + (g & 1) * 640, wo1 = wrow + ((g & 1) ^ 1) * 640;
    const int rotc = m & 3, rotd = (ct & 7) % 5, rote = (m & 3) * 2;

    if (w >= 4) {
        const int hw = w - 4;
        __builtin_amdgcn_s_setprio(2);
        if (m < 4) {
            const char* src = m < 2 ? wk : wv;
#pragma unroll 1
            for (int p = hw; p < 25; p += 4) glds(lane * 16, src + p * 1024, L + L_WKV + p * 1024);
        }
        {
            const unsigned vx0 = (lane >> 3) * 1280 + (((lane & 7) ^ g) << 4), vx1 = (8 + (lane >> 3)) * 1280 + (((lane & 7) ^ (4 + g)) << 4);
            const char* xb = x + (size_t)(tb * 64 + hw * 16) * 1280;
            int sg = (m & 3) * 2;
#pragma unroll 1
            for (int i = 0; i < 10; ++i) {
                glds(vx0, xb + sg * 128, L + L_X + (hw * 20 + sg) * 1024);
                glds(vx1, xb + sg * 128, L + L_X + (hw * 20 + 10 + sg) * 1024);
                sg = sg == 9 ? 0 : sg + 1;
                if (i == 0) bar_raw();
                if (i == 4) { wait_vm<10>(); cnt_arrive(CNT(C_WKV), lane); }
            }
        }
        {
            unsigned vw[5];
#pragma unroll
            for (int i = 0; i < 5; ++i) {
                const int ci = i * 64 + lane, pos = ci / 10, ch = ci - pos * 10, k = pos ^ (((pos >> 3) & 1) << 2);
                vw[i] = k * 1280 + ch * 16;
            }
            const char* wb = wq + m * 160 + hw * 40960;
            f32x4 cs[5];
            u32x4 one = ones;
            PIN(one);
            zero_acc(cs);
            auto wqload = [&](int r) {
#pragma unroll
                for (int i = 0; i < 5; ++i) glds(vw[i], wb + r * 163840, L + WQ_AT(r) + hw * 5120 + i * 1024);
            };
            auto wqdone = [&](int r) {
                cnt_arrive(CNT(C_WQ + r), lane);
                u32x4 f[5];
#pragma unroll
                for (int s = 0; s < 5; ++s) f[s] = ldstr2(wo0 + WQ_AT(r) + hw * 5120 + s * 32, wo1 + WQ_AT(r) + hw * 5120 + s * 32);
#pragma unroll
                for (int s = 0; s < 5; ++s) mma16(cs[s], f[s], one);
            };
            wqload(0);
            wait_vm<5>();
            cnt_arrive(CNT(C_X), lane);
            STAMP(1);
            wqload(1);
            MAKE_TAG();
            cnt_wait(CNT(C_XUSED), 4);
            wqload(2); wait_vm<10>(); wqdone(0);
            wqload(3); wait_vm<10>(); wqdone(1);
            wqload(4); wait_vm<10>(); wqdone(2);
            wait_vm<5>(); wqdone(3);
            wait_vm<0>(); wqdone(4);
            mma_done();
            if (c16 == 0) {
#pragma unroll
                for (int s = 0; s < 5; ++s) *(LAS f32x4*)(size_t)(L + L_CSX + hw * 320 + s * 64 + g * 16) = cs[s];
            }
            if (cnt_arrive(CNT(C_CSP), lane) == 3) {
                const unsigned ca = L + L_CSX + (lane < 20 ? lane : 0) * 16;
                const f32x4 c4 = (ldsf4(ca) + ldsf4(ca + 320)) + (ldsf4(ca + 640) + ldsf4(ca + 960));
                *(LAS f32x4*)(size_t)(ca + 1280) = c4;
                cnt_arrive(CNT(C_CS), lane);
            }
        }
        const char* kv = ws + WS_KV;
        STAMP(2);
        poll(ws, FL_KV, lane < 32 ? lane : hw * 32 + lane - 32, tag);
        STAMP(3);
        copy_dev(kv, L, 41, hw, lane);
        unsigned pv[7], pdst, pcs = 0;
        const char* psrc;
        int pmat, pc;
        {
            int u = blockIdx.x * 4 + hw, ks;
            pmat = u < 160 ? 0 : (u < 480 ? 1 : (u < 912 ? 2 : 3));
            if (pmat == 0) { pc = u / 20; ks = u - pc * 20; psrc = wo + pc * 160; pdst = WS_WO + u * 5120; }
            else if (pmat == 1) { u -= 160; pc = u / 20; ks = u - pc * 20; psrc = w1 + pc * 224; pdst = WS_W1 + u * 7168; pcs = WS_CS + (ks * 1792 + pc * 112) * 4; }
            else { u = pmat == 2 ? u - 480 : 0; pc = u / 54; ks = u - pc * 54; psrc = w2 + pc * 160; pdst = WS_W2 + u * 5120; }
            int rb, a3, a2, a1;
            if (pmat == 0) { if (ks < 16) { rb = 40 * ks; a3 = 20; a2 = 4; a1 = 8; } else { rb = 160 * (ks - 16) + 16; a3 = 80; a2 = 40; a1 = 20; } }
            else if (pmat == 1) { if (ks < 16) { rb = 80 * (ks >> 1) + 32 * (ks & 1); a3 = 16; a2 = 8; a1 = 4; } else { rb = 160 * (ks - 16) + 64; a3 = 8; a2 = 4; a1 = 80; } }
            else { a3 = 8; a2 = 4; if (ks < 46) { rb = 112 * (ks / 3) + 32 * (ks % 3); a1 = 16; } else { rb = 224 * (ks - 46) + 96; a1 = ks < 53 ? 112 : 48; } }
            const int nch = pmat == 1 ? 14 : 10, ld = pmat == 1 ? 3456 : 1280, chmax = (pmat == 1 && pc == 15) ? 5 : 13;
#pragma unroll
            for (int i = 0; i < 7; ++i) {
                const int ci = i * 64 + lane, pos = (pmat == 1 ? ci / 14 : ci / 10) & 31, ch = min(ci - pos * nch, chmax);
                pv[i] = (rb + a3 * (pos >> 4) + a2 * ((pos >> 3) & 1) + a1 * ((pos >> 2) & 1) + (pos & 3)) * ld + ch * 16;
                PIN(pv[i]);
            }
        }
        wait_vm<0>();
        STAMP(4);
        bar_raw();
        copy_dev(kv + CHUNK, L + CHUNK, 82, hw, lane);
        wait_vm<0>();
        bar_raw();
        if (pmat < 3) {
            const unsigned pb = L + L_AUX + hw * 7168;
#pragma unroll
            for (int i = 0; i < 5; ++i) glds(pv[i], psrc, pb + i * 1024);
            if (pmat == 1) {
                glds(pv[5], psrc, pb + 5120); glds(pv[6], psrc, pb + 6144);
                wait_vm<0>();
                const unsigned a0 = pb + (8 * g + (c16 >> 2)) * 224 + (lane & 3) * 8;
#pragma unroll
                for (int s = 0; s < 7; ++s) {
                    const unsigned a1 = a0 + (s == 6 ? (pc == 15 ? 64 : 192) : s * 32);
                    const u32x4 f = ldstr2(a1, a1 + 4 * 224);
                    bst16(rs, f, lane * 16, pdst + s * 1024);
                    const f32x4 cs = mfma16(f, ones, z4);
                    bst16(rs, __builtin_bit_cast(u32x4, cs), c16 == 0 ? g * 16 : 0x80000000u, pcs + s * 64);
                }
            } else {
                wait_vm<0>();
                const unsigned a0 = pb + (8 * g + (c16 >> 2)) * 160 + (lane & 3) * 8, a1 = a0 + (lane & 3) * 8;
#pragma unroll
                for (int s = 0; s < 5; ++s) {
                    const unsigned a2 = s < 4 ? a1 + (s >> 1) * 64 + (s & 1) * 8 : a0 + 128;
                    bst16(rs, ldstr2(a2, a2 + 4 * 160), lane * 16, pdst + s * 1024);
                }
            }
        }
        wait_vm<0>();
        STAMP(5);
        bar_raw();
        if (hw == 0 && lane == 0) set_flag(ws, FL_PR + cid * 128, tag);
        copy_dev(kv + 3 * CHUNK, L, 41, hw, lane);
        poll(ws, FL_PR, hw * 64 + lane, tag);
        wait_vm<0>();
        STAMP(6);
        bar_raw();
        if (blockIdx.x == 0 && hw == 0 && lane == 0) set_flag(ws, WS_FL, tag);
        if (hw == 3) {
            u32x4 c[20];
            const int l28 = lane < 28 ? lane : 0;
#pragma unroll
            for (int ks = 0; ks < 20; ++ks) c[ks] = bld16(rs, l28 * 16, WS_CS + (ks * 1792 + ct * 112) * 4);
            f32x4 c4 = z4;
#pragma unroll
            for (int ks = 0; ks < 20; ++ks) c4 += __builtin_bit_cast(f32x4, c[ks]);
            if (lane == 32) c4 = z4;
            if (lane < 28 || lane == 32) *(LAS f32x4*)(size_t)(L + L_MISC + lane * 16) = c4;
        }
        auto wocopy = [&](int p0, int p1) {
#pragma unroll 1
            for (int p = p0 + hw; p < p1; p += 4) {
                const int q = p + rotc * 20;
                glds_dev(lane * 16, ws + WS_WO + m * 102400 + (q >= 100 ? q - 100 : q) * 1024, L + L_WO + p * 1024);
            }
        };
        wocopy(0, 41);
        u32x4 r1[35], r2[9][8];
        unsigned g1[5], g2[9];
        rot_bases(g1, WS_W1 + ct * 143360, rotd, 28672);
        rot_bases(g2, WS_W2 + m * 276480, rote, 30720);
        const unsigned vp = lane * 16 + hw * 1024, vq = lane * 16 + min(28 + hw, 29) * 1024;
        unsigned lp0 = L + vp, lp1 = lp0 + 61440, lp2 = lp0 + 122880, lq0 = L + vq, lq1 = lq0 + 61440, lq2 = lq0 + 122880;
        PIN(lp0); PIN(lp1); PIN(lp2); PIN(lq0); PIN(lq1); PIN(lq2);
#define LD2(G, THR) do { _Pragma("unroll") for (int j_ = 0; j_ < 7; ++j_) { r2[G][j_] = bld16(rs, vp, g2[G] + j_ * 4096); wait_vm<THR>(); } \
        r2[G][7] = bld16(rs, vq, g2[G]); wait_vm<THR>(); } while (0)
#define ST2(G) do { constexpr int sl_ = (G) % 5; const unsigned bp_ = (sl_ < 2 ? lp0 : sl_ < 4 ? lp1 : lp2) + (sl_ & 1) * 30720, \
        bq_ = (sl_ < 2 ? lq0 : sl_ < 4 ? lq1 : lq2) + (sl_ & 1) * 30720; \
        _Pragma("unroll") for (int j_ = 0; j_ < 7; ++j_) ldsw16(bp_ + j_ * 4096, r2[G][j_]); \
        ldsw16(bq_, r2[G][7]); SBAR(); } while (0)
        LD2(5, 63); LD2(6, 63);
        bar();
        wocopy(41, 100);
#pragma unroll
        for (int i = 0; i < 35; ++i) r1[i] = bld16(rs, vp, g1[i / 7] + (i % 7) * 4096);
        SBAR();
        {
            u32x4 t[9];
#pragma unroll
            for (int i = 0; i < 9; ++i) t[i] = bld16(rs, lane * 16, WS_W2 + (xg & 1) * 1105920 + min((idx * 4 + hw) * 9 + i, 1079) * 1024);
#pragma unroll
            for (int i = 0; i < 9; ++i) asm volatile("" ::"v"(t[i]));
        }
        wait_vm<0>();
        STAMP(7);
        bar_raw();
        if (hw == 0) { poll(ws, FL_O, lane < 32 ? tb * 32 + lane : -1, tag); cnt_arrive(L + L_MISC + M_O, lane); }
#pragma unroll
        for (int i = 0; i < 8; ++i) ldsw16(lp0 + i * 4096, r1[i]);
        SBAR();
        LD2(7, THROT);
        bar();
        cnt_wait(L + L_MISC + M_LDS, 4);
#pragma unroll
        for (int i = 8; i < 35; ++i) ldsw16((i < 15 ? lp0 : i < 30 ? lp1 : lp2) + (i % 15) * 4096, r1[i]);
        SBAR();
        if (hw == 0) { poll(ws, FL_H, (tb >> 1) * 64 + lane, tag); cnt_arrive(L + L_MISC + M_H, lane); }
        STAMP(8);
        bar();
        LD2(8, THROT); LD2(0, THROT); LD2(1, THROT);
        STAMP(9);
        bar_raw();
        ST2(0); ST2(1);
        if (hw == 0) {
            poll(ws, FL_F, lane < 32 ? ((tb >> 1) * 16 + (lane >> 1)) * 4 + (tb & 1) * 2 + (lane & 1) : -1, tag);
            cnt_arrive(L + L_MISC + M_F, lane);
        } else {
            const unsigned gs = hw == 1 ? g2[2] : (hw == 2 ? g2[3] : g2[4]);
#pragma unroll 1
            for (int p = 0; p < 30; ++p) { glds_dev(lane * 16, ws + gs + p * 1024, L + (hw + 1) * 30720 + p * 1024); wait_vm<THROT2>(); }
        }
        wait_vm<0>();
        STAMP(10);
        bar();
        bar_raw(); ST2(5);
        bar(); ST2(6);
        bar(); ST2(7);
        bar(); ST2(8);
        bar();
        STAMP_OUT(1);
        return;
    }

    const int r32 = lane & 31, h = lane >> 5;
    u32x4 xr0, xr1; u32x2 xr2;
    u32x4 qa1, qb1; u32x2 qa2, qb2;
    const int kh = w & 1;
    {
        u32x4 xf[20], kf[20];
        if (w == 0 && lane < C_NUM) *(LAS unsigned*)(size_t)CNT(lane) = 0u;
        bar();
        const unsigned xrow = L + L_X + w * 20480 + (c16 >> 3) * 10240 + (c16 & 7) * 128;
        const int sw = c16 >> 1, t32 = (w & 1) * 16 + c16;
        unsigned xa = xrow + ((g ^ sw) << 4), xb = xrow + (((4 + g) ^ sw) << 4), xra[3], qw[5], kvo;
        {
            const int c0 = m * 10 + g, c1 = c0 + 4, c2 = m * 10 + 8 + (g >> 1);
            xra[0] = xrow + (c0 >> 3) * 1024 + (((c0 & 7) ^ sw) << 4);
            xra[1] = xrow + (c1 >> 3) * 1024 + (((c1 & 7) ^ sw) << 4);
            xra[2] = xrow + (c2 >> 3) * 1024 + (((c2 & 7) ^ sw) << 4) + (g & 1) * 8;
#pragma unroll
            for (int s = 0; s < 5; ++s) {
                const int f0 = s * 16 + 4 * g, hh = f0 / 20, d0 = f0 - hh * 20;
                qw[s] = L + L_QX + (hh * 2 + (w >> 1)) * 1280 + (d0 < 16 ? ((d0 >> 3) * 32 + t32) * 16 + (d0 & 4) * 2 : 1024 + t32 * 8);
                PIN(qw[s]);
            }
            const int d = (m & 1) * 16 + c16;
            kvo = m == 0 ? ((g >> 1) * 32 + t32) * 16 + (g & 1) * 8
                : m == 1 ? (g == 0 ? 1024 + t32 * 8 : 0x80000000u)
                         : (d <= 20 ? 1280 + (((w & 1) * 2 + (g & 1)) * 21 + d) * 16 + (g >> 1) * 8 : 0x80000000u);
            PIN(xa); PIN(xb); PIN(xra[0]); PIN(xra[1]); PIN(xra[2]); PIN(kvo);
        }
        f32x4 ka = z4, kc = z4, ssm = z4, ssq = z4, acc[5];
        zero_acc(acc);
        if (m < 4) {
            cnt_wait(CNT(C_WKV), 4);
            const unsigned a0 = L + L_WKV + (8 * g + (c16 >> 2)) * 40 + (m & 1) * 32 + (lane & 3) * 8;
#pragma unroll
            for (int j = 0; j < 20; ++j) kf[j] = ldstr2(a0 + j * 1280, a0 + j * 1280 + 160);
            if (m < 2) {
#pragma unroll
                for (int j = 0; j < 20; ++j) kc = mfma16(kf[j], ones, kc);
            } else {
#pragma unroll
                for (int j = 0; j < 20; ++j) kc = mfma16(ones, kf[j], kc);
            }
            PIN(kc);
        }
        cnt_wait(CNT(C_X), 4);
#pragma unroll
        for (int j = 0; j < 10; ++j) { xf[2 * j] = lds16(xa + j * 1024); xf[2 * j + 1] = lds16(xb + j * 1024); }
        xr0 = lds16(xra[0]); xr1 = lds16(xra[1]); xr2 = lds8(xra[2]);
        SBAR();
#pragma unroll
        for (int j = 0; j < 8; ++j) { ssm = mfma16(ones, xf[j], ssm); ssq = mfma16(xf[j], xf[j], ssq); }
        PIN(ssm); PIN(ssq);
#pragma unroll
        for (int j = 0; j < 20; ++j) PIN(xf[j]);
        PIN(xr0); PIN(xr1); PIN(xr2);
        cnt_arrive(CNT(C_XUSED), lane);
        STAMP(1);
#pragma unroll
        for (int j = 8; j < 20; ++j) { ssm = mfma16(ones, xf[j], ssm); ssq = mfma16(xf[j], xf[j], ssq); }
        PIN(ssm); PIN(ssq);
        mma_done();
        if (m < 2) {
#pragma unroll
            for (int j = 0; j < 20; ++j) ka = mfma16(kf[j], xf[j], ka);
        } else if (m < 4) {
#pragma unroll
            for (int j = 0; j < 20; ++j) ka = mfma16(xf[j], kf[j], ka);
        }
        const float sd = (c16 & 2) ? ((c16 & 1) ? ssq[3] : ssq[2]) : ((c16 & 1) ? ssq[1] : ssq[0]);
        const float mean = ssm[0] * (1.f / D), rstd = rsqrtf(fmaxf(__shfl(sd, (c16 >> 2) * 16 + c16) * (1.f / D) - mean * mean, 0.f) + LN_EPS);
        if (m < 4) {
            u32x2 pv;
            if (m < 2) {
                pv = u32x2{pk2((ka[0] - mean * kc[0]) * rstd, (ka[1] - mean * kc[1]) * rstd), pk2((ka[2] - mean * kc[2]) * rstd, (ka[3] - mean * kc[3]) * rstd)};
            } else {
                float v[4];
#pragma unroll
                for (int r = 0; r < 4; ++r) v[r] = (ka[r] - __shfl(mean, 4 * g + r) * kc[0]) * __shfl(rstd, 4 * g + r);
                pv = u32x2{pk2(v[0], v[1]), pk2(v[2], v[3])};
                if (m == 3 && c16 == 4) pv = u32x2{ONES2, ONES2};
            }
            bst8(rs, pv, kvo, WS_KV + (tb * 2 + (w >> 1)) * KV_TILE);
        }
        STAMP(2);
        u32x4 f[2][5];
#pragma unroll
        for (int r = 0; r < 5; ++r) {
            unsigned b0 = wo0 + WQ_AT(r), b1 = wo1 + WQ_AT(r);
            PIN(b0); PIN(b1);
            cnt_wait(CNT(C_WQ + r), 4);
#pragma unroll
            for (int s = 0; s < 5; ++s) f[0][s] = ldstr2(b0 + s * 32, b1 + s * 32);
            SBAR();
#pragma unroll
            for (int j = 0; j < 4; ++j) {
#pragma unroll
                for (int s = 0; s < 5; ++s) {
                    if (j < 3) f[(j + 1) & 1][s] = ldstr2(b0 + (j + 1) * 5120 + s * 32, b1 + (j + 1) * 5120 + s * 32);
                    SBAR();
                    mma16(acc[s], f[j & 1][s], xf[4 * r + j]);
                    SBAR();
                }
            }
            if (r == 1) {
                wait_vm<0>();
                MAKE_TAG();
                if (cnt_arrive(CNT(C_KVST), lane) == 3 && m < 4 && lane == 0) set_flag(ws, FL_KV + (tb * 4 + m) * 128, tag);
            }
        }
        STAMP(3);
        cnt_wait(CNT(C_KVST), 4);
        cnt_wait(CNT(C_CS), 1);
        mma_done();
        const float sc = rstd * QSCALE, ms = -mean * sc;
#pragma unroll
        for (int s = 0; s < 5; ++s) {
            const f32x4 v = acc[s] * sc + ldsf4(L + L_CSX + 1280 + s * 64 + g * 16) * ms;
            *(LAS u32x2*)(size_t)qw[s] = u32x2{pk2(v[0], v[1]), pk2(v[2], v[3])};
        }
        bar();
        const unsigned qb = L + L_QX + w * 2560;
        qa1 = lds16(qb + lane * 16); qb1 = lds16(qb + 1280 + lane * 16);
        qa2 = u32x2{lds4(qb + 1024 + r32 * 8 + h * 4), 0u}; qb2 = u32x2{lds4(qb + 1280 + 1024 + r32 * 8 + h * 4), 0u};
    }
    STAMP(4);
    {
        const int vrow = r32 <= 20 ? r32 : (r32 < 28 ? 20 : 4);
        const unsigned kconst = h == 0 ? 0x00003f80u : 0u;
        unsigned ak1 = L + lane * 16, ak2 = L + 1024 + r32 * 8 + h * 4, av = L + 1280 + (h * 21 + vrow) * 16;
        const f32x16 zero16 = {0, 0, 0, 0, 0, 0, 0, 0, 0, 0, 0, 0, 0, 0, 0, 0};
        f32x16 sa, sb, oa = zero16, ob = zero16;
        u32x4 ka1 = lds16(ak1), kb1 = lds16(ak1 + KV_TILE), va0 = lds16(av), va1 = lds16(av + 672), vb0 = va0, vb1 = va0;
        u32x2 ka2 = {lds4(ak2), kconst}, kb2 = {lds4(ak2 + KV_TILE), kconst};
        sa = mfma32(ka1, qa1, mfma32k8(ka2, qa2, zero16));
        sb = mfma32(ka1, qb1, mfma32k8(ka2, qb2, zero16));
        {
            const float ma = halfmax(max16(sa)), mb = halfmax(max16(sb));
            qa2[1] = h == 0 ? (pk2(-ma, 0.f) & 0xffffu) : 0u;
            qb2[1] = h == 0 ? (pk2(-mb, 0.f) & 0xffffu) : 0u;
        }
        sa = mfma32(ka1, qa1, mfma32k8(ka2, qa2, zero16));
        u32x4 pa0 = {0u, 0u, 0u, 0u}, pa1 = pa0, pb0 = pa0, pb1 = pa0;
        float e2 = 0.f, e3 = 0.f;
        unsigned vo0 = (r32 >> 4) * 20480 + h * 256 + (r32 & 15) * 16, vo1 = g * 20480 + c16 * 16;
        PIN(pa0); PIN(pa1); PIN(pb0); PIN(pb1); PIN(ka2); PIN(kb2); PIN(vo0); PIN(vo1);
#define SLOT(R, MF, X, S, i, PP, jp, P, j) do { R = MF; PIN(R); SBAR(); X; SBAR(); \
        float e0 = EX2(S[i]), e1 = EX2(S[i + 1]); PIN(e0); PIN(e1); SBAR(); \
        PP[jp] = pk2(e2, e3); PIN(PP); SBAR(); \
        e2 = EX2(S[i + 2]); e3 = EX2(S[i + 3]); PIN(e2); PIN(e3); SBAR(); \
        P[j] = pk2(e0, e1); PIN(P); SBAR(); } while (0)
#define STEP(KC1, KC2, KN1, KN2, VC0, VC1, VP0, VP1, AK1, AK2, AV) do { \
        SLOT(sb, mfma32k8(KC2, qb2, zero16), (void)0, sa, 0, pb1, 3, pa0, 0); \
        SLOT(sb, mfma32(KC1, qb1, sb), __builtin_amdgcn_s_waitcnt(0xC07F), sa, 4, pa0, 1, pa0, 2); \
        SLOT(ob, mfma32(VP1, pb1, ob), KC1 = lds16(AK1), sa, 8, pa0, 3, pa1, 0); \
        SLOT(oa, mfma32(VC0, pa0, oa), KC2[0] = lds4(AK2), sa, 12, pa1, 1, pa1, 2); \
        SLOT(sa, mfma32k8(KN2, qa2, zero16), (void)0, sb, 0, pa1, 3, pb0, 0); \
        SLOT(sa, mfma32(KN1, qa1, sa), VP0 = lds16(AV), sb, 4, pb0, 1, pb0, 2); \
        SLOT(oa, mfma32(VC1, pa1, oa), VP1 = lds16((AV) + 672), sb, 8, pb0, 3, pb1, 0); \
        SLOT(ob, mfma32(VC0, pb0, ob), (void)0, sb, 12, pb1, 1, pb1, 2); } while (0)
        STAMP(5);
#pragma unroll 1
        for (int hc = 0; hc < 8; ++hc) {
            if ((0x6a >> hc) & 1) bar_raw();
            const int dn = hc == 5 ? 8 * KV_TILE - 3 * CHUNK : 8 * KV_TILE;
            const unsigned nk1 = ak1 + dn, nk2 = ak2 + dn, nv = av + dn;
            SBAR();
#pragma unroll
            for (int j = 0; j < 8; j += 2) {
                STEP(ka1, ka2, kb1, kb2, va0, va1, vb0, vb1, j < 6 ? ak1 + (j + 2) * KV_TILE : nk1, j < 6 ? ak2 + (j + 2) * KV_TILE : nk2,
                     av + (j + 1) * KV_TILE);
                STEP(kb1, kb2, ka1, ka2, vb0, vb1, va0, va1, j < 6 ? ak1 + (j + 3) * KV_TILE : nk1 + KV_TILE,
                     j < 6 ? ak2 + (j + 3) * KV_TILE : nk2 + KV_TILE, j < 6 ? av + (j + 2) * KV_TILE : nv);
            }
            ak1 = nk1; ak2 = nk2; av = nv;
        }
        pb1[3] = pk2(e2, e3);
        ob = mfma32(vb1, pb1, ob);
        STAMP(6);
        float la = upper(oa[8]), lb = upper(ob[8]);
        if (__builtin_expect(!__all(row_ok(oa, la, h) && row_ok(ob, lb, h)), 0)) {
            __builtin_amdgcn_fence(__ATOMIC_ACQUIRE, "agent");
#pragma unroll 1
            for (int qt = 0; qt < 2; ++qt) {
                ob = attn_exact(ws + WS_KV, qt ? qb1 : qa1, qt ? qb2[0] : qa2[0], lane, vrow);
                if (qt == 0) oa = ob;
            }
            la = upper(oa[8]); lb = upper(ob[8]);
        }
        const float ra = __builtin_amdgcn_rcpf(la), rb = __builtin_amdgcn_rcpf(lb);
        const int hd = 4 * m + w;
        const unsigned so = WS_O + (tb * 80 + (hd >> 1)) * 1024 + (hd & 1) * 512;
        bst16(rs, u32x4{pk2(oa[0] * ra, oa[1] * ra), pk2(oa[2] * ra, oa[3] * ra), pk2(oa[4] * ra, oa[5] * ra), pk2(oa[6] * ra, oa[7] * ra)}, vo0, so);
        bst16(rs, u32x4{pk2(ob[0] * rb, ob[1] * rb), pk2(ob[2] * rb, ob[3] * rb), pk2(ob[4] * rb, ob[5] * rb), pk2(ob[6] * rb, ob[7] * rb)}, vo0, so + 40960);
        const auto s0 = __builtin_amdgcn_permlane32_swap(pk2(oa[8] * ra, oa[9] * ra), pk2(ob[8] * rb, ob[9] * rb), false, false);
        const auto s1 = __builtin_amdgcn_permlane32_swap(pk2(oa[10] * ra, oa[11] * ra), pk2(ob[10] * rb, ob[11] * rb), false, false);
        bst8(rs, u32x2{s0[0], s1[0]}, vo1, WS_O + (tb * 80 + 16 + (hd >> 3)) * 1024 + ((hd >> 1) & 3) * 256 + (hd & 1) * 8);
        STAMP(7);
        bar();
    }
#define PUBLISH(ARR) do { wait_vm<0>(); if (lane == 0) set_flag(ws, ARR + (cid * 4 + w) * 128, tag); } while (0)
    const unsigned wl = L + lane * 16;
    {
        f32x4 acc[2][5];
        u32x4 act[5][2], wf[3][5];
        unsigned wa = wl + L_WO + kh * 5120, wb = wa + 51200, gb[5];
        PIN(wa); PIN(wb);
#pragma unroll
        for (int s = 0; s < 5; ++s) wf[0][s] = lds16(wa + s * 1024);
#pragma unroll
        for (int s = 0; s < 5; ++s) wf[1][s] = lds16(wa + 10240 + s * 1024);
        PUBLISH(FL_O);
        res_init(acc[0], xr0, xr1, xr2); zero_acc(acc[1]);
        rot_bases(gb, WS_O + tb * 81920 + kh * 1024, rotc, 4096);
        unsigned v0 = lane * 16 + w * 20480, v1 = lane * 16 + (w ^ 1) * 20480;
        unsigned xs = wl + (w == 2 ? 35840 : 144384 + (w ^ 1) * 5120), xv = wl + (w == 3 ? 35840 : 144384 + w * 5120), sv = g == 0 ? c16 * 64 : 0x80000000u;
        PIN(v0); PIN(v1); PIN(xs); PIN(xv); PIN(sv);
        cnt_wait(L + L_MISC + M_O, 1);
        kstart<5, 2, 2048>(act, rs, v0, v1, gb);
        STAMP(8);
        kloop<0, 5, 10, 5, 10240, 2, 5, 2, 2048>(acc, act, wf, wa, wb, rs, v0, v1, gb);
        kloop<5, 5, 10, 5, 10240, 2, 5, 2, 2048>(acc, act, wf, wb, wa, rs, v0, v1, gb);
        mma_done();
        send_half(acc[1], xs);
        bar();
        recv_half(acc[0], xv);
        res_pack(xr0, xr1, xr2, acc[0]);
        const unsigned hb = WS_H + (tb * 4 + w) * 20480;
        bst16(rs, xr0, lane * 16, hb + 2 * m * 1024);
        bst16(rs, xr1, lane * 16, hb + (2 * m + 1) * 1024);
        bst8(rs, xr2, lane * 16, hb + (16 + (m >> 1)) * 1024 + (m & 1) * 8);
        float sm = 0.f, sq = 0.f, sm2 = 0.f, sq2 = 0.f;
#pragma unroll
        for (int i = 0; i < 4; ++i) { sm = dot2(xr0[i], ONES2, sm); sq = dot2(xr0[i], xr0[i], sq); sm2 = dot2(xr1[i], ONES2, sm2); sq2 = dot2(xr1[i], xr1[i], sq2); }
        sm = dot2(xr2[0], ONES2, sm); sq = dot2(xr2[0], xr2[0], sq); sm2 = dot2(xr2[1], ONES2, sm2); sq2 = dot2(xr2[1], xr2[1], sq2);
        sm = xsum16_32(sm + sm2); sq = xsum16_32(sq + sq2);
        bst8(rs, u32x2{__float_as_uint(sm), __float_as_uint(sq)}, sv, WS_ST + (tb * 64 + w * 16) * 64 + m * 8);
        SBAR();
        STAMP(9);
    }
    {
        f32x4 acc[2][7], c4[7];
        u32x4 act[6][2], wf[2][7], st[2];
        const int t16 = (tb >> 1) * 8 + 2 * w;
        unsigned wa = wl, wb = wl + 57344, wc = wl + 114688, gb[5], v0 = lane * 16, v1 = lane * 16 + 20480, sv = c16 * 64 + g * 16;
        PIN(wa); PIN(wb); PIN(wc); PIN(v1); PIN(sv);
#pragma unroll
        for (int s = 0; s < 7; ++s) { wf[0][s] = lds16(wa + s * 1024); c4[s] = ldsf4(L + L_MISC + s * 64 + g * 16); }
#pragma unroll
        for (int s = 0; s < 7; ++s) { PIN(c4[s]); PIN(wf[0][s]); }
        cnt_arrive(L + L_MISC + M_LDS, lane);
        PUBLISH(FL_H);
        zero_acc(acc[0]); zero_acc(acc[1]);
        rot_bases(gb, WS_H + t16 * 20480, rotd, 4096);
        cnt_wait(L + L_MISC + M_H, 1);
        kstart<6, 4, 1024>(act, rs, v0, v1, gb);
        st[0] = bld16(rs, sv, WS_ST + t16 * 1024); st[1] = bld16(rs, sv, WS_ST + t16 * 1024 + 1024);
        SBAR();
        STAMP(10);
        kloop<0, 3, 20, 7, 7168, 1, 6, 4, 1024>(acc, act, wf, wa, wa + 21504, rs, v0, v1, gb);
        bar_raw();
        kloop<3, 5, 20, 7, 7168, 1, 6, 4, 1024>(acc, act, wf, wa + 21504, wb, rs, v0, v1, gb);
        kloop<8, 8, 20, 7, 7168, 1, 6, 4, 1024>(acc, act, wf, wb, wc, rs, v0, v1, gb);
        float mu[2], rp[2];
#pragma unroll
        for (int t = 0; t < 2; ++t) {
            const float sm = xsum16_32(__uint_as_float(st[t][0]) + __uint_as_float(st[t][2]));
            const float sq = xsum16_32(__uint_as_float(st[t][1]) + __uint_as_float(st[t][3]));
            mu[t] = sm * (-1.f / D);
            rp[t] = rsqrtf(fmaxf(sq * (1.f / D) - mu[t] * mu[t], 0.f) + LN_EPS) * LOG2E;
        }
        PIN(mu[0]); PIN(mu[1]); PIN(rp[0]); PIN(rp[1]);
        SBAR();
        kloop<16, 4, 20, 7, 7168, 1, 6, 4, 1024>(acc, act, wf, wc, wa, rs, v0, v1, gb);
        bar_raw();
        mma_done();
        STAMP(11);
        auto silu4 = [&](int t, int s) {
            const f32x4 y = (acc[t][s] + mu[t] * c4[s]) * rp[t];
            f32x4 e;
#pragma unroll
            for (int r = 0; r < 4; ++r) e[r] = EX2(-y[r]);
            e = e * LOG2E + LOG2E;
#pragma unroll
            for (int r = 0; r < 4; ++r) e[r] = __builtin_amdgcn_rcpf(e[r]);
            e *= y;
            return u32x2{pk2(e[0], e[1]), pk2(e[2], e[3])};
        };
        const unsigned fb = WS_F + t16 * 55296;
#pragma unroll
        for (int t = 0; t < 2; ++t) bst8(rs, silu4(t, 6), lane * 16, fb + t * 55296 + (46 + (ct >> 1)) * 1024 + (ct & 1) * 8);
#pragma unroll
        for (int P = 0; P < 3; ++P) {
            if (P == 0 || ct < 15) {
#pragma unroll
                for (int t = 0; t < 2; ++t) {
                    const u32x2 lo = silu4(t, 2 * P), hi = silu4(t, 2 * P + 1);
                    bst16(rs, u32x4{lo[0], lo[1], hi[0], hi[1]}, lane * 16, fb + t * 55296 + (3 * ct + P) * 1024);
                }
            }
        }
        SBAR();
        STAMP(12);
    }
    {
        f32x4 acc[2][5];
        u32x4 act[6][2], wf[3][5];
        res_init(acc[0], xr0, xr1, xr2); zero_acc(acc[1]);
        unsigned a0 = wl + kh * 5120, a1 = a0 + 61440, a2 = a0 + 122880, gb[9];
        unsigned v0 = lane * 16 + w * 55296, v1 = lane * 16 + (w ^ 1) * 55296;
        PIN(a0); PIN(a1); PIN(a2); PIN(v0); PIN(v1);
        rot_bases(gb, WS_F + tb * 221184 + kh * 1024, rote, 6144);
        PUBLISH(FL_F);
        cnt_wait(L + L_MISC + M_F, 1);
        kstart<6, 3, 2048>(act, rs, v0, v1, gb);
        bar_raw();
#pragma unroll
        for (int s = 0; s < 5; ++s) wf[0][s] = lds16(a0 + s * 1024);
#pragma unroll
        for (int s = 0; s < 5; ++s) wf[1][s] = lds16(a0 + 10240 + s * 1024);
        SBAR();
        STAMP(13);
#define GRP(G, WA, WN) kloop<3 * G, 3, 27, 5, 10240, 2, 6, 3, 2048>(acc, act, wf, WA, WN, rs, v0, v1, gb)
        GRP(0, a0, a0 + 30720); bar_raw();
        GRP(1, a0 + 30720, a1); bar_raw();
        GRP(2, a1, a1 + 30720); bar_raw();
        GRP(3, a1 + 30720, a2); bar_raw();
        GRP(4, a2, a0); bar_raw();
        GRP(5, a0, a0 + 30720);
        GRP(6, a0 + 30720, a1);
        GRP(7, a1, a1 + 30720);
        GRP(8, a1 + 30720, a1);
        mma_done();
        send_half(acc[1], wl + 122880 + (w ^ 1) * 5120);
        bar();
        recv_half(acc[0], wl + 122880 + w * 5120);
        STAMP(14);
        res_pack(xr0, xr1, xr2, acc[0]);
        char* ob = out + (size_t)(tb * 64 + w * 16 + c16) * 1280 + m * 160;
        asm volatile("global_store_dwordx4 %0, %1, off sc0 sc1\n\ts_nop 1" ::"v"(ob + g * 16), "v"(xr0) : "memory");
        asm volatile("global_store_dwordx4 %0, %1, off sc0 sc1\n\ts_nop 1" ::"v"(ob + 64 + g * 16), "v"(xr1) : "memory");
        asm volatile("global_store_dwordx2 %0, %1, off sc0 sc1\n\ts_nop 1" ::"v"(ob + 128 + g * 8), "v"(xr2) : "memory");
    }
    STAMP_OUT(0);
}

extern "C" void kernel_launch(void* const* d_in, const int* in_sizes, int n_in, void* d_out, int out_size, void* d_ws, size_t ws_size,
                              hipStream_t stream) {
    (void)hipFuncSetAttribute((const void*)k_block, hipFuncAttributeMaxDynamicSharedMemorySize, LDS_BYTES);
    k_block<<<256, 512, LDS_BYTES, stream>>>((const char*)d_in[0], (const char*)d_in[1], (const char*)d_in[2], (const char*)d_in[3],
                                             (const char*)d_in[4], (const char*)d_in[6], (const char*)d_in[7], (char*)d_ws, (char*)d_out);
}
```

```cpp
#include <hip/hip_runtime.h>
#include <hip/hip_bf16.h>
#include <stdint.h>

typedef __attribute__((ext_vector_type(8))) __bf16 bf16x8;
typedef __attribute__((ext_vector_type(2))) __bf16 bf16x2;
typedef __attribute__((ext_vector_type(4))) short s16x4;
typedef __attribute__((ext_vector_type(4))) float f32x4;
typedef __attribute__((ext_vector_type(2))) float f32x2;
typedef __attribute__((ext_vector_type(16))) float f32x16;
typedef __attribute__((ext_vector_type(4))) unsigned u32x4;
typedef __attribute__((ext_vector_type(2))) unsigned u32x2;
typedef unsigned long long u64;
#define LAS __attribute__((address_space(3)))
#define GAS __attribute__((address_space(1)))
#define DI __device__ __forceinline__

constexpr int D = 640;
constexpr float LN_EPS = 1e-5f;
constexpr float LOG2E = 1.4426950408889634f;
constexpr float QSCALE = 0.22360679774997896f * LOG2E;

constexpr int KV_TILE = 2688;
constexpr int VS = 704;
constexpr unsigned WS_FL = 0;
constexpr unsigned FL_KV = 32768, FL_PR = 2 * 32768, FL_MK = 3 * 32768;
constexpr unsigned WS_KV = 4 * 32768;
constexpr unsigned WS_WO = WS_KV + 64 * KV_TILE;
constexpr unsigned WS_W1 = WS_WO + 8 * 20 * 5 * 1024;
constexpr unsigned WS_W2 = WS_W1 + 16 * 20 * 7 * 1024;
constexpr unsigned WS_CS = WS_W2 + 8 * 54 * 5 * 1024;
constexpr unsigned WS_O  = WS_CS + 20 * 1792 * 4;
constexpr unsigned WS_H  = WS_O + 128 * 20 * 1024;
constexpr unsigned WS_F  = WS_H + 128 * 20 * 1024;
constexpr unsigned WS_ST = WS_F + 128 * 54 * 1024;
constexpr unsigned WS_END = WS_ST + 2048 * 64;
constexpr unsigned EMPTY = 0xffffffffu;
static_assert((WS_END - WS_O) == 256 * 48640, "fill share per workgroup");

constexpr int CHUNK = 16 * KV_TILE;
constexpr int L_WKV = 0, L_X = 25664, L_WQ2 = 47104, L_WQ1 = 122880, L_CSX = 111104, L_QX = 152576, L_MISC = 162816;
constexpr int L_AUX = 3 * CHUNK, L_WO = CHUNK, L_CNT = 110592, XCH = L_WO + 102400, LDS_BYTES = 163840;

#define STAMP_INIT()
#define STAMP(I)
#define STAMP_OUT(K)

#define THROT 3
#define THROT2 3
#define RC 6
#define RD 7
#define RE 7
#define TAILPRIO 0
#define FIRSTFILL 4
#define ONEWAY 1
#define B0AT 0
#define NEARLY 12
#define PFDELAY 12
#define Q0 57
#define QS 1
DI void glds(unsigned voff, const void* sbase, unsigned lds) {
    asm volatile("s_mov_b32 m0, %2\n\ts_nop 0\n\tglobal_load_lds_dwordx4 %0, %1" ::"v"(voff), "s"(sbase), "s"(lds) : "memory");
}
DI void glds_dev(unsigned voff, const void* sbase, unsigned lds) {
    asm volatile("s_mov_b32 m0, %2\n\ts_nop 0\n\tglobal_load_lds_dwordx4 %0, %1 sc1" ::"v"(voff), "s"(sbase), "s"(lds) : "memory");
}
template <int N> DI void wait_vm() { asm volatile("s_waitcnt vmcnt(%0)" ::"i"(N) : "memory"); }
DI void bar() { asm volatile("s_waitcnt lgkmcnt(0)\n\ts_barrier" ::: "memory"); }
DI void bar_raw() { asm volatile("s_barrier" ::: "memory"); }
#define PIN(x) asm volatile("" : "+v"(x))
#define SBAR() __builtin_amdgcn_sched_barrier(0)
DI unsigned pk2(float a, float b) { bf16x2 v = {(__bf16)a, (__bf16)b}; return __builtin_bit_cast(unsigned, v); }
DI float bflo(unsigned u) { return __uint_as_float(u << 16); }
DI float bfhi(unsigned u) { return __uint_as_float(u & 0xffff0000u); }
DI f32x4 mfma16(u32x4 a, u32x4 b, f32x4 c) {
    return __builtin_amdgcn_mfma_f32_16x16x32_bf16(__builtin_bit_cast(bf16x8, a), __builtin_bit_cast(bf16x8, b), c, 0, 0, 0);
}
DI f32x16 mfma32(u32x4 a, u32x4 b, f32x16 c) {
    return __builtin_amdgcn_mfma_f32_32x32x16_bf16(__builtin_bit_cast(bf16x8, a), __builtin_bit_cast(bf16x8, b), c, 0, 0, 0);
}
DI f32x16 mfma32k8(u32x2 a, u32x2 b, f32x16 c) {
    return __builtin_amdgcn_mfma_f32_32x32x8bf16_1k(__builtin_bit_cast(s16x4, a), __builtin_bit_cast(s16x4, b), c, 0, 0, 0);
}
DI u32x4 lds16(unsigned a) { return *(const LAS u32x4*)(size_t)a; }
DI u32x2 lds8(unsigned a) { return *(const LAS u32x2*)(size_t)a; }
DI unsigned lds4(unsigned a) { return *(const LAS unsigned*)(size_t)a; }
DI f32x4 ldsf4(unsigned a) { return *(const LAS f32x4*)(size_t)a; }
DI u32x2 ldstr(unsigned a) { return __builtin_bit_cast(u32x2, __builtin_amdgcn_ds_read_tr16_b64_v4i16((LAS s16x4*)(size_t)a)); }
DI u32x4 ldstr2(unsigned a0, unsigned a1) { u32x2 lo = ldstr(a0), hi = ldstr(a1); return u32x4{lo[0], lo[1], hi[0], hi[1]}; }
DI float dot2(unsigned a, unsigned b, float c) {
    return __builtin_amdgcn_fdot2_f32_bf16(__builtin_bit_cast(bf16x2, a), __builtin_bit_cast(bf16x2, b), c, false);
}
DI float xsum16_32(float v) {
    const auto a = __builtin_amdgcn_permlane16_swap(__float_as_uint(v), __float_as_uint(v), false, false);
    v = __uint_as_float(a[0]) + __uint_as_float(a[1]);
    const auto b = __builtin_amdgcn_permlane32_swap(__float_as_uint(v), __float_as_uint(v), false, false);
    return __uint_as_float(b[0]) + __uint_as_float(b[1]);
}
constexpr unsigned ONES2 = 0x3f803f80u;
#define EX2(v) __builtin_amdgcn_exp2f(v)
typedef __amdgpu_buffer_rsrc_t rsrc_t;
constexpr int SC1 = 16;
DI u32x4 bld16(rsrc_t r, unsigned v, unsigned s) { return __builtin_amdgcn_raw_buffer_load_b128(r, v, s, SC1); }
DI void bst16(rsrc_t r, u32x4 d, unsigned v, unsigned s) {
    __builtin_amdgcn_raw_buffer_store_b128(d, r, v, s, SC1);
    asm volatile("s_nop 1" ::"v"(d) : "memory");
}
DI void bst8(rsrc_t r, u32x2 d, unsigned v, unsigned s) { __builtin_amdgcn_raw_buffer_store_b64(d, r, v, s, SC1); }

DI void set_flag(char* ws, unsigned off, u64 tag) { __hip_atomic_store((GAS u64*)(ws + off), tag, __ATOMIC_RELAXED, __HIP_MEMORY_SCOPE_AGENT); }
DI void poll(char* ws, unsigned arr, int index, u64 tag) {
    const GAS u64* p = (const GAS u64*)(ws + arr + index * 128);
    bool ok = index < 0;
#pragma unroll 1
    for (int spin = 0; spin < (1 << 18); ++spin) {
        if (!ok) ok = __hip_atomic_load(p, __ATOMIC_RELAXED, __HIP_MEMORY_SCOPE_AGENT) == tag;
        if (__all(ok)) break;
        __builtin_amdgcn_s_sleep(1);
    }
    asm volatile("" ::: "memory");
}
DI void copy_dev(const char* src, unsigned lds, int n, int hw, int lane) {
#pragma unroll 1
    for (int p = hw; p < n; p += 4) glds_dev(lane * 16, src + p * 1024, lds + p * 1024);
}

DI void cnt_add(unsigned a) {
    asm volatile("s_waitcnt lgkmcnt(0)\n\ts_mov_b64 exec, 1\n\tds_add_u32 %0, %1\n\ts_mov_b64 exec, -1" ::"v"(a), "v"(1u) : "memory");
}
DI unsigned cnt_arrive(unsigned a) {
    unsigned r;
    asm volatile("s_waitcnt lgkmcnt(0)\n\ts_mov_b64 exec, 1\n\tds_add_rtn_u32 %0, %1, %2\n\ts_mov_b64 exec, -1\n\ts_waitcnt lgkmcnt(0)"
                 : "=&v"(r) : "v"(a), "v"(1u) : "memory");
    return __builtin_amdgcn_readfirstlane(r);
}
DI void cnt_spin(unsigned a, unsigned n) {
#pragma unroll 1
    while ((unsigned)__builtin_amdgcn_readfirstlane(*(volatile LAS unsigned*)(size_t)a) < n) {}
    asm volatile("" ::: "memory");
}
DI void cnt_wait(unsigned a, unsigned n) {
#pragma unroll 1
    while ((unsigned)__builtin_amdgcn_readfirstlane(*(volatile LAS unsigned*)(size_t)a) < n) __builtin_amdgcn_s_sleep(1);
    asm volatile("" ::: "memory");
}
DI unsigned keep(unsigned v) { return min(v, EMPTY - 1); }
DI bool missing(const u32x4& a, const u32x4& b) { return __any(max(max(a[0], a[2]), max(b[0], b[2])) == EMPTY); }
DI void refetch(u32x4& a, u32x4& b, const char* base, unsigned v0, unsigned v1) {
#pragma unroll 1
    for (int spin = 0; spin < (1 << 16); ++spin) {
        asm volatile("global_load_dwordx4 %0, %2, %4 sc1\n\tglobal_load_dwordx4 %1, %3, %4 sc1\n\ts_waitcnt vmcnt(0)"
                     : "+v"(a), "+v"(b) : "v"(v0), "v"(v1), "s"(base) : "memory");
        if (!missing(a, b)) break;
    }
}
DI u32x4 rowpair(const u32x2& a, const u32x2& b) {
    const auto s0 = __builtin_amdgcn_permlane16_swap(a[0], b[0], false, false), s1 = __builtin_amdgcn_permlane16_swap(a[1], b[1], false, false);
    return u32x4{s0[0], s1[0], s0[1], s1[1]};
}
DI int lane_id() {
    int l;
    asm volatile("v_mbcnt_lo_u32_b32 %0, -1, 0\n\tv_mbcnt_hi_u32_b32 %0, -1, %0" : "=v"(l));
    return l;
}
#define LANE_VARS() const int lane = lane_id(), g = lane >> 4, c16 = lane & 15; (void)g; (void)c16
DI float halfmax(float m) {
    auto r = __builtin_amdgcn_permlane32_swap(__float_as_uint(m), __float_as_uint(m), false, false);
    return fmaxf(__uint_as_float(r[0]), __uint_as_float(r[1]));
}
DI float upper(float v) {
    auto r = __builtin_amdgcn_permlane32_swap(__float_as_uint(v), __float_as_uint(v), false, false);
    return __uint_as_float(r[1]);
}
DI float max16(const f32x16& s) {
    float a = fmaxf(fmaxf(s[0], s[1]), s[2]), b = fmaxf(fmaxf(s[3], s[4]), s[5]);
    a = fmaxf(fmaxf(a, s[6]), s[7]); b = fmaxf(fmaxf(b, s[8]), s[9]);
    a = fmaxf(fmaxf(a, s[10]), s[11]); b = fmaxf(fmaxf(b, s[12]), s[13]);
    a = fmaxf(fmaxf(a, s[14]), s[15]);
    return fmaxf(a, b);
}
DI bool row_ok(const f32x16& o, float l, int h) {
    float am = 0.f;
#pragma unroll
    for (int i = 0; i < 8; ++i) am = fmaxf(am, fabsf(o[i]));
    if (h == 0) {
#pragma unroll
        for (int i = 8; i < 12; ++i) am = fmaxf(am, fabsf(o[i]));
    }
    return (l >= 1e-18f) && (l <= 1e18f) && (am <= 1e37f);
}
DI f32x16 attn_exact(const char* kv, u32x4 q1, unsigned q2lo, int lane, int vrow) {
    const int r32 = lane & 31, h = lane >> 5;
    const f32x16 zero16 = {0, 0, 0, 0, 0, 0, 0, 0, 0, 0, 0, 0, 0, 0, 0, 0};
    const u32x2 q2 = {q2lo, 0u};
    float m = -INFINITY;
    f32x16 o = zero16;
#pragma unroll 1
    for (int t = 0; t < 64; ++t) {
        const GAS char* tp = (const GAS char*)kv + t * KV_TILE;
        const u32x4 fk1 = *(const GAS u32x4*)(tp + lane * 16);
        const u32x2 fk2 = {*(const GAS unsigned*)(tp + 1024 + r32 * 8 + h * 4), 0u};
        const u32x4 fv0 = *(const GAS u32x4*)(tp + 1280 + (h * 22 + vrow) * 16), fv1 = *(const GAS u32x4*)(tp + 1280 + VS + (h * 22 + vrow) * 16);
        f32x16 s = mfma32(fk1, q1, mfma32k8(fk2, q2, zero16));
        const float mn = fmaxf(m, halfmax(max16(s))), al = EX2(m - mn);
        m = mn;
        u32x4 p0, p1;
#pragma unroll
        for (int i = 0; i < 4; ++i) { p0[i] = pk2(EX2(s[2 * i] - m), EX2(s[2 * i + 1] - m)); p1[i] = pk2(EX2(s[8 + 2 * i] - m), EX2(s[9 + 2 * i] - m)); }
#pragma unroll
        for (int i = 0; i < 16; ++i) o[i] *= al;
        o = mfma32(fv1, p1, mfma32(fv0, p0, o));
    }
    return o;
}

DI void mma16(f32x4& c, const u32x4& a, const u32x4& b) {
    asm volatile("v_mfma_f32_16x16x32_bf16 %0, %1, %2, %0" : "+v"(c) : "v"(a), "v"(b));
}
DI void mma_done() { SBAR(); asm volatile("s_nop 7\n\ts_nop 1"); SBAR(); }
template <int NS> DI void zero_acc(f32x4 (&a)[NS]) {
#pragma unroll
    for (int s = 0; s < NS; ++s) { a[s] = f32x4{0, 0, 0, 0}; PIN(a[s]); }
}
#define LGKM(n) __builtin_amdgcn_s_waitcnt(0xC07F | ((n) << 8))
template <int GS, int ASTR, int NG> DI u32x4 aload(rsrc_t rs, unsigned v, const unsigned (&gb)[NG], int j) {
    const int o = (j % GS) * ASTR;
    return bld16(rs, v + (o & 4095), gb[j / GS] + (o & ~4095));
}
template <int K0, int NK, int KT, int NS, int NSN, int LA, int PD, int GS, int ASTR, int NA, int RING, int NG, class WAddr>
DI void kloop(f32x4 (&acc)[2][NA], u32x4 (&act)[RING][2], u32x4 (&wf)[LA + 1][NA], WAddr waddr, rsrc_t rs, const char* ws, unsigned v0,
              unsigned v1, const unsigned (&gb)[NG]) {
#pragma unroll
    for (int k = 0; k < NK; ++k) {
        const int j = K0 + k, jn = j - 1 + PD < KT ? j - 1 + PD : 0, jc = j + 1 < KT ? j + 1 : 0;
        const int nr = k + LA < NK ? NS : NSN;
        unsigned t = 0;
        u64 bad = 0;
        if (j == 0) { LGKM((LA - 1) * NS); SBAR(); }
#pragma unroll
        for (int i = 0; i < 2 * NS; ++i) {
            mma16(acc[i & 1][i >> 1], wf[j % (LA + 1)][i >> 1], act[j % RING][i & 1]);
            SBAR();
            if (i < nr && j + LA < KT) wf[(j + LA) % (LA + 1)][i] = lds16(waddr(j + LA, i));
            if (i == NS && j > 0 && jn) act[jn % RING][0] = aload<GS, ASTR>(rs, v0, gb, jn);
            if (i == NS + 1 && j > 0 && jn) act[jn % RING][1] = aload<GS, ASTR>(rs, v1, gb, jn);
            if (jc) {
                if (i == 2 * NS - 5) { t = max(act[jc % RING][1][0], act[jc % RING][1][2]); PIN(t); }
                if (i == 2 * NS - 4) { t = max(max(t, act[jc % RING][0][0]), act[jc % RING][0][2]); PIN(t); }
                if (i == 2 * NS - 3) { bad = __ballot(t == EMPTY); asm volatile("" : "+s"(bad)); }
                if (i == 2 * NS - 2 && __builtin_expect(bad != 0, 0))
                    refetch(act[jc % RING][0], act[jc % RING][1], ws + gb[jc / GS] + (jc % GS) * ASTR, v0, v1);
                if (i == 2 * NS - 1) { if (j + LA < KT) LGKM((LA - 1) * NS); else LGKM(0); }
            }
            SBAR();
        }
    }
}
template <int PD, int GS, int ASTR, int RING, int NG, class Probe, class Before, class After>
DI void kstart(u32x4 (&act)[RING][2], rsrc_t rs, const char* ws, unsigned v0, unsigned v1, const unsigned (&gb)[NG], Probe probe,
               Before before, After after) {
    before();
    SBAR();
#pragma unroll 1
    for (int spin = 0; spin < (1 << 16); ++spin) {
        const bool miss = probe();
        SBAR();
#pragma unroll
        for (int k = 0; k < FIRSTFILL; ++k) {
            act[k][0] = aload<GS, ASTR>(rs, v0, gb, k); SBAR();
            act[k][1] = aload<GS, ASTR>(rs, v1, gb, k); SBAR();
        }
        if (!__any(miss)) break;
        asm volatile("" ::: "memory");
    }
    SBAR();
#pragma unroll
    for (int k = FIRSTFILL; k < PD; ++k) {
        act[k][0] = aload<GS, ASTR>(rs, v0, gb, k); SBAR();
        act[k][1] = aload<GS, ASTR>(rs, v1, gb, k); SBAR();
    }
    after();
    SBAR();
    if (__builtin_expect(missing(act[0][0], act[0][1]), 0)) refetch(act[0][0], act[0][1], ws + gb[0], v0, v1);
    SBAR();
}
DI void e_bases(unsigned (&gb)[9], unsigned base, int m, int stride) {
    const int m3 = m % 3;
#pragma unroll
    for (int i = 0; i < 9; ++i) {
        const int q = i < 4 ? (i + m) & 3 : i < 6 ? 4 + ((i + (m >> 2)) & 1) : 6 + (i - 6 + m3 >= 3 ? i - 9 + m3 : i - 6 + m3);
        gb[i] = base + q * stride;
        asm volatile("" : "+s"(gb[i]));
    }
}
template <int NG> DI void rot_bases(unsigned (&gb)[NG], unsigned base, int rot, int stride) {
#pragma unroll
    for (int i = 0; i < NG; ++i) { const int q = i + rot; gb[i] = base + (q >= NG ? q - NG : q) * stride; asm volatile("" : "+s"(gb[i])); }
}
DI void send_half(const f32x4 (&a)[5], unsigned dst) {
#pragma unroll
    for (int s = 0; s < 5; ++s) *(LAS f32x4*)(size_t)(dst + s * 1024) = a[s];
}
DI void recv_half(f32x4 (&a)[5], unsigned src) {
#pragma unroll
    for (int s = 0; s < 5; ++s) a[s] += ldsf4(src + s * 1024);
}
DI void ldsw16(unsigned a, const u32x4& v) { *(LAS u32x4*)(size_t)a = v; }
DI f32x4 unpk(unsigned r0, unsigned r1) { return f32x4{bflo(r0), bfhi(r0), bflo(r1), bfhi(r1)}; }
DI void res_init(f32x4 (&a)[5], const u32x4& x0, const u32x4& x1, const u32x2& x2) {
    a[0] = unpk(x0[0], x0[1]); a[1] = unpk(x0[2], x0[3]); a[2] = unpk(x1[0], x1[1]); a[3] = unpk(x1[2], x1[3]); a[4] = unpk(x2[0], x2[1]);
#pragma unroll
    for (int s = 0; s < 5; ++s) PIN(a[s]);
}
DI void res_pack(u32x4& x0, u32x4& x1, u32x2& x2, const f32x4 (&a)[5]) {
    x0 = u32x4{pk2(a[0][0], a[0][1]), pk2(a[0][2], a[0][3]), pk2(a[1][0], a[1][1]), pk2(a[1][2], a[1][3])};
    x1 = u32x4{pk2(a[2][0], a[2][1]), pk2(a[2][2], a[2][3]), pk2(a[3][0], a[3][1]), pk2(a[3][2], a[3][3])};
    x2 = u32x2{pk2(a[4][0], a[4][1]), pk2(a[4][2], a[4][3])};
}
enum { C_WKV, C_X, C_XUSED, C_WQ, C_CSP = C_WQ + 5, C_CS, C_KVST, C_XCH, C_NUM = C_XCH + 2 };
enum { E_C1, E_C2, E_C3, E_MK, P_NONE, P_C0, P_C1, P_C2, H_PREP, H_FILL, M_LDS, M_K8, M_NUM };

__global__ __launch_bounds__(512, 2) void k_block(const char* __restrict__ x, const char* __restrict__ wq, const char* __restrict__ wk,
                                                  const char* __restrict__ wv, const char* __restrict__ wo, const char* __restrict__ w1,
                                                  const char* __restrict__ w2, char* ws, char* __restrict__ out) {
    extern __shared__ __attribute__((aligned(16))) char smem[];
    const unsigned L = (unsigned)(size_t)(LAS char*)smem;
    const int w = __builtin_amdgcn_readfirstlane(threadIdx.x >> 6);
    const int xg = blockIdx.x & 7, idx = blockIdx.x >> 3;
    const int tb = ONEWAY ? xg * 4 + (idx >> 3) : (xg >> 1) * 8 + (idx >> 2), m = ONEWAY ? (idx & 7) : (xg & 1) * 4 + (idx & 3), cid = tb * 8 + m;
    const int ct = (m >> 2) * 8 + (tb & 1) * 4 + (m & 3);
    const rsrc_t rs = __builtin_amdgcn_make_buffer_rsrc(ws, 0, WS_END, 0x00020000);
    const u32x4 ones = {ONES2, ONES2, ONES2, ONES2};
    const f32x4 z4 = {0, 0, 0, 0};
    STAMP_INIT();

#define LOAD_TAG() u64 tag; asm volatile("global_load_dwordx2 %0, %1, %2 sc1" : "=v"(tag) : "v"(0u), "s"(ws + WS_FL) : "memory")
#define MAKE_TAG() do { PIN(tag); tag = (tag ^ 0x9E3779B97F4A7C15ull) * 0xBF58476D1CE4E5B9ull + 0x632BE59BD9B4E019ull; } while (0)

#define CNT(i) (L + L_CNT + (i) * 4)
#define CN2(i) (L + L_MISC + 512 + (i) * 4)
#define WQ_AT(r) ((r) < 2 ? L_WQ1 + (r) * 20480 : L_WQ2 + ((r) - 2) * 20480)
#define WQ_ADDR() const unsigned wrow = L + (8 * g + (c16 >> 2)) * 160 + (lane & 3) * 8, wo0 = wrow + (g & 1) * 640, wo1 = wrow + ((g & 1) ^ 1) * 640
    const int rotc = ONEWAY ? m % 5 : m & 3, rotd = ONEWAY ? ct % 5 : (ct & 7) % 5;

    if (w >= 4) {
        const int hw = w - 4;
        LANE_VARS();
        LOAD_TAG();
        __builtin_amdgcn_s_setprio(2);
        if (m < 4) {
            const char* src = m < 2 ? wk : wv;
#pragma unroll 1
            for (int p = hw; p < 25; p += 4) glds(lane * 16, src + p * 1024, L + L_WKV + p * 1024);
        }
        {
            const unsigned vx0 = (lane >> 3) * 1280 + (((lane & 7) ^ g) << 4), vx1 = (8 + (lane >> 3)) * 1280 + (((lane & 7) ^ (4 + g)) << 4);
            const char* xb = x + (size_t)(tb * 64 + hw * 16) * 1280;
            int sg = ONEWAY ? m : (m & 3) * 2;
#pragma unroll 1
            for (int i = 0; i < 10; ++i) {
                glds(vx0, xb + sg * 128, L + L_X + (hw * 20 + sg) * 1024);
                glds(vx1, xb + sg * 128, L + L_X + (hw * 20 + 10 + sg) * 1024);
                sg = sg == 9 ? 0 : sg + 1;
                if (i == B0AT) bar_raw();
                if (i == 4) { wait_vm<10>(); cnt_add(CNT(C_WKV)); }
            }
        }
        {
            unsigned vw[5];
#pragma unroll
            for (int i = 0; i < 5; ++i) {
                const int ci = i * 64 + lane, pos = ci / 10, ch = ci - pos * 10, k = pos ^ (((pos >> 3) & 1) << 2);
                vw[i] = k * 1280 + ch * 16;
            }
            const char* wb = wq + m * 160 + hw * 40960;
            WQ_ADDR();
            f32x4 cs[5];
            u32x4 one = ones;
            PIN(one);
            zero_acc(cs);
            auto wqload = [&](int r) {
#pragma unroll
                for (int i = 0; i < 5; ++i) glds(vw[i], wb + r * 163840, L + WQ_AT(r) + hw * 5120 + i * 1024);
            };
            auto wqdone = [&](int r) {
                cnt_add(CNT(C_WQ + r));
                u32x4 f[5];
#pragma unroll
                for (int s = 0; s < 5; ++s) f[s] = ldstr2(wo0 + WQ_AT(r) + hw * 5120 + s * 32, wo1 + WQ_AT(r) + hw * 5120 + s * 32);
#pragma unroll
                for (int s = 0; s < 5; ++s) mma16(cs[s], f[s], one);
            };
            wqload(0);
            wait_vm<5>();
            cnt_add(CNT(C_X));
            STAMP(1);
            wqload(1);
            MAKE_TAG();
            cnt_wait(CNT(C_XUSED), 4);
            wqload(2); wait_vm<10>(); wqdone(0);
            wqload(3); wait_vm<10>(); wqdone(1);
            wqload(4); wait_vm<10>(); wqdone(2);
            wait_vm<5>(); wqdone(3);
            wait_vm<0>(); wqdone(4);
            mma_done();
            if (c16 == 0) {
#pragma unroll
                for (int s = 0; s < 5; ++s) *(LAS f32x4*)(size_t)(L + L_CSX + hw * 320 + s * 64 + g * 16) = cs[s];
            }
            if (cnt_arrive(CNT(C_CSP)) == 3) {
                const unsigned ca = L + L_CSX + (lane < 20 ? lane : 0) * 16;
                const f32x4 c4 = (ldsf4(ca) + ldsf4(ca + 320)) + (ldsf4(ca + 640) + ldsf4(ca + 960));
                *(LAS f32x4*)(size_t)(ca + 1280) = c4;
                cnt_add(CNT(C_CS));
            }
        }
        const char* kv = ws + WS_KV;
        STAMP(2);
        const int c0 = tb >> 3;
        poll(ws, FL_KV, lane < 32 ? c0 * 32 + lane : -1, tag);
        STAMP(3);
        copy_dev(kv + c0 * CHUNK, L, 42, hw, lane);
        unsigned pv[7], pdst, pd2 = 0, pcs = 0;
        const char* psrc;
        int pmat, pc;
        {
            int u = blockIdx.x * 4 + hw, ks;
            pmat = u < 160 ? 0 : (u < 480 ? 1 : (u < 912 ? 2 : 3));
            if (pmat == 0) { pc = u / 20; ks = u - pc * 20; psrc = wo + pc * 160; pdst = WS_WO + u * 5120; }
            else if (pmat == 1) { u -= 160; pc = u / 20; ks = u - pc * 20; psrc = w1 + pc * 224; pdst = WS_W1 + pc * 143360 + ks * 5120; pd2 = WS_W1 + pc * 143360 + 102400 + ks * 2048; pcs = WS_CS + (ks * 1792 + pc * 112) * 4; }
            else { u = pmat == 2 ? u - 480 : 0; pc = u / 54; ks = u - pc * 54; psrc = w2 + pc * 160; pdst = WS_W2 + u * 5120; }
            int rb, a3, a2, a1;
            if (pmat == 0) { if (ks < 16) { rb = 40 * ks; a3 = 20; a2 = 4; a1 = 8; } else { rb = 160 * (ks - 16) + 16; a3 = 80; a2 = 40; a1 = 20; } }
            else if (pmat == 1) { if (ks < 16) { rb = 80 * (ks >> 1) + 32 * (ks & 1); a3 = 16; a2 = 8; a1 = 4; } else { rb = 160 * (ks - 16) + 64; a3 = 80; a2 = 8; a1 = 4; } }
            else if (ks >= 16 && ks < 24) { rb = 224 * (ks - 16) + 96; a3 = ks < 23 ? 112 : 48; a2 = 8; a1 = 4; }
            else { rb = ks < 16 ? 112 * ks : (ks < 39 ? 112 * (ks - 24) + 32 : 112 * (ks - 39) + 64); a3 = 8; a2 = 4; a1 = 16; }
            const int nch = pmat == 1 ? 14 : 10, ld = pmat == 1 ? 3456 : 1280, chmax = pmat != 1 ? 9 : (pc == 15 ? 5 : 13);
#pragma unroll
            for (int i = 0; i < 7; ++i) {
                const int ci = i * 64 + lane, pos = (pmat == 1 ? ci / 14 : ci / 10) & 31, ch = min(ci - pos * nch, chmax);
                pv[i] = (rb + a3 * (pos >> 4) + a2 * ((pos >> 3) & 1) + a1 * ((pos >> 2) & 1) + (pos & 3)) * ld + ch * 16;
                PIN(pv[i]);
            }
        }
        wait_vm<4>();
        STAMP(4);
        bar_raw();
        const unsigned pb = L + 2 * CHUNK + hw * 7168;
        poll(ws, FL_KV, (c0 * 32 + 32 + lane) & 127, tag);
        copy_dev(kv + ((c0 + 1) & 3) * CHUNK, L + CHUNK, 42, hw, lane);
#pragma unroll
        for (int i = 0; i < 7; ++i) glds(pv[i], psrc, pb + i * 1024);
        wait_vm<7>();
        cnt_add(CN2(E_C1));
        wait_vm<0>();
        if (pmat == 1) {
            const unsigned a0 = pb + (8 * g + (c16 >> 2)) * 224 + (lane & 3) * 8;
#pragma unroll
            for (int s = 0; s < 7; ++s) {
                const unsigned a1 = a0 + (s == 6 ? (pc == 15 ? 64 : 192) : s * 32);
                const u32x4 f = ldstr2(a1, a1 + 4 * 224);
                bst16(rs, f, lane * 16, s < 2 ? pdst + s * 1024 : s < 4 ? pdst + (s + 1) * 1024 : s < 6 ? pd2 + (s - 4) * 1024 : pdst + 2048);
                const f32x4 cs = mfma16(f, ones, z4);
                bst16(rs, __builtin_bit_cast(u32x4, cs), c16 == 0 ? g * 16 : 0x80000000u, pcs + s * 64);
            }
        } else if (pmat < 3) {
            const unsigned a0 = pb + (8 * g + (c16 >> 2)) * 160 + (lane & 3) * 8, a1 = a0 + (lane & 3) * 8;
#pragma unroll
            for (int s = 0; s < 5; ++s) {
                const unsigned a2 = s < 4 ? a1 + (s >> 1) * 64 + (s & 1) * 8 : a0 + 128;
                bst16(rs, ldstr2(a2, a2 + 4 * 160), lane * 16, pdst + s * 1024);
            }
        }
        wait_vm<0>();
        if (cnt_arrive(CN2(H_PREP)) == 3 && lane == 0) set_flag(ws, FL_PR + cid * 128, tag);
        STAMP(5);
        cnt_wait(CN2(H_PREP), 4);
        copy_dev(kv + ((c0 + 2) & 3) * CHUNK, L + 2 * CHUNK, 42, hw, lane);
        {
            const u32x4 e = {EMPTY, EMPTY, EMPTY, EMPTY};
#pragma unroll 1
            for (int p = hw; p < 44; p += 4) bst16(rs, e, lane * 16, WS_O + blockIdx.x * 48640 + p * 1024);
            bst16(rs, e, hw < 3 || lane < 32 ? lane * 16 : 0x80000000u, WS_O + blockIdx.x * 48640 + (44 + hw) * 1024);
        }
        wait_vm<12>();
        cnt_add(CN2(E_C2));
        poll(ws, FL_KV, (c0 * 32 + 96 + (lane & 31)) & 127, tag);
        wait_vm<0>();
        if (cnt_arrive(CN2(H_FILL)) == 3 && lane == 0) set_flag(ws, FL_MK + cid * 128, tag);
        cnt_wait(CN2(P_C0), 4);
        copy_dev(kv + ((c0 + 3) & 3) * CHUNK, L, 42, hw, lane);
        poll(ws, FL_PR, hw * 64 + lane, tag);
        wait_vm<0>();
        cnt_add(CN2(E_C3));
        cnt_wait(CN2(E_C3), 4);
        STAMP(6);
        if (blockIdx.x == 0 && hw == 1 && lane == 0) set_flag(ws, WS_FL, tag);
        if (hw == 3) {
            u32x4 c[20];
            const int l28 = lane < 28 ? lane : 0;
#pragma unroll
            for (int ks = 0; ks < 20; ++ks) c[ks] = bld16(rs, l28 * 16, WS_CS + (ks * 1792 + ct * 112) * 4);
            f32x4 c4 = z4;
#pragma unroll
            for (int ks = 0; ks < 20; ++ks) c4 += __builtin_bit_cast(f32x4, c[ks]);
            if (lane < 28) *(LAS f32x4*)(size_t)(L + L_MISC + lane * 16) = c4;
        }
        auto wocopy = [&](int p0, int p1) {
#pragma unroll 1
            for (int p = p0 + hw; p < p1; p += 4) {
                const int q = p + rotc * 20;
                glds_dev(lane * 16, ws + WS_WO + m * 102400 + (q >= 100 ? q - 100 : q) * 1024, L + L_WO + p * 1024);
            }
        };
        u32x4 r1[35], r2[9][8];
        unsigned g1[5], g1b[5], g2[9];
        rot_bases(g1, WS_W1 + ct * 143360, rotd, 20480);
        rot_bases(g1b, WS_W1 + ct * 143360 + 102400, rotd, 8192);
        e_bases(g2, WS_W2 + m * 276480, m, 30720);
        const unsigned vp = lane * 16 + hw * 1024, vq = lane * 16 + min(28 + hw, 29) * 1024;
        unsigned lp0 = L + vp, lp1 = lp0 + 61440, lp2 = lp0 + 122880, lq0 = L + vq, lq1 = lq0 + 61440, lq2 = lq0 + 122880;
        PIN(lp0); PIN(lp1); PIN(lp2); PIN(lq0); PIN(lq1); PIN(lq2);
#define LD2(G, THR) do { _Pragma("unroll") for (int j_ = 0; j_ < 7; ++j_) { r2[G][j_] = bld16(rs, vp, g2[G] + j_ * 4096); wait_vm<THR>(); } \
        r2[G][7] = bld16(rs, vq, g2[G]); wait_vm<THR>(); } while (0)
#define ST2E(G, j) do { constexpr int sl_ = (G) % 5; \
        ldsw16((j) < 7 ? (sl_ < 2 ? lp0 : sl_ < 4 ? lp1 : lp2) + (sl_ & 1) * 30720 + (j) * 4096 : (sl_ < 2 ? lq0 : sl_ < 4 ? lq1 : lq2) + (sl_ & 1) * 30720, r2[G][j]); } while (0)
#pragma unroll
        for (int i = 0; i < 35; ++i) r1[i] = bld16(rs, vp, i < 25 ? g1[i / 5] + (i % 5) * 4096 : g1b[(i - 25) / 2] + ((i - 25) & 1) * 4096);
        SBAR();
        LD2(7, 63); LD2(8, 63);
        poll(ws, FL_MK, hw * 64 + lane, tag);
        cnt_add(CN2(E_MK));
        cnt_wait(CN2(P_C1), 4);
        wocopy(0, 42);
        cnt_wait(CN2(P_C2), 4);
        wocopy(42, 100);
#pragma unroll
        for (int G = 0; G < 7; ++G) {
            const int p = (tb & 3) * 8 + hw * 2;
            glds_dev(lane * 16, ws + g2[G] + min(p, 28) * 1024, L + XCH + hw * 1024);
            glds_dev(lane * 16, ws + g2[G] + min(p + 1, 29) * 1024, L + XCH + hw * 1024);
        }
        wait_vm<0>();
        STAMP(7);
        __builtin_amdgcn_s_setprio(TAILPRIO);
        bar_raw();
#pragma unroll
        for (int i = 0; i < 8; ++i) ldsw16(lp0 + i * 4096, r1[i < 4 ? 25 + i : i - 4]);
        SBAR();
        LD2(6, THROT);
        bar();
        cnt_wait(CN2(M_LDS), 4);
#pragma unroll
        for (int i = 8; i < 35; ++i) {
            ldsw16((i < 15 ? lp0 : i < 30 ? lp1 : lp2) + (i % 15) * 4096, r1[i < 29 ? i - 4 : i]);
            SBAR();
            if (i >= 11 && i - 11 < NEARLY) {
                const int e = i - 11, G = 3 + (e >> 3), j = e & 7;
                r2[G][j] = bld16(rs, j < 7 ? vp : vq, g2[G] + (j < 7 ? j * 4096 : 0));
                SBAR();
            }
        }
        STAMP(8);
        bar();
#pragma unroll
        for (int e = NEARLY; e < 24; ++e) {
            const int G = 3 + (e >> 3), j = e & 7;
            r2[G][j] = bld16(rs, j < 7 ? vp : vq, g2[G] + (j < 7 ? j * 4096 : 0));
            wait_vm<THROT>();
        }
        cnt_wait(CN2(M_K8), 4);
#pragma unroll 1
        for (int p = hw; p < 60; p += 4) { glds_dev(lane * 16, ws + (p < 30 ? g2[0] + p * 1024 : g2[1] + (p - 30) * 1024), L + p * 1024); wait_vm<THROT>(); }
        bar_raw();
        copy_dev(ws + g2[2], L + 61440, 30, hw, lane);
        wait_vm<7>();
        STAMP(9);
        bar_raw();
#pragma unroll
        for (int iv = 0; iv < 7; ++iv) {
#pragma unroll
            for (int e = iv < 3 ? iv * 8 - (iv == 2) * 0 : 24 + (iv - 3) * 6; e < (iv < 3 ? iv * 8 + 8 : 24 + (iv - 2) * 6); ++e) {
                switch (e >> 3) {
                    case 0: ST2E(3, e & 7); break;
                    case 1: ST2E(4, e & 7); break;
                    case 2: ST2E(5, e & 7); break;
                    case 3: ST2E(6, e & 7); break;
                    case 4: ST2E(7, e & 7); break;
                    default: ST2E(8, e & 7); break;
                }
                SBAR();
            }
            wait_vm<0>();
            bar();
        }
        asm volatile(".Lpf_end2:");
        STAMP_OUT(1);
        __builtin_amdgcn_endpgm();
    }

    u32x4 xr0, xr1; u32x2 xr2;
    u32x4 qa1, qb1; u32x2 qa2, qb2;
    const int kh = w & 1;
    {
        LANE_VARS();
        LOAD_TAG();
        u32x4 xf[20], xo[10], kf[20];
        unsigned pf[4];
        {
            static_assert(ONEWAY, "");
            const unsigned vo = lane * 128;
            const int wi = idx * 4 + w;
            asm volatile("s_mov_b64 exec, 0xfffff\n\tglobal_load_dword %0, %2, %3\n\ts_mov_b64 exec, 15\n\tglobal_load_dword %1, %2, %4\n\ts_mov_b64 exec, -1"
                         : "=&v"(pf[0]), "=&v"(pf[1])
                         : "v"(vo), "s"(x + xg * 327680 + wi * 2560), "s"(((wi & 1) ? wv : wk) + min(wi >> 1, 49) * 512) : "memory");
            u64 pc; int l1, l2;
            asm volatile("s_getpc_b64 %0\n.Lpf_pc:\n\ts_mov_b32 %1, .Lpf_end1-.Lpf_pc\n\ts_mov_b32 %2, .Lpf_end2-.Lpf_pc" : "=s"(pc), "=s"(l1), "=s"(l2));
            const unsigned len = max(max(l1, l2), 4);
            const unsigned co = min((unsigned)(wi * 4 + (lane & 3)) * 128u, len - 4) & ~3u;
            asm volatile("global_load_dword %0, %1, %2" : "=v"(pf[3]) : "v"(co), "s"(pc) : "memory");
        }
        if (w == 0 && lane < C_NUM) *(LAS unsigned*)(size_t)CNT(lane) = 0u;
        bar();
        const unsigned xrow = L + L_X + w * 20480 + (c16 >> 3) * 10240 + (c16 & 7) * 128;
        const int sw = c16 >> 1, t32 = (w & 1) * 16 + c16;
        unsigned xa = xrow + (((kh * 4 + g) ^ sw) << 4), xb = xrow + ((((kh ^ 1) * 4 + g) ^ sw) << 4), xc = xa + ((w ^ 1) - w) * 20480, xra[3], qw[5], kvo;
        {
            const int c0 = m * 10 + g, c1 = c0 + 4, c2 = m * 10 + 8 + (g >> 1);
            xra[0] = xrow + (c0 >> 3) * 1024 + (((c0 & 7) ^ sw) << 4);
            xra[1] = xrow + (c1 >> 3) * 1024 + (((c1 & 7) ^ sw) << 4);
            xra[2] = xrow + (c2 >> 3) * 1024 + (((c2 & 7) ^ sw) << 4) + (g & 1) * 8;
#pragma unroll
            for (int s = 0; s < 5; ++s) {
                const int f0 = s * 16 + 4 * g, hh = f0 / 20, d0 = f0 - hh * 20;
                qw[s] = L + L_QX + (hh * 2 + (w >> 1)) * 1280 + (d0 < 16 ? ((d0 >> 3) * 32 + t32) * 16 + (d0 & 4) * 2 : 1024 + t32 * 8);
                PIN(qw[s]);
            }
            const int d = (m & 1) * 16 + c16;
            kvo = m == 0 ? ((g >> 1) * 32 + t32) * 16 + (g & 1) * 8
                : m == 1 ? (g == 0 ? 1024 + t32 * 8 : 0x80000000u)
                         : (d <= 21 ? 1280 + (((w & 1) * 2 + (g & 1)) * 22 + d) * 16 + (g >> 1) * 8 : 0x80000000u);
            PIN(xa); PIN(xb); PIN(xc); PIN(xra[0]); PIN(xra[1]); PIN(xra[2]); PIN(kvo);
        }
        __builtin_amdgcn_s_sleep(PFDELAY);
        asm volatile("s_mov_b64 exec, %3\n\tglobal_load_dword %0, %1, %2\n\ts_mov_b64 exec, -1"
                     : "=v"(pf[2]) : "v"(lane * 128), "s"(wq + (idx * 4 + w) * 6400), "s"(0x3ffffffffffffull) : "memory");
        f32x4 ka = z4, kc = z4, ssm = z4, ssq = z4, acc[2][5];
        if (m < 4) {
            cnt_wait(CNT(C_WKV), 4);
            const unsigned a0 = L + L_WKV + (8 * g + (c16 >> 2)) * 40 + (m & 1) * 32 + (lane & 3) * 8;
            const unsigned a1 = a0 + kh * 1280, a2 = a0 + (kh ^ 1) * 1280;
#pragma unroll
            for (int j = 0; j < 10; ++j) { kf[j] = ldstr2(a1 + j * 2560, a1 + j * 2560 + 160); kf[10 + j] = ldstr2(a2 + j * 2560, a2 + j * 2560 + 160); }
            if (m < 2) {
#pragma unroll
                for (int j = 0; j < 20; ++j) kc = mfma16(kf[j], ones, kc);
            } else {
#pragma unroll
                for (int j = 0; j < 20; ++j) kc = mfma16(ones, kf[j], kc);
            }
            PIN(kc);
        }
        cnt_spin(CNT(C_X), 4);
#pragma unroll
        for (int j = 0; j < 10; ++j) xf[j] = lds16(xa + j * 1024);
#pragma unroll
        for (int j = 0; j < 10; ++j) xf[10 + j] = lds16(xb + j * 1024);
#pragma unroll
        for (int j = 0; j < 10; ++j) xo[j] = lds16(xc + j * 1024);
        xr0 = lds16(xra[0]); xr1 = lds16(xra[1]); xr2 = lds8(xra[2]);
        SBAR();
#pragma unroll
        for (int j = 0; j < 8; ++j) { ssm = mfma16(ones, xf[j], ssm); ssq = mfma16(xf[j], xf[j], ssq); }
        PIN(ssm); PIN(ssq);
#pragma unroll
        for (int j = 0; j < 10; ++j) { PIN(xf[j]); PIN(xf[10 + j]); PIN(xo[j]); }
        PIN(xr0); PIN(xr1); PIN(xr2);
        cnt_add(CNT(C_XUSED));
        STAMP(1);
#pragma unroll
        for (int j = 8; j < 20; ++j) { ssm = mfma16(ones, xf[j], ssm); ssq = mfma16(xf[j], xf[j], ssq); }
        PIN(ssm); PIN(ssq);
        mma_done();
        if (m < 2) {
#pragma unroll
            for (int j = 0; j < 20; ++j) ka = mfma16(kf[j], xf[j], ka);
        } else if (m < 4) {
#pragma unroll
            for (int j = 0; j < 20; ++j) ka = mfma16(xf[j], kf[j], ka);
        }
        const float sd = (c16 & 2) ? ((c16 & 1) ? ssq[3] : ssq[2]) : ((c16 & 1) ? ssq[1] : ssq[0]);
        const float mean = ssm[0] * (1.f / D), rstd = __builtin_amdgcn_rsqf(fmaxf(__shfl(sd, (c16 >> 2) * 16 + c16) * (1.f / D) - mean * mean, 0.f) + LN_EPS);
        if (m < 4) {
            u32x2 pv;
            if (m < 2) {
                pv = u32x2{pk2((ka[0] - mean * kc[0]) * rstd, (ka[1] - mean * kc[1]) * rstd), pk2((ka[2] - mean * kc[2]) * rstd, (ka[3] - mean * kc[3]) * rstd)};
            } else {
                float v[4];
#pragma unroll
                for (int r = 0; r < 4; ++r) v[r] = (ka[r] - __shfl(mean, 4 * g + r) * kc[0]) * __shfl(rstd, 4 * g + r);
                pv = u32x2{pk2(v[0], v[1]), pk2(v[2], v[3])};
                if (m == 3 && c16 >= 4) pv = c16 == 4 ? u32x2{ONES2, ONES2} : u32x2{0u, 0u};
            }
            bst8(rs, pv, kvo, WS_KV + (tb * 2 + (w >> 1)) * KV_TILE);
        }
        STAMP(2);
        u32x4 f[2][5];
        zero_acc(acc[0]); zero_acc(acc[1]);
        float sc = rstd * QSCALE, ms = -mean * sc;
        unsigned xs = L + L_WQ1 + (w ^ 1) * 5120 + lane * 16, xv = L + L_WQ1 + w * 5120 + lane * 16;
        PIN(sc); PIN(ms); PIN(xs); PIN(xv);
        WQ_ADDR();
        unsigned b0 = wo0 + L_WQ1 + kh * 5120, b1 = wo1 + L_WQ1 + kh * 5120, b2 = wo0 + L_WQ2 + kh * 5120, b3 = wo1 + L_WQ2 + kh * 5120, cq = CNT(0), cv = 0;
        PIN(b0); PIN(b1); PIN(b2); PIN(b3); PIN(cq);
        auto qfrag = [&](int j, int s) { return j < 4 ? ldstr2(b0 + j * 10240 + s * 32, b1 + j * 10240 + s * 32)
                                                      : ldstr2(b2 + (j - 4) * 10240 + s * 32, b3 + (j - 4) * 10240 + s * 32); };
        cnt_spin(CNT(C_WQ), 4);
#pragma unroll
        for (int s = 0; s < 5; ++s) f[0][s] = qfrag(0, s);
        SBAR();
#pragma unroll
        for (int j = 0; j < 10; ++j) {
            if (j < 8 && !(j & 1)) cv = *(volatile LAS unsigned*)(size_t)(cq + (C_WQ + j / 2 + 1) * 4);
            if (j < 9 && (j & 1) && __builtin_expect((unsigned)__builtin_amdgcn_readfirstlane(cv) < 4u, 0)) cnt_spin(CNT(C_WQ + j / 2 + 1), 4);
            asm volatile("" ::: "memory");
            SBAR();
#pragma unroll
            for (int s = 0; s < 5; ++s) {
                if (j < 9) f[(j + 1) & 1][s] = qfrag(j + 1, s);
                SBAR();
                mma16(acc[0][s], f[j & 1][s], xf[j]); mma16(acc[1][s], f[j & 1][s], xo[j]);
                SBAR();
            }
            if (j == 2) {
                wait_vm<0>();
                asm volatile("" ::"v"(pf[0]), "v"(pf[1]), "v"(pf[2]), "v"(pf[3]));
                MAKE_TAG();
                if (cnt_arrive(CNT(C_KVST)) == 3 && m < 4 && lane == 0) set_flag(ws, FL_KV + (tb * 4 + m) * 128, tag);
            }
            if (j == 7) cnt_spin(CNT(C_KVST), 4);
        }
        STAMP(3);
        mma_done();
        send_half(acc[1], xs);
        cnt_add(CNT(C_XCH + (w >> 1)));
        if (w == 0 && lane < M_NUM) *(LAS unsigned*)(size_t)CN2(lane) = 0u;
        f32x4 c5[5], p5[5];
#pragma unroll 1
        for (;;) {
            const unsigned n1 = *(volatile LAS unsigned*)(size_t)CNT(C_CS), n2 = *(volatile LAS unsigned*)(size_t)CNT(C_XCH + (w >> 1));
#pragma unroll
            for (int s = 0; s < 5; ++s) {
                c5[s] = *(volatile LAS f32x4*)(size_t)(L + L_CSX + 1280 + s * 64 + g * 16);
                p5[s] = *(volatile LAS f32x4*)(size_t)(xv + s * 1024);
            }
            if (__builtin_amdgcn_readfirstlane(n1 + n2) == 3) break;
        }
#pragma unroll
        for (int s = 0; s < 5; ++s) {
            const f32x4 v = (acc[0][s] + p5[s]) * sc + c5[s] * ms;
            *(LAS u32x2*)(size_t)qw[s] = u32x2{pk2(v[0], v[1]), pk2(v[2], v[3])};
        }
        bar();
        const int r32 = lane & 31, h = lane >> 5;
        const unsigned qb = L + L_QX + ((w & 1) * 4 + (w >> 1)) * 1280;
        qa1 = lds16(qb + lane * 16); qb1 = lds16(qb + 2560 + lane * 16);
        qa2 = u32x2{lds4(qb + 1024 + r32 * 8 + h * 4), 0u}; qb2 = u32x2{lds4(qb + 2560 + 1024 + r32 * 8 + h * 4), 0u};
    }
    STAMP(4);
    {
        LANE_VARS();
        const int r32 = lane & 31, h = lane >> 5, vrow = min(r32, 21);
        const unsigned kconst = h == 0 ? 0x00003f80u : 0u;
        unsigned ak1 = L + lane * 16, ak2 = L + 1024 + r32 * 8 + h * 4, av = L + 1280 + (h * 22 + vrow) * 16;
        const f32x16 zero16 = {0, 0, 0, 0, 0, 0, 0, 0, 0, 0, 0, 0, 0, 0, 0, 0};
        f32x16 sa, sb, oa = zero16, ob = zero16;
        u32x4 ka1 = lds16(ak1), kb1 = lds16(ak1 + KV_TILE), va0 = lds16(av), va1 = lds16(av + VS), vb0 = va0, vb1 = va0;
        u32x2 ka2 = {lds4(ak2), kconst}, kb2 = {lds4(ak2 + KV_TILE), kconst};
        sa = mfma32(ka1, qa1, mfma32k8(ka2, qa2, zero16));
        sb = mfma32(ka1, qb1, mfma32k8(ka2, qb2, zero16));
        {
            const float ma = halfmax(max16(sa)), mb = halfmax(max16(sb));
            qa2[1] = h == 0 ? (pk2(-ma, 0.f) & 0xffffu) : 0u;
            qb2[1] = h == 0 ? (pk2(-mb, 0.f) & 0xffffu) : 0u;
        }
        sa = mfma32(ka1, qa1, mfma32k8(ka2, qa2, zero16));
        u32x4 pa0 = {0u, 0u, 0u, 0u}, pa1 = pa0, pb0 = pa0, pb1 = pa0;
        float e2 = 0.f, e3 = 0.f;
        unsigned vo0 = (r32 >> 4) * 20480 + h * 256 + (r32 & 15) * 16, vo1 = h ? 0x80000000u : vo0;
        PIN(pa0); PIN(pa1); PIN(pb0); PIN(pb1); PIN(ka2); PIN(kb2); PIN(vo0); PIN(vo1);
#define SLOT(R, MF, X, S, i, PP, jp, P, j) do { R = MF; PIN(R); SBAR(); X; SBAR(); \
        float e0 = EX2(S[i]), e1 = EX2(S[i + 1]); PIN(e0); PIN(e1); SBAR(); \
        PP[jp] = pk2(e2, e3); PIN(PP); SBAR(); \
        e2 = EX2(S[i + 2]); e3 = EX2(S[i + 3]); PIN(e2); PIN(e3); SBAR(); \
        P[j] = pk2(e0, e1); PIN(P); SBAR(); } while (0)
#define STEP(KC1, KC2, KN1, KN2, VC0, VC1, VP0, VP1, OK, OV, X0, X7) do { \
        SLOT(sb, mfma32k8(KC2, qb2, zero16), X0, sa, 0, pb1, 3, pa0, 0); \
        SLOT(sb, mfma32(KC1, qb1, sb), __builtin_amdgcn_s_waitcnt(0xC07F), sa, 4, pa0, 1, pa0, 2); \
        SLOT(ob, mfma32(VP1, pb1, ob), KC1 = lds16(ak1 + (OK) * KV_TILE), sa, 8, pa0, 3, pa1, 0); \
        SLOT(oa, mfma32(VC0, pa0, oa), KC2[0] = lds4(ak2 + (OK) * KV_TILE), sa, 12, pa1, 1, pa1, 2); \
        SLOT(sa, mfma32k8(KN2, qa2, zero16), (void)0, sb, 0, pa1, 3, pb0, 0); \
        SLOT(sa, mfma32(KN1, qa1, sa), VP0 = lds16(av + (OV) * KV_TILE), sb, 4, pb0, 1, pb0, 2); \
        SLOT(oa, mfma32(VC1, pa1, oa), VP1 = lds16(av + (OV) * KV_TILE + VS), sb, 8, pb0, 3, pb1, 0); \
        SLOT(ob, mfma32(VC0, pb0, ob), X7, sb, 12, pb1, 1, pb1, 2); } while (0)
        unsigned ea = CN2(E_C1), ev = 0, one = 1;
        PIN(ea); PIN(one); PIN(ak1); PIN(ak2); PIN(av);
        auto hook0 = [&](int j, int dn) {
            if (j == 14) { ak1 += dn; ak2 += dn; PIN(ak1); PIN(ak2); }
            if (j == 15) { av += dn; ea += 4; PIN(av); PIN(ea); }
        };
        auto hook7 = [&](int j) {
            if (j == 0)
                asm volatile("s_mov_b64 exec, 1\n\tds_add_u32 %0, %1 offset:%2\n\ts_mov_b64 exec, -1" ::"v"(ea), "v"(one), "i"((P_NONE - E_C1) * 4) : "memory");
            if (j == 5) ev = *(volatile LAS unsigned*)(size_t)ea;
            if (j == 6 && __builtin_expect((unsigned)__builtin_amdgcn_readfirstlane(ev) < 4u, 0))
                cnt_wait(ea, 4);
        };
        STAMP(5);
#pragma unroll 1
        for (int c = 0; c < 4; ++c) {
            const int dn = c == 2 ? -2 * CHUNK : CHUNK;
#pragma unroll
            for (int j = 0; j < 16; j += 2) {
                STEP(ka1, ka2, kb1, kb2, va0, va1, vb0, vb1, (j + 2) & 15, j + 1, hook0(j, dn), hook7(j));
                STEP(kb1, kb2, ka1, ka2, vb0, vb1, va0, va1, (j + 3) & 15, (j + 2) & 15, hook0(j + 1, dn), hook7(j + 1));
            }
        }
        pb1[3] = pk2(e2, e3);
        ob = mfma32(vb1, pb1, ob);
        STAMP(6);
        float la = upper(oa[8]), lb = upper(ob[8]);
        if (__builtin_expect(!__all(row_ok(oa, la, h) && row_ok(ob, lb, h)), 0)) {
            __builtin_amdgcn_fence(__ATOMIC_ACQUIRE, "agent");
#pragma unroll 1
            for (int qt = 0; qt < 2; ++qt) {
                ob = attn_exact(ws + WS_KV, qt ? qb1 : qa1, qt ? qb2[0] : qa2[0], lane, vrow);
                if (qt == 0) oa = ob;
            }
            la = upper(oa[8]); lb = upper(ob[8]);
        }
        const float ra = __builtin_amdgcn_rcpf(la), rb = __builtin_amdgcn_rcpf(lb);
        const int pr = 2 * m + (w & 1);
        const unsigned so = WS_O + (tb * 4 + (w & 2)) * 20480;
        bst16(rs, u32x4{keep(pk2(oa[0] * ra, oa[1] * ra)), pk2(oa[2] * ra, oa[3] * ra), keep(pk2(oa[4] * ra, oa[5] * ra)), pk2(oa[6] * ra, oa[7] * ra)}, vo0, so + pr * 1024);
        bst16(rs, u32x4{keep(pk2(ob[0] * rb, ob[1] * rb)), pk2(ob[2] * rb, ob[3] * rb), keep(pk2(ob[4] * rb, ob[5] * rb)), pk2(ob[6] * rb, ob[7] * rb)}, vo0, so + pr * 1024 + 512);
        bst16(rs, u32x4{keep(pk2(oa[8] * ra, oa[9] * ra)), pk2(oa[10] * ra, oa[11] * ra), keep(pk2(ob[8] * rb, ob[9] * rb)), pk2(ob[10] * rb, ob[11] * rb)}, vo1, so + 16384 + pr * 256);
        STAMP(7);
    }
    u32x4 wd[2][7], ae[RE][2], we[3][5];
    unsigned ge[9], e0, e1, ehv, eprobe, a0;
    const unsigned ehs = WS_F + (tb * 4 + (w & 2)) * 55296 + 16384;
    {
        LANE_VARS();
        const unsigned wl = L + lane * 16;
        f32x4 acc[2][5];
        u32x4 act[RC][2], wf[3][5];
        unsigned wa = wl + L_WO + kh * 5120, wb = wa + 51200, gb[5];
        rot_bases(gb, WS_O + tb * 81920 + kh * 1024, rotc, 4096);
        unsigned v0 = lane * 16 + w * 20480, v1 = lane * 16 + (w ^ 1) * 20480, hv = c16 * 256 + (g & 1) * 20480;
        unsigned xs = wl + (w == 2 ? 35840 : XCH + (w ^ 1) * 5120), xv = wl + (w == 3 ? 35840 : XCH + w * 5120);
        unsigned sv = g == 0 ? c16 * 8 : 0x80000000u, h2v = (g & 1) ? 0x80000000u : (g >> 1) * 256 + c16 * 16;
        PIN(v0); PIN(v1); PIN(hv);
        kstart<RC, 2, 2048>(act, rs, ws, v0, v1, gb,
                           [&] { return __builtin_amdgcn_raw_buffer_load_b32(rs, hv, WS_O + (tb * 4 + (w & 2)) * 20480 + 16384, SC1) == EMPTY; }, [&] {
            bar();
            PIN(wa); PIN(wb); PIN(xs); PIN(xv); PIN(sv); PIN(h2v);
#pragma unroll
            for (int s = 0; s < 5; ++s) wf[0][s] = lds16(wa + s * 1024);
#pragma unroll
            for (int s = 0; s < 5; ++s) wf[1][s] = lds16(wa + 10240 + s * 1024);
            res_init(acc[0], xr0, xr1, xr2); zero_acc(acc[1]);

            wait_vm<0>();
        }, [&] {
        });
        STAMP(8);
        kloop<0, 10, 10, 5, 5, 2, RC, 2, 2048>(acc, act, wf, [&](int j, int s) { return (j < 5 ? wa : wb) + (j % 5) * 10240 + s * 1024; }, rs, ws, v0, v1, gb);
        mma_done();
        send_half(acc[1], xs);
        bar();
        recv_half(acc[0], xv);
#pragma unroll
        for (int s = 0; s < 7; ++s) wd[0][s] = lds16(wl + (s < 5 ? 16384 + s * 1024 : (s - 5) * 1024));
        SBAR();
        res_pack(xr0, xr1, xr2, acc[0]);
        xr0[0] = keep(xr0[0]); xr0[2] = keep(xr0[2]); xr1[0] = keep(xr1[0]); xr1[2] = keep(xr1[2]); xr2[0] = keep(xr2[0]);
        const unsigned hb = WS_H + (tb * 4 + w) * 20480;
        bst16(rs, xr0, lane * 16, hb + 2 * m * 1024);
        bst16(rs, xr1, lane * 16, hb + (2 * m + 1) * 1024);
        bst16(rs, rowpair(xr2, xr2), h2v, hb + 16384 + m * 512);
#pragma unroll
        for (int s = 0; s < 7; ++s) PIN(wd[0][s]);
        cnt_add(CN2(M_LDS));
        float sm = 0.f, sq = 0.f, sm2 = 0.f, sq2 = 0.f;
#pragma unroll
        for (int i = 0; i < 4; ++i) { sm = dot2(xr0[i], ONES2, sm); sq = dot2(xr0[i], xr0[i], sq); sm2 = dot2(xr1[i], ONES2, sm2); sq2 = dot2(xr1[i], xr1[i], sq2); }
        sm = dot2(xr2[0], ONES2, sm); sq = dot2(xr2[0], xr2[0], sq); sm2 = dot2(xr2[1], ONES2, sm2); sq2 = dot2(xr2[1], xr2[1], sq2);
        sm = xsum16_32(sm + sm2); sq = xsum16_32(sq + sq2);
        bst8(rs, u32x2{keep(__float_as_uint(sm)), __float_as_uint(sq)}, sv, WS_ST + (tb * 4 + w) * 1024 + m * 128);
        SBAR();
        STAMP(9);
    }
    {
        f32x4 acc[2][7], c4[7];
        u32x4 act[12][2], wf[3][2];
        u32x2 st[4];
        float mu[2], rp[2];
        const int t16 = (tb >> 1) * 8 + 2 * w;
        unsigned v0 = lane_id() * 16, v1 = v0 + 20480, sv = v0 >> 1, wa = L + v0, wb = wa + 16384 + 46080, gb[5];
        PIN(sv); PIN(wa); PIN(wb); PIN(v0); PIN(v1);
        zero_acc(acc[0]); zero_acc(acc[1]);
        rot_bases(gb, WS_H + t16 * 20480, rotd, 4096);
        e_bases(ge, WS_F + tb * 221184 + kh * 1024, m, 6144);
        unsigned fs[7], wc, fw, f2v, ca;
        {
            const unsigned fb = WS_F + t16 * 55296 + ct * 1024;
            fs[0] = fb; fs[1] = fb + 55296; fs[2] = fb + 16384 - ct * 512; fs[3] = fb + 24576; fs[4] = fb + 24576 + 55296; fs[5] = fb + 39936; fs[6] = fb + 39936 + 55296;
#pragma unroll
            for (int i = 0; i < 7; ++i) asm volatile("" : "+s"(fs[i]));
            const int lane = v0 >> 4, g = lane >> 4, c16 = lane & 15;
            wc = wa + 118784; fw = ct < 15 ? v0 : 0x80000000u; f2v = (g & 1) * 55296 + (g >> 1) * 256 + c16 * 16; ca = L + L_MISC + g * 16;
            e0 = v0 + w * 55296; e1 = v0 + (w ^ 1) * 55296; ehv = (g & 1) * 55296 + c16 * 512; a0 = wa + kh * 5120;
            PIN(wc); PIN(fw); PIN(f2v); PIN(ca); PIN(e0); PIN(e1); PIN(ehv); PIN(a0);
        }
        kstart<RD, 4, 1024>(act, rs, ws, v0, v1, gb, [&] {
#pragma unroll
            for (int i = 0; i < 4; ++i) st[i] = __builtin_amdgcn_raw_buffer_load_b64(rs, sv + i * 512, WS_ST + t16 * 1024, SC1);
            return max(max(st[0][0], st[1][0]), max(st[2][0], st[3][0])) == EMPTY; }, [&] { wait_vm<0>(); }, [&] {
#pragma unroll
            for (int t = 0; t < 2; ++t) {
                const float sm = xsum16_32(__uint_as_float(st[2 * t][0]) + __uint_as_float(st[2 * t + 1][0]));
                const float sq = xsum16_32(__uint_as_float(st[2 * t][1]) + __uint_as_float(st[2 * t + 1][1]));
                mu[t] = sm * (-1.f / D);
                rp[t] = __builtin_amdgcn_rsqf(fmaxf(sq * (1.f / D) - mu[t] * mu[t], 0.f) + LN_EPS) * LOG2E;
                PIN(mu[t]); PIN(rp[t]);
            } });
        STAMP(10);
        auto wad = [&](int j, int s) { return s >= 5 ? wa + j * 2048 + (s - 5) * 1024 : (j < 9 ? wa + 16384 + j * 5120 : wb + (j - 9) * 5120) + s * 1024; };
        kloop<0, 2, 20, 7, 7, 1, RD, 4, 1024>(acc, act, wd, wad, rs, ws, v0, v1, gb);
        bar_raw();
        kloop<2, 6, 20, 7, 5, 1, RD, 4, 1024>(acc, act, wd, wad, rs, ws, v0, v1, gb);
        kloop<8, 1, 20, 5, 5, 1, RD, 4, 1024>(acc, act, wd, wad, rs, ws, v0, v1, gb);
        cnt_add(CN2(M_K8));
        kloop<9, 8, 20, 5, 5, 1, RD, 4, 1024>(acc, act, wd, wad, rs, ws, v0, v1, gb);
#pragma unroll
        for (int i = 0; i < 4; ++i) wf[i >> 1][i & 1] = lds16(wc + i * 1024);
#pragma unroll
        for (int s = 0; s < 7; ++s) c4[s] = ldsf4(ca + s * 64);
        SBAR();
        kloop<17, 3, 20, 5, 5, 1, RD, 4, 1024>(acc, act, wd, wad, rs, ws, v0, v1, gb);
        bar_raw();
        STAMP(11);
        u32x2 lo, hi;
        auto silu = [&](const f32x4& a, const f32x4& c, int t, u32x2& r, auto side) {
            f32x4 y = (a + mu[t] * c) * rp[t], e;
            PIN(y); SBAR();
            side(0); SBAR();
            e[0] = EX2(-y[0]); e[1] = EX2(-y[1]); PIN(e); SBAR();
            side(1); SBAR();
            e[2] = EX2(-y[2]); e[3] = EX2(-y[3]); PIN(e); SBAR();
            e = e * LOG2E + LOG2E; PIN(e); SBAR();
            side(2); SBAR();
            e[0] = __builtin_amdgcn_rcpf(e[0]); e[1] = __builtin_amdgcn_rcpf(e[1]); PIN(e); SBAR();
            side(3); SBAR();
            e[2] = __builtin_amdgcn_rcpf(e[2]); e[3] = __builtin_amdgcn_rcpf(e[3]); PIN(e); SBAR();
            e *= y;
            r = u32x2{keep(pk2(e[0], e[1])), pk2(e[2], e[3])};
            PIN(r); SBAR();
        };
        auto bstep = [&](int b, int i) {
            mma16(acc[i & 1][5 + (i >> 1)], wf[b % 3][i >> 1], act[(b + 8) % 12][i & 1]);
            SBAR();
            if (i < 2 && b + 2 < 12) wf[(b + 2) % 3][i] = lds16(wc + (b + 2) * 2048 + i * 1024);
        };
        auto early = [&](int q) {
            if (q == Q0 - 1) eprobe = __builtin_amdgcn_raw_buffer_load_b32(rs, ehv, ehs, SC1);
            const int n = (q - Q0) / QS;
            if (q >= Q0 && (q - Q0) % QS == 0 && n < 2 * RE) ae[n >> 1][n & 1] = aload<3, 2048>(rs, (n & 1) ? e1 : e0, ge, n >> 1);
        };
#pragma unroll
        for (int i = 0; i < 8; ++i) { bstep(i >> 2, i & 3); SBAR(); }
#pragma unroll
        for (int b = 0; b < 10; ++b) {
            const int ut = b < 4 ? b >> 1 : b < 6 ? b & 1 : (b - 6) >> 1, ui = b < 4 ? b & 1 : b < 6 ? 2 : 3 + (b & 1), us = ui < 2 ? ui : ui == 2 ? 6 : ui - 1;
            silu(acc[ut][ui], c4[us], ut, (b & 1) ? hi : lo, [&](int i) { bstep(b + 2, i); early(b * 4 + i); });
            if (b == 5) bst16(rs, rowpair(lo, hi), f2v, fs[2]);
            else if (b & 1) bst16(rs, u32x4{lo[0], lo[1], hi[0], hi[1]}, b < 5 ? v0 : fw, fs[b >> 1]);
            SBAR();
        }
        bar_raw();
        mma_done();
#pragma unroll
        for (int u = 0; u < 4; ++u) {
            silu(acc[u >> 1][5 + (u & 1)], c4[4 + (u & 1)], u >> 1, (u & 1) ? hi : lo, [&](int i) {
                const int n = u * 4 + i;
                early(40 + n);
                if (n < 10) we[n / 5][n % 5] = lds16(a0 + (n / 5) * 10240 + (n % 5) * 1024); });
            if (u & 1) bst16(rs, u32x4{lo[0], lo[1], hi[0], hi[1]}, fw, fs[5 + (u >> 1)]);
            SBAR();
        }
#pragma unroll
        for (int q = 56; q < Q0 + 2 * RE * QS; ++q) { early(q); SBAR(); }
        STAMP(12);
    }
    {
        f32x4 acc[2][5];
        unsigned a1 = a0 + 61440, a2 = a0 + 122880;
        PIN(a1); PIN(a2);
        res_init(acc[0], xr0, xr1, xr2); zero_acc(acc[1]);
        if (__any(eprobe == EMPTY))
            kstart<RE, 3, 2048>(ae, rs, ws, e0, e1, ge, [&] { return __builtin_amdgcn_raw_buffer_load_b32(rs, ehv, ehs, SC1) == EMPTY; }, [] {}, [] {});
        if (__builtin_expect(missing(ae[0][0], ae[0][1]), 0)) refetch(ae[0][0], ae[0][1], ws + ge[0], e0, e1);
        SBAR();
        STAMP(13);
        auto wae = [&](int j, int s) { const int q = j % 15; return (q < 6 ? a0 : q < 12 ? a1 : a2) + (q % 6) * 10240 + s * 1024; };
#define GRP(G, WA, WN) kloop<3 * G, 3, 27, 5, 5, 2, RE, 3, 2048>(acc, ae, we, wae, rs, ws, e0, e1, ge)
        GRP(0, a0, a0 + 30720);
        bar_raw();

        GRP(1, a0 + 30720, a1);
        bar_raw();
        GRP(2, a1, a1 + 30720);
        bar_raw();
        GRP(3, a1 + 30720, a2); bar_raw();
        GRP(4, a2, a0); bar_raw();
        GRP(5, a0, a0 + 30720); bar_raw();
        GRP(6, a0 + 30720, a1); bar_raw();
        GRP(7, a1, a1 + 30720);
        GRP(8, a1 + 30720, a1);
        mma_done();
        LANE_VARS();
        const unsigned wl = L + lane * 16;
        send_half(acc[1], wl + 122880 + (w ^ 1) * 5120);
        bar();
        recv_half(acc[0], wl + 122880 + w * 5120);
        STAMP(14);
        res_pack(xr0, xr1, xr2, acc[0]);
        char* ob = out + (size_t)(tb * 64 + w * 16 + c16) * 1280 + m * 160;
        asm volatile("global_store_dwordx4 %0, %1, off sc0 sc1\n\tglobal_store_dwordx4 %0, %2, off offset:64 sc0 sc1\n\t"
                     "global_store_dwordx2 %3, %4, off offset:128 sc0 sc1\n\ts_nop 1" ::"v"(ob + g * 16), "v"(xr0), "v"(xr1), "v"(ob + g * 8), "v"(xr2) : "memory");
    }
    asm volatile(".Lpf_end1:");
    STAMP_OUT(0);
    __builtin_amdgcn_endpgm();
}

extern "C" void kernel_launch(void* const* d_in, const int* in_sizes, int n_in, void* d_out, int out_size, void* d_ws, size_t ws_size,
                              hipStream_t stream) {
    (void)hipFuncSetAttribute((const void*)k_block, hipFuncAttributeMaxDynamicSharedMemorySize, LDS_BYTES);
    k_block<<<256, 512, LDS_BYTES, stream>>>((const char*)d_in[0], (const char*)d_in[1], (const char*)d_in[2], (const char*)d_in[3],
                                             (const char*)d_in[4], (const char*)d_in[6], (const char*)d_in[7], (char*)d_ws, (char*)d_out);
}
```

```cpp
#include <hip/hip_runtime.h>
#include <hip/hip_bf16.h>
#include <stdint.h>

typedef __attribute__((ext_vector_type(8))) __bf16 bf16x8;
typedef __attribute__((ext_vector_type(2))) __bf16 bf16x2;
typedef __attribute__((ext_vector_type(4))) short s16x4;
typedef __attribute__((ext_vector_type(4))) float f32x4;
typedef __attribute__((ext_vector_type(2))) float f32x2;
typedef __attribute__((ext_vector_type(16))) float f32x16;
typedef __attribute__((ext_vector_type(4))) unsigned u32x4;
typedef __attribute__((ext_vector_type(2))) unsigned u32x2;
typedef unsigned long long u64;
#define LAS __attribute__((address_space(3)))
#define GAS __attribute__((address_space(1)))
#define DI __device__ __forceinline__

constexpr int D = 640;
constexpr float LN_EPS = 1e-5f;
constexpr float LOG2E = 1.4426950408889634f;
constexpr float QSCALE = 0.22360679774997896f * LOG2E;

constexpr int KV_TILE = 2688;
constexpr int VS = 704;
constexpr unsigned WS_FL = 0;
constexpr unsigned FL_KV = 32768, FL_PR = 2 * 32768, FL_MK = 3 * 32768;
constexpr unsigned WS_KV = 4 * 32768;
constexpr unsigned WS_WO = WS_KV + 64 * KV_TILE;
constexpr unsigned WS_W1 = WS_WO + 8 * 20 * 5 * 1024;
constexpr unsigned WS_W2 = WS_W1 + 16 * 20 * 7 * 1024;
constexpr unsigned WS_CS = WS_W2 + 8 * 54 * 5 * 1024;
constexpr unsigned WS_O  = WS_CS + 20 * 1792 * 4;
constexpr unsigned WS_H  = WS_O + 128 * 20 * 1024;
constexpr unsigned WS_F  = WS_H + 128 * 20 * 1024;
constexpr unsigned WS_ST = WS_F + 128 * 54 * 1024;
constexpr unsigned WS_END = WS_ST + 2048 * 64;
constexpr unsigned EMPTY = 0xffffffffu;
static_assert((WS_END - WS_O) == 256 * 48640, "fill share per workgroup");

constexpr int CHUNK = 16 * KV_TILE;
constexpr int L_X = 0, L_WQ2 = 43008, L_WKV = 83968, L_WQ1 = 122880,
               L_CSX = 111104, L_QX = 152576, L_MISC = 162816;
constexpr int L_AUX = 3 * CHUNK, L_WO = CHUNK, L_CNT = 110592, XCH = L_WO + 102400, LDS_BYTES = 163840;

#define STAMP_INIT()
#define STAMP(I)
#define STAMP_OUT(K)

#define THROT 3
#define THROT2 3
#define RC 6
#define RD 7
#define RE 7
#define TAILPRIO 0
#define FIRSTFILL 4
#define ONEWAY 1
#define B0AT 0
#define NEARLY 12
#define PFDELAY 6
#define Q0 57
#define QS 1
#define WKVAT 2
#define FLAGAT 0
#define P1PRIO 1
DI void glds(unsigned voff, const void* sbase, unsigned lds) {
    asm volatile("s_mov_b32 m0, %2\n\ts_nop 0\n\tglobal_load_lds_dwordx4 %0, %1" ::"v"(voff), "s"(sbase), "s"(lds) : "memory");
}
DI void glds_dev(unsigned voff, const void* sbase, unsigned lds) {
    asm volatile("s_mov_b32 m0, %2\n\ts_nop 0\n\tglobal_load_lds_dwordx4 %0, %1 sc1" ::"v"(voff), "s"(sbase), "s"(lds) : "memory");
}
template <int N> DI void wait_vm() { asm volatile("s_waitcnt vmcnt(%0)" ::"i"(N) : "memory"); }
DI void bar() { asm volatile("s_waitcnt lgkmcnt(0)\n\ts_barrier" ::: "memory"); }
DI void bar_raw() { asm volatile("s_barrier" ::: "memory"); }
#define PIN(x) asm volatile("" : "+v"(x))
#define SBAR() __builtin_amdgcn_sched_barrier(0)
DI unsigned pk2(float a, float b) { bf16x2 v = {(__bf16)a, (__bf16)b}; return __builtin_bit_cast(unsigned, v); }
DI float bflo(unsigned u) { return __uint_as_float(u << 16); }
DI float bfhi(unsigned u) { return __uint_as_float(u & 0xffff0000u); }
DI f32x4 mfma16(u32x4 a, u32x4 b, f32x4 c) {
    return __builtin_amdgcn_mfma_f32_16x16x32_bf16(__builtin_bit_cast(bf16x8, a), __builtin_bit_cast(bf16x8, b), c, 0, 0, 0);
}
DI f32x16 mfma32(u32x4 a, u32x4 b, f32x16 c) {
    return __builtin_amdgcn_mfma_f32_32x32x16_bf16(__builtin_bit_cast(bf16x8, a), __builtin_bit_cast(bf16x8, b), c, 0, 0, 0);
}
DI f32x16 mfma32k8(u32x2 a, u32x2 b, f32x16 c) {
    return __builtin_amdgcn_mfma_f32_32x32x8bf16_1k(__builtin_bit_cast(s16x4, a), __builtin_bit_cast(s16x4, b), c, 0, 0, 0);
}
DI u32x4 lds16(unsigned a) { return *(const LAS u32x4*)(size_t)a; }
DI u32x2 lds8(unsigned a) { return *(const LAS u32x2*)(size_t)a; }
DI unsigned lds4(unsigned a) { return *(const LAS unsigned*)(size_t)a; }
DI f32x4 ldsf4(unsigned a) { return *(const LAS f32x4*)(size_t)a; }
DI u32x2 ldstr(unsigned a) { return __builtin_bit_cast(u32x2, __builtin_amdgcn_ds_read_tr16_b64_v4i16((LAS s16x4*)(size_t)a)); }
DI u32x4 ldstr2(unsigned a0, unsigned a1) { u32x2 lo = ldstr(a0), hi = ldstr(a1); return u32x4{lo[0], lo[1], hi[0], hi[1]}; }
DI float dot2(unsigned a, unsigned b, float c) {
    return __builtin_amdgcn_fdot2_f32_bf16(__builtin_bit_cast(bf16x2, a), __builtin_bit_cast(bf16x2, b), c, false);
}
DI float xsum16_32(float v) {
    const auto a = __builtin_amdgcn_permlane16_swap(__float_as_uint(v), __float_as_uint(v), false, false);
    v = __uint_as_float(a[0]) + __uint_as_float(a[1]);
    const auto b = __builtin_amdgcn_permlane32_swap(__float_as_uint(v), __float_as_uint(v), false, false);
    return __uint_as_float(b[0]) + __uint_as_float(b[1]);
}
constexpr unsigned ONES2 = 0x3f803f80u;
#define EX2(v) __builtin_amdgcn_exp2f(v)
typedef __amdgpu_buffer_rsrc_t rsrc_t;
constexpr int SC1 = 16;
DI u32x4 bld16(rsrc_t r, unsigned v, unsigned s) { return __builtin_amdgcn_raw_buffer_load_b128(r, v, s, SC1); }
DI void bst16(rsrc_t r, u32x4 d, unsigned v, unsigned s) {
    __builtin_amdgcn_raw_buffer_store_b128(d, r, v, s, SC1);
    asm volatile("s_nop 1" ::"v"(d) : "memory");
}
DI void bst8(rsrc_t r, u32x2 d, unsigned v, unsigned s) { __builtin_amdgcn_raw_buffer_store_b64(d, r, v, s, SC1); }

DI void set_flag(char* ws, unsigned off, u64 tag) { __hip_atomic_store((GAS u64*)(ws + off), tag, __ATOMIC_RELAXED, __HIP_MEMORY_SCOPE_AGENT); }
DI void poll(char* ws, unsigned arr, int index, u64 tag) {
    const GAS u64* p = (const GAS u64*)(ws + arr + index * 128);
    bool ok = index < 0;
#pragma unroll 1
    for (int spin = 0; spin < (1 << 18); ++spin) {
        if (!ok) ok = __hip_atomic_load(p, __ATOMIC_RELAXED, __HIP_MEMORY_SCOPE_AGENT) == tag;
        if (__all(ok)) break;
        __builtin_amdgcn_s_sleep(1);
    }
    asm volatile("" ::: "memory");
}
DI void copy_dev(const char* src, unsigned lds, int n, int hw, int lane) {
#pragma unroll 1
    for (int p = hw; p < n; p += 4) glds_dev(lane * 16, src + p * 1024, lds + p * 1024);
}

DI void cnt_add(unsigned a) {
    asm volatile("s_waitcnt lgkmcnt(0)\n\ts_mov_b64 exec, 1\n\tds_add_u32 %0, %1\n\ts_mov_b64 exec, -1" ::"v"(a), "v"(1u) : "memory");
}
DI unsigned cnt_arrive(unsigned a) {
    unsigned r;
    asm volatile("s_waitcnt lgkmcnt(0)\n\ts_mov_b64 exec, 1\n\tds_add_rtn_u32 %0, %1, %2\n\ts_mov_b64 exec, -1\n\ts_waitcnt lgkmcnt(0)"
                 : "=&v"(r) : "v"(a), "v"(1u) : "memory");
    return __builtin_amdgcn_readfirstlane(r);
}
DI void cnt_spin(unsigned a, unsigned n) {
#pragma unroll 1
    while ((unsigned)__builtin_amdgcn_readfirstlane(*(volatile LAS unsigned*)(size_t)a) < n) {}
    asm volatile("" ::: "memory");
}
DI void cnt_wait(unsigned a, unsigned n) {
#pragma unroll 1
    while ((unsigned)__builtin_amdgcn_readfirstlane(*(volatile LAS unsigned*)(size_t)a) < n) __builtin_amdgcn_s_sleep(1);
    asm volatile("" ::: "memory");
}
DI unsigned keep(unsigned v) { return min(v, EMPTY - 1); }
DI bool missing(const u32x4& a, const u32x4& b) { return __any(max(max(a[0], a[2]), max(b[0], b[2])) == EMPTY); }
DI void refetch(u32x4& a, u32x4& b, const char* base, unsigned v0, unsigned v1) {
#pragma unroll 1
    for (int spin = 0; spin < (1 << 16); ++spin) {
        asm volatile("global_load_dwordx4 %0, %2, %4 sc1\n\tglobal_load_dwordx4 %1, %3, %4 sc1\n\ts_waitcnt vmcnt(0)"
                     : "+v"(a), "+v"(b) : "v"(v0), "v"(v1), "s"(base) : "memory");
        if (!missing(a, b)) break;
    }
}
DI u32x4 rowpair(const u32x2& a, const u32x2& b) {
    const auto s0 = __builtin_amdgcn_permlane16_swap(a[0], b[0], false, false), s1 = __builtin_amdgcn_permlane16_swap(a[1], b[1], false, false);
    return u32x4{s0[0], s1[0], s0[1], s1[1]};
}
DI int lane_id() {
    int l;
    asm volatile("v_mbcnt_lo_u32_b32 %0, -1, 0\n\tv_mbcnt_hi_u32_b32 %0, -1, %0" : "=v"(l));
    return l;
}
#define LANE_VARS() const int lane = lane_id(), g = lane >> 4, c16 = lane & 15; (void)g; (void)c16
DI float halfmax(float m) {
    auto r = __builtin_amdgcn_permlane32_swap(__float_as_uint(m), __float_as_uint(m), false, false);
    return fmaxf(__uint_as_float(r[0]), __uint_as_float(r[1]));
}
DI float upper(float v) {
    auto r = __builtin_amdgcn_permlane32_swap(__float_as_uint(v), __float_as_uint(v), false, false);
    return __uint_as_float(r[1]);
}
DI float max16(const f32x16& s) {
    float a = fmaxf(fmaxf(s[0], s[1]), s[2]), b = fmaxf(fmaxf(s[3], s[4]), s[5]);
    a = fmaxf(fmaxf(a, s[6]), s[7]); b = fmaxf(fmaxf(b, s[8]), s[9]);
    a = fmaxf(fmaxf(a, s[10]), s[11]); b = fmaxf(fmaxf(b, s[12]), s[13]);
    a = fmaxf(fmaxf(a, s[14]), s[15]);
    return fmaxf(a, b);
}
DI bool row_ok(const f32x16& o, float l, int h) {
    float am = 0.f;
#pragma unroll
    for (int i = 0; i < 8; ++i) am = fmaxf(am, fabsf(o[i]));
    if (h == 0) {
#pragma unroll
        for (int i = 8; i < 12; ++i) am = fmaxf(am, fabsf(o[i]));
    }
    return (l >= 1e-18f) && (l <= 1e18f) && (am <= 1e37f);
}
DI f32x16 attn_exact(const char* kv, u32x4 q1, unsigned q2lo, int lane, int vrow) {
    const int r32 = lane & 31, h = lane >> 5;
    const f32x16 zero16 = {0, 0, 0, 0, 0, 0, 0, 0, 0, 0, 0, 0, 0, 0, 0, 0};
    const u32x2 q2 = {q2lo, 0u};
    float m = -INFINITY;
    f32x16 o = zero16;
#pragma unroll 1
    for (int t = 0; t < 64; ++t) {
        const GAS char* tp = (const GAS char*)kv + t * KV_TILE;
        const u32x4 fk1 = *(const GAS u32x4*)(tp + lane * 16);
        const u32x2 fk2 = {*(const GAS unsigned*)(tp + 1024 + r32 * 8 + h * 4), 0u};
        const u32x4 fv0 = *(const GAS u32x4*)(tp + 1280 + (h * 22 + vrow) * 16), fv1 = *(const GAS u32x4*)(tp + 1280 + VS + (h * 22 + vrow) * 16);
        f32x16 s = mfma32(fk1, q1, mfma32k8(fk2, q2, zero16));
        const float mn = fmaxf(m, halfmax(max16(s))), al = EX2(m - mn);
        m = mn;
        u32x4 p0, p1;
#pragma unroll
        for (int i = 0; i < 4; ++i) { p0[i] = pk2(EX2(s[2 * i] - m), EX2(s[2 * i + 1] - m)); p1[i] = pk2(EX2(s[8 + 2 * i] - m), EX2(s[9 + 2 * i] - m)); }
#pragma unroll
        for (int i = 0; i < 16; ++i) o[i] *= al;
        o = mfma32(fv1, p1, mfma32(fv0, p0, o));
    }
    return o;
}

DI void mma16(f32x4& c, const u32x4& a, const u32x4& b) {
    asm volatile("v_mfma_f32_16x16x32_bf16 %0, %1, %2, %0" : "+v"(c) : "v"(a), "v"(b));
}
DI void mma16z(f32x4& c, const u32x4& a, const u32x4& b) { asm volatile("v_mfma_f32_16x16x32_bf16 %0, %1, %2, 0" : "=&v"(c) : "v"(a), "v"(b)); }
DI void mma_done() { SBAR(); asm volatile("s_nop 7\n\ts_nop 1"); SBAR(); }
template <int NS> DI void zero_acc(f32x4 (&a)[NS]) {
#pragma unroll
    for (int s = 0; s < NS; ++s) { a[s] = f32x4{0, 0, 0, 0}; PIN(a[s]); }
}
template <int GS, int ASTR, int NG> DI u32x4 aload(rsrc_t rs, unsigned& v, const unsigned (&gb)[NG], int j) {
    const int o = (j % GS) * ASTR;
    PIN(v);
    return bld16(rs, v + (o & 4095), gb[j / GS] + (o & ~4095));
}
template <int N> DI void lgkm() { __builtin_amdgcn_s_waitcnt(0xC07F | (N << 8)); }
template <int K0, int NK, int KT, int F0, int FT, int NS, int NSN, int PD, int GS, int ASTR, int NA, int RING, int POOL, int NG, class WAddr>
DI void kloop(f32x4 (&acc)[2][NA], u32x4 (&act)[RING][2], u32x4 (&wf)[POOL], WAddr waddr, rsrc_t rs, const char* ws, unsigned& v0,
              unsigned& v1, const unsigned (&gb)[NG]) {
    if constexpr (NK > 0) {
        constexpr int j = K0, jn = j - 1 + PD < KT ? j - 1 + PD : 0, jc = j + 1 < KT ? j + 1 : 0, nn = NK > 1 ? NS : NSN;
        constexpr int issued = F0 + NS + POOL < FT ? F0 + NS + POOL : FT, need = F0 + NS + nn < FT ? F0 + NS + nn : FT;
        unsigned t = 0;
        u64 bad = 0;
        if (j == 0) { lgkm<POOL - NS>(); SBAR(); }
#pragma unroll
        for (int i = 0; i < 2 * NS; ++i) {
            const int n = F0 + (i >> 1);
            mma16(acc[i & 1][i >> 1], wf[n % POOL], act[j % RING][i & 1]);
            SBAR();
            if ((i & 1) && n + POOL < FT) wf[n % POOL] = lds16(waddr(n + POOL));
            if (i == 0 && j > 0 && jn) act[jn % RING][0] = aload<GS, ASTR>(rs, v0, gb, jn);
            if (i == 2 && j > 0 && jn) act[jn % RING][1] = aload<GS, ASTR>(rs, v1, gb, jn);
            if (jc) {
                if (i == 2 * NS - 6) {
                    t = max(act[jc % RING][1][0], act[jc % RING][1][2]);
                    t = max(max(t, act[jc % RING][0][0]), act[jc % RING][0][2]); PIN(t);
                }
                if (i == 2 * NS - 4) { bad = __ballot(t == EMPTY); asm volatile("" : "+s"(bad)); }
                if (i == 2 * NS - 2 && __builtin_expect(bad != 0, 0))
                    refetch(act[jc % RING][0], act[jc % RING][1], ws + gb[jc / GS] + (jc % GS) * ASTR, v0, v1);
                if (i == 2 * NS - 1) { SBAR(); lgkm<issued - need>(); }
            }
            SBAR();
        }
        kloop<K0 + 1, NK - 1, KT, F0 + NS, FT, NS, NSN, PD, GS, ASTR>(acc, act, wf, waddr, rs, ws, v0, v1, gb);
    }
}
template <int PD, int GS, int ASTR, int RING, int NG, class Probe, class Before, class After>
DI void kstart(u32x4 (&act)[RING][2], rsrc_t rs, const char* ws, unsigned& v0, unsigned& v1, const unsigned (&gb)[NG], Probe probe,
               Before before, After after) {
    before();
    SBAR();
#pragma unroll 1
    for (int spin = 0; spin < (1 << 16); ++spin) {
        const bool miss = probe();
        SBAR();
#pragma unroll
        for (int k = 0; k < FIRSTFILL; ++k) {
            act[k][0] = aload<GS, ASTR>(rs, v0, gb, k); SBAR();
            act[k][1] = aload<GS, ASTR>(rs, v1, gb, k); SBAR();
        }
        if (!__any(miss)) break;
        asm volatile("" ::: "memory");
    }
    SBAR();
#pragma unroll
    for (int k = FIRSTFILL; k < PD; ++k) {
        act[k][0] = aload<GS, ASTR>(rs, v0, gb, k); SBAR();
        act[k][1] = aload<GS, ASTR>(rs, v1, gb, k); SBAR();
    }
    after();
    SBAR();
    if (__builtin_expect(missing(act[0][0], act[0][1]), 0)) refetch(act[0][0], act[0][1], ws + gb[0], v0, v1);
    SBAR();
}
DI void e_bases(unsigned (&gb)[9], unsigned base, int m, int stride) {
    const int m3 = m % 3;
#pragma unroll
    for (int i = 0; i < 9; ++i) {
        const int q = i < 4 ? (i + m) & 3 : i < 6 ? 4 + ((i + (m >> 2)) & 1) : 6 + (i - 6 + m3 >= 3 ? i - 9 + m3 : i - 6 + m3);
        gb[i] = base + q * stride;
        asm volatile("" : "+s"(gb[i]));
    }
}
template <int NG> DI void rot_bases(unsigned (&gb)[NG], unsigned base, int rot, int stride) {
#pragma unroll
    for (int i = 0; i < NG; ++i) { const int q = i + rot; gb[i] = base + (q >= NG ? q - NG : q) * stride; asm volatile("" : "+s"(gb[i])); }
}
DI void send_half(const f32x4 (&a)[5], unsigned dst) {
#pragma unroll
    for (int s = 0; s < 5; ++s) *(LAS f32x4*)(size_t)(dst + s * 1024) = a[s];
}
DI void recv_half(f32x4 (&a)[5], unsigned src) {
#pragma unroll
    for (int s = 0; s < 5; ++s) a[s] += ldsf4(src + s * 1024);
}
DI void ldsw16(unsigned a, const u32x4& v) { *(LAS u32x4*)(size_t)a = v; }
DI f32x4 unpk(unsigned r0, unsigned r1) { return f32x4{bflo(r0), bfhi(r0), bflo(r1), bfhi(r1)}; }
DI void res_init(f32x4 (&a)[5], const u32x4& x0, const u32x4& x1, const u32x2& x2) {
    a[0] = unpk(x0[0], x0[1]); a[1] = unpk(x0[2], x0[3]); a[2] = unpk(x1[0], x1[1]); a[3] = unpk(x1[2], x1[3]); a[4] = unpk(x2[0], x2[1]);
#pragma unroll
    for (int s = 0; s < 5; ++s) PIN(a[s]);
}
DI void res_pack(u32x4& x0, u32x4& x1, u32x2& x2, const f32x4 (&a)[5]) {
    x0 = u32x4{pk2(a[0][0], a[0][1]), pk2(a[0][2], a[0][3]), pk2(a[1][0], a[1][1]), pk2(a[1][2], a[1][3])};
    x1 = u32x4{pk2(a[2][0], a[2][1]), pk2(a[2][2], a[2][3]), pk2(a[3][0], a[3][1]), pk2(a[3][2], a[3][3])};
    x2 = u32x2{pk2(a[4][0], a[4][1]), pk2(a[4][2], a[4][3])};
}
enum { C_WKV, C_KFUSED, C_X, C_XUSED, C_WQ, C_CSP = C_WQ + 5, C_CS, C_KVST, C_R1, C_XCH, C_NUM = C_XCH + 2 };
enum { E_C1, E_C2, E_C3, E_MK, P_NONE, P_C0, P_C1, P_C2, E_C0, H_PREP, H_FILL, M_LDS, M_K8, M_NUM };

__global__ __launch_bounds__(512, 2) void k_block(const char* __restrict__ x, const char* __restrict__ wq, const char* __restrict__ wk,
                                                  const char* __restrict__ wv, const char* __restrict__ wo, const char* __restrict__ w1,
                                                  const char* __restrict__ w2, char* ws, char* __restrict__ out) {
    extern __shared__ __attribute__((aligned(16))) char smem[];
    const unsigned L = (unsigned)(size_t)(LAS char*)smem;
    const int w = __builtin_amdgcn_readfirstlane(threadIdx.x >> 6);
    const int xg = blockIdx.x & 7, idx = blockIdx.x >> 3;
    const int tb = ONEWAY ? xg * 4 + (idx >> 3) : (xg >> 1) * 8 + (idx >> 2), m = ONEWAY ? (idx & 7) : (xg & 1) * 4 + (idx & 3), cid = tb * 8 + m;
    const int ct = (m >> 2) * 8 + (tb & 1) * 4 + (m & 3);
    const rsrc_t rs = __builtin_amdgcn_make_buffer_rsrc(ws, 0, WS_END, 0x00020000);
    const u32x4 ones = {ONES2, ONES2, ONES2, ONES2};
    const f32x4 z4 = {0, 0, 0, 0};
    STAMP_INIT();

#define LOAD_TAG() u64 tag; asm volatile("global_load_dwordx2 %0, %1, %2 sc1" : "=v"(tag) : "v"(0u), "s"(ws + WS_FL) : "memory")
#define MAKE_TAG() do { PIN(tag); \
        tag = ((u64)(unsigned)__builtin_amdgcn_readfirstlane(tag >> 32) << 32) | (unsigned)__builtin_amdgcn_readfirstlane((unsigned)tag); \
        tag = (tag ^ 0x9E3779B97F4A7C15ull) * 0xBF58476D1CE4E5B9ull + 0x632BE59BD9B4E019ull; asm volatile("" : "+s"(tag)); } while (0)

#define CNT(i) (L + L_CNT + (i) * 4)
#define CN2(i) (L + L_MISC + 512 + (i) * 4)
#define WQ_AT(r) ((r) < 2 ? L_WQ1 + (r) * 20480 : L_WQ2 + (r) % 3 * 20480)
#define WQ_ADDR() const unsigned wrow = L + (8 * g + (c16 >> 2)) * 160 + (lane & 3) * 8, wo0 = wrow + (g & 1) * 640, wo1 = wrow + ((g & 1) ^ 1) * 640
    const int rotc = ONEWAY ? m % 5 : m & 3, rotd = ONEWAY ? ct % 5 : (ct & 7) % 5;

    if (w >= 4) {
        const int hw = w - 4;
        LANE_VARS();
        LOAD_TAG();
        __builtin_amdgcn_s_setprio(P1PRIO);
        if (m < 4) {
            const char* src = m < 2 ? wk : wv;
#pragma unroll 1
            for (int p = hw; p < 25; p += 4) glds(lane * 16, src + p * 1024, L + L_WKV + p * 1024);
        }
        {
            const unsigned vx0 = (lane >> 3) * 1280 + (((lane & 7) ^ g) << 4), vx1 = (8 + (lane >> 3)) * 1280 + (((lane & 7) ^ (4 + g)) << 4);
            const char* xb = x + (size_t)(tb * 64 + hw * 16) * 1280;
            int sg = ONEWAY ? m : (m & 3) * 2;
#pragma unroll 1
            for (int i = 0; i < 10; ++i) {
                glds(vx0, xb + sg * 128, L + L_X + (hw * 20 + sg) * 1024);
                glds(vx1, xb + sg * 128, L + L_X + (hw * 20 + 10 + sg) * 1024);
                sg = sg == 9 ? 0 : sg + 1;
                if (i == B0AT) bar_raw();
                if (i == WKVAT) { wait_vm<2 * WKVAT + 2>(); cnt_add(CNT(C_WKV)); }
            }
        }
        u64 f0;
        const int c0 = tb >> 3;
        {
            unsigned vw[5];
#pragma unroll
            for (int i = 0; i < 5; ++i) {
                const int ci = i * 64 + lane, pos = ci / 10, ch = ci - pos * 10, k = pos ^ (((pos >> 3) & 1) << 2);
                vw[i] = k * 1280 + ch * 16;
            }
            const char* wb = wq + m * 160 + hw * 40960;
            WQ_ADDR();
            f32x4 cs[5];
            u32x4 one = ones;
            PIN(one);
            zero_acc(cs);
            auto wqload = [&](int r) {
#pragma unroll
                for (int i = 0; i < 5; ++i) glds(vw[i], wb + r * 163840, L + WQ_AT(r) + hw * 5120 + i * 1024);
            };
            auto wqsum = [&](int r) {
                u32x4 f[5];
#pragma unroll
                for (int s = 0; s < 5; ++s) f[s] = ldstr2(wo0 + WQ_AT(r) + hw * 5120 + s * 32, wo1 + WQ_AT(r) + hw * 5120 + s * 32);
#pragma unroll
                for (int s = 0; s < 5; ++s) mma16(cs[s], f[s], one);
            };
            wqload(0);
            wait_vm<5>();
            cnt_add(CNT(C_X));
            STAMP(1);
            wqload(1);
            MAKE_TAG();
            cnt_spin(CNT(C_KFUSED), 4);
            wqload(2); wait_vm<10>(); cnt_add(CNT(C_WQ));
            cnt_spin(CNT(C_XUSED), 4);
            wqload(3); wait_vm<10>(); cnt_add(CNT(C_WQ + 1));
            wqload(4);
            wqsum(0); wqsum(1);
            wait_vm<10>(); cnt_add(CNT(C_WQ + 2)); wqsum(2);
            wait_vm<5>(); cnt_add(CNT(C_WQ + 3)); wqsum(3);
            wait_vm<0>(); cnt_add(CNT(C_WQ + 4));
            f0 = __hip_atomic_load((const GAS u64*)(ws + FL_KV + (c0 * 32 + (lane & 31)) * 128), __ATOMIC_RELAXED, __HIP_MEMORY_SCOPE_AGENT);
            wqsum(4);
            mma_done();
            if (c16 == 0) {
#pragma unroll
                for (int s = 0; s < 5; ++s) *(LAS f32x4*)(size_t)(L + L_CSX + hw * 320 + s * 64 + g * 16) = cs[s];
            }
            if (cnt_arrive(CNT(C_CSP)) == 3) {
                const unsigned ca = L + L_CSX + (lane < 20 ? lane : 0) * 16;
                const f32x4 c4 = (ldsf4(ca) + ldsf4(ca + 320)) + (ldsf4(ca + 640) + ldsf4(ca + 960));
                *(LAS f32x4*)(size_t)(ca + 1280) = c4;
                cnt_add(CNT(C_CS));
            }
        }
        const char* kv = ws + WS_KV;
        STAMP(2);
        poll(ws, FL_KV, lane < 32 && f0 != tag ? c0 * 32 + lane : -1, tag);
        STAMP(3);
        copy_dev(kv + c0 * CHUNK, L, 42, hw, lane);
        wait_vm<7>();
        STAMP(4);
        __builtin_amdgcn_s_setprio(2);
        bar_raw();
        wait_vm<0>();
        cnt_add(CN2(E_C0));
        poll(ws, FL_KV, (c0 * 32 + 32 + lane) & 127, tag);
        copy_dev(kv + ((c0 + 1) & 3) * CHUNK, L + CHUNK, 42, hw, lane);
        {
            int u = blockIdx.x * 4 + hw;
            const int pmat = u < 160 ? 0 : (u < 480 ? 1 : (u < 912 ? 2 : 3));
            u = pmat == 0 ? u : pmat == 1 ? u - 160 : pmat == 2 ? u - 480 : 0;
            const int pc = pmat < 2 ? u / 20 : u / 54, ks = u - pc * (pmat < 2 ? 20 : 54);
            int rb, a3, a2, a1;
            if (pmat == 0) { if (ks < 16) { rb = 40 * ks; a3 = 20; a2 = 4; a1 = 8; } else { rb = 160 * (ks - 16) + 16; a3 = 80; a2 = 40; a1 = 20; } }
            else if (pmat == 1) { if (ks < 16) { rb = 80 * (ks >> 1) + 32 * (ks & 1); a3 = 16; a2 = 8; a1 = 4; } else { rb = 160 * (ks - 16) + 64; a3 = 80; a2 = 8; a1 = 4; } }
            else if (ks >= 16 && ks < 24) { rb = 224 * (ks - 16) + 96; a3 = ks < 23 ? 112 : 48; a2 = 8; a1 = 4; }
            else { rb = ks < 16 ? 112 * ks : (ks < 39 ? 112 * (ks - 24) + 32 : 112 * (ks - 39) + 64); a3 = 8; a2 = 4; a1 = 16; }
            const int ld = pmat == 1 ? 3456 : 1280, chmax = pmat != 1 ? 9 : (pc == 15 ? 5 : 13);
            const char* psrc = pmat == 0 ? wo + pc * 160 : pmat == 1 ? w1 + pc * 224 : w2 + pc * 160;
            const unsigned pb = L + 2 * CHUNK + hw * 8192, pv = g * ld + min(c16, chmax) * 16;
#pragma unroll 1
            for (int i = 0; i < 8; ++i) glds(pv, psrc + (rb + a3 * (i >> 2) + a2 * ((i >> 1) & 1) + a1 * (i & 1)) * ld, pb + i * 1024);
            wait_vm<8>();
            cnt_add(CN2(E_C1));
            wait_vm<0>();
            const unsigned a0 = pb + (8 * g + (c16 >> 2)) * 256 + (lane & 3) * 8;
            if (pmat == 1) {
                const unsigned pdst = WS_W1 + pc * 143360 + ks * 5120, pd2 = WS_W1 + pc * 143360 + 102400 + ks * 2048;
                const unsigned cv = c16 == 0 ? g * 16 : 0x80000000u;
#pragma unroll
                for (int s = 0; s < 7; ++s) {
                    const unsigned a1 = a0 + (s == 6 ? (pc == 15 ? 64 : 192) : s * 32);
                    const u32x4 f = ldstr2(a1, a1 + 1024);
                    bst16(rs, f, lane * 16, s < 2 ? pdst + s * 1024 : s < 4 ? pdst + (s + 1) * 1024 : s < 6 ? pd2 + (s - 4) * 1024 : pdst + 2048);
                    bst16(rs, __builtin_bit_cast(u32x4, mfma16(f, ones, z4)), cv, WS_CS + (ks * 1792 + pc * 112) * 4 + s * 64);
                }
            } else if (pmat < 3) {
                const unsigned a1 = a0 + (lane & 3) * 8, pdst = (pmat == 0 ? WS_WO : WS_W2) + u * 5120;
#pragma unroll
                for (int s = 0; s < 5; ++s) {
                    const unsigned a2 = s < 4 ? a1 + (s >> 1) * 64 + (s & 1) * 8 : a0 + 128;
                    bst16(rs, ldstr2(a2, a2 + 1024), lane * 16, pdst + s * 1024);
                }
            }
        }
        wait_vm<0>();
        if (cnt_arrive(CN2(H_PREP)) == 3 && lane == 0) set_flag(ws, FL_PR + cid * 128, tag);
        STAMP(5);
        cnt_wait(CN2(H_PREP), 4);
        copy_dev(kv + ((c0 + 2) & 3) * CHUNK, L + 2 * CHUNK, 42, hw, lane);
        {
            const u32x4 e = {EMPTY, EMPTY, EMPTY, EMPTY};
#pragma unroll 1
            for (int p = hw; p < 44; p += 4) bst16(rs, e, lane * 16, WS_O + blockIdx.x * 48640 + p * 1024);
            bst16(rs, e, hw < 3 || lane < 32 ? lane * 16 : 0x80000000u, WS_O + blockIdx.x * 48640 + (44 + hw) * 1024);
        }
        wait_vm<12>();
        cnt_add(CN2(E_C2));
        poll(ws, FL_KV, (c0 * 32 + 96 + (lane & 31)) & 127, tag);
        wait_vm<0>();
        if (cnt_arrive(CN2(H_FILL)) == 3 && lane == 0) set_flag(ws, FL_MK + cid * 128, tag);
        cnt_wait(CN2(P_C0), 4);
        copy_dev(kv + ((c0 + 3) & 3) * CHUNK, L, 42, hw, lane);
        poll(ws, FL_PR, hw * 64 + lane, tag);
        wait_vm<0>();
        cnt_add(CN2(E_C3));
        cnt_wait(CN2(E_C3), 4);
        STAMP(6);
        if (blockIdx.x == 0 && hw == 1 && lane == 0) set_flag(ws, WS_FL, tag);
        if (hw == 3) {
            u32x4 c[20];
            const int l28 = lane < 28 ? lane : 0;
#pragma unroll
            for (int ks = 0; ks < 20; ++ks) c[ks] = bld16(rs, l28 * 16, WS_CS + (ks * 1792 + ct * 112) * 4);
            f32x4 c4 = z4;
#pragma unroll
            for (int ks = 0; ks < 20; ++ks) c4 += __builtin_bit_cast(f32x4, c[ks]);
            if (lane < 28) *(LAS f32x4*)(size_t)(L + L_MISC + lane * 16) = c4;
        }
        auto wocopy = [&](int p0, int p1) {
#pragma unroll 1
            for (int p = p0 + hw; p < p1; p += 4) {
                const int q = p + rotc * 20;
                glds_dev(lane * 16, ws + WS_WO + m * 102400 + (q >= 100 ? q - 100 : q) * 1024, L + L_WO + p * 1024);
            }
        };
        u32x4 r1[35], r2[9][8];
        unsigned g1[5], g1b[5], g2[9];
        rot_bases(g1, WS_W1 + ct * 143360, rotd, 20480);
        rot_bases(g1b, WS_W1 + ct * 143360 + 102400, rotd, 8192);
        e_bases(g2, WS_W2 + m * 276480, m, 30720);
        const unsigned vp = lane * 16 + hw * 1024, vq = lane * 16 + min(28 + hw, 29) * 1024;
        unsigned lp0 = L + vp, lp1 = lp0 + 61440, lp2 = lp0 + 122880, lq0 = L + vq, lq1 = lq0 + 61440, lq2 = lq0 + 122880;
        PIN(lp0); PIN(lp1); PIN(lp2); PIN(lq0); PIN(lq1); PIN(lq2);
#define LD2(G, THR) do { _Pragma("unroll") for (int j_ = 0; j_ < 7; ++j_) { r2[G][j_] = bld16(rs, vp, g2[G] + j_ * 4096); wait_vm<THR>(); } \
        r2[G][7] = bld16(rs, vq, g2[G]); wait_vm<THR>(); } while (0)
#define ST2E(G, j) do { constexpr int sl_ = (G) % 5; \
        ldsw16((j) < 7 ? (sl_ < 2 ? lp0 : sl_ < 4 ? lp1 : lp2) + (sl_ & 1) * 30720 + (j) * 4096 : (sl_ < 2 ? lq0 : sl_ < 4 ? lq1 : lq2) + (sl_ & 1) * 30720, r2[G][j]); } while (0)
#pragma unroll
        for (int i = 0; i < 35; ++i) r1[i] = bld16(rs, vp, i < 25 ? g1[i / 5] + (i % 5) * 4096 : g1b[(i - 25) / 2] + ((i - 25) & 1) * 4096);
        SBAR();
        LD2(7, 63); LD2(8, 63);
        poll(ws, FL_MK, hw * 64 + lane, tag);
        cnt_add(CN2(E_MK));
        cnt_wait(CN2(P_C1), 4);
        wocopy(0, 42);
        cnt_wait(CN2(P_C2), 4);
        wocopy(42, 100);
#pragma unroll
        for (int G = 0; G < 7; ++G) {
            const int p = (tb & 3) * 8 + hw * 2;
            glds_dev(lane * 16, ws + g2[G] + min(p, 28) * 1024, L + XCH + hw * 1024);
            glds_dev(lane * 16, ws + g2[G] + min(p + 1, 29) * 1024, L + XCH + hw * 1024);
        }
        wait_vm<0>();
        STAMP(7);
        __builtin_amdgcn_s_setprio(TAILPRIO);
        bar_raw();
#pragma unroll
        for (int i = 0; i < 8; ++i) ldsw16(lp0 + i * 4096, r1[i < 4 ? 25 + i : i - 4]);
        SBAR();
        LD2(6, THROT);
        bar();
        cnt_wait(CN2(M_LDS), 4);
#pragma unroll
        for (int i = 8; i < 35; ++i) {
            ldsw16((i < 15 ? lp0 : i < 30 ? lp1 : lp2) + (i % 15) * 4096, r1[i < 29 ? i - 4 : i]);
            SBAR();
            if (i >= 11 && i - 11 < NEARLY) {
                const int e = i - 11, G = 3 + (e >> 3), j = e & 7;
                r2[G][j] = bld16(rs, j < 7 ? vp : vq, g2[G] + (j < 7 ? j * 4096 : 0));
                SBAR();
            }
        }
        STAMP(8);
        bar();
#pragma unroll
        for (int e = NEARLY; e < 24; ++e) {
            const int G = 3 + (e >> 3), j = e & 7;
            r2[G][j] = bld16(rs, j < 7 ? vp : vq, g2[G] + (j < 7 ? j * 4096 : 0));
            wait_vm<THROT>();
        }
        cnt_wait(CN2(M_K8), 4);
#pragma unroll 1
        for (int p = hw; p < 60; p += 4) { glds_dev(lane * 16, ws + (p < 30 ? g2[0] + p * 1024 : g2[1] + (p - 30) * 1024), L + p * 1024); wait_vm<THROT>(); }
        bar_raw();
        copy_dev(ws + g2[2], L + 61440, 30, hw, lane);
        wait_vm<7>();
        STAMP(9);
        bar_raw();
#pragma unroll
        for (int iv = 0; iv < 7; ++iv) {
#pragma unroll
            for (int e = iv < 3 ? iv * 8 - (iv == 2) * 0 : 24 + (iv - 3) * 6; e < (iv < 3 ? iv * 8 + 8 : 24 + (iv - 2) * 6); ++e) {
                switch (e >> 3) {
                    case 0: ST2E(3, e & 7); break;
                    case 1: ST2E(4, e & 7); break;
                    case 2: ST2E(5, e & 7); break;
                    case 3: ST2E(6, e & 7); break;
                    case 4: ST2E(7, e & 7); break;
                    default: ST2E(8, e & 7); break;
                }
                SBAR();
            }
            wait_vm<0>();
            bar();
        }
        asm volatile(".Lpf_end2:");
        STAMP_OUT(1);
        __builtin_amdgcn_endpgm();
    }

    u32x4 xr0, xr1; u32x2 xr2;
    u32x4 qa1, qb1; u32x2 qa2, qb2;
    const int kh = w & 1;
    {
        LANE_VARS();
        LOAD_TAG();
        u32x4 xf[20], xo[10], kf[20];
        unsigned pf[5] = {0, 0, 0, 0, 0};
        {
            static_assert(ONEWAY, "");
            const unsigned vo = lane * 128;
            const int wi = idx * 4 + w;
            asm volatile("s_mov_b64 exec, 0xfffff\n\tglobal_load_dword %0, %2, %3\n\ts_mov_b64 exec, 15\n\tglobal_load_dword %1, %2, %4\n\ts_mov_b64 exec, -1"
                         : "=&v"(pf[0]), "=&v"(pf[1])
                         : "v"(vo), "s"(x + xg * 327680 + wi * 2560), "s"(((wi & 1) ? wv : wk) + min(wi >> 1, 49) * 512) : "memory");
            u64 pc; int l1, l2;
            asm volatile("s_getpc_b64 %0\n.Lpf_pc:\n\ts_mov_b32 %1, .Lpf_end1-.Lpf_pc\n\ts_mov_b32 %2, .Lpf_end2-.Lpf_pc" : "=s"(pc), "=s"(l1), "=s"(l2));
            const unsigned len = max(max(l1, l2), 4);
            const unsigned co = min((unsigned)(wi * 4 + (lane & 3)) * 128u, len - 4) & ~3u;
            asm volatile("global_load_dword %0, %1, %2" : "=v"(pf[3]) : "v"(co), "s"(pc) : "memory");
        }
        if (w == 0 && lane < C_NUM) *(LAS unsigned*)(size_t)CNT(lane) = 0u;
        bar();
        const unsigned xrow = L + L_X + w * 20480 + (c16 >> 3) * 10240 + (c16 & 7) * 128;
        const int sw = c16 >> 1, t32 = (w & 1) * 16 + c16;
        unsigned xa = xrow + (((kh * 4 + g) ^ sw) << 4), xb = xrow + ((((kh ^ 1) * 4 + g) ^ sw) << 4), xc = xa + ((w ^ 1) - w) * 20480, xra[3], qw[5], kvo;
        {
            const int c0 = m * 10 + g, c1 = c0 + 4, c2 = m * 10 + 8 + (g >> 1);
            xra[0] = xrow + (c0 >> 3) * 1024 + (((c0 & 7) ^ sw) << 4);
            xra[1] = xrow + (c1 >> 3) * 1024 + (((c1 & 7) ^ sw) << 4);
            xra[2] = xrow + (c2 >> 3) * 1024 + (((c2 & 7) ^ sw) << 4) + (g & 1) * 8;
            const int d = (m & 1) * 16 + c16;
            kvo = m == 0 ? ((g >> 1) * 32 + t32) * 16 + (g & 1) * 8
                : m == 1 ? (g == 0 ? 1024 + t32 * 8 : 0x80000000u)
                         : (d <= 21 ? 1280 + (((w & 1) * 2 + (g & 1)) * 22 + d) * 16 + (g >> 1) * 8 : 0x80000000u);
            PIN(xa); PIN(xb); PIN(xc); PIN(xra[0]); PIN(xra[1]); PIN(xra[2]); PIN(kvo);
        }
#pragma unroll
        for (int s = 0; s < 5; ++s) {
            const int f0 = s * 16 + 4 * g, hh = f0 / 20, d0 = f0 - hh * 20;
            qw[s] = L + L_QX + (hh * 2 + (w >> 1)) * 1280 + (d0 < 16 ? ((d0 >> 3) * 32 + t32) * 16 + (d0 & 4) * 2 : 1024 + t32 * 8);
            PIN(qw[s]);
        }
        WQ_ADDR();
        unsigned b0 = wo0 + L_WQ1 + kh * 5120, b1 = wo1 + L_WQ1 + kh * 5120;
        PIN(b0); PIN(b1);
        wait_vm<3>();
        MAKE_TAG();
        if (m >= 4) {
            __builtin_amdgcn_s_sleep(PFDELAY);
            const char* wp = wq + (((idx >> 3) * 4 + (m & 3)) * 4 + w) * 12800;
            asm volatile("s_mov_b64 exec, %5\n\tglobal_load_dword %0, %2, %3\n\tglobal_load_dword %1, %2, %4\n\ts_mov_b64 exec, -1"
                         : "+v"(pf[2]), "+v"(pf[4]) : "v"(lane * 128), "s"(wp), "s"(wp + 6400), "s"(0x3ffffffffffffull) : "memory");
        }
        f32x4 ka = z4, kc = z4, ssm = z4, ssq = z4, acc[2][5];
        u32x4 one = ones;
        PIN(ssm); PIN(ssq); PIN(one);
        if (m < 4) {
            cnt_wait(CNT(C_WKV), 4);
            const unsigned a0 = L + L_WKV + (8 * g + (c16 >> 2)) * 40 + (m & 1) * 32 + (lane & 3) * 8;
            const unsigned a1 = a0 + kh * 1280, a2 = a0 + (kh ^ 1) * 1280;
#pragma unroll
            for (int j = 0; j < 10; ++j) { kf[j] = ldstr2(a1 + j * 2560, a1 + j * 2560 + 160); kf[10 + j] = ldstr2(a2 + j * 2560, a2 + j * 2560 + 160); }
            if (m < 2) {
#pragma unroll
                for (int j = 0; j < 20; ++j) kc = mfma16(kf[j], ones, kc);
            } else {
#pragma unroll
                for (int j = 0; j < 20; ++j) kc = mfma16(ones, kf[j], kc);
            }
            PIN(kc);
        }
        cnt_add(CNT(C_KFUSED));
        cnt_spin(CNT(C_X), 4);
#pragma unroll
        for (int j = 0; j < 10; ++j) xf[j] = lds16(xa + j * 1024);
#pragma unroll
        for (int j = 0; j < 10; ++j) xf[10 + j] = lds16(xb + j * 1024);
#pragma unroll
        for (int j = 0; j < 10; ++j) xo[j] = lds16(xc + j * 1024);
        xr0 = lds16(xra[0]); xr1 = lds16(xra[1]); xr2 = lds8(xra[2]);
        SBAR();
        if (m < 2) {
#pragma unroll
            for (int j = 0; j < 20; ++j) ka = mfma16(kf[j], xf[j], ka);
        } else if (m < 4) {
#pragma unroll
            for (int j = 0; j < 20; ++j) ka = mfma16(xf[j], kf[j], ka);
        }
        PIN(ka);
#pragma unroll
        for (int j = 0; j < 20; ++j) {
            mma16(ssm, one, xf[j]); mma16(ssq, xf[j], xf[j]);
            if (j == 4) {
#pragma unroll
                for (int i = 0; i < 10; ++i) { PIN(xf[i]); PIN(xf[10 + i]); PIN(xo[i]); }
                PIN(xr0); PIN(xr1); PIN(xr2);
                cnt_add(CNT(C_XUSED));
            }
        }
        mma_done();
        STAMP(1);
        const float sd = (c16 & 2) ? ((c16 & 1) ? ssq[3] : ssq[2]) : ((c16 & 1) ? ssq[1] : ssq[0]);
        const float mean = ssm[0] * (1.f / D), rstd = __builtin_amdgcn_rsqf(fmaxf(__shfl(sd, (c16 >> 2) * 16 + c16) * (1.f / D) - mean * mean, 0.f) + LN_EPS);
        if (m < 4) {
            u32x2 pv;
            if (m < 2) {
                pv = u32x2{pk2((ka[0] - mean * kc[0]) * rstd, (ka[1] - mean * kc[1]) * rstd), pk2((ka[2] - mean * kc[2]) * rstd, (ka[3] - mean * kc[3]) * rstd)};
            } else {
                float v[4];
#pragma unroll
                for (int r = 0; r < 4; ++r) v[r] = (ka[r] - __shfl(mean, 4 * g + r) * kc[0]) * __shfl(rstd, 4 * g + r);
                pv = u32x2{pk2(v[0], v[1]), pk2(v[2], v[3])};
                if (m == 3 && c16 >= 4) pv = c16 == 4 ? u32x2{ONES2, ONES2} : u32x2{0u, 0u};
            }
            bst8(rs, pv, kvo, WS_KV + (tb * 2 + (w >> 1)) * KV_TILE);
        }
        SBAR();
        STAMP(2);
        u32x2 f[2][5][2];
        unsigned xs, xv;
        float sc = rstd * QSCALE, ms = -mean * sc;
        PIN(sc); PIN(ms);
        unsigned b2 = b0 + (L_WQ2 - L_WQ1), b3 = b1 + (L_WQ2 - L_WQ1), cq = CNT(0), cv = 0;
        PIN(b2); PIN(b3); PIN(cq);
        auto qhalf = [&](int j, int s, int e) { return ldstr((j < 4 ? (e ? b1 : b0) + j * 10240 : (e ? b3 : b2) + j % 6 * 10240) + s * 32); };
        auto kvflag = [&] {
            if (m < 4) {
                wait_vm<0>();
                if (cnt_arrive(CNT(C_KVST)) == 3 && lane == 0) set_flag(ws, FL_KV + (tb * 4 + m) * 128, tag);
            }
        };
        if (FLAGAT < 0) kvflag();
        cnt_spin(CNT(C_WQ), 4);
#pragma unroll
        for (int s = 0; s < 5; ++s) { f[0][s][0] = qhalf(0, s, 0); f[0][s][1] = qhalf(0, s, 1); }
        SBAR();
#pragma unroll
        for (int j = 0; j < 10; ++j) {
            if (j < 8 && !(j & 1)) cv = *(volatile LAS unsigned*)(size_t)(cq + (C_WQ + j / 2 + 1) * 4);
            if (j < 9 && (j & 1) && __builtin_expect((unsigned)__builtin_amdgcn_readfirstlane(cv) < 4u, 0)) cnt_spin(CNT(C_WQ + j / 2 + 1), 4);
            asm volatile("" ::: "memory");
            SBAR();
#pragma unroll
            for (int s = 0; s < 5; ++s) {
                const u32x4 fr = {f[j & 1][s][0][0], f[j & 1][s][0][1], f[j & 1][s][1][0], f[j & 1][s][1][1]};
                if (j == 0) mma16z(acc[0][s], fr, xf[j]); else mma16(acc[0][s], fr, xf[j]);
                SBAR();
                if (j < 9) f[(j + 1) & 1][s][0] = qhalf(j + 1, s, 0);
                SBAR();
                if (j == 0) mma16z(acc[1][s], fr, xo[j]); else mma16(acc[1][s], fr, xo[j]);
                SBAR();
                if (j < 9) f[(j + 1) & 1][s][1] = qhalf(j + 1, s, 1);
                SBAR();
            }
            if (j == FLAGAT) {   }
            if (j == 0) {   } if (j == 1) {   } if (j == 2) {   } if (j == 3) {   } if (j == 4) {   }
            if (j == 5) {   } if (j == 6) {   } if (j == 7) {   } if (j == 8) {   } if (j == 9) {   }
            if (j == FLAGAT) kvflag();
            if (j == 3) asm volatile("s_mov_b64 exec, 1\n\tds_add_u32 %0, %1 offset:%2\n\ts_mov_b64 exec, -1" ::"v"(cq), "v"(1u), "i"(C_R1 * 4) : "memory");
            if (j == FLAGAT) {   }
            if (j == 5) { xs = L + L_WQ1 + (w ^ 1) * 5120 + lane * 16; xv = L + L_WQ1 + w * 5120 + lane * 16; PIN(xs); PIN(xv); }
            if (j == 7) cnt_spin(CNT(C_R1), 4);
        }
        STAMP(3);
        if (m >= 4) wait_vm<0>();
        asm volatile("" ::"v"(pf[0]), "v"(pf[1]), "v"(pf[2]), "v"(pf[3]), "v"(pf[4]));
        mma_done();
        send_half(acc[1], xs);
        cnt_add(CNT(C_XCH + (w >> 1)));
        if (w == 0 && lane < M_NUM) *(LAS unsigned*)(size_t)CN2(lane) = 0u;
        f32x4 c5[5], p5[5];
#pragma unroll 1
        for (;;) {
            const unsigned n1 = *(volatile LAS unsigned*)(size_t)CNT(C_CS), n2 = *(volatile LAS unsigned*)(size_t)CNT(C_XCH + (w >> 1));
#pragma unroll
            for (int s = 0; s < 5; ++s) {
                c5[s] = *(volatile LAS f32x4*)(size_t)(L + L_CSX + 1280 + s * 64 + g * 16);
                p5[s] = *(volatile LAS f32x4*)(size_t)(xv + s * 1024);
            }
            if (__builtin_amdgcn_readfirstlane(n1 + n2) == 3) break;
        }
#pragma unroll
        for (int s = 0; s < 5; ++s) {
            const f32x4 v = (acc[0][s] + p5[s]) * sc + c5[s] * ms;
            *(LAS u32x2*)(size_t)qw[s] = u32x2{pk2(v[0], v[1]), pk2(v[2], v[3])};
        }
        bar();
        const int r32 = lane & 31, h = lane >> 5;
        const unsigned qb = L + L_QX + ((w & 1) * 4 + (w >> 1)) * 1280;
        qa1 = lds16(qb + lane * 16); qb1 = lds16(qb + 2560 + lane * 16);
        qa2 = u32x2{lds4(qb + 1024 + r32 * 8 + h * 4), 0u}; qb2 = u32x2{lds4(qb + 2560 + 1024 + r32 * 8 + h * 4), 0u};
    }
    STAMP(4);
    {
        LANE_VARS();
        const int r32 = lane & 31, h = lane >> 5, vrow = min(r32, 21);
        const unsigned kconst = h == 0 ? 0x00003f80u : 0u;
        unsigned ak1 = L + lane * 16, ak2 = L + 1024 + r32 * 8 + h * 4, av = L + 1280 + (h * 22 + vrow) * 16;
        const f32x16 zero16 = {0, 0, 0, 0, 0, 0, 0, 0, 0, 0, 0, 0, 0, 0, 0, 0};
        f32x16 sa, sb, oa = zero16, ob = zero16;
        u32x4 ka1 = lds16(ak1), kb1 = lds16(ak1 + KV_TILE), va0 = lds16(av), va1 = lds16(av + VS), vb0 = va0, vb1 = va0;
        u32x2 ka2 = {lds4(ak2), kconst}, kb2 = {lds4(ak2 + KV_TILE), kconst};
        sa = mfma32(ka1, qa1, mfma32k8(ka2, qa2, zero16));
        sb = mfma32(ka1, qb1, mfma32k8(ka2, qb2, zero16));
        {
            const float ma = halfmax(max16(sa)), mb = halfmax(max16(sb));
            qa2[1] = h == 0 ? (pk2(-ma, 0.f) & 0xffffu) : 0u;
            qb2[1] = h == 0 ? (pk2(-mb, 0.f) & 0xffffu) : 0u;
        }
        sa = mfma32(ka1, qa1, mfma32k8(ka2, qa2, zero16));
        u32x4 pa0 = {0u, 0u, 0u, 0u}, pa1 = pa0, pb0 = pa0, pb1 = pa0;
        float e2 = 0.f, e3 = 0.f;
        unsigned vo0 = (r32 >> 4) * 20480 + h * 256 + (r32 & 15) * 16, vo1 = h ? 0x80000000u : vo0;
        PIN(pa0); PIN(pa1); PIN(pb0); PIN(pb1); PIN(ka2); PIN(kb2); PIN(vo0); PIN(vo1);
#define SLOT(R, MF, X, S, i, PP, jp, P, j) do { R = MF; PIN(R); SBAR(); X; SBAR(); \
        float e0 = EX2(S[i]), e1 = EX2(S[i + 1]); PIN(e0); PIN(e1); SBAR(); \
        PP[jp] = pk2(e2, e3); PIN(PP); SBAR(); \
        e2 = EX2(S[i + 2]); e3 = EX2(S[i + 3]); PIN(e2); PIN(e3); SBAR(); \
        P[j] = pk2(e0, e1); PIN(P); SBAR(); } while (0)
#define STEP(KC1, KC2, KN1, KN2, VC0, VC1, VP0, VP1, OK, OV, X0, X7) do { \
        SLOT(sb, mfma32k8(KC2, qb2, zero16), X0, sa, 0, pb1, 3, pa0, 0); \
        SLOT(sb, mfma32(KC1, qb1, sb), __builtin_amdgcn_s_waitcnt(0xC07F), sa, 4, pa0, 1, pa0, 2); \
        SLOT(ob, mfma32(VP1, pb1, ob), KC1 = lds16(ak1 + (OK) * KV_TILE), sa, 8, pa0, 3, pa1, 0); \
        SLOT(oa, mfma32(VC0, pa0, oa), KC2[0] = lds4(ak2 + (OK) * KV_TILE), sa, 12, pa1, 1, pa1, 2); \
        SLOT(sa, mfma32k8(KN2, qa2, zero16), (void)0, sb, 0, pa1, 3, pb0, 0); \
        SLOT(sa, mfma32(KN1, qa1, sa), VP0 = lds16(av + (OV) * KV_TILE), sb, 4, pb0, 1, pb0, 2); \
        SLOT(oa, mfma32(VC1, pa1, oa), VP1 = lds16(av + (OV) * KV_TILE + VS), sb, 8, pb0, 3, pb1, 0); \
        SLOT(ob, mfma32(VC0, pb0, ob), X7, sb, 12, pb1, 1, pb1, 2); } while (0)
        unsigned ea = CN2(E_C1), ev = 0, one = 1;
        PIN(ea); PIN(one); PIN(ak1); PIN(ak2); PIN(av);
        auto hook0 = [&](int j, int dn) {
            if (j == 14) { ak1 += dn; ak2 += dn; PIN(ak1); PIN(ak2); }
            if (j == 15) { av += dn; ea += 4; PIN(av); PIN(ea); }
        };
        auto hook7 = [&](int j) {
            if (j == 0)
                asm volatile("s_mov_b64 exec, 1\n\tds_add_u32 %0, %1 offset:%2\n\ts_mov_b64 exec, -1" ::"v"(ea), "v"(one), "i"((P_NONE - E_C1) * 4) : "memory");
            if (j == 1 && __builtin_expect((unsigned)__builtin_amdgcn_readfirstlane(ev) < 4u, 0)) cnt_spin(CN2(E_C0), 4);
            if (j == 9) ev = *(volatile LAS unsigned*)(size_t)ea;
            if (j == 10 && __builtin_expect((unsigned)__builtin_amdgcn_readfirstlane(ev) < 4u, 0))
                { cnt_wait(ea, 4); ev = 4; }
        };
        STAMP(5);
#pragma unroll 1
        for (int c = 0; c < 4; ++c) {
            const int dn = c == 2 ? -2 * CHUNK : CHUNK;
#pragma unroll
            for (int j = 0; j < 16; j += 2) {
                STEP(ka1, ka2, kb1, kb2, va0, va1, vb0, vb1, (j + 2) & 15, j + 1, hook0(j, dn), hook7(j));
                STEP(kb1, kb2, ka1, ka2, vb0, vb1, va0, va1, (j + 3) & 15, (j + 2) & 15, hook0(j + 1, dn), hook7(j + 1));
            }
        }
        pb1[3] = pk2(e2, e3);
        ob = mfma32(vb1, pb1, ob);
        STAMP(6);
        float la = upper(oa[8]), lb = upper(ob[8]);
        if (__builtin_expect(!__all(row_ok(oa, la, h) && row_ok(ob, lb, h)), 0)) {
            __builtin_amdgcn_fence(__ATOMIC_ACQUIRE, "agent");
#pragma unroll 1
            for (int qt = 0; qt < 2; ++qt) {
                ob = attn_exact(ws + WS_KV, qt ? qb1 : qa1, qt ? qb2[0] : qa2[0], lane, vrow);
                if (qt == 0) oa = ob;
            }
            la = upper(oa[8]); lb = upper(ob[8]);
        }
        const float ra = __builtin_amdgcn_rcpf(la), rb = __builtin_amdgcn_rcpf(lb);
        const int pr = 2 * m + (w & 1);
        const unsigned so = WS_O + (tb * 4 + (w & 2)) * 20480;
        bst16(rs, u32x4{keep(pk2(oa[0] * ra, oa[1] * ra)), pk2(oa[2] * ra, oa[3] * ra), keep(pk2(oa[4] * ra, oa[5] * ra)), pk2(oa[6] * ra, oa[7] * ra)}, vo0, so + pr * 1024);
        bst16(rs, u32x4{keep(pk2(ob[0] * rb, ob[1] * rb)), pk2(ob[2] * rb, ob[3] * rb), keep(pk2(ob[4] * rb, ob[5] * rb)), pk2(ob[6] * rb, ob[7] * rb)}, vo0, so + pr * 1024 + 512);
        bst16(rs, u32x4{keep(pk2(oa[8] * ra, oa[9] * ra)), pk2(oa[10] * ra, oa[11] * ra), keep(pk2(ob[8] * rb, ob[9] * rb)), pk2(ob[10] * rb, ob[11] * rb)}, vo1, so + 16384 + pr * 256);
        STAMP(7);
    }
    u32x4 wd[12], ae[RE][2], we[10];
    unsigned ge[9], e0, e1, ehv, eprobe, a0;
    const unsigned ehs = WS_F + (tb * 4 + (w & 2)) * 55296 + 16384;
    {
        LANE_VARS();
        const unsigned wl = L + lane * 16;
        f32x4 acc[2][5];
        u32x4 act[RC][2], wf[10];
        unsigned wa = wl + L_WO + kh * 5120, wb = wa + 51200, gb[5];
        rot_bases(gb, WS_O + tb * 81920 + kh * 1024, rotc, 4096);
        unsigned v0 = lane * 16 + w * 20480, v1 = lane * 16 + (w ^ 1) * 20480, hv = c16 * 256 + (g & 1) * 20480;
        unsigned xs = wl + (w == 2 ? 35840 : XCH + (w ^ 1) * 5120), xv = wl + (w == 3 ? 35840 : XCH + w * 5120);
        unsigned sv = g == 0 ? c16 * 8 : 0x80000000u, h2v = (g & 1) ? 0x80000000u : (g >> 1) * 256 + c16 * 16;
        PIN(v0); PIN(v1); PIN(hv);
        kstart<RC, 2, 2048>(act, rs, ws, v0, v1, gb,
                           [&] { return __builtin_amdgcn_raw_buffer_load_b32(rs, hv, WS_O + (tb * 4 + (w & 2)) * 20480 + 16384, SC1) == EMPTY; }, [&] {
            bar();
            PIN(wa); PIN(wb); PIN(xs); PIN(xv); PIN(sv); PIN(h2v);
#pragma unroll
            for (int n = 0; n < 10; ++n) { wf[n] = lds16(wa + (n / 5) * 10240 + (n % 5) * 1024); SBAR(); }
            res_init(acc[0], xr0, xr1, xr2); zero_acc(acc[1]);

            wait_vm<0>();
        }, [&] {
        });
        STAMP(8);
        kloop<0, 10, 10, 0, 50, 5, 5, RC, 2, 2048>(acc, act, wf, [&](int n) { return (n < 25 ? wa : wb) + (n / 5 % 5) * 10240 + (n % 5) * 1024; }, rs, ws, v0, v1, gb);
        mma_done();
        send_half(acc[1], xs);
        bar();
        recv_half(acc[0], xv);
#pragma unroll
        for (int n = 0; n < 12; ++n) wd[n] = lds16(wl + (n % 7 < 5 ? 16384 + (n / 7) * 5120 + (n % 7) * 1024 : (n / 7) * 2048 + (n % 7 - 5) * 1024));
        SBAR();
        res_pack(xr0, xr1, xr2, acc[0]);
        xr0[0] = keep(xr0[0]); xr0[2] = keep(xr0[2]); xr1[0] = keep(xr1[0]); xr1[2] = keep(xr1[2]); xr2[0] = keep(xr2[0]);
        const unsigned hb = WS_H + (tb * 4 + w) * 20480;
        bst16(rs, xr0, lane * 16, hb + 2 * m * 1024);
        bst16(rs, xr1, lane * 16, hb + (2 * m + 1) * 1024);
        bst16(rs, rowpair(xr2, xr2), h2v, hb + 16384 + m * 512);
#pragma unroll
        for (int n = 0; n < 12; ++n) PIN(wd[n]);
        cnt_add(CN2(M_LDS));
        float sm = 0.f, sq = 0.f, sm2 = 0.f, sq2 = 0.f;
#pragma unroll
        for (int i = 0; i < 4; ++i) { sm = dot2(xr0[i], ONES2, sm); sq = dot2(xr0[i], xr0[i], sq); sm2 = dot2(xr1[i], ONES2, sm2); sq2 = dot2(xr1[i], xr1[i], sq2); }
        sm = dot2(xr2[0], ONES2, sm); sq = dot2(xr2[0], xr2[0], sq); sm2 = dot2(xr2[1], ONES2, sm2); sq2 = dot2(xr2[1], xr2[1], sq2);
        sm = xsum16_32(sm + sm2); sq = xsum16_32(sq + sq2);
        bst8(rs, u32x2{keep(__float_as_uint(sm)), __float_as_uint(sq)}, sv, WS_ST + (tb * 4 + w) * 1024 + m * 128);
        SBAR();
        STAMP(9);
    }
    {
        f32x4 acc[2][7], c4[7];
        u32x4 act[12][2], wf[3][2];
        u32x2 st[4];
        float mu[2], rp[2];
        const int t16 = (tb >> 1) * 8 + 2 * w;
        unsigned v0 = lane_id() * 16, v1 = v0 + 20480, sv = v0 >> 1, wa = L + v0, wb = wa + 16384 + 46080, gb[5];
        PIN(sv); PIN(wa); PIN(wb); PIN(v0); PIN(v1);
        zero_acc(acc[0]); zero_acc(acc[1]);
        rot_bases(gb, WS_H + t16 * 20480, rotd, 4096);
        e_bases(ge, WS_F + tb * 221184 + kh * 1024, m, 6144);
        unsigned fs[7], wc, fw, f2v, ca;
        {
            const unsigned fb = WS_F + t16 * 55296 + ct * 1024;
            fs[0] = fb; fs[1] = fb + 55296; fs[2] = fb + 16384 - ct * 512; fs[3] = fb + 24576; fs[4] = fb + 24576 + 55296; fs[5] = fb + 39936; fs[6] = fb + 39936 + 55296;
#pragma unroll
            for (int i = 0; i < 7; ++i) asm volatile("" : "+s"(fs[i]));
            const int lane = v0 >> 4, g = lane >> 4, c16 = lane & 15;
            f2v = (g & 1) * 55296 + (g >> 1) * 256 + c16 * 16; ca = L + L_MISC + g * 16;
            ehv = (g & 1) * 55296 + c16 * 512;
            PIN(f2v); PIN(ca); PIN(ehv);
        }
        kstart<RD, 4, 1024>(act, rs, ws, v0, v1, gb, [&] {
#pragma unroll
            for (int i = 0; i < 4; ++i) st[i] = __builtin_amdgcn_raw_buffer_load_b64(rs, sv + i * 512, WS_ST + t16 * 1024, SC1);
            return max(max(st[0][0], st[1][0]), max(st[2][0], st[3][0])) == EMPTY; }, [&] { wait_vm<0>(); }, [&] {
#pragma unroll
            for (int t = 0; t < 2; ++t) {
                const float sm = xsum16_32(__uint_as_float(st[2 * t][0]) + __uint_as_float(st[2 * t + 1][0]));
                const float sq = xsum16_32(__uint_as_float(st[2 * t][1]) + __uint_as_float(st[2 * t + 1][1]));
                mu[t] = sm * (-1.f / D);
                rp[t] = __builtin_amdgcn_rsqf(fmaxf(sq * (1.f / D) - mu[t] * mu[t], 0.f) + LN_EPS) * LOG2E;
                PIN(mu[t]); PIN(rp[t]);
            } });
        STAMP(10);
        auto wad = [&](int n) {
            const int j = n < 56 ? n / 7 : 8 + (n - 56) / 5, s = n < 56 ? n % 7 : (n - 56) % 5;
            return s >= 5 ? wa + j * 2048 + (s - 5) * 1024 : (j < 9 ? wa + 16384 + j * 5120 : wb + (j - 9) * 5120) + s * 1024; };
        kloop<0, 1, 20, 0, 116, 7, 7, RD, 4, 1024>(acc, act, wd, wad, rs, ws, v0, v1, gb);
        bar_raw();
        kloop<1, 7, 20, 7, 116, 7, 5, RD, 4, 1024>(acc, act, wd, wad, rs, ws, v0, v1, gb);
        kloop<8, 1, 20, 56, 116, 5, 5, RD, 4, 1024>(acc, act, wd, wad, rs, ws, v0, v1, gb);
        asm volatile("s_mov_b64 exec, 1\n\tds_add_u32 %0, %1\n\ts_mov_b64 exec, -1" ::"v"(CN2(M_K8)), "v"(1u) : "memory");
        kloop<9, 10, 20, 61, 116, 5, 5, RD, 4, 1024>(acc, act, wd, wad, rs, ws, v0, v1, gb);
        PIN(wa); PIN(v0);
        wc = wa + 118784; fw = ct < 15 ? v0 : 0x80000000u; e0 = v0 + w * 55296; e1 = v0 + (w ^ 1) * 55296; a0 = wa + kh * 5120;
        PIN(wc); PIN(fw); PIN(e0); PIN(e1); PIN(a0);
#pragma unroll
        for (int i = 0; i < 4; ++i) wf[i >> 1][i & 1] = lds16(wc + i * 1024);
#pragma unroll
        for (int s = 0; s < 7; ++s) c4[s] = ldsf4(ca + s * 64);
        SBAR();
        kloop<19, 1, 20, 111, 116, 5, 5, RD, 4, 1024>(acc, act, wd, wad, rs, ws, v0, v1, gb);
        bar_raw();
        STAMP(11);
        u32x2 lo, hi;
        auto silu = [&](const f32x4& a, const f32x4& c, int t, u32x2& r, auto side) {
            f32x4 y = (a + mu[t] * c) * rp[t], e;
            PIN(y); SBAR();
            side(0); SBAR();
            e[0] = EX2(-y[0]); e[1] = EX2(-y[1]); PIN(e); SBAR();
            side(1); SBAR();
            e[2] = EX2(-y[2]); e[3] = EX2(-y[3]); PIN(e); SBAR();
            e = e * LOG2E + LOG2E; PIN(e); SBAR();
            side(2); SBAR();
            e[0] = __builtin_amdgcn_rcpf(e[0]); e[1] = __builtin_amdgcn_rcpf(e[1]); PIN(e); SBAR();
            side(3); SBAR();
            e[2] = __builtin_amdgcn_rcpf(e[2]); e[3] = __builtin_amdgcn_rcpf(e[3]); PIN(e); SBAR();
            e *= y;
            r = u32x2{keep(pk2(e[0], e[1])), pk2(e[2], e[3])};
            PIN(r); SBAR();
        };
        auto bstep = [&](int b, int i) {
            mma16(acc[i & 1][5 + (i >> 1)], wf[b % 3][i >> 1], act[(b + 8) % 12][i & 1]);
            SBAR();
            if (i < 2 && b + 2 < 12) wf[(b + 2) % 3][i] = lds16(wc + (b + 2) * 2048 + i * 1024);
        };
        auto early = [&](int q) {
            if (q == Q0 - 1) eprobe = __builtin_amdgcn_raw_buffer_load_b32(rs, ehv, ehs, SC1);
            const int n = (q - Q0) / QS;
            if (q >= Q0 && (q - Q0) % QS == 0 && n < 2 * RE) ae[n >> 1][n & 1] = aload<3, 2048>(rs, (n & 1) ? e1 : e0, ge, n >> 1);
        };
#pragma unroll
        for (int i = 0; i < 8; ++i) { bstep(i >> 2, i & 3); SBAR(); }
#pragma unroll
        for (int b = 0; b < 10; ++b) {
            const int ut = b < 4 ? b >> 1 : b < 6 ? b & 1 : (b - 6) >> 1, ui = b < 4 ? b & 1 : b < 6 ? 2 : 3 + (b & 1), us = ui < 2 ? ui : ui == 2 ? 6 : ui - 1;
            silu(acc[ut][ui], c4[us], ut, (b & 1) ? hi : lo, [&](int i) { bstep(b + 2, i); early(b * 4 + i); });
            if (b == 5) bst16(rs, rowpair(lo, hi), f2v, fs[2]);
            else if (b & 1) bst16(rs, u32x4{lo[0], lo[1], hi[0], hi[1]}, b < 5 ? v0 : fw, fs[b >> 1]);
            SBAR();
        }
        bar_raw();
        mma_done();
#pragma unroll
        for (int u = 0; u < 4; ++u) {
            silu(acc[u >> 1][5 + (u & 1)], c4[4 + (u & 1)], u >> 1, (u & 1) ? hi : lo, [&](int i) {
                const int n = u * 4 + i;
                early(40 + n);
                if (n < 10) we[n] = lds16(a0 + (n / 5) * 10240 + (n % 5) * 1024); });
            if (u & 1) bst16(rs, u32x4{lo[0], lo[1], hi[0], hi[1]}, fw, fs[5 + (u >> 1)]);
            SBAR();
        }
#pragma unroll
        for (int q = 56; q < Q0 + 2 * RE * QS; ++q) { early(q); SBAR(); }
        STAMP(12);
    }
    {
        f32x4 acc[2][5];
        unsigned a1 = a0 + 61440, a2 = a0 + 122880;
        PIN(a1); PIN(a2);
        res_init(acc[0], xr0, xr1, xr2); zero_acc(acc[1]);
        if (__any(eprobe == EMPTY))
            kstart<RE, 3, 2048>(ae, rs, ws, e0, e1, ge, [&] { return __builtin_amdgcn_raw_buffer_load_b32(rs, ehv, ehs, SC1) == EMPTY; }, [] {}, [] {});
        if (__builtin_expect(missing(ae[0][0], ae[0][1]), 0)) refetch(ae[0][0], ae[0][1], ws + ge[0], e0, e1);
        SBAR();
        STAMP(13);
        auto wae = [&](int n) { const int q = n / 5 % 15; return (q < 6 ? a0 : q < 12 ? a1 : a2) + (q % 6) * 10240 + (n % 5) * 1024; };
#define GRP(G, WA, WN) kloop<3 * G, 3, 27, 15 * G, 135, 5, 5, RE, 3, 2048>(acc, ae, we, wae, rs, ws, e0, e1, ge)
        GRP(0, a0, a0 + 30720);
        bar_raw();

        GRP(1, a0 + 30720, a1);
        bar_raw();
        GRP(2, a1, a1 + 30720);
        bar_raw();
        GRP(3, a1 + 30720, a2); bar_raw();
        GRP(4, a2, a0); bar_raw();
        GRP(5, a0, a0 + 30720); bar_raw();
        GRP(6, a0 + 30720, a1); bar_raw();
        GRP(7, a1, a1 + 30720);
        GRP(8, a1 + 30720, a1);
        mma_done();
        LANE_VARS();
        const unsigned wl = L + lane * 16;
        send_half(acc[1], wl + 122880 + (w ^ 1) * 5120);
        bar();
        recv_half(acc[0], wl + 122880 + w * 5120);
        STAMP(14);
        res_pack(xr0, xr1, xr2, acc[0]);
        char* ob = out + (size_t)(tb * 64 + w * 16 + c16) * 1280 + m * 160;
        asm volatile("global_store_dwordx4 %0, %1, off sc0 sc1\n\tglobal_store_dwordx4 %0, %2, off offset:64 sc0 sc1\n\t"
                     "global_store_dwordx2 %3, %4, off offset:128 sc0 sc1\n\ts_nop 1" ::"v"(ob + g * 16), "v"(xr0), "v"(xr1), "v"(ob + g * 8), "v"(xr2) : "memory");
    }
    asm volatile(".Lpf_end1:");
    STAMP_OUT(0);
    __builtin_amdgcn_endpgm();
}

extern "C" void kernel_launch(void* const* d_in, const int* in_sizes, int n_in, void* d_out, int out_size, void* d_ws, size_t ws_size,
                              hipStream_t stream) {
    (void)hipFuncSetAttribute((const void*)k_block, hipFuncAttributeMaxDynamicSharedMemorySize, LDS_BYTES);
    k_block<<<256, 512, LDS_BYTES, stream>>>((const char*)d_in[0], (const char*)d_in[1], (const char*)d_in[2], (const char*)d_in[3],
                                             (const char*)d_in[4], (const char*)d_in[6], (const char*)d_in[7], (char*)d_ws, (char*)d_out);
}
```

```cpp
#include <hip/hip_runtime.h>
#include <hip/hip_bf16.h>
#include <stdint.h>

typedef __attribute__((ext_vector_type(8))) __bf16 bf16x8;
typedef __attribute__((ext_vector_type(2))) __bf16 bf16x2;
typedef __attribute__((ext_vector_type(4))) short s16x4;
typedef __attribute__((ext_vector_type(4))) float f32x4;
typedef __attribute__((ext_vector_type(2))) float f32x2;
typedef __attribute__((ext_vector_type(16))) float f32x16;
typedef __attribute__((ext_vector_type(4))) unsigned u32x4;
typedef __attribute__((ext_vector_type(2))) unsigned u32x2;
typedef unsigned long long u64;
#define LAS __attribute__((address_space(3)))
#define GAS __attribute__((address_space(1)))
#define DI __device__ __forceinline__

constexpr int D = 640;
constexpr float LN_EPS = 1e-5f;
constexpr float LOG2E = 1.4426950408889634f;
constexpr float QSCALE = 0.22360679774997896f * LOG2E;

constexpr int KV_TILE = 2688;
constexpr int VS = 704;
constexpr unsigned WS_FL = 0;
constexpr unsigned FL_KV = 32768, FL_PR = 2 * 32768, FL_MK = 3 * 32768;
constexpr unsigned WS_KV = 4 * 32768;
constexpr unsigned WS_WO = WS_KV + 64 * KV_TILE;
constexpr unsigned WS_W1 = WS_WO + 8 * 20 * 5 * 1024;
constexpr unsigned WS_W2 = WS_W1 + 16 * 20 * 7 * 1024;
constexpr unsigned WS_CS = WS_W2 + 8 * 54 * 5 * 1024;
constexpr unsigned WS_O  = WS_CS + 20 * 1792 * 4;
constexpr unsigned WS_H  = WS_O + 128 * 20 * 1024;
constexpr unsigned WS_F  = WS_H + 128 * 20 * 1024;
constexpr unsigned WS_ST = WS_F + 128 * 54 * 1024;
constexpr unsigned WS_END = WS_ST + 2048 * 64;
constexpr unsigned EMPTY = 0xffffffffu;
static_assert((WS_END - WS_O) == 256 * 48640, "fill share per workgroup");

constexpr int CHUNK = 16 * KV_TILE;
constexpr int L_X = 0, L_WQ2 = 43008, L_WKV = 83968, L_WQ1 = 122880,
               L_CSX = 111104, L_QX = 152576, L_MISC = 162816;
constexpr int L_AUX = 3 * CHUNK, L_WO = CHUNK, L_CNT = 110592, XCH = L_WO + 102400, LDS_BYTES = 163840;

#define STAMP_INIT()
#define STAMP(I)
#define STAMP_OUT(K)

#define THROT 3
#define THROT2 3
#define RC 6
#define RD 7
#define RE 7
#define TAILPRIO 0
#define FIRSTFILL 4
#define ONEWAY 1
#define B0AT 0
#define NEARLY 12
#define PFDELAY 6
#define WKVAT 2
#define FLAGAT 0
#define P1PRIO 1
#define DRC -1
#define DRD 4
#define PE 10
#define NOST 0
DI void glds(unsigned voff, const void* sbase, unsigned lds) {
    asm volatile("s_mov_b32 m0, %2\n\ts_nop 0\n\tglobal_load_lds_dwordx4 %0, %1" ::"v"(voff), "s"(sbase), "s"(lds) : "memory");
}
DI void glds_dev(unsigned voff, const void* sbase, unsigned lds) {
    asm volatile("s_mov_b32 m0, %2\n\ts_nop 0\n\tglobal_load_lds_dwordx4 %0, %1 sc1" ::"v"(voff), "s"(sbase), "s"(lds) : "memory");
}
template <int N> DI void wait_vm() { asm volatile("s_waitcnt vmcnt(%0)" ::"i"(N) : "memory"); }
DI void bar() { asm volatile("s_waitcnt lgkmcnt(0)\n\ts_barrier" ::: "memory"); }
DI void bar_raw() { asm volatile("s_barrier" ::: "memory"); }
#define PIN(x) asm volatile("" : "+v"(x))
#define SBAR() __builtin_amdgcn_sched_barrier(0)
DI unsigned pk2(float a, float b) { bf16x2 v = {(__bf16)a, (__bf16)b}; return __builtin_bit_cast(unsigned, v); }
DI float bflo(unsigned u) { return __uint_as_float(u << 16); }
DI float bfhi(unsigned u) { return __uint_as_float(u & 0xffff0000u); }
DI f32x4 mfma16(u32x4 a, u32x4 b, f32x4 c) {
    return __builtin_amdgcn_mfma_f32_16x16x32_bf16(__builtin_bit_cast(bf16x8, a), __builtin_bit_cast(bf16x8, b), c, 0, 0, 0);
}
DI f32x16 mfma32(u32x4 a, u32x4 b, f32x16 c) {
    return __builtin_amdgcn_mfma_f32_32x32x16_bf16(__builtin_bit_cast(bf16x8, a), __builtin_bit_cast(bf16x8, b), c, 0, 0, 0);
}
DI f32x16 mfma32k8(u32x2 a, u32x2 b, f32x16 c) {
    return __builtin_amdgcn_mfma_f32_32x32x8bf16_1k(__builtin_bit_cast(s16x4, a), __builtin_bit_cast(s16x4, b), c, 0, 0, 0);
}
DI u32x4 lds16(unsigned a) { return *(const LAS u32x4*)(size_t)a; }
DI u32x2 lds8(unsigned a) { return *(const LAS u32x2*)(size_t)a; }
DI unsigned lds4(unsigned a) { return *(const LAS unsigned*)(size_t)a; }
DI f32x4 ldsf4(unsigned a) { return *(const LAS f32x4*)(size_t)a; }
DI u32x2 ldstr(unsigned a) { return __builtin_bit_cast(u32x2, __builtin_amdgcn_ds_read_tr16_b64_v4i16((LAS s16x4*)(size_t)a)); }
DI u32x4 ldstr2(unsigned a0, unsigned a1) { u32x2 lo = ldstr(a0), hi = ldstr(a1); return u32x4{lo[0], lo[1], hi[0], hi[1]}; }
DI float dot2(unsigned a, unsigned b, float c) {
    return __builtin_amdgcn_fdot2_f32_bf16(__builtin_bit_cast(bf16x2, a), __builtin_bit_cast(bf16x2, b), c, false);
}
DI float xsum16_32(float v) {
    const auto a = __builtin_amdgcn_permlane16_swap(__float_as_uint(v), __float_as_uint(v), false, false);
    v = __uint_as_float(a[0]) + __uint_as_float(a[1]);
    const auto b = __builtin_amdgcn_permlane32_swap(__float_as_uint(v), __float_as_uint(v), false, false);
    return __uint_as_float(b[0]) + __uint_as_float(b[1]);
}
constexpr unsigned ONES2 = 0x3f803f80u;
#define EX2(v) __builtin_amdgcn_exp2f(v)
typedef __amdgpu_buffer_rsrc_t rsrc_t;
constexpr int SC1 = 16;
DI u32x4 bld16(rsrc_t r, unsigned v, unsigned s) { return __builtin_amdgcn_raw_buffer_load_b128(r, v, s, SC1); }
DI void bst16(rsrc_t r, u32x4 d, unsigned v, unsigned s) {
    __builtin_amdgcn_raw_buffer_store_b128(d, r, v, s, SC1);
    asm volatile("s_nop 1" ::"v"(d) : "memory");
}
DI void bst8(rsrc_t r, u32x2 d, unsigned v, unsigned s) { __builtin_amdgcn_raw_buffer_store_b64(d, r, v, s, SC1); }

DI void set_flag(char* ws, unsigned off, u64 tag) { __hip_atomic_store((GAS u64*)(ws + off), tag, __ATOMIC_RELAXED, __HIP_MEMORY_SCOPE_AGENT); }
DI void poll(char* ws, unsigned arr, int index, u64 tag) {
    const GAS u64* p = (const GAS u64*)(ws + arr + index * 128);
    bool ok = index < 0;
#pragma unroll 1
    for (int spin = 0; spin < (1 << 18); ++spin) {
        if (!ok) ok = __hip_atomic_load(p, __ATOMIC_RELAXED, __HIP_MEMORY_SCOPE_AGENT) == tag;
        if (__all(ok)) break;
        __builtin_amdgcn_s_sleep(1);
    }
    asm volatile("" ::: "memory");
}
DI void copy_dev(const char* src, unsigned lds, int n, int hw, int lane) {
#pragma unroll 1
    for (int p = hw; p < n; p += 4) glds_dev(lane * 16, src + p * 1024, lds + p * 1024);
}

DI void cnt_add(unsigned a) {
    asm volatile("s_waitcnt lgkmcnt(0)\n\ts_mov_b64 exec, 1\n\tds_add_u32 %0, %1\n\ts_mov_b64 exec, -1" ::"v"(a), "v"(1u) : "memory");
}
DI unsigned cnt_arrive(unsigned a) {
    unsigned r;
    asm volatile("s_waitcnt lgkmcnt(0)\n\ts_mov_b64 exec, 1\n\tds_add_rtn_u32 %0, %1, %2\n\ts_mov_b64 exec, -1\n\ts_waitcnt lgkmcnt(0)"
                 : "=&v"(r) : "v"(a), "v"(1u) : "memory");
    return __builtin_amdgcn_readfirstlane(r);
}
DI void cnt_spin(unsigned a, unsigned n) {
#pragma unroll 1
    while ((unsigned)__builtin_amdgcn_readfirstlane(*(volatile LAS unsigned*)(size_t)a) < n) {}
    asm volatile("" ::: "memory");
}
DI void cnt_wait(unsigned a, unsigned n) {
#pragma unroll 1
    while ((unsigned)__builtin_amdgcn_readfirstlane(*(volatile LAS unsigned*)(size_t)a) < n) __builtin_amdgcn_s_sleep(1);
    asm volatile("" ::: "memory");
}
DI unsigned keep(unsigned v) { return min(v, EMPTY - 1); }
DI bool missing(const u32x4& a, const u32x4& b) { return __any(max(max(a[0], a[2]), max(b[0], b[2])) == EMPTY); }
DI void refetch(u32x4& a, u32x4& b, const char* base, unsigned v0, unsigned v1) {
#pragma unroll 1
    for (int spin = 0; spin < (1 << 16); ++spin) {
        asm volatile("global_load_dwordx4 %0, %2, %4 sc1\n\tglobal_load_dwordx4 %1, %3, %4 sc1\n\ts_waitcnt vmcnt(0)"
                     : "+v"(a), "+v"(b) : "v"(v0), "v"(v1), "s"(base) : "memory");
        if (!missing(a, b)) break;
    }
}
DI u32x4 rowpair(const u32x2& a, const u32x2& b) {
    const auto s0 = __builtin_amdgcn_permlane16_swap(a[0], b[0], false, false), s1 = __builtin_amdgcn_permlane16_swap(a[1], b[1], false, false);
    return u32x4{s0[0], s1[0], s0[1], s1[1]};
}
DI int lane_id() {
    int l;
    asm volatile("v_mbcnt_lo_u32_b32 %0, -1, 0\n\tv_mbcnt_hi_u32_b32 %0, -1, %0" : "=v"(l));
    return l;
}
#define LANE_VARS() const int lane = lane_id(), g = lane >> 4, c16 = lane & 15; (void)g; (void)c16
DI float halfmax(float m) {
    auto r = __builtin_amdgcn_permlane32_swap(__float_as_uint(m), __float_as_uint(m), false, false);
    return fmaxf(__uint_as_float(r[0]), __uint_as_float(r[1]));
}
DI float upper(float v) {
    auto r = __builtin_amdgcn_permlane32_swap(__float_as_uint(v), __float_as_uint(v), false, false);
    return __uint_as_float(r[1]);
}
DI float max16(const f32x16& s) {
    float a = fmaxf(fmaxf(s[0], s[1]), s[2]), b = fmaxf(fmaxf(s[3], s[4]), s[5]);
    a = fmaxf(fmaxf(a, s[6]), s[7]); b = fmaxf(fmaxf(b, s[8]), s[9]);
    a = fmaxf(fmaxf(a, s[10]), s[11]); b = fmaxf(fmaxf(b, s[12]), s[13]);
    a = fmaxf(fmaxf(a, s[14]), s[15]);
    return fmaxf(a, b);
}
DI float amax3(float m, float a, float b) {
    float r;
    asm("v_maximum3_f32 %0, %1, |%2|, |%3|" : "=v"(r) : "v"(m), "v"(a), "v"(b));
    return r;
}
DI f32x16 attn_exact(const char* kv, u32x4 q1, unsigned q2lo, int lane, int vrow) {
    const int r32 = lane & 31, h = lane >> 5;
    const f32x16 zero16 = {0, 0, 0, 0, 0, 0, 0, 0, 0, 0, 0, 0, 0, 0, 0, 0};
    const u32x2 q2 = {q2lo, 0u};
    float m = -INFINITY;
    f32x16 o = zero16;
#pragma unroll 1
    for (int t = 0; t < 64; ++t) {
        const GAS char* tp = (const GAS char*)kv + t * KV_TILE;
        const u32x4 fk1 = *(const GAS u32x4*)(tp + lane * 16);
        const u32x2 fk2 = {*(const GAS unsigned*)(tp + 1024 + r32 * 8 + h * 4), 0u};
        const u32x4 fv0 = *(const GAS u32x4*)(tp + 1280 + (h * 22 + vrow) * 16), fv1 = *(const GAS u32x4*)(tp + 1280 + VS + (h * 22 + vrow) * 16);
        f32x16 s = mfma32(fk1, q1, mfma32k8(fk2, q2, zero16));
        const float mn = fmaxf(m, halfmax(max16(s))), al = EX2(m - mn);
        m = mn;
        u32x4 p0, p1;
#pragma unroll
        for (int i = 0; i < 4; ++i) { p0[i] = pk2(EX2(s[2 * i] - m), EX2(s[2 * i + 1] - m)); p1[i] = pk2(EX2(s[8 + 2 * i] - m), EX2(s[9 + 2 * i] - m)); }
#pragma unroll
        for (int i = 0; i < 16; ++i) o[i] *= al;
        o = mfma32(fv1, p1, mfma32(fv0, p0, o));
    }
    return o;
}

DI void mma16(f32x4& c, const u32x4& a, const u32x4& b) {
    asm volatile("v_mfma_f32_16x16x32_bf16 %0, %1, %2, %0" : "+v"(c) : "v"(a), "v"(b));
}
DI void mma16z(f32x4& c, const u32x4& a, const u32x4& b) { asm volatile("v_mfma_f32_16x16x32_bf16 %0, %1, %2, 0" : "=&v"(c) : "v"(a), "v"(b)); }
DI void mma_done() { SBAR(); asm volatile("s_nop 7\n\ts_nop 1"); SBAR(); }
template <int NS> DI void zero_acc(f32x4 (&a)[NS]) {
#pragma unroll
    for (int s = 0; s < NS; ++s) { a[s] = f32x4{0, 0, 0, 0}; PIN(a[s]); }
}
template <int GS, int ASTR, int NG> DI u32x4 aload(rsrc_t rs, unsigned& v, const unsigned (&gb)[NG], int j) {
    const int o = (j % GS) * ASTR;
    PIN(v);
    return bld16(rs, v + (o & 4095), gb[j / GS] + (o & ~4095));
}
constexpr int areq(int j, int PD, int KT) {
    const int a = FIRSTFILL + 2 * j, b = PD + (j > 0 ? j - 1 : 0), c = a < b ? a : b;
    return c < KT ? c : KT;
}
template <int N> DI void lgkm() { __builtin_amdgcn_s_waitcnt(0xC07F | (N << 8)); }
template <int K0, int NK, int KT, int F0, int FT, int NS, int NSN, int PD, int GS, int ASTR, int NA, int RING, int POOL, int NG, class WAddr>
DI void kloop(f32x4 (&acc)[2][NA], u32x4 (&act)[RING][2], u32x4 (&wf)[POOL], WAddr waddr, rsrc_t rs, const char* ws, unsigned& v0,
              unsigned& v1, const unsigned (&gb)[NG]) {
    if constexpr (NK > 0) {
        constexpr int j = K0, q0 = areq(j, PD, KT), q1 = areq(j + 1, PD, KT), jc = j + 1 < KT ? j + 1 : 0, nn = NK > 1 ? NS : NSN;
        constexpr int issued = F0 + NS + POOL < FT ? F0 + NS + POOL : FT, need = F0 + NS + nn < FT ? F0 + NS + nn : FT;
        unsigned t = 0;
        u64 bad = 0;
        if (j == 0) { lgkm<POOL - NS>(); SBAR(); }
#pragma unroll
        for (int i = 0; i < 2 * NS; ++i) {
            const int n = F0 + (i >> 1);
            mma16(acc[i & 1][i >> 1], wf[n % POOL], act[j % RING][i & 1]);
            SBAR();
            if ((i & 1) && n + POOL < FT) wf[n % POOL] = lds16(waddr(n + POOL));
            if (!(i & 1) && q0 + i / 4 < q1) act[(q0 + i / 4) % RING][i / 2 & 1] = aload<GS, ASTR>(rs, (i & 2) ? v1 : v0, gb, q0 + i / 4);
            if (jc) {
                if (i == (NS > 2 ? 2 * NS - 6 : 0)) {
                    t = max(act[jc % RING][1][0], act[jc % RING][1][2]);
                    t = max(max(t, act[jc % RING][0][0]), act[jc % RING][0][2]); PIN(t);
                }
                if (i == (NS > 2 ? 2 * NS - 4 : 1)) { bad = __ballot(t == EMPTY); asm volatile("" : "+s"(bad)); }
                if (i == 2 * NS - 2 && __builtin_expect(bad != 0, 0))
                    refetch(act[jc % RING][0], act[jc % RING][1], ws + gb[jc / GS] + (jc % GS) * ASTR, v0, v1);
            }
            if (i == 2 * NS - 1 && need > F0 + NS) { SBAR(); lgkm<issued - need>(); }
            SBAR();
        }
        kloop<K0 + 1, NK - 1, KT, F0 + NS, FT, NS, NSN, PD, GS, ASTR>(acc, act, wf, waddr, rs, ws, v0, v1, gb);
    }
}
template <int PD, int GS, int ASTR, int RING, int NG, class Probe, class Before, class After>
DI void kstart(u32x4 (&act)[RING][2], rsrc_t rs, const char* ws, unsigned& v0, unsigned& v1, const unsigned (&gb)[NG], Probe probe,
               Before before, After after) {
    before();
    SBAR();
#pragma unroll 1
    for (int spin = 0; spin < (1 << 16); ++spin) {
        const bool miss = probe();
        SBAR();
#pragma unroll
        for (int k = 0; k < FIRSTFILL; ++k) {
            act[k][0] = aload<GS, ASTR>(rs, v0, gb, k); SBAR();
            act[k][1] = aload<GS, ASTR>(rs, v1, gb, k); SBAR();
        }
        if (!__any(miss)) break;
        asm volatile("" ::: "memory");
    }
    SBAR();
    after();
    SBAR();
    if (__builtin_expect(missing(act[0][0], act[0][1]), 0)) refetch(act[0][0], act[0][1], ws + gb[0], v0, v1);
    SBAR();
}
DI void e_bases(unsigned (&gb)[9], unsigned base, int m, int stride) {
    const int m3 = m % 3;
#pragma unroll
    for (int i = 0; i < 9; ++i) {
        const int q = i < 4 ? (i + m) & 3 : i < 6 ? 4 + ((i + (m >> 2)) & 1) : 6 + (i - 6 + m3 >= 3 ? i - 9 + m3 : i - 6 + m3);
        gb[i] = base + q * stride;
        asm volatile("" : "+s"(gb[i]));
    }
}
template <int NG> DI void rot_bases(unsigned (&gb)[NG], unsigned base, int rot, int stride) {
#pragma unroll
    for (int i = 0; i < NG; ++i) { const int q = i + rot; gb[i] = base + (q >= NG ? q - NG : q) * stride; asm volatile("" : "+s"(gb[i])); }
}
DI void send_half(const f32x4 (&a)[5], unsigned dst) {
#pragma unroll
    for (int s = 0; s < 5; ++s) *(LAS f32x4*)(size_t)(dst + s * 1024) = a[s];
}
DI void recv_half(f32x4 (&a)[5], unsigned src) {
#pragma unroll
    for (int s = 0; s < 5; ++s) a[s] += ldsf4(src + s * 1024);
}
DI void ldsw16(unsigned a, const u32x4& v) { *(LAS u32x4*)(size_t)a = v; }
DI f32x4 unpk(unsigned r0, unsigned r1) { return f32x4{bflo(r0), bfhi(r0), bflo(r1), bfhi(r1)}; }
DI void res_init(f32x4 (&a)[5], const u32x4& x0, const u32x4& x1, const u32x2& x2) {
    a[0] = unpk(x0[0], x0[1]); a[1] = unpk(x0[2], x0[3]); a[2] = unpk(x1[0], x1[1]); a[3] = unpk(x1[2], x1[3]); a[4] = unpk(x2[0], x2[1]);
#pragma unroll
    for (int s = 0; s < 5; ++s) PIN(a[s]);
}
DI void res_pack(u32x4& x0, u32x4& x1, u32x2& x2, const f32x4 (&a)[5]) {
    x0 = u32x4{pk2(a[0][0], a[0][1]), pk2(a[0][2], a[0][3]), pk2(a[1][0], a[1][1]), pk2(a[1][2], a[1][3])};
    x1 = u32x4{pk2(a[2][0], a[2][1]), pk2(a[2][2], a[2][3]), pk2(a[3][0], a[3][1]), pk2(a[3][2], a[3][3])};
    x2 = u32x2{pk2(a[4][0], a[4][1]), pk2(a[4][2], a[4][3])};
}
enum { C_WKV, C_KFUSED, C_X, C_XUSED, C_WQ, C_CSP = C_WQ + 5, C_CS, C_KVST, C_R1, C_XCH, C_NUM = C_XCH + 2 };
enum { E_C1, E_C2, E_C3, E_MK, P_NONE, P_C0, P_C1, P_C2, E_C0, H_PREP, H_FILL, M_LDS, M_K8, M_T4, M_NUM };

__global__ __launch_bounds__(512, 2) void k_block(const char* __restrict__ x, const char* __restrict__ wq, const char* __restrict__ wk,
                                                  const char* __restrict__ wv, const char* __restrict__ wo, const char* __restrict__ w1,
                                                  const char* __restrict__ w2, char* ws, char* __restrict__ out) {
    extern __shared__ __attribute__((aligned(16))) char smem[];
    const unsigned L = (unsigned)(size_t)(LAS char*)smem;
    const int w = __builtin_amdgcn_readfirstlane(threadIdx.x >> 6);
    const int xg = blockIdx.x & 7, idx = blockIdx.x >> 3;
    const int tb = ONEWAY ? xg * 4 + (idx >> 3) : (xg >> 1) * 8 + (idx >> 2), m = ONEWAY ? (idx & 7) : (xg & 1) * 4 + (idx & 3), cid = tb * 8 + m;
    const int ct = (m >> 2) * 8 + (tb & 1) * 4 + (m & 3);
    const rsrc_t rs = __builtin_amdgcn_make_buffer_rsrc(ws, 0, WS_END, 0x00020000);
    const u32x4 ones = {ONES2, ONES2, ONES2, ONES2};
    const f32x4 z4 = {0, 0, 0, 0};
    STAMP_INIT();

#define LOAD_TAG() u64 tag; asm volatile("global_load_dwordx2 %0, %1, %2 sc1" : "=v"(tag) : "v"(0u), "s"(ws + WS_FL) : "memory")
#define MAKE_TAG() do { PIN(tag); \
        tag = ((u64)(unsigned)__builtin_amdgcn_readfirstlane(tag >> 32) << 32) | (unsigned)__builtin_amdgcn_readfirstlane((unsigned)tag); \
        tag = (tag ^ 0x9E3779B97F4A7C15ull) * 0xBF58476D1CE4E5B9ull + 0x632BE59BD9B4E019ull; asm volatile("" : "+s"(tag)); } while (0)

#define CNT(i) (L + L_CNT + (i) * 4)
#define CN2(i) (L + L_MISC + 512 + (i) * 4)
#define WQ_AT(r) ((r) < 2 ? L_WQ1 + (r) * 20480 : L_WQ2 + (r) % 3 * 20480)
#define WQ_ADDR() const unsigned wrow = L + (8 * g + (c16 >> 2)) * 160 + (lane & 3) * 8, wo0 = wrow + (g & 1) * 640, wo1 = wrow + ((g & 1) ^ 1) * 640
    const int rotc = ONEWAY ? m % 5 : m & 3, rotd = ONEWAY ? ct % 5 : (ct & 7) % 5;

    if (w >= 4) {
        const int hw = w - 4;
        LANE_VARS();
        __builtin_amdgcn_s_setprio(P1PRIO);
        if (m < 4) {
            const char* src = m < 2 ? wk : wv;
#pragma unroll 1
            for (int p = hw; p < 25; p += 4) glds(lane * 16, src + p * 1024, L + L_WKV + p * 1024);
        }
        {
            const unsigned vx0 = (lane >> 3) * 1280 + (((lane & 7) ^ g) << 4), vx1 = (8 + (lane >> 3)) * 1280 + (((lane & 7) ^ (4 + g)) << 4);
            const char* xb = x + (size_t)(tb * 64 + hw * 16) * 1280;
            int sg = ONEWAY ? m : (m & 3) * 2;
#pragma unroll 1
            for (int i = 0; i < 10; ++i) {
                glds(vx0, xb + sg * 128, L + L_X + (hw * 20 + sg) * 1024);
                glds(vx1, xb + sg * 128, L + L_X + (hw * 20 + 10 + sg) * 1024);
                sg = sg == 9 ? 0 : sg + 1;
                if (i == B0AT) bar_raw();
                if (i == WKVAT) { wait_vm<2 * WKVAT + 2>(); cnt_add(CNT(C_WKV)); }
            }
        }
    }
    unsigned pf[5] = {0, 0, 0, 0, 0};
    if (w < 4) {
        static_assert(ONEWAY, "");
        const int lane = lane_id();
        const unsigned vo = lane * 128;
        const int wi = idx * 4 + w;
        asm volatile("s_mov_b64 exec, 0xfffff\n\tglobal_load_dword %0, %2, %3\n\ts_mov_b64 exec, 15\n\tglobal_load_dword %1, %2, %4\n\ts_mov_b64 exec, -1"
                     : "+v"(pf[0]), "+v"(pf[1])
                     : "v"(vo), "s"(x + xg * 327680 + wi * 2560), "s"(((wi & 1) ? wv : wk) + min(wi >> 1, 49) * 512) : "memory");
        u64 pc; int l1, l2;
        asm volatile("s_getpc_b64 %0\n.Lpf_pc:\n\ts_mov_b32 %1, .Lpf_end1-.Lpf_pc\n\ts_mov_b32 %2, .Lpf_end2-.Lpf_pc" : "=s"(pc), "=s"(l1), "=s"(l2));
        const unsigned len = max(max(l1, l2), 4);
        const unsigned co = min((unsigned)(wi * 4 + (lane & 3)) * 128u, len - 4) & ~3u;
        asm volatile("global_load_dword %0, %1, %2" : "+v"(pf[3]) : "v"(co), "s"(pc) : "memory");
    }

    if (w >= 4) {
        const int hw = w - 4;
        LANE_VARS();
        LOAD_TAG();
        u64 f0;
        const int c0 = tb >> 3;
        {
            unsigned vw[5];
#pragma unroll
            for (int i = 0; i < 5; ++i) {
                const int ci = i * 64 + lane, pos = ci / 10, ch = ci - pos * 10, k = pos ^ (((pos >> 3) & 1) << 2);
                vw[i] = k * 1280 + ch * 16;
            }
            const char* wb = wq + m * 160 + hw * 40960;
            WQ_ADDR();
            f32x4 cs[5];
            u32x4 one = ones;
            PIN(one);
            zero_acc(cs);
            auto wqload = [&](int r) {
#pragma unroll
                for (int i = 0; i < 5; ++i) glds(vw[i], wb + r * 163840, L + WQ_AT(r) + hw * 5120 + i * 1024);
            };
            auto wqsum = [&](int r) {
                u32x4 f[5];
#pragma unroll
                for (int s = 0; s < 5; ++s) f[s] = ldstr2(wo0 + WQ_AT(r) + hw * 5120 + s * 32, wo1 + WQ_AT(r) + hw * 5120 + s * 32);
#pragma unroll
                for (int s = 0; s < 5; ++s) mma16(cs[s], f[s], one);
            };
            wqload(0);
            wait_vm<5>();
            cnt_add(CNT(C_X));
            STAMP(1);
            wqload(1);
            MAKE_TAG();
            cnt_spin(CNT(C_KFUSED), 4);
            wqload(2); wait_vm<10>(); cnt_add(CNT(C_WQ));
            cnt_spin(CNT(C_XUSED), 4);
            wqload(3); wait_vm<10>(); cnt_add(CNT(C_WQ + 1));
            wqload(4);
            wqsum(0); wqsum(1);
            wait_vm<10>(); cnt_add(CNT(C_WQ + 2)); wqsum(2);
            wait_vm<5>(); cnt_add(CNT(C_WQ + 3)); wqsum(3);
            wait_vm<0>(); cnt_add(CNT(C_WQ + 4));
            f0 = __hip_atomic_load((const GAS u64*)(ws + FL_KV + (c0 * 32 + (lane & 31)) * 128), __ATOMIC_RELAXED, __HIP_MEMORY_SCOPE_AGENT);
            wqsum(4);
            mma_done();
            if (c16 == 0) {
#pragma unroll
                for (int s = 0; s < 5; ++s) *(LAS f32x4*)(size_t)(L + L_CSX + hw * 320 + s * 64 + g * 16) = cs[s];
            }
            if (cnt_arrive(CNT(C_CSP)) == 3) {
                const unsigned ca = L + L_CSX + (lane < 20 ? lane : 0) * 16;
                const f32x4 c4 = (ldsf4(ca) + ldsf4(ca + 320)) + (ldsf4(ca + 640) + ldsf4(ca + 960));
                *(LAS f32x4*)(size_t)(ca + 1280) = c4;
                cnt_add(CNT(C_CS));
            }
        }
        const char* kv = ws + WS_KV;
        STAMP(2);
        poll(ws, FL_KV, lane < 32 && f0 != tag ? c0 * 32 + lane : -1, tag);
        STAMP(3);
        copy_dev(kv + c0 * CHUNK, L, 42, hw, lane);
        wait_vm<7>();
        STAMP(4);
        __builtin_amdgcn_s_setprio(2);
        bar_raw();
        wait_vm<0>();
        cnt_add(CN2(E_C0));
        poll(ws, FL_KV, (c0 * 32 + 32 + lane) & 127, tag);
        copy_dev(kv + ((c0 + 1) & 3) * CHUNK, L + CHUNK, 42, hw, lane);
        {
            int u = blockIdx.x * 4 + hw;
            const int pmat = u < 160 ? 0 : (u < 480 ? 1 : (u < 912 ? 2 : 3));
            u = pmat == 0 ? u : pmat == 1 ? u - 160 : pmat == 2 ? u - 480 : 0;
            const int pc = pmat < 2 ? u / 20 : u / 54, ks = u - pc * (pmat < 2 ? 20 : 54);
            int rb, a3, a2, a1;
            if (pmat == 0) { if (ks < 16) { rb = 40 * ks; a3 = 20; a2 = 4; a1 = 8; } else { rb = 160 * (ks - 16) + 16; a3 = 80; a2 = 40; a1 = 20; } }
            else if (pmat == 1) { if (ks < 16) { rb = 80 * (ks >> 1) + 32 * (ks & 1); a3 = 16; a2 = 8; a1 = 4; } else { rb = 160 * (ks - 16) + 64; a3 = 80; a2 = 8; a1 = 4; } }
            else if (ks >= 16 && ks < 24) { rb = 224 * (ks - 16) + 96; a3 = ks < 23 ? 112 : 48; a2 = 8; a1 = 4; }
            else { rb = ks < 16 ? 112 * ks : (ks < 39 ? 112 * (ks - 24) + 32 : 112 * (ks - 39) + 64); a3 = 8; a2 = 4; a1 = 16; }
            const int ld = pmat == 1 ? 3456 : 1280, chmax = pmat != 1 ? 9 : (pc == 15 ? 5 : 13);
            const char* psrc = pmat == 0 ? wo + pc * 160 : pmat == 1 ? w1 + pc * 224 : w2 + pc * 160;
            const unsigned pb = L + 2 * CHUNK + hw * 8192, pv = g * ld + min(c16, chmax) * 16;
#pragma unroll 1
            for (int i = 0; i < 8; ++i) glds(pv, psrc + (rb + a3 * (i >> 2) + a2 * ((i >> 1) & 1) + a1 * (i & 1)) * ld, pb + i * 1024);
            wait_vm<8>();
            cnt_add(CN2(E_C1));
            wait_vm<0>();
            const unsigned a0 = pb + (8 * g + (c16 >> 2)) * 256 + (lane & 3) * 8;
            if (pmat == 1) {
                const unsigned pdst = WS_W1 + pc * 143360 + ks * 5120, pd2 = WS_W1 + pc * 143360 + 102400 + ks * 2048;
                const unsigned cv = c16 == 0 ? g * 16 : 0x80000000u;
#pragma unroll
                for (int s = 0; s < 7; ++s) {
                    const unsigned a1 = a0 + (s == 6 ? (pc == 15 ? 64 : 192) : s * 32);
                    const u32x4 f = ldstr2(a1, a1 + 1024);
                    bst16(rs, f, lane * 16, s < 2 ? pdst + s * 1024 : s < 4 ? pdst + (s + 1) * 1024 : s < 6 ? pd2 + (s - 4) * 1024 : pdst + 2048);
                    bst16(rs, __builtin_bit_cast(u32x4, mfma16(f, ones, z4)), cv, WS_CS + (ks * 1792 + pc * 112) * 4 + s * 64);
                }
            } else if (pmat < 3) {
                const unsigned a1 = a0 + (lane & 3) * 8, pdst = (pmat == 0 ? WS_WO : WS_W2) + u * 5120;
#pragma unroll
                for (int s = 0; s < 5; ++s) {
                    const unsigned a2 = s < 4 ? a1 + (s >> 1) * 64 + (s & 1) * 8 : a0 + 128;
                    bst16(rs, ldstr2(a2, a2 + 1024), lane * 16, pdst + s * 1024);
                }
            }
        }
        wait_vm<0>();
        if (cnt_arrive(CN2(H_PREP)) == 3 && lane == 0) set_flag(ws, FL_PR + cid * 128, tag);
        STAMP(5);
        cnt_wait(CN2(H_PREP), 4);
        copy_dev(kv + ((c0 + 2) & 3) * CHUNK, L + 2 * CHUNK, 42, hw, lane);
        {
            const u32x4 e = {EMPTY, EMPTY, EMPTY, EMPTY};
#pragma unroll 1
            for (int p = hw; p < 44; p += 4) bst16(rs, e, lane * 16, WS_O + blockIdx.x * 48640 + p * 1024);
            bst16(rs, e, hw < 3 || lane < 32 ? lane * 16 : 0x80000000u, WS_O + blockIdx.x * 48640 + (44 + hw) * 1024);
        }
        wait_vm<12>();
        cnt_add(CN2(E_C2));
        poll(ws, FL_KV, (c0 * 32 + 96 + (lane & 31)) & 127, tag);
        wait_vm<0>();
        if (cnt_arrive(CN2(H_FILL)) == 3 && lane == 0) set_flag(ws, FL_MK + cid * 128, tag);
        cnt_wait(CN2(P_C0), 4);
        copy_dev(kv + ((c0 + 3) & 3) * CHUNK, L, 42, hw, lane);
        poll(ws, FL_PR, hw * 64 + lane, tag);
        wait_vm<0>();
        cnt_add(CN2(E_C3));
        cnt_wait(CN2(E_C3), 4);
        STAMP(6);
        if (blockIdx.x == 0 && hw == 1 && lane == 0) set_flag(ws, WS_FL, tag);
        if (hw == 3) {
            u32x4 c[20];
            const int l28 = lane < 28 ? lane : 0;
#pragma unroll
            for (int ks = 0; ks < 20; ++ks) c[ks] = bld16(rs, l28 * 16, WS_CS + (ks * 1792 + ct * 112) * 4);
            f32x4 c4 = z4;
#pragma unroll
            for (int ks = 0; ks < 20; ++ks) c4 += __builtin_bit_cast(f32x4, c[ks]);
            if (lane < 28) *(LAS f32x4*)(size_t)(L + L_MISC + lane * 16) = c4;
        }
        auto wocopy = [&](int p0, int p1) {
#pragma unroll 1
            for (int p = p0 + hw; p < p1; p += 4) {
                const int q = p + rotc * 20;
                glds_dev(lane * 16, ws + WS_WO + m * 102400 + (q >= 100 ? q - 100 : q) * 1024, L + L_WO + p * 1024);
            }
        };
        u32x4 r1[35], r2[9][8];
        unsigned g1[5], g1b[5], g2[9];
        rot_bases(g1, WS_W1 + ct * 143360, rotd, 20480);
        rot_bases(g1b, WS_W1 + ct * 143360 + 102400, rotd, 8192);
        e_bases(g2, WS_W2 + m * 276480, m, 30720);
        const unsigned vp = lane * 16 + hw * 1024, vq = lane * 16 + min(28 + hw, 29) * 1024;
        unsigned lp0 = L + vp, lp1 = lp0 + 61440, lp2 = lp0 + 122880, lq0 = L + vq, lq1 = lq0 + 61440, lq2 = lq0 + 122880;
        PIN(lp0); PIN(lp1); PIN(lp2); PIN(lq0); PIN(lq1); PIN(lq2);
#define LD2(G, THR) do { _Pragma("unroll") for (int j_ = 0; j_ < 7; ++j_) { r2[G][j_] = bld16(rs, vp, g2[G] + j_ * 4096); wait_vm<THR>(); } \
        r2[G][7] = bld16(rs, vq, g2[G]); wait_vm<THR>(); } while (0)
#define ST2E(G, j) do { constexpr int sl_ = (G) % 5; \
        ldsw16((j) < 7 ? (sl_ < 2 ? lp0 : sl_ < 4 ? lp1 : lp2) + (sl_ & 1) * 30720 + (j) * 4096 : (sl_ < 2 ? lq0 : sl_ < 4 ? lq1 : lq2) + (sl_ & 1) * 30720, r2[G][j]); } while (0)
#pragma unroll
        for (int i = 0; i < 35; ++i) r1[i] = bld16(rs, vp, i < 25 ? g1[i / 5] + (i % 5) * 4096 : g1b[(i - 25) / 2] + ((i - 25) & 1) * 4096);
        SBAR();
        LD2(7, 63); LD2(8, 63);
        poll(ws, FL_MK, hw * 64 + lane, tag);
        cnt_add(CN2(E_MK));
        cnt_wait(CN2(P_C1), 4);
        wocopy(0, 42);
        cnt_wait(CN2(P_C2), 4);
        wocopy(42, 100);
#pragma unroll
        for (int G = 0; G < 7; ++G) {
            const int p = (tb & 3) * 8 + hw * 2;
            glds_dev(lane * 16, ws + g2[G] + min(p, 28) * 1024, L + XCH + hw * 1024);
            glds_dev(lane * 16, ws + g2[G] + min(p + 1, 29) * 1024, L + XCH + hw * 1024);
        }
        wait_vm<0>();
        STAMP(7);
        __builtin_amdgcn_s_setprio(TAILPRIO);
        bar_raw();
#pragma unroll
        for (int i = 0; i < 8; ++i) ldsw16(lp0 + i * 4096, r1[i < 4 ? 25 + i : i - 4]);
        SBAR();
        LD2(6, THROT);
        bar();
        cnt_wait(CN2(M_LDS), 4);
#pragma unroll
        for (int i = 8; i < 35; ++i) {
            ldsw16((i < 15 ? lp0 : i < 30 ? lp1 : lp2) + (i % 15) * 4096, r1[i < 29 ? i - 4 : i]);
            SBAR();
            if (i >= 11 && i - 11 < NEARLY) {
                const int e = i - 11, G = 3 + (e >> 3), j = e & 7;
                r2[G][j] = bld16(rs, j < 7 ? vp : vq, g2[G] + (j < 7 ? j * 4096 : 0));
                SBAR();
            }
        }
        STAMP(8);
        bar();
#pragma unroll
        for (int e = NEARLY; e < 24; ++e) {
            const int G = 3 + (e >> 3), j = e & 7;
            r2[G][j] = bld16(rs, j < 7 ? vp : vq, g2[G] + (j < 7 ? j * 4096 : 0));
            wait_vm<THROT>();
        }
        cnt_wait(CN2(M_K8), 4);
#pragma unroll 1
        for (int p = hw; p < 60; p += 4) { glds_dev(lane * 16, ws + (p < 30 ? g2[0] + p * 1024 : g2[1] + (p - 30) * 1024), L + p * 1024); wait_vm<THROT>(); }
        cnt_wait(CN2(M_T4), 4);
        copy_dev(ws + g2[2], L + 61440, 30, hw, lane);
        wait_vm<7>();
        STAMP(9);
        bar_raw();
#pragma unroll
        for (int iv = 0; iv < 7; ++iv) {
#pragma unroll
            for (int e = iv < 3 ? iv * 8 - (iv == 2) * 0 : 24 + (iv - 3) * 6; e < (iv < 3 ? iv * 8 + 8 : 24 + (iv - 2) * 6); ++e) {
                if (!(NOST && iv > 0)) switch (e >> 3) {
                    case 0: ST2E(3, e & 7); break;
                    case 1: ST2E(4, e & 7); break;
                    case 2: ST2E(5, e & 7); break;
                    case 3: ST2E(6, e & 7); break;
                    case 4: ST2E(7, e & 7); break;
                    default: ST2E(8, e & 7); break;
                }
                SBAR();
            }
            wait_vm<0>();
            bar();
        }
        asm volatile(".Lpf_end2:");
        STAMP_OUT(1);
        __builtin_amdgcn_endpgm();
    }

    u32x4 xr0, xr1; u32x2 xr2;
    u32x4 qa1, qb1; u32x2 qa2, qb2;
    const int kh = w & 1;
    {
        LANE_VARS();
        LOAD_TAG();
        u32x4 xf[20], xo[10], kf[20];
        if (w == 0 && lane < C_NUM) *(LAS unsigned*)(size_t)CNT(lane) = 0u;
        bar();
        const unsigned xrow = L + L_X + w * 20480 + (c16 >> 3) * 10240 + (c16 & 7) * 128;
        const int sw = c16 >> 1, t32 = (w & 1) * 16 + c16;
        unsigned xa = xrow + (((kh * 4 + g) ^ sw) << 4), xb = xrow + ((((kh ^ 1) * 4 + g) ^ sw) << 4), xc = xa + ((w ^ 1) - w) * 20480, xra[3], qw[5], kvo;
        {
            const int c0 = m * 10 + g, c1 = c0 + 4, c2 = m * 10 + 8 + (g >> 1);
            xra[0] = xrow + (c0 >> 3) * 1024 + (((c0 & 7) ^ sw) << 4);
            xra[1] = xrow + (c1 >> 3) * 1024 + (((c1 & 7) ^ sw) << 4);
            xra[2] = xrow + (c2 >> 3) * 1024 + (((c2 & 7) ^ sw) << 4) + (g & 1) * 8;
            const int d = (m & 1) * 16 + c16;
            kvo = m == 0 ? ((g >> 1) * 32 + t32) * 16 + (g & 1) * 8
                : m == 1 ? (g == 0 ? 1024 + t32 * 8 : 0x80000000u)
                         : (d <= 21 ? 1280 + (((w & 1) * 2 + (g & 1)) * 22 + d) * 16 + (g >> 1) * 8 : 0x80000000u);
            PIN(xa); PIN(xb); PIN(xc); PIN(xra[0]); PIN(xra[1]); PIN(xra[2]); PIN(kvo);
        }
#pragma unroll
        for (int s = 0; s < 5; ++s) {
            const int f0 = s * 16 + 4 * g, hh = f0 / 20, d0 = f0 - hh * 20;
            qw[s] = L + L_QX + (hh * 2 + (w >> 1)) * 1280 + (d0 < 16 ? ((d0 >> 3) * 32 + t32) * 16 + (d0 & 4) * 2 : 1024 + t32 * 8);
            PIN(qw[s]);
        }
        WQ_ADDR();
        unsigned b0 = wo0 + L_WQ1 + kh * 5120, b1 = wo1 + L_WQ1 + kh * 5120;
        PIN(b0); PIN(b1);
        wait_vm<0>();
        MAKE_TAG();
        if (m >= 4) {
            __builtin_amdgcn_s_sleep(PFDELAY);
            const char* wp = wq + (((idx >> 3) * 4 + (m & 3)) * 4 + w) * 12800;
            asm volatile("s_mov_b64 exec, %5\n\tglobal_load_dword %0, %2, %3\n\tglobal_load_dword %1, %2, %4\n\ts_mov_b64 exec, -1"
                         : "+v"(pf[2]), "+v"(pf[4]) : "v"(lane * 128), "s"(wp), "s"(wp + 6400), "s"(0x3ffffffffffffull) : "memory");
        }
        f32x4 ka = z4, kc = z4, ssm = z4, ssq = z4, acc[2][5];
        u32x4 one = ones;
        PIN(ssm); PIN(ssq); PIN(one);
        if (m < 4) {
            cnt_wait(CNT(C_WKV), 4);
            const unsigned a0 = L + L_WKV + (8 * g + (c16 >> 2)) * 40 + (m & 1) * 32 + (lane & 3) * 8;
            const unsigned a1 = a0 + kh * 1280, a2 = a0 + (kh ^ 1) * 1280;
#pragma unroll
            for (int j = 0; j < 10; ++j) { kf[j] = ldstr2(a1 + j * 2560, a1 + j * 2560 + 160); kf[10 + j] = ldstr2(a2 + j * 2560, a2 + j * 2560 + 160); }
            if (m < 2) {
#pragma unroll
                for (int j = 0; j < 20; ++j) kc = mfma16(kf[j], ones, kc);
            } else {
#pragma unroll
                for (int j = 0; j < 20; ++j) kc = mfma16(ones, kf[j], kc);
            }
            PIN(kc);
        }
        cnt_add(CNT(C_KFUSED));
        cnt_spin(CNT(C_X), 4);
#pragma unroll
        for (int j = 0; j < 10; ++j) xf[j] = lds16(xa + j * 1024);
#pragma unroll
        for (int j = 0; j < 10; ++j) xf[10 + j] = lds16(xb + j * 1024);
#pragma unroll
        for (int j = 0; j < 10; ++j) xo[j] = lds16(xc + j * 1024);
        xr0 = lds16(xra[0]); xr1 = lds16(xra[1]); xr2 = lds8(xra[2]);
        SBAR();
        if (m < 2) {
#pragma unroll
            for (int j = 0; j < 20; ++j) ka = mfma16(kf[j], xf[j], ka);
        } else if (m < 4) {
#pragma unroll
            for (int j = 0; j < 20; ++j) ka = mfma16(xf[j], kf[j], ka);
        }
        PIN(ka);
#pragma unroll
        for (int j = 0; j < 20; ++j) {
            mma16(ssm, one, xf[j]); mma16(ssq, xf[j], xf[j]);
            if (j == 4) {
#pragma unroll
                for (int i = 0; i < 10; ++i) { PIN(xf[i]); PIN(xf[10 + i]); PIN(xo[i]); }
                PIN(xr0); PIN(xr1); PIN(xr2);
                cnt_add(CNT(C_XUSED));
            }
        }
        mma_done();
        STAMP(1);
        const float sd = (c16 & 2) ? ((c16 & 1) ? ssq[3] : ssq[2]) : ((c16 & 1) ? ssq[1] : ssq[0]);
        const float mean = ssm[0] * (1.f / D), rstd = __builtin_amdgcn_rsqf(fmaxf(__shfl(sd, (c16 >> 2) * 16 + c16) * (1.f / D) - mean * mean, 0.f) + LN_EPS);
        if (m < 4) {
            u32x2 pv;
            if (m < 2) {
                pv = u32x2{pk2((ka[0] - mean * kc[0]) * rstd, (ka[1] - mean * kc[1]) * rstd), pk2((ka[2] - mean * kc[2]) * rstd, (ka[3] - mean * kc[3]) * rstd)};
            } else {
                float v[4];
#pragma unroll
                for (int r = 0; r < 4; ++r) v[r] = (ka[r] - __shfl(mean, 4 * g + r) * kc[0]) * __shfl(rstd, 4 * g + r);
                pv = u32x2{pk2(v[0], v[1]), pk2(v[2], v[3])};
                if (m == 3 && c16 >= 4) pv = c16 == 4 ? u32x2{ONES2, ONES2} : u32x2{0u, 0u};
            }
            bst8(rs, pv, kvo, WS_KV + (tb * 2 + (w >> 1)) * KV_TILE);
        }
        SBAR();
        STAMP(2);
        u32x2 f[2][5][2];
        unsigned xs, xv;
        float sc = rstd * QSCALE, ms = -mean * sc;
        PIN(sc); PIN(ms);
        unsigned b2 = b0 + (L_WQ2 - L_WQ1), b3 = b1 + (L_WQ2 - L_WQ1), cq = CNT(0), cv = 0;
        PIN(b2); PIN(b3); PIN(cq);
        auto qhalf = [&](int j, int s, int e) { return ldstr((j < 4 ? (e ? b1 : b0) + j * 10240 : (e ? b3 : b2) + j % 6 * 10240) + s * 32); };
        auto kvflag = [&] {
            if (m < 4) {
                wait_vm<0>();
                if (cnt_arrive(CNT(C_KVST)) == 3 && lane == 0) set_flag(ws, FL_KV + (tb * 4 + m) * 128, tag);
            }
        };
        if (FLAGAT < 0) kvflag();
        cnt_spin(CNT(C_WQ), 4);
#pragma unroll
        for (int s = 0; s < 5; ++s) { f[0][s][0] = qhalf(0, s, 0); f[0][s][1] = qhalf(0, s, 1); }
        SBAR();
#pragma unroll
        for (int j = 0; j < 10; ++j) {
            if (j < 8 && !(j & 1)) cv = *(volatile LAS unsigned*)(size_t)(cq + (C_WQ + j / 2 + 1) * 4);
            if (j < 9 && (j & 1) && __builtin_expect((unsigned)__builtin_amdgcn_readfirstlane(cv) < 4u, 0)) cnt_spin(CNT(C_WQ + j / 2 + 1), 4);
            asm volatile("" ::: "memory");
            SBAR();
#pragma unroll
            for (int s = 0; s < 5; ++s) {
                const u32x4 fr = {f[j & 1][s][0][0], f[j & 1][s][0][1], f[j & 1][s][1][0], f[j & 1][s][1][1]};
                if (j == 0) mma16z(acc[0][s], fr, xf[j]); else mma16(acc[0][s], fr, xf[j]);
                SBAR();
                if (j < 9) f[(j + 1) & 1][s][0] = qhalf(j + 1, s, 0);
                SBAR();
                if (j == 0) mma16z(acc[1][s], fr, xo[j]); else mma16(acc[1][s], fr, xo[j]);
                SBAR();
                if (j < 9) f[(j + 1) & 1][s][1] = qhalf(j + 1, s, 1);
                SBAR();
            }
            if (j == FLAGAT) {   }
            if (j == FLAGAT) kvflag();
            if (j == 3) asm volatile("s_mov_b64 exec, 1\n\tds_add_u32 %0, %1 offset:%2\n\ts_mov_b64 exec, -1" ::"v"(cq), "v"(1u), "i"(C_R1 * 4) : "memory");
            if (j == FLAGAT) {   }
            if (j == 5) { xs = L + L_WQ1 + (w ^ 1) * 5120 + lane * 16; xv = L + L_WQ1 + w * 5120 + lane * 16; PIN(xs); PIN(xv); }
            if (j == 7) cnt_spin(CNT(C_R1), 4);
        }
        STAMP(3);
        if (m >= 4) wait_vm<0>();
        asm volatile("" ::"v"(pf[0]), "v"(pf[1]), "v"(pf[2]), "v"(pf[3]), "v"(pf[4]));
        mma_done();
        send_half(acc[1], xs);
        cnt_add(CNT(C_XCH + (w >> 1)));
        if (w == 0 && lane < M_NUM) *(LAS unsigned*)(size_t)CN2(lane) = 0u;
        f32x4 c5[5], p5[5];
#pragma unroll 1
        for (;;) {
            const unsigned n1 = *(volatile LAS unsigned*)(size_t)CNT(C_CS), n2 = *(volatile LAS unsigned*)(size_t)CNT(C_XCH + (w >> 1));
#pragma unroll
            for (int s = 0; s < 5; ++s) {
                c5[s] = *(volatile LAS f32x4*)(size_t)(L + L_CSX + 1280 + s * 64 + g * 16);
                p5[s] = *(volatile LAS f32x4*)(size_t)(xv + s * 1024);
            }
            if (__builtin_amdgcn_readfirstlane(n1 + n2) == 3) break;
        }
#pragma unroll
        for (int s = 0; s < 5; ++s) {
            const f32x4 v = (acc[0][s] + p5[s]) * sc + c5[s] * ms;
            *(LAS u32x2*)(size_t)qw[s] = u32x2{pk2(v[0], v[1]), pk2(v[2], v[3])};
        }
        bar();
        const int r32 = lane & 31, h = lane >> 5;
        const unsigned qb = L + L_QX + ((w & 1) * 4 + (w >> 1)) * 1280;
        qa1 = lds16(qb + lane * 16); qb1 = lds16(qb + 2560 + lane * 16);
        qa2 = u32x2{lds4(qb + 1024 + r32 * 8 + h * 4), 0u}; qb2 = u32x2{lds4(qb + 2560 + 1024 + r32 * 8 + h * 4), 0u};
    }
    STAMP(4);
    {
        LANE_VARS();
        const int r32 = lane & 31, h = lane >> 5, vrow = min(r32, 21);
        const unsigned kconst = h == 0 ? 0x00003f80u : 0u;
        unsigned ak1 = L + lane * 16, ak2 = L + 1024 + r32 * 8 + h * 4, av = L + 1280 + (h * 22 + vrow) * 16;
        const f32x16 zero16 = {0, 0, 0, 0, 0, 0, 0, 0, 0, 0, 0, 0, 0, 0, 0, 0};
        f32x16 sa, sb, oa = zero16, ob = zero16;
        u32x4 ka1 = lds16(ak1), kb1 = lds16(ak1 + KV_TILE), va0 = lds16(av), va1 = lds16(av + VS), vb0 = va0, vb1 = va0;
        u32x2 ka2 = {lds4(ak2), kconst}, kb2 = {lds4(ak2 + KV_TILE), kconst};
        sa = mfma32(ka1, qa1, mfma32k8(ka2, qa2, zero16));
        sb = mfma32(ka1, qb1, mfma32k8(ka2, qb2, zero16));
        {
            const float ma = halfmax(max16(sa)), mb = halfmax(max16(sb));
            qa2[1] = h == 0 ? (pk2(-ma, 0.f) & 0xffffu) : 0u;
            qb2[1] = h == 0 ? (pk2(-mb, 0.f) & 0xffffu) : 0u;
        }
        sa = mfma32(ka1, qa1, mfma32k8(ka2, qa2, zero16));
        u32x4 pa0 = {0u, 0u, 0u, 0u}, pa1 = pa0, pb0 = pa0, pb1 = pa0;
        float e2 = 0.f, e3 = 0.f;
        unsigned vo0 = (r32 >> 4) * 20480 + h * 256 + (r32 & 15) * 16, vo1 = h ? 0x80000000u : vo0;
        PIN(pa0); PIN(pa1); PIN(pb0); PIN(pb1); PIN(ka2); PIN(kb2); PIN(vo0); PIN(vo1);
#define SLOT(R, MF, X, S, i, PP, jp, P, j) do { R = MF; PIN(R); SBAR(); X; SBAR(); \
        float e0 = EX2(S[i]), e1 = EX2(S[i + 1]); PIN(e0); PIN(e1); SBAR(); \
        PP[jp] = pk2(e2, e3); PIN(PP); SBAR(); \
        e2 = EX2(S[i + 2]); e3 = EX2(S[i + 3]); PIN(e2); PIN(e3); SBAR(); \
        P[j] = pk2(e0, e1); PIN(P); SBAR(); } while (0)
#define STEP(KC1, KC2, KN1, KN2, VC0, VC1, VP0, VP1, OK, OV, X0, X7) do { \
        SLOT(sb, mfma32k8(KC2, qb2, zero16), X0, sa, 0, pb1, 3, pa0, 0); \
        SLOT(sb, mfma32(KC1, qb1, sb), __builtin_amdgcn_s_waitcnt(0xC07F), sa, 4, pa0, 1, pa0, 2); \
        SLOT(ob, mfma32(VP1, pb1, ob), KC1 = lds16(ak1 + (OK) * KV_TILE), sa, 8, pa0, 3, pa1, 0); \
        SLOT(oa, mfma32(VC0, pa0, oa), KC2[0] = lds4(ak2 + (OK) * KV_TILE), sa, 12, pa1, 1, pa1, 2); \
        SLOT(sa, mfma32k8(KN2, qa2, zero16), (void)0, sb, 0, pa1, 3, pb0, 0); \
        SLOT(sa, mfma32(KN1, qa1, sa), VP0 = lds16(av + (OV) * KV_TILE), sb, 4, pb0, 1, pb0, 2); \
        SLOT(oa, mfma32(VC1, pa1, oa), VP1 = lds16(av + (OV) * KV_TILE + VS), sb, 8, pb0, 3, pb1, 0); \
        SLOT(ob, mfma32(VC0, pb0, ob), X7, sb, 12, pb1, 1, pb1, 2); } while (0)
        unsigned so1 = WS_O + (tb * 4 + (w & 2)) * 20480, so0 = so1 + (2 * m + (w & 1)) * 1024;
        so1 += 16384 + (2 * m + (w & 1)) * 256;
        asm volatile("" : "+s"(so0), "+s"(so1));
        unsigned ea = CN2(E_C1), ev = 0, one = 1;
        PIN(ea); PIN(one); PIN(ak1); PIN(ak2); PIN(av);
        auto hook0 = [&](int j, int dn) {
            if (j == 14) { ak1 += dn; ak2 += dn; PIN(ak1); PIN(ak2); }
            if (j == 15) { av += dn; ea += 4; PIN(av); PIN(ea); }
        };
        auto hook7 = [&](int j) {
            if (j == 0)
                asm volatile("s_mov_b64 exec, 1\n\tds_add_u32 %0, %1 offset:%2\n\ts_mov_b64 exec, -1" ::"v"(ea), "v"(one), "i"((P_NONE - E_C1) * 4) : "memory");
            if (j == 1 && __builtin_expect((unsigned)__builtin_amdgcn_readfirstlane(ev) < 4u, 0)) cnt_spin(CN2(E_C0), 4);
            if (j == 9) ev = *(volatile LAS unsigned*)(size_t)ea;
            if (j == 10 && __builtin_expect((unsigned)__builtin_amdgcn_readfirstlane(ev) < 4u, 0))
                { cnt_wait(ea, 4); ev = 4; }
        };
        STAMP(5);
#pragma unroll 1
        for (int c = 0; c < 4; ++c) {
            const int dn = c == 2 ? -2 * CHUNK : CHUNK;
#pragma unroll
            for (int j = 0; j < 16; j += 2) {
                STEP(ka1, ka2, kb1, kb2, va0, va1, vb0, vb1, (j + 2) & 15, j + 1, hook0(j, dn), hook7(j));
                STEP(kb1, kb2, ka1, ka2, vb0, vb1, va0, va1, (j + 3) & 15, (j + 2) & 15, hook0(j + 1, dn), hook7(j + 1));
            }
        }
        pb1[3] = pk2(e2, e3);
        ob = mfma32(vb1, pb1, ob);
        STAMP(6);
        u32x4 r0, r1, r2;
        auto finish = [&](const f32x16& pa, const f32x16& pb, float la, float lb) {
            const float ra = __builtin_amdgcn_rcpf(la), rb = __builtin_amdgcn_rcpf(lb);
            r0 = u32x4{keep(pk2(pa[0] * ra, pa[1] * ra)), pk2(pa[2] * ra, pa[3] * ra), keep(pk2(pa[4] * ra, pa[5] * ra)), pk2(pa[6] * ra, pa[7] * ra)};
            r1 = u32x4{keep(pk2(pb[0] * rb, pb[1] * rb)), pk2(pb[2] * rb, pb[3] * rb), keep(pk2(pb[4] * rb, pb[5] * rb)), pk2(pb[6] * rb, pb[7] * rb)};
            r2 = u32x4{keep(pk2(pa[8] * ra, pa[9] * ra)), pk2(pa[10] * ra, pa[11] * ra), keep(pk2(pb[8] * rb, pb[9] * rb)), pk2(pb[10] * rb, pb[11] * rb)};
        };
        {
            float am = 0.f;
#pragma unroll
            for (int i = 0; i < 12; i += 2) am = amax3(am, oa[i], oa[i + 1]);
            const float la = upper(oa[8]);
            asm volatile("s_nop 7" : "+v"(ob));
            const float lb = upper(ob[8]);
#pragma unroll
            for (int i = 0; i < 12; i += 2) am = amax3(am, ob[i], ob[i + 1]);
            finish(oa, ob, la, lb);
            const bool ok = (fminf(la, lb) >= 1e-18f) & (fmaxf(la, lb) <= 1e18f) & (am <= 1e37f);
            if (__builtin_expect(__ballot(!ok) != 0, 0)) {
                __builtin_amdgcn_fence(__ATOMIC_ACQUIRE, "agent");
                f32x16 xa, xb;
#pragma unroll 1
                for (int qt = 0; qt < 2; ++qt) {
                    xb = attn_exact(ws + WS_KV, qt ? qb1 : qa1, qt ? qb2[0] : qa2[0], lane, vrow);
                    if (qt == 0) xa = xb;
                }
                finish(xa, xb, upper(xa[8]), upper(xb[8]));
            }
        }
        bst16(rs, r0, vo0, so0);
        bst16(rs, r1, vo0 + 512, so0);
        bst16(rs, r2, vo1, so1);
        STAMP(7);
    }
    u32x4 wd[12], ae[RE][2], we[PE];
    unsigned ge[9], e0, e1, ehv, eprobe, a0;
    const unsigned ehs = WS_F + (tb * 4 + (w & 2)) * 55296 + 16384;
    {
        LANE_VARS();
        const unsigned wl = L + lane * 16;
        f32x4 acc[2][5];
        u32x4 act[RC][2], wf[10];
        unsigned wa = wl + L_WO + kh * 5120, wb = wa + 51200, gb[5];
        rot_bases(gb, WS_O + tb * 81920 + kh * 1024, rotc, 4096);
        unsigned v0 = lane * 16 + w * 20480, v1 = lane * 16 + (w ^ 1) * 20480, hv = c16 * 256 + (g & 1) * 20480;
        unsigned xs = wl + (w == 2 ? 35840 : XCH + (w ^ 1) * 5120), xv = wl + (w == 3 ? 35840 : XCH + w * 5120);
        unsigned sv = g == 0 ? c16 * 8 : 0x80000000u, h2v = (g & 1) ? 0x80000000u : (g >> 1) * 256 + c16 * 16;
        PIN(v0); PIN(v1); PIN(hv);
        kstart<RC, 2, 2048>(act, rs, ws, v0, v1, gb,
                           [&] { return __builtin_amdgcn_raw_buffer_load_b32(rs, hv, WS_O + (tb * 4 + (w & 2)) * 20480 + 16384, SC1) == EMPTY; }, [&] {
            bar();
            PIN(wa); PIN(wb); PIN(xs); PIN(xv); PIN(sv); PIN(h2v);
#pragma unroll
            for (int n = 0; n < 10; ++n) { wf[n] = lds16(wa + (n / 5) * 10240 + (n % 5) * 1024); SBAR(); }
            res_init(acc[0], xr0, xr1, xr2); zero_acc(acc[1]);
            if (DRC < 0) wait_vm<0>(); else __builtin_amdgcn_s_sleep(DRC < 0 ? 0 : DRC);
        }, [&] {
        });
        STAMP(8);
        kloop<0, 10, 10, 0, 50, 5, 5, RC, 2, 2048>(acc, act, wf, [&](int n) { return (n < 25 ? wa : wb) + (n / 5 % 5) * 10240 + (n % 5) * 1024; }, rs, ws, v0, v1, gb);
        mma_done();
        send_half(acc[1], xs);
        bar();
        recv_half(acc[0], xv);
#pragma unroll
        for (int n = 0; n < 12; ++n) wd[n] = lds16(wl + (n % 7 < 5 ? 16384 + (n / 7) * 5120 + (n % 7) * 1024 : (n / 7) * 2048 + (n % 7 - 5) * 1024));
        SBAR();
        res_pack(xr0, xr1, xr2, acc[0]);
        xr0[0] = keep(xr0[0]); xr0[2] = keep(xr0[2]); xr1[0] = keep(xr1[0]); xr1[2] = keep(xr1[2]); xr2[0] = keep(xr2[0]);
        const unsigned hb = WS_H + (tb * 4 + w) * 20480;
        bst16(rs, xr0, lane * 16, hb + 2 * m * 1024);
        bst16(rs, xr1, lane * 16, hb + (2 * m + 1) * 1024);
        bst16(rs, rowpair(xr2, xr2), h2v, hb + 16384 + m * 512);
#pragma unroll
        for (int n = 0; n < 12; ++n) PIN(wd[n]);
        cnt_add(CN2(M_LDS));
        float sm = 0.f, sq = 0.f, sm2 = 0.f, sq2 = 0.f;
#pragma unroll
        for (int i = 0; i < 4; ++i) { sm = dot2(xr0[i], ONES2, sm); sq = dot2(xr0[i], xr0[i], sq); sm2 = dot2(xr1[i], ONES2, sm2); sq2 = dot2(xr1[i], xr1[i], sq2); }
        sm = dot2(xr2[0], ONES2, sm); sq = dot2(xr2[0], xr2[0], sq); sm2 = dot2(xr2[1], ONES2, sm2); sq2 = dot2(xr2[1], xr2[1], sq2);
        sm = xsum16_32(sm + sm2); sq = xsum16_32(sq + sq2);
        bst8(rs, u32x2{keep(__float_as_uint(sm)), __float_as_uint(sq)}, sv, WS_ST + (tb * 4 + w) * 1024 + m * 128);
        SBAR();
        STAMP(9);
    }
    {
        f32x4 acc[2][7], c4[7];
        u32x4 act[12][2];
        u32x2 st[4];
        float mu[2], rp[2];
        const int t16 = (tb >> 1) * 8 + 2 * w;
        unsigned v0 = lane_id() * 16, v1 = v0 + 20480, sv = v0 >> 1, wa = L + v0, wb = wa + 16384 + 46080, gb[5];
        PIN(sv); PIN(wa); PIN(wb); PIN(v0); PIN(v1);
        zero_acc(acc[0]); zero_acc(acc[1]);
        rot_bases(gb, WS_H + t16 * 20480, rotd, 4096);
        e_bases(ge, WS_F + tb * 221184 + kh * 1024, m, 6144);
        unsigned fs[7], wc, fw, f2v, ca;
        {
            const unsigned fb = WS_F + t16 * 55296 + ct * 1024;
            fs[0] = fb; fs[1] = fb + 55296; fs[2] = fb + 16384 - ct * 512; fs[3] = fb + 24576; fs[4] = fb + 24576 + 55296; fs[5] = fb + 39936; fs[6] = fb + 39936 + 55296;
#pragma unroll
            for (int i = 0; i < 7; ++i) asm volatile("" : "+s"(fs[i]));
            const int lane = v0 >> 4, g = lane >> 4, c16 = lane & 15;
            f2v = (g & 1) * 55296 + (g >> 1) * 256 + c16 * 16; ca = L + L_MISC + g * 16;
            ehv = (g & 1) * 55296 + c16 * 512;
            wc = wa + 118784;
            PIN(f2v); PIN(ca); PIN(ehv); PIN(wc);
        }
        kstart<RD, 4, 1024>(act, rs, ws, v0, v1, gb, [&] {
#pragma unroll
            for (int i = 0; i < 4; ++i) st[i] = __builtin_amdgcn_raw_buffer_load_b64(rs, sv + i * 512, WS_ST + t16 * 1024, SC1);
            return max(max(st[0][0], st[1][0]), max(st[2][0], st[3][0])) == EMPTY; }, [&] { if (DRD < 0) wait_vm<0>(); else __builtin_amdgcn_s_sleep(DRD < 0 ? 0 : DRD); }, [&] {
#pragma unroll
            for (int t = 0; t < 2; ++t) {
                const float sm = xsum16_32(__uint_as_float(st[2 * t][0]) + __uint_as_float(st[2 * t + 1][0]));
                const float sq = xsum16_32(__uint_as_float(st[2 * t][1]) + __uint_as_float(st[2 * t + 1][1]));
                mu[t] = sm * (-1.f / D);
                rp[t] = __builtin_amdgcn_rsqf(fmaxf(sq * (1.f / D) - mu[t] * mu[t], 0.f) + LN_EPS) * LOG2E;
                PIN(mu[t]); PIN(rp[t]);
            } });
        STAMP(10);
        auto wad = [&](int n) {
            const int j = n < 56 ? n / 7 : n < 86 ? 8 + (n - 56) / 5 : n < 98 ? 14 + (n - 86) / 2 : 14 + (n - 98) / 3;
            const int s = n < 56 ? n % 7 : n < 86 ? (n - 56) % 5 : n < 98 ? (n - 86) % 2 : 2 + (n - 98) % 3;
            return n >= 116 ? wc + (n - 116) * 1024 : s >= 5 ? wa + j * 2048 + (s - 5) * 1024 : (j < 9 ? wa + 16384 + j * 5120 : wb + (j - 9) * 5120) + s * 1024; };
        kloop<0, 1, 20, 0, 140, 7, 7, RD, 4, 1024>(acc, act, wd, wad, rs, ws, v0, v1, gb);
        bar_raw();
        kloop<1, 7, 20, 7, 140, 7, 5, RD, 4, 1024>(acc, act, wd, wad, rs, ws, v0, v1, gb);
        kloop<8, 1, 20, 56, 140, 5, 5, RD, 4, 1024>(acc, act, wd, wad, rs, ws, v0, v1, gb);
        asm volatile("s_mov_b64 exec, 1\n\tds_add_u32 %0, %1\n\ts_mov_b64 exec, -1" ::"v"(CN2(M_K8)), "v"(1u) : "memory");
        kloop<9, 5, 20, 61, 140, 5, 2, RD, 4, 1024>(acc, act, wd, wad, rs, ws, v0, v1, gb);
        c4[0] = ldsf4(ca); c4[1] = ldsf4(ca + 64);
        SBAR();
        kloop<14, 6, 20, 86, 140, 2, 4, RD, 4, 1024>(acc, act, wd, wad, rs, ws, v0, v1, gb);
        STAMP(11);
        u32x2 lo, hi;
        auto silu = [&](const f32x4& a, const f32x4& c, int t, u32x2& r, auto side) {
            f32x4 y = (a + mu[t] * c) * rp[t], e;
            PIN(y); SBAR();
#pragma unroll
            for (int i = 0; i < 4; ++i) { side(i); SBAR(); e[i] = EX2(-y[i]); PIN(e); SBAR(); }
            e = e * LOG2E + LOG2E; PIN(e); SBAR();
#pragma unroll
            for (int i = 0; i < 4; ++i) { side(4 + i); SBAR(); e[i] = __builtin_amdgcn_rcpf(e[i]); PIN(e); SBAR(); }
            e *= y;
            r = u32x2{keep(pk2(e[0], e[1])), pk2(e[2], e[3])};
            PIN(r); SBAR();
        };
        auto rest = [&](int q) {
            const int n = 98 + (q >> 1), j = n < 116 ? 14 + (n - 98) / 3 : 8 + (n - 116) / 2, s = n < 116 ? 2 + (n - 98) % 3 : 5 + (n - 116) % 2;
            mma16(acc[q & 1][s], wd[n % 12], act[j % 12][q & 1]);
            SBAR();
            if ((q & 1) && n + 12 < 140) wd[n % 12] = lds16(wad(n + 12));
            if (q >= 24 && q < 74 && q % 10 == 4) c4[q == 24 ? 6 : q / 10 - 1] = ldsf4(ca + (q == 24 ? 6 : q / 10 - 1) * 64);
        };
        auto early = [&](int q) {
            if (q == 0) eprobe = __builtin_amdgcn_raw_buffer_load_b32(rs, ehv, ehs, SC1);
            ae[q >> 1][q & 1] = aload<3, 2048>(rs, (q & 1) ? e1 : e0, ge, q >> 1);
        };
#pragma unroll
        for (int q = 0; q < 8; ++q) { rest(q); SBAR(); }
#pragma unroll
        for (int b = 0; b < 10; ++b) {
            const int ut = b < 4 ? b >> 1 : b < 6 ? b & 1 : (b - 6) >> 1, ui = b < 4 ? b & 1 : b < 6 ? 2 : 3 + (b & 1), us = ui < 2 ? ui : ui == 2 ? 6 : ui - 1;
            silu(acc[ut][ui], c4[us], ut, (b & 1) ? hi : lo, [&](int i) { if (8 + b * 8 + i < 84) rest(8 + b * 8 + i); });
            if (b == 5) bst16(rs, rowpair(lo, hi), f2v, fs[2]);
            else if (b & 1) bst16(rs, u32x4{lo[0], lo[1], hi[0], hi[1]}, b < 5 ? v0 : fw, fs[b >> 1]);
            if (b == 1) cnt_add(CN2(M_T4));
            if (b == 1) {   } if (b == 3) {   } if (b == 5) {   } if (b == 7) {   } if (b == 9) {   }
            if (b == 5) {
                PIN(v0);
                fw = ct < 15 ? v0 : 0x80000000u; e0 = v0 + w * 55296; e1 = v0 + (w ^ 1) * 55296; a0 = L + v0 + kh * 5120;
                PIN(fw); PIN(e0); PIN(e1); PIN(a0);
            }
            SBAR();
        }
        bar_raw();
        mma_done();
#pragma unroll
        for (int n = 0; n < PE; ++n) we[n] = lds16(a0 + (n / 5) * 10240 + (n % 5) * 1024);
#pragma unroll
        for (int t = 0; t < 2; ++t) {
            u32x4 r;
#pragma unroll
            for (int u = 0; u < 2; ++u) {
                const f32x4 y = (acc[t][5 + u] + mu[t] * c4[4 + u]) * rp[t];
                f32x4 e = f32x4{EX2(-y[0]), EX2(-y[1]), EX2(-y[2]), EX2(-y[3])} * LOG2E + LOG2E;
                e = f32x4{__builtin_amdgcn_rcpf(e[0]), __builtin_amdgcn_rcpf(e[1]), __builtin_amdgcn_rcpf(e[2]), __builtin_amdgcn_rcpf(e[3])} * y;
                r[2 * u] = keep(pk2(e[0], e[1])); r[2 * u + 1] = pk2(e[2], e[3]);
            }
            bst16(rs, r, fw, fs[5 + t]);
        }
        SBAR();
#pragma unroll
        for (int q = 0; q < 2 * FIRSTFILL; ++q) { early(q); SBAR(); }
        STAMP(12);
    }
    {
        f32x4 acc[2][5];
        unsigned a1 = a0 + 61440, a2 = a0 + 122880;
        PIN(a1); PIN(a2);
        res_init(acc[0], xr0, xr1, xr2); zero_acc(acc[1]);
        if (__any(eprobe == EMPTY))
            kstart<RE, 3, 2048>(ae, rs, ws, e0, e1, ge, [&] { return __builtin_amdgcn_raw_buffer_load_b32(rs, ehv, ehs, SC1) == EMPTY; }, [] {}, [] {});
        if (__builtin_expect(missing(ae[0][0], ae[0][1]), 0)) refetch(ae[0][0], ae[0][1], ws + ge[0], e0, e1);
        SBAR();
        STAMP(13);
        auto wae = [&](int n) { const int q = n / 5 % 15; return (q < 6 ? a0 : q < 12 ? a1 : a2) + (q % 6) * 10240 + (n % 5) * 1024; };
#define GRP(G, WA, WN) kloop<3 * G, 3, 27, 15 * G, 135, 5, 5, RE, 3, 2048>(acc, ae, we, wae, rs, ws, e0, e1, ge)
        GRP(0, a0, a0 + 30720);
        bar_raw();

        GRP(1, a0 + 30720, a1);
        bar_raw();
        GRP(2, a1, a1 + 30720);
        bar_raw();
        GRP(3, a1 + 30720, a2); bar_raw();
        GRP(4, a2, a0); bar_raw();

        GRP(5, a0, a0 + 30720); bar_raw();
        GRP(6, a0 + 30720, a1); bar_raw();
        GRP(7, a1, a1 + 30720);
        GRP(8, a1 + 30720, a1);
        mma_done();
        LANE_VARS();
        const unsigned wl = L + lane * 16;
        send_half(acc[1], wl + 122880 + (w ^ 1) * 5120);
        bar();
        recv_half(acc[0], wl + 122880 + w * 5120);
        STAMP(14);
        res_pack(xr0, xr1, xr2, acc[0]);
        char* ob = out + (size_t)(tb * 64 + w * 16 + c16) * 1280 + m * 160;
        asm volatile("global_store_dwordx4 %0, %1, off nt\n\tglobal_store_dwordx4 %0, %2, off offset:64 nt\n\t"
                     "global_store_dwordx2 %3, %4, off offset:128 nt\n\ts_nop 1" ::"v"(ob + g * 16), "v"(xr0), "v"(xr1), "v"(ob + g * 8), "v"(xr2) : "memory");
    }
    asm volatile(".Lpf_end1:");
    STAMP_OUT(0);
    __builtin_amdgcn_endpgm();
}

extern "C" void kernel_launch(void* const* d_in, const int* in_sizes, int n_in, void* d_out, int out_size, void* d_ws, size_t ws_size,
                              hipStream_t stream) {
    (void)hipFuncSetAttribute((const void*)k_block, hipFuncAttributeMaxDynamicSharedMemorySize, LDS_BYTES);
    k_block<<<256, 512, LDS_BYTES, stream>>>((const char*)d_in[0], (const char*)d_in[1], (const char*)d_in[2], (const char*)d_in[3],
                                             (const char*)d_in[4], (const char*)d_in[6], (const char*)d_in[7], (char*)d_ws, (char*)d_out);
}
```

```cpp
#include <hip/hip_runtime.h>
#include <hip/hip_bf16.h>
#include <stdint.h>

typedef __attribute__((ext_vector_type(8))) __bf16 bf16x8;
typedef __attribute__((ext_vector_type(2))) __bf16 bf16x2;
typedef __attribute__((ext_vector_type(4))) short s16x4;
typedef __attribute__((ext_vector_type(4))) float f32x4;
typedef __attribute__((ext_vector_type(2))) float f32x2;
typedef __attribute__((ext_vector_type(16))) float f32x16;
typedef __attribute__((ext_vector_type(4))) unsigned u32x4;
typedef __attribute__((ext_vector_type(2))) unsigned u32x2;
typedef unsigned long long u64;
#define LAS __attribute__((address_space(3)))
#define GAS __attribute__((address_space(1)))
#define DI __device__ __forceinline__

constexpr int D = 640;
constexpr float LN_EPS = 1e-5f;
constexpr float LOG2E = 1.4426950408889634f;
constexpr float QSCALE = 0.22360679774997896f * LOG2E;

constexpr int KV_TILE = 2688;
constexpr int VS = 704;
constexpr unsigned WS_FL = 0;
constexpr unsigned FL_KV = 32768, FL_PR = 2 * 32768, FL_MK = 3 * 32768;
constexpr unsigned WS_KV = 4 * 32768;
constexpr unsigned WS_WO = WS_KV + 64 * KV_TILE;
constexpr unsigned WS_W1 = WS_WO + 8 * 20 * 5 * 1024;
constexpr unsigned WS_W2 = WS_W1 + 16 * 20 * 7 * 1024;
constexpr unsigned WS_CS = WS_W2 + 8 * 54 * 5 * 1024;
constexpr unsigned WS_O  = WS_CS + 20 * 1792 * 4;
constexpr unsigned WS_H  = WS_O + 128 * 20 * 1024;
constexpr unsigned WS_F  = WS_H + 128 * 20 * 1024;
constexpr unsigned WS_ST = WS_F + 128 * 54 * 1024;
constexpr unsigned WS_END = WS_ST + 2048 * 64;
constexpr unsigned EMPTY = 0xffffffffu;
static_assert((WS_END - WS_O) == 256 * 48640, "fill share per workgroup");

constexpr int CHUNK = 16 * KV_TILE;
constexpr int L_X = 0, L_WQ2 = 43008, L_WKV = 83968, L_WQ1 = 122880,
               L_CSX = 111104, L_QX = 152576, L_MISC = 162816;
constexpr int L_AUX = 3 * CHUNK, L_WO = CHUNK, L_CNT = 110592, XCH = L_WO + 102400, LDS_BYTES = 163840;

#define ENCK 0
#define STAMP_INIT() const u64 rt0_ = __builtin_amdgcn_s_memrealtime(); u64 rtk_ = rt0_
#define STAMP(I) do { if ((I) == ENCK) { __builtin_amdgcn_sched_barrier(0); asm volatile("s_memrealtime %0\n\ts_waitcnt lgkmcnt(0)" : "=s"(rtk_) :: "memory"); __builtin_amdgcn_sched_barrier(0); } } while (0)
#define STAMP_OUT(K) do { if ((K) == 0) { while (__builtin_amdgcn_s_memrealtime() - rt0_ < 6000 + (rtk_ - rt0_)) __builtin_amdgcn_s_sleep(2); } } while (0)

#define THROT 3
#define THROT2 3
#define RC 6
#define RD 7
#define RE 7
#define TAILPRIO 0
#define FIRSTFILL 4
#define ONEWAY 1
#define B0AT 0
#define NEARLY 12
#define PFDELAY 6
#define WKVAT 2
#define FLAGAT 0
#define P1PRIO 1
#define DRC -1
#define DRD 4
#define PE 10
#define NOST 0
DI void glds(unsigned voff, const void* sbase, unsigned lds) {
    asm volatile("s_mov_b32 m0, %2\n\ts_nop 0\n\tglobal_load_lds_dwordx4 %0, %1" ::"v"(voff), "s"(sbase), "s"(lds) : "memory");
}
DI void glds_dev(unsigned voff, const void* sbase, unsigned lds) {
    asm volatile("s_mov_b32 m0, %2\n\ts_nop 0\n\tglobal_load_lds_dwordx4 %0, %1 sc1" ::"v"(voff), "s"(sbase), "s"(lds) : "memory");
}
template <int N> DI void wait_vm() { asm volatile("s_waitcnt vmcnt(%0)" ::"i"(N) : "memory"); }
DI void bar() { asm volatile("s_waitcnt lgkmcnt(0)\n\ts_barrier" ::: "memory"); }
DI void bar_raw() { asm volatile("s_barrier" ::: "memory"); }
#define PIN(x) asm volatile("" : "+v"(x))
#define SBAR() __builtin_amdgcn_sched_barrier(0)
DI unsigned pk2(float a, float b) { bf16x2 v = {(__bf16)a, (__bf16)b}; return __builtin_bit_cast(unsigned, v); }
DI float bflo(unsigned u) { return __uint_as_float(u << 16); }
DI float bfhi(unsigned u) { return __uint_as_float(u & 0xffff0000u); }
DI f32x4 mfma16(u32x4 a, u32x4 b, f32x4 c) {
    return __builtin_amdgcn_mfma_f32_16x16x32_bf16(__builtin_bit_cast(bf16x8, a), __builtin_bit_cast(bf16x8, b), c, 0, 0, 0);
}
DI f32x16 mfma32(u32x4 a, u32x4 b, f32x16 c) {
    return __builtin_amdgcn_mfma_f32_32x32x16_bf16(__builtin_bit_cast(bf16x8, a), __builtin_bit_cast(bf16x8, b), c, 0, 0, 0);
}
DI f32x16 mfma32k8(u32x2 a, u32x2 b, f32x16 c) {
    return __builtin_amdgcn_mfma_f32_32x32x8bf16_1k(__builtin_bit_cast(s16x4, a), __builtin_bit_cast(s16x4, b), c, 0, 0, 0);
}
DI u32x4 lds16(unsigned a) { return *(const LAS u32x4*)(size_t)a; }
DI u32x2 lds8(unsigned a) { return *(const LAS u32x2*)(size_t)a; }
DI unsigned lds4(unsigned a) { return *(const LAS unsigned*)(size_t)a; }
DI f32x4 ldsf4(unsigned a) { return *(const LAS f32x4*)(size_t)a; }
DI u32x2 ldstr(unsigned a) { return __builtin_bit_cast(u32x2, __builtin_amdgcn_ds_read_tr16_b64_v4i16((LAS s16x4*)(size_t)a)); }
DI u32x4 ldstr2(unsigned a0, unsigned a1) { u32x2 lo = ldstr(a0), hi = ldstr(a1); return u32x4{lo[0], lo[1], hi[0], hi[1]}; }
DI float dot2(unsigned a, unsigned b, float c) {
    return __builtin_amdgcn_fdot2_f32_bf16(__builtin_bit_cast(bf16x2, a), __builtin_bit_cast(bf16x2, b), c, false);
}
DI float xsum16_32(float v) {
    const auto a = __builtin_amdgcn_permlane16_swap(__float_as_uint(v), __float_as_uint(v), false, false);
    v = __uint_as_float(a[0]) + __uint_as_float(a[1]);
    const auto b = __builtin_amdgcn_permlane32_swap(__float_as_uint(v), __float_as_uint(v), false, false);
    return __uint_as_float(b[0]) + __uint_as_float(b[1]);
}
constexpr unsigned ONES2 = 0x3f803f80u;
#define EX2(v) __builtin_amdgcn_exp2f(v)
typedef __amdgpu_buffer_rsrc_t rsrc_t;
constexpr int SC1 = 16;
DI u32x4 bld16(rsrc_t r, unsigned v, unsigned s) { return __builtin_amdgcn_raw_buffer_load_b128(r, v, s, SC1); }
DI void bst16(rsrc_t r, u32x4 d, unsigned v, unsigned s) {
    __builtin_amdgcn_raw_buffer_store_b128(d, r, v, s, SC1);
    asm volatile("s_nop 1" ::"v"(d) : "memory");
}
DI void bst8(rsrc_t r, u32x2 d, unsigned v, unsigned s) { __builtin_amdgcn_raw_buffer_store_b64(d, r, v, s, SC1); }

DI void set_flag(char* ws, unsigned off, u64 tag) { __hip_atomic_store((GAS u64*)(ws + off), tag, __ATOMIC_RELAXED, __HIP_MEMORY_SCOPE_AGENT); }
DI void poll(char* ws, unsigned arr, int index, u64 tag) {
    const GAS u64* p = (const GAS u64*)(ws + arr + index * 128);
    bool ok = index < 0;
#pragma unroll 1
    for (int spin = 0; spin < (1 << 18); ++spin) {
        if (!ok) ok = __hip_atomic_load(p, __ATOMIC_RELAXED, __HIP_MEMORY_SCOPE_AGENT) == tag;
        if (__all(ok)) break;
        __builtin_amdgcn_s_sleep(1);
    }
    asm volatile("" ::: "memory");
}
DI void copy_dev(const char* src, unsigned lds, int n, int hw, int lane) {
#pragma unroll 1
    for (int p = hw; p < n; p += 4) glds_dev(lane * 16, src + p * 1024, lds + p * 1024);
}

DI void cnt_add(unsigned a) {
    asm volatile("s_waitcnt lgkmcnt(0)\n\ts_mov_b64 exec, 1\n\tds_add_u32 %0, %1\n\ts_mov_b64 exec, -1" ::"v"(a), "v"(1u) : "memory");
}
DI unsigned cnt_arrive(unsigned a) {
    unsigned r;
    asm volatile("s_waitcnt lgkmcnt(0)\n\ts_mov_b64 exec, 1\n\tds_add_rtn_u32 %0, %1, %2\n\ts_mov_b64 exec, -1\n\ts_waitcnt lgkmcnt(0)"
                 : "=&v"(r) : "v"(a), "v"(1u) : "memory");
    return __builtin_amdgcn_readfirstlane(r);
}
DI void cnt_spin(unsigned a, unsigned n) {
#pragma unroll 1
    while ((unsigned)__builtin_amdgcn_readfirstlane(*(volatile LAS unsigned*)(size_t)a) < n) {}
    asm volatile("" ::: "memory");
}
DI void cnt_wait(unsigned a, unsigned n) {
#pragma unroll 1
    while ((unsigned)__builtin_amdgcn_readfirstlane(*(volatile LAS unsigned*)(size_t)a) < n) __builtin_amdgcn_s_sleep(1);
    asm volatile("" ::: "memory");
}
DI unsigned keep(unsigned v) { return min(v, EMPTY - 1); }
DI bool missing(const u32x4& a, const u32x4& b) { return __any(max(max(a[0], a[2]), max(b[0], b[2])) == EMPTY); }
DI void refetch(u32x4& a, u32x4& b, const char* base, unsigned v0, unsigned v1) {
#pragma unroll 1
    for (int spin = 0; spin < (1 << 16); ++spin) {
        asm volatile("global_load_dwordx4 %0, %2, %4 sc1\n\tglobal_load_dwordx4 %1, %3, %4 sc1\n\ts_waitcnt vmcnt(0)"
                     : "+v"(a), "+v"(b) : "v"(v0), "v"(v1), "s"(base) : "memory");
        if (!missing(a, b)) break;
    }
}
DI u32x4 rowpair(const u32x2& a, const u32x2& b) {
    const auto s0 = __builtin_amdgcn_permlane16_swap(a[0], b[0], false, false), s1 = __builtin_amdgcn_permlane16_swap(a[1], b[1], false, false);
    return u32x4{s0[0], s1[0], s0[1], s1[1]};
}
DI int lane_id() {
    int l;
    asm volatile("v_mbcnt_lo_u32_b32 %0, -1, 0\n\tv_mbcnt_hi_u32_b32 %0, -1, %0" : "=v"(l));
    return l;
}
#define LANE_VARS() const int lane = lane_id(), g = lane >> 4, c16 = lane & 15; (void)g; (void)c16
DI float halfmax(float m) {
    auto r = __builtin_amdgcn_permlane32_swap(__float_as_uint(m), __float_as_uint(m), false, false);
    return fmaxf(__uint_as_float(r[0]), __uint_as_float(r[1]));
}
DI float upper(float v) {
    auto r = __builtin_amdgcn_permlane32_swap(__float_as_uint(v), __float_as_uint(v), false, false);
    return __uint_as_float(r[1]);
}
DI float max16(const f32x16& s) {
    float a = fmaxf(fmaxf(s[0], s[1]), s[2]), b = fmaxf(fmaxf(s[3], s[4]), s[5]);
    a = fmaxf(fmaxf(a, s[6]), s[7]); b = fmaxf(fmaxf(b, s[8]), s[9]);
    a = fmaxf(fmaxf(a, s[10]), s[11]); b = fmaxf(fmaxf(b, s[12]), s[13]);
    a = fmaxf(fmaxf(a, s[14]), s[15]);
    return fmaxf(a, b);
}
DI float amax3(float m, float a, float b) {
    float r;
    asm("v_maximum3_f32 %0, %1, |%2|, |%3|" : "=v"(r) : "v"(m), "v"(a), "v"(b));
    return r;
}
DI f32x16 attn_exact(const char* kv, u32x4 q1, unsigned q2lo, int lane, int vrow) {
    const int r32 = lane & 31, h = lane >> 5;
    const f32x16 zero16 = {0, 0, 0, 0, 0, 0, 0, 0, 0, 0, 0, 0, 0, 0, 0, 0};
    const u32x2 q2 = {q2lo, 0u};
    float m = -INFINITY;
    f32x16 o = zero16;
#pragma unroll 1
    for (int t = 0; t < 64; ++t) {
        const GAS char* tp = (const GAS char*)kv + t * KV_TILE;
        const u32x4 fk1 = *(const GAS u32x4*)(tp + lane * 16);
        const u32x2 fk2 = {*(const GAS unsigned*)(tp + 1024 + r32 * 8 + h * 4), 0u};
        const u32x4 fv0 = *(const GAS u32x4*)(tp + 1280 + (h * 22 + vrow) * 16), fv1 = *(const GAS u32x4*)(tp + 1280 + VS + (h * 22 + vrow) * 16);
        f32x16 s = mfma32(fk1, q1, mfma32k8(fk2, q2, zero16));
        const float mn = fmaxf(m, halfmax(max16(s))), al = EX2(m - mn);
        m = mn;
        u32x4 p0, p1;
#pragma unroll
        for (int i = 0; i < 4; ++i) { p0[i] = pk2(EX2(s[2 * i] - m), EX2(s[2 * i + 1] - m)); p1[i] = pk2(EX2(s[8 + 2 * i] - m), EX2(s[9 + 2 * i] - m)); }
#pragma unroll
        for (int i = 0; i < 16; ++i) o[i] *= al;
        o = mfma32(fv1, p1, mfma32(fv0, p0, o));
    }
    return o;
}

DI void mma16(f32x4& c, const u32x4& a, const u32x4& b) {
    asm volatile("v_mfma_f32_16x16x32_bf16 %0, %1, %2, %0" : "+v"(c) : "v"(a), "v"(b));
}
DI void mma16z(f32x4& c, const u32x4& a, const u32x4& b) { asm volatile("v_mfma_f32_16x16x32_bf16 %0, %1, %2, 0" : "=&v"(c) : "v"(a), "v"(b)); }
DI void mma_done() { SBAR(); asm volatile("s_nop 7\n\ts_nop 1"); SBAR(); }
template <int NS> DI void zero_acc(f32x4 (&a)[NS]) {
#pragma unroll
    for (int s = 0; s < NS; ++s) { a[s] = f32x4{0, 0, 0, 0}; PIN(a[s]); }
}
template <int GS, int ASTR, int NG> DI u32x4 aload(rsrc_t rs, unsigned& v, const unsigned (&gb)[NG], int j) {
    const int o = (j % GS) * ASTR;
    PIN(v);
    return bld16(rs, v + (o & 4095), gb[j / GS] + (o & ~4095));
}
constexpr int areq(int j, int PD, int KT) {
    const int a = FIRSTFILL + 2 * j, b = PD + (j > 0 ? j - 1 : 0), c = a < b ? a : b;
    return c < KT ? c : KT;
}
template <int N> DI void lgkm() { __builtin_amdgcn_s_waitcnt(0xC07F | (N << 8)); }
template <int K0, int NK, int KT, int F0, int FT, int NS, int NSN, int PD, int GS, int ASTR, int NA, int RING, int POOL, int NG, class WAddr>
DI void kloop(f32x4 (&acc)[2][NA], u32x4 (&act)[RING][2], u32x4 (&wf)[POOL], WAddr waddr, rsrc_t rs, const char* ws, unsigned& v0,
              unsigned& v1, const unsigned (&gb)[NG]) {
    if constexpr (NK > 0) {
        constexpr int j = K0, q0 = areq(j, PD, KT), q1 = areq(j + 1, PD, KT), jc = j + 1 < KT ? j + 1 : 0, nn = NK > 1 ? NS : NSN;
        constexpr int issued = F0 + NS + POOL < FT ? F0 + NS + POOL : FT, need = F0 + NS + nn < FT ? F0 + NS + nn : FT;
        unsigned t = 0;
        u64 bad = 0;
        if (j == 0) { lgkm<POOL - NS>(); SBAR(); }
#pragma unroll
        for (int i = 0; i < 2 * NS; ++i) {
            const int n = F0 + (i >> 1);
            mma16(acc[i & 1][i >> 1], wf[n % POOL], act[j % RING][i & 1]);
            SBAR();
            if ((i & 1) && n + POOL < FT) wf[n % POOL] = lds16(waddr(n + POOL));
            if (!(i & 1) && q0 + i / 4 < q1) act[(q0 + i / 4) % RING][i / 2 & 1] = aload<GS, ASTR>(rs, (i & 2) ? v1 : v0, gb, q0 + i / 4);
            if (jc) {
                if (i == (NS > 2 ? 2 * NS - 6 : 0)) {
                    t = max(act[jc % RING][1][0], act[jc % RING][1][2]);
                    t = max(max(t, act[jc % RING][0][0]), act[jc % RING][0][2]); PIN(t);
                }
                if (i == (NS > 2 ? 2 * NS - 4 : 1)) { bad = __ballot(t == EMPTY); asm volatile("" : "+s"(bad)); }
                if (i == 2 * NS - 2 && __builtin_expect(bad != 0, 0))
                    refetch(act[jc % RING][0], act[jc % RING][1], ws + gb[jc / GS] + (jc % GS) * ASTR, v0, v1);
            }
            if (i == 2 * NS - 1 && need > F0 + NS) { SBAR(); lgkm<issued - need>(); }
            SBAR();
        }
        kloop<K0 + 1, NK - 1, KT, F0 + NS, FT, NS, NSN, PD, GS, ASTR>(acc, act, wf, waddr, rs, ws, v0, v1, gb);
    }
}
template <int PD, int GS, int ASTR, int RING, int NG, class Probe, class Before, class After>
DI void kstart(u32x4 (&act)[RING][2], rsrc_t rs, const char* ws, unsigned& v0, unsigned& v1, const unsigned (&gb)[NG], Probe probe,
               Before before, After after) {
    before();
    SBAR();
#pragma unroll 1
    for (int spin = 0; spin < (1 << 16); ++spin) {
        const bool miss = probe();
        SBAR();
#pragma unroll
        for (int k = 0; k < FIRSTFILL; ++k) {
            act[k][0] = aload<GS, ASTR>(rs, v0, gb, k); SBAR();
            act[k][1] = aload<GS, ASTR>(rs, v1, gb, k); SBAR();
        }
        if (!__any(miss)) break;
        asm volatile("" ::: "memory");
    }
    SBAR();
    after();
    SBAR();
    if (__builtin_expect(missing(act[0][0], act[0][1]), 0)) refetch(act[0][0], act[0][1], ws + gb[0], v0, v1);
    SBAR();
}
DI void e_bases(unsigned (&gb)[9], unsigned base, int m, int stride) {
    const int m3 = m % 3;
#pragma unroll
    for (int i = 0; i < 9; ++i) {
        const int q = i < 4 ? (i + m) & 3 : i < 6 ? 4 + ((i + (m >> 2)) & 1) : 6 + (i - 6 + m3 >= 3 ? i - 9 + m3 : i - 6 + m3);
        gb[i] = base + q * stride;
        asm volatile("" : "+s"(gb[i]));
    }
}
template <int NG> DI void rot_bases(unsigned (&gb)[NG], unsigned base, int rot, int stride) {
#pragma unroll
    for (int i = 0; i < NG; ++i) { const int q = i + rot; gb[i] = base + (q >= NG ? q - NG : q) * stride; asm volatile("" : "+s"(gb[i])); }
}
DI void send_half(const f32x4 (&a)[5], unsigned dst) {
#pragma unroll
    for (int s = 0; s < 5; ++s) *(LAS f32x4*)(size_t)(dst + s * 1024) = a[s];
}
DI void recv_half(f32x4 (&a)[5], unsigned src) {
#pragma unroll
    for (int s = 0; s < 5; ++s) a[s] += ldsf4(src + s * 1024);
}
DI void ldsw16(unsigned a, const u32x4& v) { *(LAS u32x4*)(size_t)a = v; }
DI f32x4 unpk(unsigned r0, unsigned r1) { return f32x4{bflo(r0), bfhi(r0), bflo(r1), bfhi(r1)}; }
DI void res_init(f32x4 (&a)[5], const u32x4& x0, const u32x4& x1, const u32x2& x2) {
    a[0] = unpk(x0[0], x0[1]); a[1] = unpk(x0[2], x0[3]); a[2] = unpk(x1[0], x1[1]); a[3] = unpk(x1[2], x1[3]); a[4] = unpk(x2[0], x2[1]);
#pragma unroll
    for (int s = 0; s < 5; ++s) PIN(a[s]);
}
DI void res_pack(u32x4& x0, u32x4& x1, u32x2& x2, const f32x4 (&a)[5]) {
    x0 = u32x4{pk2(a[0][0], a[0][1]), pk2(a[0][2], a[0][3]), pk2(a[1][0], a[1][1]), pk2(a[1][2], a[1][3])};
    x1 = u32x4{pk2(a[2][0], a[2][1]), pk2(a[2][2], a[2][3]), pk2(a[3][0], a[3][1]), pk2(a[3][2], a[3][3])};
    x2 = u32x2{pk2(a[4][0], a[4][1]), pk2(a[4][2], a[4][3])};
}
enum { C_WKV, C_KFUSED, C_X, C_XUSED, C_WQ, C_CSP = C_WQ + 5, C_CS, C_KVST, C_R1, C_XCH, C_NUM = C_XCH + 2 };
enum { E_C1, E_C2, E_C3, E_MK, P_NONE, P_C0, P_C1, P_C2, E_C0, H_PREP, H_FILL, M_LDS, M_K8, M_T4, M_NUM };

__global__ __launch_bounds__(512, 2) void k_block(const char* __restrict__ x, const char* __restrict__ wq, const char* __restrict__ wk,
                                                  const char* __restrict__ wv, const char* __restrict__ wo, const char* __restrict__ w1,
                                                  const char* __restrict__ w2, char* ws, char* __restrict__ out) {
    extern __shared__ __attribute__((aligned(16))) char smem[];
    const unsigned L = (unsigned)(size_t)(LAS char*)smem;
    const int w = __builtin_amdgcn_readfirstlane(threadIdx.x >> 6);
    const int xg = blockIdx.x & 7, idx = blockIdx.x >> 3;
    const int tb = ONEWAY ? xg * 4 + (idx >> 3) : (xg >> 1) * 8 + (idx >> 2), m = ONEWAY ? (idx & 7) : (xg & 1) * 4 + (idx & 3), cid = tb * 8 + m;
    const int ct = (m >> 2) * 8 + (tb & 1) * 4 + (m & 3);
    const rsrc_t rs = __builtin_amdgcn_make_buffer_rsrc(ws, 0, WS_END, 0x00020000);
    const u32x4 ones = {ONES2, ONES2, ONES2, ONES2};
    const f32x4 z4 = {0, 0, 0, 0};
    STAMP_INIT();

#define LOAD_TAG() u64 tag; asm volatile("global_load_dwordx2 %0, %1, %2 sc1" : "=v"(tag) : "v"(0u), "s"(ws + WS_FL) : "memory")
#define MAKE_TAG() do { PIN(tag); \
        tag = ((u64)(unsigned)__builtin_amdgcn_readfirstlane(tag >> 32) << 32) | (unsigned)__builtin_amdgcn_readfirstlane((unsigned)tag); \
        tag = (tag ^ 0x9E3779B97F4A7C15ull) * 0xBF58476D1CE4E5B9ull + 0x632BE59BD9B4E019ull; asm volatile("" : "+s"(tag)); } while (0)

#define CNT(i) (L + L_CNT + (i) * 4)
#define CN2(i) (L + L_MISC + 512 + (i) * 4)
#define WQ_AT(r) ((r) < 2 ? L_WQ1 + (r) * 20480 : L_WQ2 + (r) % 3 * 20480)
#define WQ_ADDR() const unsigned wrow = L + (8 * g + (c16 >> 2)) * 160 + (lane & 3) * 8, wo0 = wrow + (g & 1) * 640, wo1 = wrow + ((g & 1) ^ 1) * 640
    const int rotc = ONEWAY ? m % 5 : m & 3, rotd = ONEWAY ? ct % 5 : (ct & 7) % 5;

    if (w >= 4) {
        const int hw = w - 4;
        LANE_VARS();
        __builtin_amdgcn_s_setprio(P1PRIO);
        if (m < 4) {
            const char* src = m < 2 ? wk : wv;
#pragma unroll 1
            for (int p = hw; p < 25; p += 4) glds(lane * 16, src + p * 1024, L + L_WKV + p * 1024);
        }
        {
            const unsigned vx0 = (lane >> 3) * 1280 + (((lane & 7) ^ g) << 4), vx1 = (8 + (lane >> 3)) * 1280 + (((lane & 7) ^ (4 + g)) << 4);
            const char* xb = x + (size_t)(tb * 64 + hw * 16) * 1280;
            int sg = ONEWAY ? m : (m & 3) * 2;
#pragma unroll 1
            for (int i = 0; i < 10; ++i) {
                glds(vx0, xb + sg * 128, L + L_X + (hw * 20 + sg) * 1024);
                glds(vx1, xb + sg * 128, L + L_X + (hw * 20 + 10 + sg) * 1024);
                sg = sg == 9 ? 0 : sg + 1;
                if (i == B0AT) bar_raw();
                if (i == WKVAT) { wait_vm<2 * WKVAT + 2>(); cnt_add(CNT(C_WKV)); }
            }
        }
    }
    unsigned pf[5] = {0, 0, 0, 0, 0};
    if (w < 4) {
        static_assert(ONEWAY, "");
        const int lane = lane_id();
        const unsigned vo = lane * 128;
        const int wi = idx * 4 + w;
        asm volatile("s_mov_b64 exec, 0xfffff\n\tglobal_load_dword %0, %2, %3\n\ts_mov_b64 exec, 15\n\tglobal_load_dword %1, %2, %4\n\ts_mov_b64 exec, -1"
                     : "+v"(pf[0]), "+v"(pf[1])
                     : "v"(vo), "s"(x + xg * 327680 + wi * 2560), "s"(((wi & 1) ? wv : wk) + min(wi >> 1, 49) * 512) : "memory");
        u64 pc; int l1, l2;
        asm volatile("s_getpc_b64 %0\n.Lpf_pc:\n\ts_mov_b32 %1, .Lpf_end1-.Lpf_pc\n\ts_mov_b32 %2, .Lpf_end2-.Lpf_pc" : "=s"(pc), "=s"(l1), "=s"(l2));
        const unsigned len = max(max(l1, l2), 4);
        const unsigned co = min((unsigned)(wi * 4 + (lane & 3)) * 128u, len - 4) & ~3u;
        asm volatile("global_load_dword %0, %1, %2" : "+v"(pf[3]) : "v"(co), "s"(pc) : "memory");
    }

    if (w >= 4) {
        const int hw = w - 4;
        LANE_VARS();
        LOAD_TAG();
        u64 f0;
        const int c0 = tb >> 3;
        {
            unsigned vw[5];
#pragma unroll
            for (int i = 0; i < 5; ++i) {
                const int ci = i * 64 + lane, pos = ci / 10, ch = ci - pos * 10, k = pos ^ (((pos >> 3) & 1) << 2);
                vw[i] = k * 1280 + ch * 16;
            }
            const char* wb = wq + m * 160 + hw * 40960;
            WQ_ADDR();
            f32x4 cs[5];
            u32x4 one = ones;
            PIN(one);
            zero_acc(cs);
            auto wqload = [&](int r) {
#pragma unroll
                for (int i = 0; i < 5; ++i) glds(vw[i], wb + r * 163840, L + WQ_AT(r) + hw * 5120 + i * 1024);
            };
            auto wqsum = [&](int r) {
                u32x4 f[5];
#pragma unroll
                for (int s = 0; s < 5; ++s) f[s] = ldstr2(wo0 + WQ_AT(r) + hw * 5120 + s * 32, wo1 + WQ_AT(r) + hw * 5120 + s * 32);
#pragma unroll
                for (int s = 0; s < 5; ++s) mma16(cs[s], f[s], one);
            };
            wqload(0);
            wait_vm<5>();
            cnt_add(CNT(C_X));
            STAMP(1);
            wqload(1);
            MAKE_TAG();
            cnt_spin(CNT(C_KFUSED), 4);
            wqload(2); wait_vm<10>(); cnt_add(CNT(C_WQ));
            cnt_spin(CNT(C_XUSED), 4);
            wqload(3); wait_vm<10>(); cnt_add(CNT(C_WQ + 1));
            wqload(4);
            wqsum(0); wqsum(1);
            wait_vm<10>(); cnt_add(CNT(C_WQ + 2)); wqsum(2);
            wait_vm<5>(); cnt_add(CNT(C_WQ + 3)); wqsum(3);
            wait_vm<0>(); cnt_add(CNT(C_WQ + 4));
            f0 = __hip_atomic_load((const GAS u64*)(ws + FL_KV + (c0 * 32 + (lane & 31)) * 128), __ATOMIC_RELAXED, __HIP_MEMORY_SCOPE_AGENT);
            wqsum(4);
            mma_done();
            if (c16 == 0) {
#pragma unroll
                for (int s = 0; s < 5; ++s) *(LAS f32x4*)(size_t)(L + L_CSX + hw * 320 + s * 64 + g * 16) = cs[s];
            }
            if (cnt_arrive(CNT(C_CSP)) == 3) {
                const unsigned ca = L + L_CSX + (lane < 20 ? lane : 0) * 16;
                const f32x4 c4 = (ldsf4(ca) + ldsf4(ca + 320)) + (ldsf4(ca + 640) + ldsf4(ca + 960));
                *(LAS f32x4*)(size_t)(ca + 1280) = c4;
                cnt_add(CNT(C_CS));
            }
        }
        const char* kv = ws + WS_KV;
        STAMP(2);
        poll(ws, FL_KV, lane < 32 && f0 != tag ? c0 * 32 + lane : -1, tag);
        STAMP(3);
        copy_dev(kv + c0 * CHUNK, L, 42, hw, lane);
        wait_vm<7>();
        STAMP(4);
        __builtin_amdgcn_s_setprio(2);
        bar_raw();
        wait_vm<0>();
        cnt_add(CN2(E_C0));
        poll(ws, FL_KV, (c0 * 32 + 32 + lane) & 127, tag);
        copy_dev(kv + ((c0 + 1) & 3) * CHUNK, L + CHUNK, 42, hw, lane);
        {
            int u = blockIdx.x * 4 + hw;
            const int pmat = u < 160 ? 0 : (u < 480 ? 1 : (u < 912 ? 2 : 3));
            u = pmat == 0 ? u : pmat == 1 ? u - 160 : pmat == 2 ? u - 480 : 0;
            const int pc = pmat < 2 ? u / 20 : u / 54, ks = u - pc * (pmat < 2 ? 20 : 54);
            int rb, a3, a2, a1;
            if (pmat == 0) { if (ks < 16) { rb = 40 * ks; a3 = 20; a2 = 4; a1 = 8; } else { rb = 160 * (ks - 16) + 16; a3 = 80; a2 = 40; a1 = 20; } }
            else if (pmat == 1) { if (ks < 16) { rb = 80 * (ks >> 1) + 32 * (ks & 1); a3 = 16; a2 = 8; a1 = 4; } else { rb = 160 * (ks - 16) + 64; a3 = 80; a2 = 8; a1 = 4; } }
            else if (ks >= 16 && ks < 24) { rb = 224 * (ks - 16) + 96; a3 = ks < 23 ? 112 : 48; a2 = 8; a1 = 4; }
            else { rb = ks < 16 ? 112 * ks : (ks < 39 ? 112 * (ks - 24) + 32 : 112 * (ks - 39) + 64); a3 = 8; a2 = 4; a1 = 16; }
            const int ld = pmat == 1 ? 3456 : 1280, chmax = pmat != 1 ? 9 : (pc == 15 ? 5 : 13);
            const char* psrc = pmat == 0 ? wo + pc * 160 : pmat == 1 ? w1 + pc * 224 : w2 + pc * 160;
            const unsigned pb = L + 2 * CHUNK + hw * 8192, pv = g * ld + min(c16, chmax) * 16;
#pragma unroll 1
            for (int i = 0; i < 8; ++i) glds(pv, psrc + (rb + a3 * (i >> 2) + a2 * ((i >> 1) & 1) + a1 * (i & 1)) * ld, pb + i * 1024);
            wait_vm<8>();
            cnt_add(CN2(E_C1));
            wait_vm<0>();
            const unsigned a0 = pb + (8 * g + (c16 >> 2)) * 256 + (lane & 3) * 8;
            if (pmat == 1) {
                const unsigned pdst = WS_W1 + pc * 143360 + ks * 5120, pd2 = WS_W1 + pc * 143360 + 102400 + ks * 2048;
                const unsigned cv = c16 == 0 ? g * 16 : 0x80000000u;
#pragma unroll
                for (int s = 0; s < 7; ++s) {
                    const unsigned a1 = a0 + (s == 6 ? (pc == 15 ? 64 : 192) : s * 32);
                    const u32x4 f = ldstr2(a1, a1 + 1024);
                    bst16(rs, f, lane * 16, s < 2 ? pdst + s * 1024 : s < 4 ? pdst + (s + 1) * 1024 : s < 6 ? pd2 + (s - 4) * 1024 : pdst + 2048);
                    bst16(rs, __builtin_bit_cast(u32x4, mfma16(f, ones, z4)), cv, WS_CS + (ks * 1792 + pc * 112) * 4 + s * 64);
                }
            } else if (pmat < 3) {
                const unsigned a1 = a0 + (lane & 3) * 8, pdst = (pmat == 0 ? WS_WO : WS_W2) + u * 5120;
#pragma unroll
                for (int s = 0; s < 5; ++s) {
                    const unsigned a2 = s < 4 ? a1 + (s >> 1) * 64 + (s & 1) * 8 : a0 + 128;
                    bst16(rs, ldstr2(a2, a2 + 1024), lane * 16, pdst + s * 1024);
                }
            }
        }
        wait_vm<0>();
        if (cnt_arrive(CN2(H_PREP)) == 3 && lane == 0) set_flag(ws, FL_PR + cid * 128, tag);
        STAMP(5);
        cnt_wait(CN2(H_PREP), 4);
        copy_dev(kv + ((c0 + 2) & 3) * CHUNK, L + 2 * CHUNK, 42, hw, lane);
        {
            const u32x4 e = {EMPTY, EMPTY, EMPTY, EMPTY};
#pragma unroll 1
            for (int p = hw; p < 44; p += 4) bst16(rs, e, lane * 16, WS_O + blockIdx.x * 48640 + p * 1024);
            bst16(rs, e, hw < 3 || lane < 32 ? lane * 16 : 0x80000000u, WS_O + blockIdx.x * 48640 + (44 + hw) * 1024);
        }
        wait_vm<12>();
        cnt_add(CN2(E_C2));
        poll(ws, FL_KV, (c0 * 32 + 96 + (lane & 31)) & 127, tag);
        wait_vm<0>();
        if (cnt_arrive(CN2(H_FILL)) == 3 && lane == 0) set_flag(ws, FL_MK + cid * 128, tag);
        cnt_wait(CN2(P_C0), 4);
        copy_dev(kv + ((c0 + 3) & 3) * CHUNK, L, 42, hw, lane);
        poll(ws, FL_PR, hw * 64 + lane, tag);
        wait_vm<0>();
        cnt_add(CN2(E_C3));
        cnt_wait(CN2(E_C3), 4);
        STAMP(6);
        if (blockIdx.x == 0 && hw == 1 && lane == 0) set_flag(ws, WS_FL, tag);
        if (hw == 3) {
            u32x4 c[20];
            const int l28 = lane < 28 ? lane : 0;
#pragma unroll
            for (int ks = 0; ks < 20; ++ks) c[ks] = bld16(rs, l28 * 16, WS_CS + (ks * 1792 + ct * 112) * 4);
            f32x4 c4 = z4;
#pragma unroll
            for (int ks = 0; ks < 20; ++ks) c4 += __builtin_bit_cast(f32x4, c[ks]);
            if (lane < 28) *(LAS f32x4*)(size_t)(L + L_MISC + lane * 16) = c4;
        }
        auto wocopy = [&](int p0, int p1) {
#pragma unroll 1
            for (int p = p0 + hw; p < p1; p += 4) {
                const int q = p + rotc * 20;
                glds_dev(lane * 16, ws + WS_WO + m * 102400 + (q >= 100 ? q - 100 : q) * 1024, L + L_WO + p * 1024);
            }
        };
        u32x4 r1[35], r2[9][8];
        unsigned g1[5], g1b[5], g2[9];
        rot_bases(g1, WS_W1 + ct * 143360, rotd, 20480);
        rot_bases(g1b, WS_W1 + ct * 143360 + 102400, rotd, 8192);
        e_bases(g2, WS_W2 + m * 276480, m, 30720);
        const unsigned vp = lane * 16 + hw * 1024, vq = lane * 16 + min(28 + hw, 29) * 1024;
        unsigned lp0 = L + vp, lp1 = lp0 + 61440, lp2 = lp0 + 122880, lq0 = L + vq, lq1 = lq0 + 61440, lq2 = lq0 + 122880;
        PIN(lp0); PIN(lp1); PIN(lp2); PIN(lq0); PIN(lq1); PIN(lq2);
#define LD2(G, THR) do { _Pragma("unroll") for (int j_ = 0; j_ < 7; ++j_) { r2[G][j_] = bld16(rs, vp, g2[G] + j_ * 4096); wait_vm<THR>(); } \
        r2[G][7] = bld16(rs, vq, g2[G]); wait_vm<THR>(); } while (0)
#define ST2E(G, j) do { constexpr int sl_ = (G) % 5; \
        ldsw16((j) < 7 ? (sl_ < 2 ? lp0 : sl_ < 4 ? lp1 : lp2) + (sl_ & 1) * 30720 + (j) * 4096 : (sl_ < 2 ? lq0 : sl_ < 4 ? lq1 : lq2) + (sl_ & 1) * 30720, r2[G][j]); } while (0)
#pragma unroll
        for (int i = 0; i < 35; ++i) r1[i] = bld16(rs, vp, i < 25 ? g1[i / 5] + (i % 5) * 4096 : g1b[(i - 25) / 2] + ((i - 25) & 1) * 4096);
        SBAR();
        LD2(7, 63); LD2(8, 63);
        poll(ws, FL_MK, hw * 64 + lane, tag);
        cnt_add(CN2(E_MK));
        cnt_wait(CN2(P_C1), 4);
        wocopy(0, 42);
        cnt_wait(CN2(P_C2), 4);
        wocopy(42, 100);
#pragma unroll
        for (int G = 0; G < 7; ++G) {
            const int p = (tb & 3) * 8 + hw * 2;
            glds_dev(lane * 16, ws + g2[G] + min(p, 28) * 1024, L + XCH + hw * 1024);
            glds_dev(lane * 16, ws + g2[G] + min(p + 1, 29) * 1024, L + XCH + hw * 1024);
        }
        wait_vm<0>();
        STAMP(7);
        __builtin_amdgcn_s_setprio(TAILPRIO);
        bar_raw();
#pragma unroll
        for (int i = 0; i < 8; ++i) ldsw16(lp0 + i * 4096, r1[i < 4 ? 25 + i : i - 4]);
        SBAR();
        LD2(6, THROT);
        bar();
        cnt_wait(CN2(M_LDS), 4);
#pragma unroll
        for (int i = 8; i < 35; ++i) {
            ldsw16((i < 15 ? lp0 : i < 30 ? lp1 : lp2) + (i % 15) * 4096, r1[i < 29 ? i - 4 : i]);
            SBAR();
            if (i >= 11 && i - 11 < NEARLY) {
                const int e = i - 11, G = 3 + (e >> 3), j = e & 7;
                r2[G][j] = bld16(rs, j < 7 ? vp : vq, g2[G] + (j < 7 ? j * 4096 : 0));
                SBAR();
            }
        }
        STAMP(8);
        bar();
#pragma unroll
        for (int e = NEARLY; e < 24; ++e) {
            const int G = 3 + (e >> 3), j = e & 7;
            r2[G][j] = bld16(rs, j < 7 ? vp : vq, g2[G] + (j < 7 ? j * 4096 : 0));
            wait_vm<THROT>();
        }
        cnt_wait(CN2(M_K8), 4);
#pragma unroll 1
        for (int p = hw; p < 60; p += 4) { glds_dev(lane * 16, ws + (p < 30 ? g2[0] + p * 1024 : g2[1] + (p - 30) * 1024), L + p * 1024); wait_vm<THROT>(); }
        cnt_wait(CN2(M_T4), 4);
        copy_dev(ws + g2[2], L + 61440, 30, hw, lane);
        wait_vm<7>();
        STAMP(9);
        bar_raw();
#pragma unroll
        for (int iv = 0; iv < 7; ++iv) {
#pragma unroll
            for (int e = iv < 3 ? iv * 8 - (iv == 2) * 0 : 24 + (iv - 3) * 6; e < (iv < 3 ? iv * 8 + 8 : 24 + (iv - 2) * 6); ++e) {
                if (!(NOST && iv > 0)) switch (e >> 3) {
                    case 0: ST2E(3, e & 7); break;
                    case 1: ST2E(4, e & 7); break;
                    case 2: ST2E(5, e & 7); break;
                    case 3: ST2E(6, e & 7); break;
                    case 4: ST2E(7, e & 7); break;
                    default: ST2E(8, e & 7); break;
                }
                SBAR();
            }
            wait_vm<0>();
            bar();
        }
        asm volatile(".Lpf_end2:");
        STAMP_OUT(1);
        __builtin_amdgcn_endpgm();
    }

    u32x4 xr0, xr1; u32x2 xr2;
    u32x4 qa1, qb1; u32x2 qa2, qb2;
    const int kh = w & 1;
    {
        LANE_VARS();
        LOAD_TAG();
        u32x4 xf[20], xo[10], kf[20];
        if (w == 0 && lane < C_NUM) *(LAS unsigned*)(size_t)CNT(lane) = 0u;
        bar();
        const unsigned xrow = L + L_X + w * 20480 + (c16 >> 3) * 10240 + (c16 & 7) * 128;
        const int sw = c16 >> 1, t32 = (w & 1) * 16 + c16;
        unsigned xa = xrow + (((kh * 4 + g) ^ sw) << 4), xb = xrow + ((((kh ^ 1) * 4 + g) ^ sw) << 4), xc = xa + ((w ^ 1) - w) * 20480, xra[3], qw[5], kvo;
        {
            const int c0 = m * 10 + g, c1 = c0 + 4, c2 = m * 10 + 8 + (g >> 1);
            xra[0] = xrow + (c0 >> 3) * 1024 + (((c0 & 7) ^ sw) << 4);
            xra[1] = xrow + (c1 >> 3) * 1024 + (((c1 & 7) ^ sw) << 4);
            xra[2] = xrow + (c2 >> 3) * 1024 + (((c2 & 7) ^ sw) << 4) + (g & 1) * 8;
            const int d = (m & 1) * 16 + c16;
            kvo = m == 0 ? ((g >> 1) * 32 + t32) * 16 + (g & 1) * 8
                : m == 1 ? (g == 0 ? 1024 + t32 * 8 : 0x80000000u)
                         : (d <= 21 ? 1280 + (((w & 1) * 2 + (g & 1)) * 22 + d) * 16 + (g >> 1) * 8 : 0x80000000u);
            PIN(xa); PIN(xb); PIN(xc); PIN(xra[0]); PIN(xra[1]); PIN(xra[2]); PIN(kvo);
        }
#pragma unroll
        for (int s = 0; s < 5; ++s) {
            const int f0 = s * 16 + 4 * g, hh = f0 / 20, d0 = f0 - hh * 20;
            qw[s] = L + L_QX + (hh * 2 + (w >> 1)) * 1280 + (d0 < 16 ? ((d0 >> 3) * 32 + t32) * 16 + (d0 & 4) * 2 : 1024 + t32 * 8);
            PIN(qw[s]);
        }
        WQ_ADDR();
        unsigned b0 = wo0 + L_WQ1 + kh * 5120, b1 = wo1 + L_WQ1 + kh * 5120;
        PIN(b0); PIN(b1);
        wait_vm<0>();
        MAKE_TAG();
        if (m >= 4) {
            __builtin_amdgcn_s_sleep(PFDELAY);
            const char* wp = wq + (((idx >> 3) * 4 + (m & 3)) * 4 + w) * 12800;
            asm volatile("s_mov_b64 exec, %5\n\tglobal_load_dword %0, %2, %3\n\tglobal_load_dword %1, %2, %4\n\ts_mov_b64 exec, -1"
                         : "+v"(pf[2]), "+v"(pf[4]) : "v"(lane * 128), "s"(wp), "s"(wp + 6400), "s"(0x3ffffffffffffull) : "memory");
        }
        f32x4 ka = z4, kc = z4, ssm = z4, ssq = z4, acc[2][5];
        u32x4 one = ones;
        PIN(ssm); PIN(ssq); PIN(one);
        if (m < 4) {
            cnt_wait(CNT(C_WKV), 4);
            const unsigned a0 = L + L_WKV + (8 * g + (c16 >> 2)) * 40 + (m & 1) * 32 + (lane & 3) * 8;
            const unsigned a1 = a0 + kh * 1280, a2 = a0 + (kh ^ 1) * 1280;
#pragma unroll
            for (int j = 0; j < 10; ++j) { kf[j] = ldstr2(a1 + j * 2560, a1 + j * 2560 + 160); kf[10 + j] = ldstr2(a2 + j * 2560, a2 + j * 2560 + 160); }
            if (m < 2) {
#pragma unroll
                for (int j = 0; j < 20; ++j) kc = mfma16(kf[j], ones, kc);
            } else {
#pragma unroll
                for (int j = 0; j < 20; ++j) kc = mfma16(ones, kf[j], kc);
            }
            PIN(kc);
        }
        cnt_add(CNT(C_KFUSED));
        cnt_spin(CNT(C_X), 4);
#pragma unroll
        for (int j = 0; j < 10; ++j) xf[j] = lds16(xa + j * 1024);
#pragma unroll
        for (int j = 0; j < 10; ++j) xf[10 + j] = lds16(xb + j * 1024);
#pragma unroll
        for (int j = 0; j < 10; ++j) xo[j] = lds16(xc + j * 1024);
        xr0 = lds16(xra[0]); xr1 = lds16(xra[1]); xr2 = lds8(xra[2]);
        SBAR();
        if (m < 2) {
#pragma unroll
            for (int j = 0; j < 20; ++j) ka = mfma16(kf[j], xf[j], ka);
        } else if (m < 4) {
#pragma unroll
            for (int j = 0; j < 20; ++j) ka = mfma16(xf[j], kf[j], ka);
        }
        PIN(ka);
#pragma unroll
        for (int j = 0; j < 20; ++j) {
            mma16(ssm, one, xf[j]); mma16(ssq, xf[j], xf[j]);
            if (j == 4) {
#pragma unroll
                for (int i = 0; i < 10; ++i) { PIN(xf[i]); PIN(xf[10 + i]); PIN(xo[i]); }
                PIN(xr0); PIN(xr1); PIN(xr2);
                cnt_add(CNT(C_XUSED));
            }
        }
        mma_done();
        STAMP(1);
        const float sd = (c16 & 2) ? ((c16 & 1) ? ssq[3] : ssq[2]) : ((c16 & 1) ? ssq[1] : ssq[0]);
        const float mean = ssm[0] * (1.f / D), rstd = __builtin_amdgcn_rsqf(fmaxf(__shfl(sd, (c16 >> 2) * 16 + c16) * (1.f / D) - mean * mean, 0.f) + LN_EPS);
        if (m < 4) {
            u32x2 pv;
            if (m < 2) {
                pv = u32x2{pk2((ka[0] - mean * kc[0]) * rstd, (ka[1] - mean * kc[1]) * rstd), pk2((ka[2] - mean * kc[2]) * rstd, (ka[3] - mean * kc[3]) * rstd)};
            } else {
                float v[4];
#pragma unroll
                for (int r = 0; r < 4; ++r) v[r] = (ka[r] - __shfl(mean, 4 * g + r) * kc[0]) * __shfl(rstd, 4 * g + r);
                pv = u32x2{pk2(v[0], v[1]), pk2(v[2], v[3])};
                if (m == 3 && c16 >= 4) pv = c16 == 4 ? u32x2{ONES2, ONES2} : u32x2{0u, 0u};
            }
            bst8(rs, pv, kvo, WS_KV + (tb * 2 + (w >> 1)) * KV_TILE);
        }
        SBAR();
        STAMP(2);
        u32x2 f[2][5][2];
        unsigned xs, xv;
        float sc = rstd * QSCALE, ms = -mean * sc;
        PIN(sc); PIN(ms);
        unsigned b2 = b0 + (L_WQ2 - L_WQ1), b3 = b1 + (L_WQ2 - L_WQ1), cq = CNT(0), cv = 0;
        PIN(b2); PIN(b3); PIN(cq);
        auto qhalf = [&](int j, int s, int e) { return ldstr((j < 4 ? (e ? b1 : b0) + j * 10240 : (e ? b3 : b2) + j % 6 * 10240) + s * 32); };
        auto kvflag = [&] {
            if (m < 4) {
                wait_vm<0>();
                if (cnt_arrive(CNT(C_KVST)) == 3 && lane == 0) set_flag(ws, FL_KV + (tb * 4 + m) * 128, tag);
            }
        };
        if (FLAGAT < 0) kvflag();
        cnt_spin(CNT(C_WQ), 4);
#pragma unroll
        for (int s = 0; s < 5; ++s) { f[0][s][0] = qhalf(0, s, 0); f[0][s][1] = qhalf(0, s, 1); }
        SBAR();
#pragma unroll
        for (int j = 0; j < 10; ++j) {
            if (j < 8 && !(j & 1)) cv = *(volatile LAS unsigned*)(size_t)(cq + (C_WQ + j / 2 + 1) * 4);
            if (j < 9 && (j & 1) && __builtin_expect((unsigned)__builtin_amdgcn_readfirstlane(cv) < 4u, 0)) cnt_spin(CNT(C_WQ + j / 2 + 1), 4);
            asm volatile("" ::: "memory");
            SBAR();
#pragma unroll
            for (int s = 0; s < 5; ++s) {
                const u32x4 fr = {f[j & 1][s][0][0], f[j & 1][s][0][1], f[j & 1][s][1][0], f[j & 1][s][1][1]};
                if (j == 0) mma16z(acc[0][s], fr, xf[j]); else mma16(acc[0][s], fr, xf[j]);
                SBAR();
                if (j < 9) f[(j + 1) & 1][s][0] = qhalf(j + 1, s, 0);
                SBAR();
                if (j == 0) mma16z(acc[1][s], fr, xo[j]); else mma16(acc[1][s], fr, xo[j]);
                SBAR();
                if (j < 9) f[(j + 1) & 1][s][1] = qhalf(j + 1, s, 1);
                SBAR();
            }
            if (j == FLAGAT) {   }
            if (j == FLAGAT) kvflag();
            if (j == 3) asm volatile("s_mov_b64 exec, 1\n\tds_add_u32 %0, %1 offset:%2\n\ts_mov_b64 exec, -1" ::"v"(cq), "v"(1u), "i"(C_R1 * 4) : "memory");
            if (j == FLAGAT) {   }
            if (j == 5) { xs = L + L_WQ1 + (w ^ 1) * 5120 + lane * 16; xv = L + L_WQ1 + w * 5120 + lane * 16; PIN(xs); PIN(xv); }
            if (j == 7) cnt_spin(CNT(C_R1), 4);
        }
        STAMP(3);
        if (m >= 4) wait_vm<0>();
        asm volatile("" ::"v"(pf[0]), "v"(pf[1]), "v"(pf[2]), "v"(pf[3]), "v"(pf[4]));
        mma_done();
        send_half(acc[1], xs);
        cnt_add(CNT(C_XCH + (w >> 1)));
        if (w == 0 && lane < M_NUM) *(LAS unsigned*)(size_t)CN2(lane) = 0u;
        f32x4 c5[5], p5[5];
#pragma unroll 1
        for (;;) {
            const unsigned n1 = *(volatile LAS unsigned*)(size_t)CNT(C_CS), n2 = *(volatile LAS unsigned*)(size_t)CNT(C_XCH + (w >> 1));
#pragma unroll
            for (int s = 0; s < 5; ++s) {
                c5[s] = *(volatile LAS f32x4*)(size_t)(L + L_CSX + 1280 + s * 64 + g * 16);
                p5[s] = *(volatile LAS f32x4*)(size_t)(xv + s * 1024);
            }
            if (__builtin_amdgcn_readfirstlane(n1 + n2) == 3) break;
        }
#pragma unroll
        for (int s = 0; s < 5; ++s) {
            const f32x4 v = (acc[0][s] + p5[s]) * sc + c5[s] * ms;
            *(LAS u32x2*)(size_t)qw[s] = u32x2{pk2(v[0], v[1]), pk2(v[2], v[3])};
        }
        bar();
        const int r32 = lane & 31, h = lane >> 5;
        const unsigned qb = L + L_QX + ((w & 1) * 4 + (w >> 1)) * 1280;
        qa1 = lds16(qb + lane * 16); qb1 = lds16(qb + 2560 + lane * 16);
        qa2 = u32x2{lds4(qb + 1024 + r32 * 8 + h * 4), 0u}; qb2 = u32x2{lds4(qb + 2560 + 1024 + r32 * 8 + h * 4), 0u};
    }
    STAMP(4);
    {
        LANE_VARS();
        const int r32 = lane & 31, h = lane >> 5, vrow = min(r32, 21);
        const unsigned kconst = h == 0 ? 0x00003f80u : 0u;
        unsigned ak1 = L + lane * 16, ak2 = L + 1024 + r32 * 8 + h * 4, av = L + 1280 + (h * 22 + vrow) * 16;
        const f32x16 zero16 = {0, 0, 0, 0, 0, 0, 0, 0, 0, 0, 0, 0, 0, 0, 0, 0};
        f32x16 sa, sb, oa = zero16, ob = zero16;
        u32x4 ka1 = lds16(ak1), kb1 = lds16(ak1 + KV_TILE), va0 = lds16(av), va1 = lds16(av + VS), vb0 = va0, vb1 = va0;
        u32x2 ka2 = {lds4(ak2), kconst}, kb2 = {lds4(ak2 + KV_TILE), kconst};
        sa = mfma32(ka1, qa1, mfma32k8(ka2, qa2, zero16));
        sb = mfma32(ka1, qb1, mfma32k8(ka2, qb2, zero16));
        {
            const float ma = halfmax(max16(sa)), mb = halfmax(max16(sb));
            qa2[1] = h == 0 ? (pk2(-ma, 0.f) & 0xffffu) : 0u;
            qb2[1] = h == 0 ? (pk2(-mb, 0.f) & 0xffffu) : 0u;
        }
        sa = mfma32(ka1, qa1, mfma32k8(ka2, qa2, zero16));
        u32x4 pa0 = {0u, 0u, 0u, 0u}, pa1 = pa0, pb0 = pa0, pb1 = pa0;
        float e2 = 0.f, e3 = 0.f;
        unsigned vo0 = (r32 >> 4) * 20480 + h * 256 + (r32 & 15) * 16, vo1 = h ? 0x80000000u : vo0;
        PIN(pa0); PIN(pa1); PIN(pb0); PIN(pb1); PIN(ka2); PIN(kb2); PIN(vo0); PIN(vo1);
#define SLOT(R, MF, X, S, i, PP, jp, P, j) do { R = MF; PIN(R); SBAR(); X; SBAR(); \
        float e0 = EX2(S[i]), e1 = EX2(S[i + 1]); PIN(e0); PIN(e1); SBAR(); \
        PP[jp] = pk2(e2, e3); PIN(PP); SBAR(); \
        e2 = EX2(S[i + 2]); e3 = EX2(S[i + 3]); PIN(e2); PIN(e3); SBAR(); \
        P[j] = pk2(e0, e1); PIN(P); SBAR(); } while (0)
#define STEP(KC1, KC2, KN1, KN2, VC0, VC1, VP0, VP1, OK, OV, X0, X7) do { \
        SLOT(sb, mfma32k8(KC2, qb2, zero16), X0, sa, 0, pb1, 3, pa0, 0); \
        SLOT(sb, mfma32(KC1, qb1, sb), __builtin_amdgcn_s_waitcnt(0xC07F), sa, 4, pa0, 1, pa0, 2); \
        SLOT(ob, mfma32(VP1, pb1, ob), KC1 = lds16(ak1 + (OK) * KV_TILE), sa, 8, pa0, 3, pa1, 0); \
        SLOT(oa, mfma32(VC0, pa0, oa), KC2[0] = lds4(ak2 + (OK) * KV_TILE), sa, 12, pa1, 1, pa1, 2); \
        SLOT(sa, mfma32k8(KN2, qa2, zero16), (void)0, sb, 0, pa1, 3, pb0, 0); \
        SLOT(sa, mfma32(KN1, qa1, sa), VP0 = lds16(av + (OV) * KV_TILE), sb, 4, pb0, 1, pb0, 2); \
        SLOT(oa, mfma32(VC1, pa1, oa), VP1 = lds16(av + (OV) * KV_TILE + VS), sb, 8, pb0, 3, pb1, 0); \
        SLOT(ob, mfma32(VC0, pb0, ob), X7, sb, 12, pb1, 1, pb1, 2); } while (0)
        unsigned so1 = WS_O + (tb * 4 + (w & 2)) * 20480, so0 = so1 + (2 * m + (w & 1)) * 1024;
        so1 += 16384 + (2 * m + (w & 1)) * 256;
        asm volatile("" : "+s"(so0), "+s"(so1));
        unsigned ea = CN2(E_C1), ev = 0, one = 1;
        PIN(ea); PIN(one); PIN(ak1); PIN(ak2); PIN(av);
        auto hook0 = [&](int j, int dn) {
            if (j == 14) { ak1 += dn; ak2 += dn; PIN(ak1); PIN(ak2); }
            if (j == 15) { av += dn; ea += 4; PIN(av); PIN(ea); }
        };
        auto hook7 = [&](int j) {
            if (j == 0)
                asm volatile("s_mov_b64 exec, 1\n\tds_add_u32 %0, %1 offset:%2\n\ts_mov_b64 exec, -1" ::"v"(ea), "v"(one), "i"((P_NONE - E_C1) * 4) : "memory");
            if (j == 1 && __builtin_expect((unsigned)__builtin_amdgcn_readfirstlane(ev) < 4u, 0)) cnt_spin(CN2(E_C0), 4);
            if (j == 9) ev = *(volatile LAS unsigned*)(size_t)ea;
            if (j == 10 && __builtin_expect((unsigned)__builtin_amdgcn_readfirstlane(ev) < 4u, 0))
                { cnt_wait(ea, 4); ev = 4; }
        };
        STAMP(5);
#pragma unroll 1
        for (int c = 0; c < 4; ++c) {
            const int dn = c == 2 ? -2 * CHUNK : CHUNK;
#pragma unroll
            for (int j = 0; j < 16; j += 2) {
                STEP(ka1, ka2, kb1, kb2, va0, va1, vb0, vb1, (j + 2) & 15, j + 1, hook0(j, dn), hook7(j));
                STEP(kb1, kb2, ka1, ka2, vb0, vb1, va0, va1, (j + 3) & 15, (j + 2) & 15, hook0(j + 1, dn), hook7(j + 1));
            }
        }
        pb1[3] = pk2(e2, e3);
        ob = mfma32(vb1, pb1, ob);
        STAMP(6);
        u32x4 r0, r1, r2;
        auto finish = [&](const f32x16& pa, const f32x16& pb, float la, float lb) {
            const float ra = __builtin_amdgcn_rcpf(la), rb = __builtin_amdgcn_rcpf(lb);
            r0 = u32x4{keep(pk2(pa[0] * ra, pa[1] * ra)), pk2(pa[2] * ra, pa[3] * ra), keep(pk2(pa[4] * ra, pa[5] * ra)), pk2(pa[6] * ra, pa[7] * ra)};
            r1 = u32x4{keep(pk2(pb[0] * rb, pb[1] * rb)), pk2(pb[2] * rb, pb[3] * rb), keep(pk2(pb[4] * rb, pb[5] * rb)), pk2(pb[6] * rb, pb[7] * rb)};
            r2 = u32x4{keep(pk2(pa[8] * ra, pa[9] * ra)), pk2(pa[10] * ra, pa[11] * ra), keep(pk2(pb[8] * rb, pb[9] * rb)), pk2(pb[10] * rb, pb[11] * rb)};
        };
        {
            float am = 0.f;
#pragma unroll
            for (int i = 0; i < 12; i += 2) am = amax3(am, oa[i], oa[i + 1]);
            const float la = upper(oa[8]);
            asm volatile("s_nop 7" : "+v"(ob));
            const float lb = upper(ob[8]);
#pragma unroll
            for (int i = 0; i < 12; i += 2) am = amax3(am, ob[i], ob[i + 1]);
            finish(oa, ob, la, lb);
            const bool ok = (fminf(la, lb) >= 1e-18f) & (fmaxf(la, lb) <= 1e18f) & (am <= 1e37f);
            if (__builtin_expect(__ballot(!ok) != 0, 0)) {
                __builtin_amdgcn_fence(__ATOMIC_ACQUIRE, "agent");
                f32x16 xa, xb;
#pragma unroll 1
                for (int qt = 0; qt < 2; ++qt) {
                    xb = attn_exact(ws + WS_KV, qt ? qb1 : qa1, qt ? qb2[0] : qa2[0], lane, vrow);
                    if (qt == 0) xa = xb;
                }
                finish(xa, xb, upper(xa[8]), upper(xb[8]));
            }
        }
        bst16(rs, r0, vo0, so0);
        bst16(rs, r1, vo0 + 512, so0);
        bst16(rs, r2, vo1, so1);
        STAMP(7);
    }
    u32x4 wd[12], ae[RE][2], we[PE];
    unsigned ge[9], e0, e1, ehv, eprobe, a0;
    const unsigned ehs = WS_F + (tb * 4 + (w & 2)) * 55296 + 16384;
    {
        LANE_VARS();
        const unsigned wl = L + lane * 16;
        f32x4 acc[2][5];
        u32x4 act[RC][2], wf[10];
        unsigned wa = wl + L_WO + kh * 5120, wb = wa + 51200, gb[5];
        rot_bases(gb, WS_O + tb * 81920 + kh * 1024, rotc, 4096);
        unsigned v0 = lane * 16 + w * 20480, v1 = lane * 16 + (w ^ 1) * 20480, hv = c16 * 256 + (g & 1) * 20480;
        unsigned xs = wl + (w == 2 ? 35840 : XCH + (w ^ 1) * 5120), xv = wl + (w == 3 ? 35840 : XCH + w * 5120);
        unsigned sv = g == 0 ? c16 * 8 : 0x80000000u, h2v = (g & 1) ? 0x80000000u : (g >> 1) * 256 + c16 * 16;
        PIN(v0); PIN(v1); PIN(hv);
        kstart<RC, 2, 2048>(act, rs, ws, v0, v1, gb,
                           [&] { return __builtin_amdgcn_raw_buffer_load_b32(rs, hv, WS_O + (tb * 4 + (w & 2)) * 20480 + 16384, SC1) == EMPTY; }, [&] {
            bar();
            PIN(wa); PIN(wb); PIN(xs); PIN(xv); PIN(sv); PIN(h2v);
#pragma unroll
            for (int n = 0; n < 10; ++n) { wf[n] = lds16(wa + (n / 5) * 10240 + (n % 5) * 1024); SBAR(); }
            res_init(acc[0], xr0, xr1, xr2); zero_acc(acc[1]);
            if (DRC < 0) wait_vm<0>(); else __builtin_amdgcn_s_sleep(DRC < 0 ? 0 : DRC);
        }, [&] {
        });
        STAMP(8);
        kloop<0, 10, 10, 0, 50, 5, 5, RC, 2, 2048>(acc, act, wf, [&](int n) { return (n < 25 ? wa : wb) + (n / 5 % 5) * 10240 + (n % 5) * 1024; }, rs, ws, v0, v1, gb);
        mma_done();
        send_half(acc[1], xs);
        bar();
        recv_half(acc[0], xv);
#pragma unroll
        for (int n = 0; n < 12; ++n) wd[n] = lds16(wl + (n % 7 < 5 ? 16384 + (n / 7) * 5120 + (n % 7) * 1024 : (n / 7) * 2048 + (n % 7 - 5) * 1024));
        SBAR();
        res_pack(xr0, xr1, xr2, acc[0]);
        xr0[0] = keep(xr0[0]); xr0[2] = keep(xr0[2]); xr1[0] = keep(xr1[0]); xr1[2] = keep(xr1[2]); xr2[0] = keep(xr2[0]);
        const unsigned hb = WS_H + (tb * 4 + w) * 20480;
        bst16(rs, xr0, lane * 16, hb + 2 * m * 1024);
        bst16(rs, xr1, lane * 16, hb + (2 * m + 1) * 1024);
        bst16(rs, rowpair(xr2, xr2), h2v, hb + 16384 + m * 512);
#pragma unroll
        for (int n = 0; n < 12; ++n) PIN(wd[n]);
        cnt_add(CN2(M_LDS));
        float sm = 0.f, sq = 0.f, sm2 = 0.f, sq2 = 0.f;
#pragma unroll
        for (int i = 0; i < 4; ++i) { sm = dot2(xr0[i], ONES2, sm); sq = dot2(xr0[i], xr0[i], sq); sm2 = dot2(xr1[i], ONES2, sm2); sq2 = dot2(xr1[i], xr1[i], sq2); }
        sm = dot2(xr2[0], ONES2, sm); sq = dot2(xr2[0], xr2[0], sq); sm2 = dot2(xr2[1], ONES2, sm2); sq2 = dot2(xr2[1], xr2[1], sq2);
        sm = xsum16_32(sm + sm2); sq = xsum16_32(sq + sq2);
        bst8(rs, u32x2{keep(__float_as_uint(sm)), __float_as_uint(sq)}, sv, WS_ST + (tb * 4 + w) * 1024 + m * 128);
        SBAR();
        STAMP(9);
    }
    {
        f32x4 acc[2][7], c4[7];
        u32x4 act[12][2];
        u32x2 st[4];
        float mu[2], rp[2];
        const int t16 = (tb >> 1) * 8 + 2 * w;
        unsigned v0 = lane_id() * 16, v1 = v0 + 20480, sv = v0 >> 1, wa = L + v0, wb = wa + 16384 + 46080, gb[5];
        PIN(sv); PIN(wa); PIN(wb); PIN(v0); PIN(v1);
        zero_acc(acc[0]); zero_acc(acc[1]);
        rot_bases(gb, WS_H + t16 * 20480, rotd, 4096);
        e_bases(ge, WS_F + tb * 221184 + kh * 1024, m, 6144);
        unsigned fs[7], wc, fw, f2v, ca;
        {
            const unsigned fb = WS_F + t16 * 55296 + ct * 1024;
            fs[0] = fb; fs[1] = fb + 55296; fs[2] = fb + 16384 - ct * 512; fs[3] = fb + 24576; fs[4] = fb + 24576 + 55296; fs[5] = fb + 39936; fs[6] = fb + 39936 + 55296;
#pragma unroll
            for (int i = 0; i < 7; ++i) asm volatile("" : "+s"(fs[i]));
            const int lane = v0 >> 4, g = lane >> 4, c16 = lane & 15;
            f2v = (g & 1) * 55296 + (g >> 1) * 256 + c16 * 16; ca = L + L_MISC + g * 16;
            ehv = (g & 1) * 55296 + c16 * 512;
            wc = wa + 118784;
            PIN(f2v); PIN(ca); PIN(ehv); PIN(wc);
        }
        kstart<RD, 4, 1024>(act, rs, ws, v0, v1, gb, [&] {
#pragma unroll
            for (int i = 0; i < 4; ++i) st[i] = __builtin_amdgcn_raw_buffer_load_b64(rs, sv + i * 512, WS_ST + t16 * 1024, SC1);
            return max(max(st[0][0], st[1][0]), max(st[2][0], st[3][0])) == EMPTY; }, [&] { if (DRD < 0) wait_vm<0>(); else __builtin_amdgcn_s_sleep(DRD < 0 ? 0 : DRD); }, [&] {
#pragma unroll
            for (int t = 0; t < 2; ++t) {
                const float sm = xsum16_32(__uint_as_float(st[2 * t][0]) + __uint_as_float(st[2 * t + 1][0]));
                const float sq = xsum16_32(__uint_as_float(st[2 * t][1]) + __uint_as_float(st[2 * t + 1][1]));
                mu[t] = sm * (-1.f / D);
                rp[t] = __builtin_amdgcn_rsqf(fmaxf(sq * (1.f / D) - mu[t] * mu[t], 0.f) + LN_EPS) * LOG2E;
                PIN(mu[t]); PIN(rp[t]);
            } });
        STAMP(10);
        auto wad = [&](int n) {
            const int j = n < 56 ? n / 7 : n < 86 ? 8 + (n - 56) / 5 : n < 98 ? 14 + (n - 86) / 2 : 14 + (n - 98) / 3;
            const int s = n < 56 ? n % 7 : n < 86 ? (n - 56) % 5 : n < 98 ? (n - 86) % 2 : 2 + (n - 98) % 3;
            return n >= 116 ? wc + (n - 116) * 1024 : s >= 5 ? wa + j * 2048 + (s - 5) * 1024 : (j < 9 ? wa + 16384 + j * 5120 : wb + (j - 9) * 5120) + s * 1024; };
        kloop<0, 1, 20, 0, 140, 7, 7, RD, 4, 1024>(acc, act, wd, wad, rs, ws, v0, v1, gb);
        bar_raw();
        kloop<1, 7, 20, 7, 140, 7, 5, RD, 4, 1024>(acc, act, wd, wad, rs, ws, v0, v1, gb);
        kloop<8, 1, 20, 56, 140, 5, 5, RD, 4, 1024>(acc, act, wd, wad, rs, ws, v0, v1, gb);
        asm volatile("s_mov_b64 exec, 1\n\tds_add_u32 %0, %1\n\ts_mov_b64 exec, -1" ::"v"(CN2(M_K8)), "v"(1u) : "memory");
        kloop<9, 5, 20, 61, 140, 5, 2, RD, 4, 1024>(acc, act, wd, wad, rs, ws, v0, v1, gb);
        c4[0] = ldsf4(ca); c4[1] = ldsf4(ca + 64);
        SBAR();
        kloop<14, 6, 20, 86, 140, 2, 4, RD, 4, 1024>(acc, act, wd, wad, rs, ws, v0, v1, gb);
        STAMP(11);
        u32x2 lo, hi;
        auto silu = [&](const f32x4& a, const f32x4& c, int t, u32x2& r, auto side) {
            f32x4 y = (a + mu[t] * c) * rp[t], e;
            PIN(y); SBAR();
#pragma unroll
            for (int i = 0; i < 4; ++i) { side(i); SBAR(); e[i] = EX2(-y[i]); PIN(e); SBAR(); }
            e = e * LOG2E + LOG2E; PIN(e); SBAR();
#pragma unroll
            for (int i = 0; i < 4; ++i) { side(4 + i); SBAR(); e[i] = __builtin_amdgcn_rcpf(e[i]); PIN(e); SBAR(); }
            e *= y;
            r = u32x2{keep(pk2(e[0], e[1])), pk2(e[2], e[3])};
            PIN(r); SBAR();
        };
        auto rest = [&](int q) {
            const int n = 98 + (q >> 1), j = n < 116 ? 14 + (n - 98) / 3 : 8 + (n - 116) / 2, s = n < 116 ? 2 + (n - 98) % 3 : 5 + (n - 116) % 2;
            mma16(acc[q & 1][s], wd[n % 12], act[j % 12][q & 1]);
            SBAR();
            if ((q & 1) && n + 12 < 140) wd[n % 12] = lds16(wad(n + 12));
            if (q >= 24 && q < 74 && q % 10 == 4) c4[q == 24 ? 6 : q / 10 - 1] = ldsf4(ca + (q == 24 ? 6 : q / 10 - 1) * 64);
        };
        auto early = [&](int q) {
            if (q == 0) eprobe = __builtin_amdgcn_raw_buffer_load_b32(rs, ehv, ehs, SC1);
            ae[q >> 1][q & 1] = aload<3, 2048>(rs, (q & 1) ? e1 : e0, ge, q >> 1);
        };
#pragma unroll
        for (int q = 0; q < 8; ++q) { rest(q); SBAR(); }
#pragma unroll
        for (int b = 0; b < 10; ++b) {
            const int ut = b < 4 ? b >> 1 : b < 6 ? b & 1 : (b - 6) >> 1, ui = b < 4 ? b & 1 : b < 6 ? 2 : 3 + (b & 1), us = ui < 2 ? ui : ui == 2 ? 6 : ui - 1;
            silu(acc[ut][ui], c4[us], ut, (b & 1) ? hi : lo, [&](int i) { if (8 + b * 8 + i < 84) rest(8 + b * 8 + i); });
            if (b == 5) bst16(rs, rowpair(lo, hi), f2v, fs[2]);
            else if (b & 1) bst16(rs, u32x4{lo[0], lo[1], hi[0], hi[1]}, b < 5 ? v0 : fw, fs[b >> 1]);
            if (b == 1) cnt_add(CN2(M_T4));
            if (b == 1) {   } if (b == 3) {   } if (b == 5) {   } if (b == 7) {   } if (b == 9) {   }
            if (b == 5) {
                PIN(v0);
                fw = ct < 15 ? v0 : 0x80000000u; e0 = v0 + w * 55296; e1 = v0 + (w ^ 1) * 55296; a0 = L + v0 + kh * 5120;
                PIN(fw); PIN(e0); PIN(e1); PIN(a0);
            }
            SBAR();
        }
        bar_raw();
        mma_done();
#pragma unroll
        for (int n = 0; n < PE; ++n) we[n] = lds16(a0 + (n / 5) * 10240 + (n % 5) * 1024);
#pragma unroll
        for (int t = 0; t < 2; ++t) {
            u32x4 r;
#pragma unroll
            for (int u = 0; u < 2; ++u) {
                const f32x4 y = (acc[t][5 + u] + mu[t] * c4[4 + u]) * rp[t];
                f32x4 e = f32x4{EX2(-y[0]), EX2(-y[1]), EX2(-y[2]), EX2(-y[3])} * LOG2E + LOG2E;
                e = f32x4{__builtin_amdgcn_rcpf(e[0]), __builtin_amdgcn_rcpf(e[1]), __builtin_amdgcn_rcpf(e[2]), __builtin_amdgcn_rcpf(e[3])} * y;
                r[2 * u] = keep(pk2(e[0], e[1])); r[2 * u + 1] = pk2(e[2], e[3]);
            }
            bst16(rs, r, fw, fs[5 + t]);
        }
        SBAR();
#pragma unroll
        for (int q = 0; q < 2 * FIRSTFILL; ++q) { early(q); SBAR(); }
        STAMP(12);
    }
    {
        f32x4 acc[2][5];
        unsigned a1 = a0 + 61440, a2 = a0 + 122880;
        PIN(a1); PIN(a2);
        res_init(acc[0], xr0, xr1, xr2); zero_acc(acc[1]);
        if (__any(eprobe == EMPTY))
            kstart<RE, 3, 2048>(ae, rs, ws, e0, e1, ge, [&] { return __builtin_amdgcn_raw_buffer_load_b32(rs, ehv, ehs, SC1) == EMPTY; }, [] {}, [] {});
        if (__builtin_expect(missing(ae[0][0], ae[0][1]), 0)) refetch(ae[0][0], ae[0][1], ws + ge[0], e0, e1);
        SBAR();
        STAMP(13);
        auto wae = [&](int n) { const int q = n / 5 % 15; return (q < 6 ? a0 : q < 12 ? a1 : a2) + (q % 6) * 10240 + (n % 5) * 1024; };
#define GRP(G, WA, WN) kloop<3 * G, 3, 27, 15 * G, 135, 5, 5, RE, 3, 2048>(acc, ae, we, wae, rs, ws, e0, e1, ge)
        GRP(0, a0, a0 + 30720);
        bar_raw();

        GRP(1, a0 + 30720, a1);
        bar_raw();
        GRP(2, a1, a1 + 30720);
        bar_raw();
        GRP(3, a1 + 30720, a2); bar_raw();
        GRP(4, a2, a0); bar_raw();

        GRP(5, a0, a0 + 30720); bar_raw();
        GRP(6, a0 + 30720, a1); bar_raw();
        GRP(7, a1, a1 + 30720);
        GRP(8, a1 + 30720, a1);
        mma_done();
        LANE_VARS();
        const unsigned wl = L + lane * 16;
        send_half(acc[1], wl + 122880 + (w ^ 1) * 5120);
        bar();
        recv_half(acc[0], wl + 122880 + w * 5120);
        STAMP(14);
        res_pack(xr0, xr1, xr2, acc[0]);
        char* ob = out + (size_t)(tb * 64 + w * 16 + c16) * 1280 + m * 160;
        asm volatile("global_store_dwordx4 %0, %1, off nt\n\tglobal_store_dwordx4 %0, %2, off offset:64 nt\n\t"
                     "global_store_dwordx2 %3, %4, off offset:128 nt\n\ts_nop 1" ::"v"(ob + g * 16), "v"(xr0), "v"(xr1), "v"(ob + g * 8), "v"(xr2) : "memory");
    }
    asm volatile(".Lpf_end1:");
    STAMP_OUT(0);
    __builtin_amdgcn_endpgm();
}

extern "C" void kernel_launch(void* const* d_in, const int* in_sizes, int n_in, void* d_out, int out_size, void* d_ws, size_t ws_size,
                              hipStream_t stream) {
    (void)hipFuncSetAttribute((const void*)k_block, hipFuncAttributeMaxDynamicSharedMemorySize, LDS_BYTES);
    k_block<<<256, 512, LDS_BYTES, stream>>>((const char*)d_in[0], (const char*)d_in[1], (const char*)d_in[2], (const char*)d_in[3],
                                             (const char*)d_in[4], (const char*)d_in[6], (const char*)d_in[7], (char*)d_ws, (char*)d_out);
}
```

```cpp
#include <hip/hip_runtime.h>
#include <hip/hip_bf16.h>
#include <stdint.h>

typedef __attribute__((ext_vector_type(8))) __bf16 bf16x8;
typedef __attribute__((ext_vector_type(2))) __bf16 bf16x2;
typedef __attribute__((ext_vector_type(4))) short s16x4;
typedef __attribute__((ext_vector_type(4))) float f32x4;
typedef __attribute__((ext_vector_type(2))) float f32x2;
typedef __attribute__((ext_vector_type(16))) float f32x16;
typedef __attribute__((ext_vector_type(4))) unsigned u32x4;
typedef __attribute__((ext_vector_type(2))) unsigned u32x2;
typedef unsigned long long u64;
#define LAS __attribute__((address_space(3)))
#define GAS __attribute__((address_space(1)))
#define DI __device__ __forceinline__

constexpr int D = 640;
constexpr float LN_EPS = 1e-5f;
constexpr float LOG2E = 1.4426950408889634f;
constexpr float QSCALE = 0.22360679774997896f * LOG2E;

constexpr int KV_TILE = 2688;
constexpr int VS = 704;
constexpr unsigned WS_FL = 0;
constexpr unsigned FL_KV = 32768, FL_PR = 2 * 32768, FL_MK = 3 * 32768;
constexpr unsigned WS_KV = 4 * 32768;
constexpr unsigned WS_WO = WS_KV + 64 * KV_TILE;
constexpr unsigned WS_W1 = WS_WO + 8 * 20 * 5 * 1024;
constexpr unsigned WS_W2 = WS_W1 + 16 * 20 * 7 * 1024;
constexpr unsigned WS_CS = WS_W2 + 8 * 54 * 5 * 1024;
constexpr unsigned WS_O  = WS_CS + 20 * 1792 * 4;
constexpr unsigned WS_H  = WS_O + 128 * 20 * 1024;
constexpr unsigned WS_F  = WS_H + 128 * 20 * 1024;
constexpr unsigned WS_ST = WS_F + 128 * 54 * 1024;
constexpr unsigned WS_END = WS_ST + 2048 * 64;
constexpr unsigned EMPTY = 0xffffffffu;
static_assert((WS_END - WS_O) == 256 * 48640, "fill share per workgroup");

constexpr int CHUNK = 16 * KV_TILE;
constexpr int L_X = 0, L_WQ2 = 43008, L_WKV = 83968, L_WQ1 = 122880,
               L_CSX = 111104, L_QX = 152576, L_MISC = 162816;
constexpr int L_AUX = 3 * CHUNK, L_WO = CHUNK, L_CNT = 110592, L_JUNK = 113664, XCH = L_WO + 102400, LDS_BYTES = 163840;

#define STAMP_INIT()
#define STAMP(I)
#define STAMP_OUT(K)

#define THROT 3
#define THROT2 3
#define RC 6
#define RD 7
#define RE 7
#define TAILPRIO 0
#define FIRSTFILL 4
#define ONEWAY 1
#define B0AT 0
#define NEARLY 12
#define PFDELAY 6
#define WKVAT 2
#define FLAGAT 0
#define P1PRIO 1
#define DRC -1
#define DRD 0
#define PE 10
#define NOST 0
#define LONGNAP 10
DI void glds(unsigned voff, const void* sbase, unsigned lds) {
    asm volatile("s_mov_b32 m0, %2\n\ts_nop 0\n\tglobal_load_lds_dwordx4 %0, %1" ::"v"(voff), "s"(sbase), "s"(lds) : "memory");
}
DI void glds_dev(unsigned voff, const void* sbase, unsigned lds) {
    asm volatile("s_mov_b32 m0, %2\n\ts_nop 0\n\tglobal_load_lds_dwordx4 %0, %1 sc1" ::"v"(voff), "s"(sbase), "s"(lds) : "memory");
}
DI void touch(unsigned voff, const void* sbase, unsigned lds, u64 mask) {
    asm volatile("s_mov_b32 m0, %2\n\ts_mov_b64 exec, %3\n\tglobal_load_lds_dword %0, %1\n\ts_mov_b64 exec, -1" ::"v"(voff), "s"(sbase), "s"(lds), "s"(mask) : "memory");
}
template <int N> DI void wait_vm() { asm volatile("s_waitcnt vmcnt(%0)" ::"i"(N) : "memory"); }
DI void bar() { asm volatile("s_waitcnt lgkmcnt(0)\n\ts_barrier" ::: "memory"); }
DI void bar_raw() { asm volatile("s_barrier" ::: "memory"); }
#define PIN(x) asm volatile("" : "+v"(x))
#define SBAR() __builtin_amdgcn_sched_barrier(0)
DI unsigned pk2(float a, float b) { bf16x2 v = {(__bf16)a, (__bf16)b}; return __builtin_bit_cast(unsigned, v); }
DI float bflo(unsigned u) { return __uint_as_float(u << 16); }
DI float bfhi(unsigned u) { return __uint_as_float(u & 0xffff0000u); }
DI f32x4 mfma16(u32x4 a, u32x4 b, f32x4 c) {
    return __builtin_amdgcn_mfma_f32_16x16x32_bf16(__builtin_bit_cast(bf16x8, a), __builtin_bit_cast(bf16x8, b), c, 0, 0, 0);
}
DI f32x16 mfma32(u32x4 a, u32x4 b, f32x16 c) {
    return __builtin_amdgcn_mfma_f32_32x32x16_bf16(__builtin_bit_cast(bf16x8, a), __builtin_bit_cast(bf16x8, b), c, 0, 0, 0);
}
DI f32x16 mfma32k8(u32x2 a, u32x2 b, f32x16 c) {
    return __builtin_amdgcn_mfma_f32_32x32x8bf16_1k(__builtin_bit_cast(s16x4, a), __builtin_bit_cast(s16x4, b), c, 0, 0, 0);
}
DI u32x4 lds16(unsigned a) { return *(const LAS u32x4*)(size_t)a; }
DI u32x2 lds8(unsigned a) { return *(const LAS u32x2*)(size_t)a; }
DI unsigned lds4(unsigned a) { return *(const LAS unsigned*)(size_t)a; }
DI f32x4 ldsf4(unsigned a) { return *(const LAS f32x4*)(size_t)a; }
DI u32x2 ldstr(unsigned a) { return __builtin_bit_cast(u32x2, __builtin_amdgcn_ds_read_tr16_b64_v4i16((LAS s16x4*)(size_t)a)); }
DI u32x4 ldstr2(unsigned a0, unsigned a1) { u32x2 lo = ldstr(a0), hi = ldstr(a1); return u32x4{lo[0], lo[1], hi[0], hi[1]}; }
DI float dot2(unsigned a, unsigned b, float c) {
    return __builtin_amdgcn_fdot2_f32_bf16(__builtin_bit_cast(bf16x2, a), __builtin_bit_cast(bf16x2, b), c, false);
}
DI float xsum16_32(float v) {
    const auto a = __builtin_amdgcn_permlane16_swap(__float_as_uint(v), __float_as_uint(v), false, false);
    v = __uint_as_float(a[0]) + __uint_as_float(a[1]);
    const auto b = __builtin_amdgcn_permlane32_swap(__float_as_uint(v), __float_as_uint(v), false, false);
    return __uint_as_float(b[0]) + __uint_as_float(b[1]);
}
constexpr unsigned ONES2 = 0x3f803f80u;
#define EX2(v) __builtin_amdgcn_exp2f(v)
typedef __amdgpu_buffer_rsrc_t rsrc_t;
constexpr int SC1 = 16;
DI u32x4 bld16(rsrc_t r, unsigned v, unsigned s) { return __builtin_amdgcn_raw_buffer_load_b128(r, v, s, SC1); }
DI void bst16(rsrc_t r, u32x4 d, unsigned v, unsigned s) {
    __builtin_amdgcn_raw_buffer_store_b128(d, r, v, s, SC1);
    asm volatile("s_nop 1" ::"v"(d) : "memory");
}
DI void bst8(rsrc_t r, u32x2 d, unsigned v, unsigned s) { __builtin_amdgcn_raw_buffer_store_b64(d, r, v, s, SC1); }

DI void set_flag(char* ws, unsigned off, u64 tag) { __hip_atomic_store((GAS u64*)(ws + off), tag, __ATOMIC_RELAXED, __HIP_MEMORY_SCOPE_AGENT); }
DI void poll(char* ws, unsigned arr, int index, u64 tag) {
    const GAS u64* p = (const GAS u64*)(ws + arr + index * 128);
    bool ok = index < 0;
#pragma unroll 1
    for (int spin = 0; spin < (1 << 18); ++spin) {
        if (!ok) ok = __hip_atomic_load(p, __ATOMIC_RELAXED, __HIP_MEMORY_SCOPE_AGENT) == tag;
        if (__all(ok)) break;
        __builtin_amdgcn_s_sleep(1);
    }
    asm volatile("" ::: "memory");
}
DI void copy_dev(const char* src, unsigned lds, int n, int hw, int lane) {
#pragma unroll 1
    for (int p = hw; p < n; p += 4) glds_dev(lane * 16, src + p * 1024, lds + p * 1024);
}

DI void cnt_add(unsigned a) {
    asm volatile("s_waitcnt lgkmcnt(0)\n\ts_mov_b64 exec, 1\n\tds_add_u32 %0, %1\n\ts_mov_b64 exec, -1" ::"v"(a), "v"(1u) : "memory");
}
DI unsigned cnt_arrive(unsigned a) {
    unsigned r;
    asm volatile("s_waitcnt lgkmcnt(0)\n\ts_mov_b64 exec, 1\n\tds_add_rtn_u32 %0, %1, %2\n\ts_mov_b64 exec, -1\n\ts_waitcnt lgkmcnt(0)"
                 : "=&v"(r) : "v"(a), "v"(1u) : "memory");
    return __builtin_amdgcn_readfirstlane(r);
}
DI void cnt_spin(unsigned a, unsigned n) {
#pragma unroll 1
    while ((unsigned)__builtin_amdgcn_readfirstlane(*(volatile LAS unsigned*)(size_t)a) < n) {}
    asm volatile("" ::: "memory");
}
template <int NAP = 1> DI void cnt_wait(unsigned a, unsigned n) {
#pragma unroll 1
    while ((unsigned)__builtin_amdgcn_readfirstlane(*(volatile LAS unsigned*)(size_t)a) < n) __builtin_amdgcn_s_sleep(NAP);
    asm volatile("" ::: "memory");
}
DI unsigned keep(unsigned v) { return min(v, EMPTY - 1); }
DI bool missing(const u32x4& a, const u32x4& b) { return __any(max(max(a[0], a[2]), max(b[0], b[2])) == EMPTY); }
DI void refetch(u32x4& a, u32x4& b, const char* base, unsigned v0, unsigned v1) {
#pragma unroll 1
    for (int spin = 0; spin < (1 << 16); ++spin) {
        asm volatile("global_load_dwordx4 %0, %2, %4 sc1\n\tglobal_load_dwordx4 %1, %3, %4 sc1\n\ts_waitcnt vmcnt(0)"
                     : "+v"(a), "+v"(b) : "v"(v0), "v"(v1), "s"(base) : "memory");
        if (!missing(a, b)) break;
    }
}
DI u32x4 rowpair(const u32x2& a, const u32x2& b) {
    const auto s0 = __builtin_amdgcn_permlane16_swap(a[0], b[0], false, false), s1 = __builtin_amdgcn_permlane16_swap(a[1], b[1], false, false);
    return u32x4{s0[0], s1[0], s0[1], s1[1]};
}
DI int lane_id() {
    int l;
    asm volatile("v_mbcnt_lo_u32_b32 %0, -1, 0\n\tv_mbcnt_hi_u32_b32 %0, -1, %0" : "=v"(l));
    return l;
}
#define LANE_VARS() const int lane = lane_id(), g = lane >> 4, c16 = lane & 15; (void)g; (void)c16
DI float halfmax(float m) {
    auto r = __builtin_amdgcn_permlane32_swap(__float_as_uint(m), __float_as_uint(m), false, false);
    return fmaxf(__uint_as_float(r[0]), __uint_as_float(r[1]));
}
DI float upper(float v) {
    auto r = __builtin_amdgcn_permlane32_swap(__float_as_uint(v), __float_as_uint(v), false, false);
    return __uint_as_float(r[1]);
}
DI float max16(const f32x16& s) {
    float a = fmaxf(fmaxf(s[0], s[1]), s[2]), b = fmaxf(fmaxf(s[3], s[4]), s[5]);
    a = fmaxf(fmaxf(a, s[6]), s[7]); b = fmaxf(fmaxf(b, s[8]), s[9]);
    a = fmaxf(fmaxf(a, s[10]), s[11]); b = fmaxf(fmaxf(b, s[12]), s[13]);
    a = fmaxf(fmaxf(a, s[14]), s[15]);
    return fmaxf(a, b);
}
DI float amax3(float m, float a, float b) {
    float r;
    asm("v_maximum3_f32 %0, %1, |%2|, |%3|" : "=v"(r) : "v"(m), "v"(a), "v"(b));
    return r;
}
DI f32x16 attn_exact(const char* kv, u32x4 q1, unsigned q2lo, int lane, int vrow) {
    const int r32 = lane & 31, h = lane >> 5;
    const f32x16 zero16 = {0, 0, 0, 0, 0, 0, 0, 0, 0, 0, 0, 0, 0, 0, 0, 0};
    const u32x2 q2 = {q2lo, 0u};
    float m = -INFINITY;
    f32x16 o = zero16;
#pragma unroll 1
    for (int t = 0; t < 64; ++t) {
        const GAS char* tp = (const GAS char*)kv + t * KV_TILE;
        const u32x4 fk1 = *(const GAS u32x4*)(tp + lane * 16);
        const u32x2 fk2 = {*(const GAS unsigned*)(tp + 1024 + r32 * 8 + h * 4), 0u};
        const u32x4 fv0 = *(const GAS u32x4*)(tp + 1280 + (h * 22 + vrow) * 16), fv1 = *(const GAS u32x4*)(tp + 1280 + VS + (h * 22 + vrow) * 16);
        f32x16 s = mfma32(fk1, q1, mfma32k8(fk2, q2, zero16));
        const float mn = fmaxf(m, halfmax(max16(s))), al = EX2(m - mn);
        m = mn;
        u32x4 p0, p1;
#pragma unroll
        for (int i = 0; i < 4; ++i) { p0[i] = pk2(EX2(s[2 * i] - m), EX2(s[2 * i + 1] - m)); p1[i] = pk2(EX2(s[8 + 2 * i] - m), EX2(s[9 + 2 * i] - m)); }
#pragma unroll
        for (int i = 0; i < 16; ++i) o[i] *= al;
        o = mfma32(fv1, p1, mfma32(fv0, p0, o));
    }
    return o;
}

DI void mma16(f32x4& c, const u32x4& a, const u32x4& b) {
    asm volatile("v_mfma_f32_16x16x32_bf16 %0, %1, %2, %0" : "+v"(c) : "v"(a), "v"(b));
}
DI void mma16z(f32x4& c, const u32x4& a, const u32x4& b) { asm volatile("v_mfma_f32_16x16x32_bf16 %0, %1, %2, 0" : "=&v"(c) : "v"(a), "v"(b)); }
DI void mma_done() { SBAR(); asm volatile("s_nop 7\n\ts_nop 1"); SBAR(); }
template <int NS> DI void zero_acc(f32x4 (&a)[NS]) {
#pragma unroll
    for (int s = 0; s < NS; ++s) { a[s] = f32x4{0, 0, 0, 0}; PIN(a[s]); }
}
template <int GS, int ASTR, int NG> DI u32x4 aload(rsrc_t rs, unsigned& v, const unsigned (&gb)[NG], int j) {
    const int o = (j % GS) * ASTR;
    PIN(v);
    return bld16(rs, v + (o & 4095), gb[j / GS] + (o & ~4095));
}
constexpr int areq(int j, int PD, int KT) {
    const int a = FIRSTFILL + 2 * j, b = PD + (j > 0 ? j - 1 : 0), c = a < b ? a : b;
    return c < KT ? c : KT;
}
template <int N> struct Int { static constexpr int v = N; };
template <int A, int B, class Fn> DI void static_for(Fn fn) { if constexpr (A < B) { fn(Int<A>{}); static_for<A + 1, B>(fn); } }
template <int N> DI void lgkm() { __builtin_amdgcn_s_waitcnt(0xC07F | (N << 8)); }
template <int K0, int NK, int KT, int F0, int FT, int NS, int NSN, int PD, int GS, int ASTR, int NA, int RING, int POOL, int NG, class WAddr, class Side>
DI void kloop(f32x4 (&acc)[2][NA], u32x4 (&act)[RING][2], u32x4 (&wf)[POOL], WAddr waddr, rsrc_t rs, const char* ws, unsigned& v0,
              unsigned& v1, const unsigned (&gb)[NG], Side side) {
    if constexpr (NK > 0) {
        constexpr int j = K0, q0 = areq(j, PD, KT), q1 = areq(j + 1, PD, KT), jc = j + 1 < KT ? j + 1 : 0, nn = NK > 1 ? NS : NSN;
        constexpr int issued = F0 + NS + POOL < FT ? F0 + NS + POOL : FT, need = F0 + NS + nn < FT ? F0 + NS + nn : FT;
        unsigned t = 0;
        u64 bad = 0;
        if (j == 0) { lgkm<POOL - NS>(); SBAR(); }
#pragma unroll
        for (int i = 0; i < 2 * NS; ++i) {
            const int n = F0 + (i >> 1);
            mma16(acc[i & 1][i >> 1], wf[n % POOL], act[j % RING][i & 1]);
            SBAR();
            if ((i & 1) && n + POOL < FT) wf[n % POOL] = lds16(waddr(n + POOL));
            if (!(i & 1) && q0 + i / 4 < q1) act[(q0 + i / 4) % RING][i / 2 & 1] = aload<GS, ASTR>(rs, (i & 2) ? v1 : v0, gb, q0 + i / 4);
            if (jc) {
                if (i == (NS > 2 ? 2 * NS - 6 : 0)) {
                    t = max(act[jc % RING][1][0], act[jc % RING][1][2]);
                    t = max(max(t, act[jc % RING][0][0]), act[jc % RING][0][2]); PIN(t);
                }
                if (i == (NS > 2 ? 2 * NS - 4 : 1)) { bad = __ballot(t == EMPTY); asm volatile("" : "+s"(bad)); }
                if (i == 2 * NS - 2 && __builtin_expect(bad != 0, 0))
                    refetch(act[jc % RING][0], act[jc % RING][1], ws + gb[jc / GS] + (jc % GS) * ASTR, v0, v1);
            }
            side(j, i);
            if (i == 2 * NS - 1 && need > F0 + NS) { SBAR(); lgkm<issued - need>(); }
            SBAR();
        }
        kloop<K0 + 1, NK - 1, KT, F0 + NS, FT, NS, NSN, PD, GS, ASTR>(acc, act, wf, waddr, rs, ws, v0, v1, gb, side);
    }
}
template <int PD, int GS, int ASTR, int RING, int NG, class Probe, class Before, class After>
DI void kstart(u32x4 (&act)[RING][2], rsrc_t rs, const char* ws, unsigned& v0, unsigned& v1, const unsigned (&gb)[NG], Probe probe,
               Before before, After after) {
    before();
    SBAR();
#pragma unroll 1
    for (int spin = 0; spin < (1 << 16); ++spin) {
        const bool miss = probe();
        SBAR();
#pragma unroll
        for (int k = 0; k < FIRSTFILL; ++k) {
            act[k][0] = aload<GS, ASTR>(rs, v0, gb, k); SBAR();
            act[k][1] = aload<GS, ASTR>(rs, v1, gb, k); SBAR();
        }
        if (!__any(miss)) break;
        asm volatile("" ::: "memory");
    }
    SBAR();
    after();
    SBAR();
    if (__builtin_expect(missing(act[0][0], act[0][1]), 0)) refetch(act[0][0], act[0][1], ws + gb[0], v0, v1);
    SBAR();
}
DI void e_bases(unsigned (&gb)[9], unsigned base, int m, int stride) {
    const int m3 = m % 3;
#pragma unroll
    for (int i = 0; i < 9; ++i) {
        const int q = i < 4 ? (i + m) & 3 : i < 6 ? 4 + ((i + (m >> 2)) & 1) : 6 + (i - 6 + m3 >= 3 ? i - 9 + m3 : i - 6 + m3);
        gb[i] = base + q * stride;
        asm volatile("" : "+s"(gb[i]));
    }
}
DI void d_bases(unsigned (&gb)[5], unsigned base, int ct, int stride) {
    const int c3 = ct % 3;
#pragma unroll
    for (int i = 0; i < 5; ++i) {
        gb[i] = base + (i < 3 ? (i + c3 >= 3 ? i + c3 - 3 : i + c3) : 3 + ((i + 1 + ct) & 1)) * stride;
        asm volatile("" : "+s"(gb[i]));
    }
}
template <int NG> DI void rot_bases(unsigned (&gb)[NG], unsigned base, int rot, int stride) {
#pragma unroll
    for (int i = 0; i < NG; ++i) { const int q = i + rot; gb[i] = base + (q >= NG ? q - NG : q) * stride; asm volatile("" : "+s"(gb[i])); }
}
DI void send_half(const f32x4 (&a)[5], unsigned dst) {
#pragma unroll
    for (int s = 0; s < 5; ++s) *(LAS f32x4*)(size_t)(dst + s * 1024) = a[s];
}
DI void recv_half(f32x4 (&a)[5], unsigned src) {
#pragma unroll
    for (int s = 0; s < 5; ++s) a[s] += ldsf4(src + s * 1024);
}
DI void ldsw16(unsigned a, const u32x4& v) { *(LAS u32x4*)(size_t)a = v; }
DI f32x4 unpk(unsigned r0, unsigned r1) { return f32x4{bflo(r0), bfhi(r0), bflo(r1), bfhi(r1)}; }
DI void res_init(f32x4 (&a)[5], const u32x4& x0, const u32x4& x1, const u32x2& x2) {
    a[0] = unpk(x0[0], x0[1]); a[1] = unpk(x0[2], x0[3]); a[2] = unpk(x1[0], x1[1]); a[3] = unpk(x1[2], x1[3]); a[4] = unpk(x2[0], x2[1]);
#pragma unroll
    for (int s = 0; s < 5; ++s) PIN(a[s]);
}
DI void res_pack(u32x4& x0, u32x4& x1, u32x2& x2, const f32x4 (&a)[5]) {
    x0 = u32x4{pk2(a[0][0], a[0][1]), pk2(a[0][2], a[0][3]), pk2(a[1][0], a[1][1]), pk2(a[1][2], a[1][3])};
    x1 = u32x4{pk2(a[2][0], a[2][1]), pk2(a[2][2], a[2][3]), pk2(a[3][0], a[3][1]), pk2(a[3][2], a[3][3])};
    x2 = u32x2{pk2(a[4][0], a[4][1]), pk2(a[4][2], a[4][3])};
}
enum { C_WKV, C_KFUSED, C_X, C_XUSED, C_WQ, C_CSP = C_WQ + 5, C_CS, C_KVST, C_R1, C_XCH, C_NUM = C_XCH + 2 };
enum { E_C1, E_C2, E_C3, E_MK, P_NONE, P_C0, P_C1, P_C2, E_C0, H_PREP, H_FILL, M_LDS, M_W1A, M_W1, M_K8, M_T4, M_NUM };

__global__ __launch_bounds__(512, 2) void k_block(const char* __restrict__ x, const char* __restrict__ wq, const char* __restrict__ wk,
                                                  const char* __restrict__ wv, const char* __restrict__ wo, const char* __restrict__ w1,
                                                  const char* __restrict__ w2, char* ws, char* __restrict__ out) {
    extern __shared__ __attribute__((aligned(16))) char smem[];
    const unsigned L = (unsigned)(size_t)(LAS char*)smem;
    const int w = __builtin_amdgcn_readfirstlane(threadIdx.x >> 6);
    const int xg = blockIdx.x & 7, idx = blockIdx.x >> 3;
    const int tb = ONEWAY ? xg * 4 + (idx >> 3) : (xg >> 1) * 8 + (idx >> 2), m = ONEWAY ? (idx & 7) : (xg & 1) * 4 + (idx & 3), cid = tb * 8 + m;
    const int ct = (m >> 2) * 8 + (tb & 1) * 4 + (m & 3);
    const rsrc_t rs = __builtin_amdgcn_make_buffer_rsrc(ws, 0, WS_END, 0x00020000);
    const u32x4 ones = {ONES2, ONES2, ONES2, ONES2};
    const f32x4 z4 = {0, 0, 0, 0};
    STAMP_INIT();

#define LOAD_TAG() u64 tag; asm volatile("global_load_dwordx2 %0, %1, %2 sc1" : "=v"(tag) : "v"(0u), "s"(ws + WS_FL) : "memory")
#define MAKE_TAG() do { PIN(tag); \
        tag = ((u64)(unsigned)__builtin_amdgcn_readfirstlane(tag >> 32) << 32) | (unsigned)__builtin_amdgcn_readfirstlane((unsigned)tag); \
        tag = (tag ^ 0x9E3779B97F4A7C15ull) * 0xBF58476D1CE4E5B9ull + 0x632BE59BD9B4E019ull; asm volatile("" : "+s"(tag)); } while (0)

#define CNT(i) (L + L_CNT + (i) * 4)
#define CN2(i) (L + L_MISC + 512 + (i) * 4)
#define WQ_AT(r) ((r) < 2 ? L_WQ1 + (r) * 20480 : L_WQ2 + (r) % 3 * 20480)
#define WQ_ADDR() const unsigned wrow = L + (8 * g + (c16 >> 2)) * 160 + (lane & 3) * 8, wo0 = wrow + (g & 1) * 640, wo1 = wrow + ((g & 1) ^ 1) * 640
    const int rotc = ONEWAY ? m % 5 : m & 3;

    if (w >= 4) {
        const int hw = w - 4;
        LANE_VARS();
        LOAD_TAG();
        __builtin_amdgcn_s_setprio(P1PRIO);
        if (m < 4) {
            const char* src = m < 2 ? wk : wv;
#pragma unroll 1
            for (int p = hw; p < 25; p += 4) glds(lane * 16, src + p * 1024, L + L_WKV + p * 1024);
        }
        {
            const unsigned vx0 = (lane >> 3) * 1280 + (((lane & 7) ^ g) << 4), vx1 = (8 + (lane >> 3)) * 1280 + (((lane & 7) ^ (4 + g)) << 4);
            const char* xb = x + (size_t)(tb * 64 + hw * 16) * 1280;
            int sg = ONEWAY ? m : (m & 3) * 2;
#pragma unroll 1
            for (int i = 0; i < 10; ++i) {
                glds(vx0, xb + sg * 128, L + L_X + (hw * 20 + sg) * 1024);
                glds(vx1, xb + sg * 128, L + L_X + (hw * 20 + 10 + sg) * 1024);
                sg = sg == 9 ? 0 : sg + 1;
                if (i == B0AT) bar_raw();
                if (i == WKVAT) { wait_vm<2 * WKVAT + 2>(); cnt_add(CNT(C_WKV)); }
            }
        }
        u64 f0;
        const int c0 = tb >> 3;
        {
            unsigned vw[5];
#pragma unroll
            for (int i = 0; i < 5; ++i) {
                const int ci = i * 64 + lane, pos = ci / 10, ch = ci - pos * 10, k = pos ^ (((pos >> 3) & 1) << 2);
                vw[i] = k * 1280 + ch * 16;
            }
            const char* wb = wq + m * 160 + hw * 40960;
            WQ_ADDR();
            f32x4 cs[5];
            u32x4 one = ones;
            PIN(one);
            zero_acc(cs);
            auto wqload = [&](int r) {
#pragma unroll
                for (int i = 0; i < 5; ++i) glds(vw[i], wb + r * 163840, L + WQ_AT(r) + hw * 5120 + i * 1024);
            };
            auto wqsum = [&](int r) {
                u32x4 f[5];
#pragma unroll
                for (int s = 0; s < 5; ++s) f[s] = ldstr2(wo0 + WQ_AT(r) + hw * 5120 + s * 32, wo1 + WQ_AT(r) + hw * 5120 + s * 32);
#pragma unroll
                for (int s = 0; s < 5; ++s) mma16(cs[s], f[s], one);
            };
            wqload(0);
            wait_vm<5>();
            cnt_add(CNT(C_X));
            STAMP(1);
            wqload(1);
            MAKE_TAG();
            cnt_spin(CNT(C_KFUSED), 4);
            wqload(2); wait_vm<10>(); cnt_add(CNT(C_WQ));
            cnt_spin(CNT(C_XUSED), 4);
            wqload(3); wait_vm<10>(); cnt_add(CNT(C_WQ + 1));
            wqload(4);
            wqsum(0); wqsum(1);
            wait_vm<10>(); cnt_add(CNT(C_WQ + 2)); wqsum(2);
            wait_vm<5>(); cnt_add(CNT(C_WQ + 3)); wqsum(3);
            wait_vm<0>(); cnt_add(CNT(C_WQ + 4));
            f0 = __hip_atomic_load((const GAS u64*)(ws + FL_KV + (c0 * 32 + (lane & 31)) * 128), __ATOMIC_RELAXED, __HIP_MEMORY_SCOPE_AGENT);
            wqsum(4);
            mma_done();
            if (c16 == 0) {
#pragma unroll
                for (int s = 0; s < 5; ++s) *(LAS f32x4*)(size_t)(L + L_CSX + hw * 320 + s * 64 + g * 16) = cs[s];
            }
            if (cnt_arrive(CNT(C_CSP)) == 3) {
                const unsigned ca = L + L_CSX + (lane < 20 ? lane : 0) * 16;
                const f32x4 c4 = (ldsf4(ca) + ldsf4(ca + 320)) + (ldsf4(ca + 640) + ldsf4(ca + 960));
                *(LAS f32x4*)(size_t)(ca + 1280) = c4;
                cnt_add(CNT(C_CS));
            }
        }
        const char* kv = ws + WS_KV;
        STAMP(2);
        poll(ws, FL_KV, lane < 32 && f0 != tag ? c0 * 32 + lane : -1, tag);
        STAMP(3);
        copy_dev(kv + c0 * CHUNK, L, 42, hw, lane);
        u64 f1;
        asm volatile("global_load_dwordx2 %0, %1, %2 sc1" : "=v"(f1) : "v"(((c0 * 32 + 32 + lane) & 127) * 128), "s"(ws + FL_KV) : "memory");
        wait_vm<8>();
        STAMP(4);
        __builtin_amdgcn_s_setprio(2);
        bar_raw();
        wait_vm<0>();
        cnt_add(CN2(E_C0));
        PIN(f1);
        poll(ws, FL_KV, f1 != tag ? (c0 * 32 + 32 + lane) & 127 : -1, tag);
        copy_dev(kv + ((c0 + 1) & 3) * CHUNK, L + CHUNK, 42, hw, lane);
        {
            int u = blockIdx.x * 4 + hw;
            const int pmat = u < 160 ? 0 : (u < 480 ? 1 : (u < 912 ? 2 : 3));
            u = pmat == 0 ? u : pmat == 1 ? u - 160 : pmat == 2 ? u - 480 : 0;
            const int pc = pmat < 2 ? u / 20 : u / 54, ks = u - pc * (pmat < 2 ? 20 : 54);
            int rb, a3, a2, a1;
            if (pmat == 0) { if (ks < 16) { rb = 40 * ks; a3 = 20; a2 = 4; a1 = 8; } else { rb = 160 * (ks - 16) + 16; a3 = 80; a2 = 40; a1 = 20; } }
            else if (pmat == 1) { rb = ks < 8 ? 80 * ks : ks < 12 ? 160 * (ks - 8) + 64 : 80 * (ks - 12) + 32; a3 = ks >= 8 && ks < 12 ? 80 : 16; a2 = 8; a1 = 4; }
            else if (ks >= 16 && ks < 24) { rb = 224 * (ks - 16) + 96; a3 = ks < 23 ? 112 : 48; a2 = 8; a1 = 4; }
            else { rb = ks < 16 ? 112 * ks : (ks < 39 ? 112 * (ks - 24) + 32 : 112 * (ks - 39) + 64); a3 = 8; a2 = 4; a1 = 16; }
            const int ld = pmat == 1 ? 3456 : 1280, chmax = pmat != 1 ? 9 : (pc == 15 ? 5 : 13);
            const char* psrc = pmat == 0 ? wo + pc * 160 : pmat == 1 ? w1 + pc * 224 : w2 + pc * 160;
            const unsigned pb = L + 3 * CHUNK + hw * 8192, pv = g * ld + min(c16, chmax) * 16;
            cnt_wait(CN2(P_NONE), 4);
#pragma unroll 1
            for (int i = 0; i < 8; ++i) glds(pv, psrc + (rb + a3 * (i >> 2) + a2 * ((i >> 1) & 1) + a1 * (i & 1)) * ld, pb + i * 1024);
            wait_vm<8>();
            cnt_add(CN2(E_C1));
            copy_dev(kv + ((c0 + 2) & 3) * CHUNK, L + 2 * CHUNK, 42, hw, lane);
            wait_vm<10>();
            const unsigned a0 = pb + (8 * g + (c16 >> 2)) * 256 + (lane & 3) * 8;
            if (pmat == 1) {
                const unsigned pdst = WS_W1 + pc * 143360 + ks * 5120, pd2 = WS_W1 + pc * 143360 + 102400 + ks * 2048;
                const unsigned cv = c16 == 0 ? g * 16 : 0x80000000u;
#pragma unroll
                for (int s = 0; s < 7; ++s) {
                    const unsigned a1 = a0 + (s == 6 ? (pc == 15 ? 64 : 192) : s * 32);
                    const u32x4 f = ldstr2(a1, a1 + 1024);
                    bst16(rs, f, lane * 16, s < 2 ? pdst + s * 1024 : s < 4 ? pdst + (s + 1) * 1024 : s < 6 ? pd2 + (s - 4) * 1024 : pdst + 2048);
                    bst16(rs, __builtin_bit_cast(u32x4, mfma16(f, ones, z4)), cv, WS_CS + (ks * 1792 + pc * 112) * 4 + s * 64);
                }
            } else if (pmat < 3) {
                const unsigned a1 = a0 + (lane & 3) * 8, pdst = (pmat == 0 ? WS_WO : WS_W2) + u * 5120;
#pragma unroll
                for (int s = 0; s < 5; ++s) {
                    const unsigned a2 = s < 4 ? a1 + (s >> 1) * 64 + (s & 1) * 8 : a0 + 128;
                    bst16(rs, ldstr2(a2, a2 + 1024), lane * 16, pdst + s * 1024);
                }
            }
        }
        wait_vm<0>();
        cnt_add(CN2(E_C2));
        if (cnt_arrive(CN2(H_PREP)) == 3 && lane == 0) set_flag(ws, FL_PR + cid * 128, tag);
        STAMP(5);
        cnt_wait<3>(CN2(H_PREP), 4);
        {
            const u32x4 e = {EMPTY, EMPTY, EMPTY, EMPTY};
#pragma unroll 1
            for (int p = hw; p < 44; p += 4) bst16(rs, e, lane * 16, WS_O + blockIdx.x * 48640 + p * 1024);
            bst16(rs, e, hw < 3 || lane < 32 ? lane * 16 : 0x80000000u, WS_O + blockIdx.x * 48640 + (44 + hw) * 1024);
        }
        poll(ws, FL_KV, (c0 * 32 + 96 + (lane & 31)) & 127, tag);
        wait_vm<0>();
        if (cnt_arrive(CN2(H_FILL)) == 3 && lane == 0) set_flag(ws, FL_MK + cid * 128, tag);
        cnt_wait<LONGNAP>(CN2(P_C0), 4);
        copy_dev(kv + ((c0 + 3) & 3) * CHUNK, L, 42, hw, lane);
        poll(ws, FL_PR, hw * 64 + lane, tag);
        wait_vm<0>();
        cnt_add(CN2(E_C3));
        cnt_wait<3>(CN2(E_C3), 4);
        STAMP(6);
        if (blockIdx.x == 0 && hw == 1 && lane == 0) set_flag(ws, WS_FL, tag);
        auto wocopy = [&](int p0, int p1) {
#pragma unroll 1
            for (int p = p0 + hw; p < p1; p += 4) {
                const int q = p + rotc * 20;
                glds_dev(lane * 16, ws + WS_WO + m * 102400 + (q >= 100 ? q - 100 : q) * 1024, L + L_WO + p * 1024);
            }
        };
        u32x4 r1[35], r2[9][8];
        unsigned g1[5], g1b[5], g2[9];
        d_bases(g1, WS_W1 + ct * 143360, ct, 20480);
        d_bases(g1b, WS_W1 + ct * 143360 + 102400, ct, 8192);
        e_bases(g2, WS_W2 + m * 276480, m, 30720);
        const unsigned vp = lane * 16 + hw * 1024, vq = lane * 16 + min(28 + hw, 29) * 1024;
        unsigned lp0 = L + vp, lp1 = lp0 + 61440, lp2 = lp0 + 122880, lq0 = L + vq, lq1 = lq0 + 61440, lq2 = lq0 + 122880;
        PIN(lp0); PIN(lp1); PIN(lp2); PIN(lq0); PIN(lq1); PIN(lq2);
#define LD2(G, THR) do { _Pragma("unroll") for (int j_ = 0; j_ < 7; ++j_) { r2[G][j_] = bld16(rs, vp, g2[G] + j_ * 4096); wait_vm<THR>(); } \
        r2[G][7] = bld16(rs, vq, g2[G]); wait_vm<THR>(); } while (0)
#define ST2E(G, j) do { constexpr int sl_ = (G) % 5; \
        ldsw16((j) < 7 ? (sl_ < 2 ? lp0 : sl_ < 4 ? lp1 : lp2) + (sl_ & 1) * 30720 + (j) * 4096 : (sl_ < 2 ? lq0 : sl_ < 4 ? lq1 : lq2) + (sl_ & 1) * 30720, r2[G][j]); } while (0)
#pragma unroll
        for (int i = 0; i < 35; ++i) r1[i] = bld16(rs, vp, i < 25 ? g1[i / 5] + (i % 5) * 4096 : g1b[(i - 25) / 2] + ((i - 25) & 1) * 4096);
        SBAR();
        if (hw == 3) {
            u32x4 c[10];
            const unsigned cv = min(lane & 31, 27) * 16 + (lane >> 5) * 71680;
#pragma unroll
            for (int ks = 0; ks < 10; ++ks) c[ks] = bld16(rs, cv, WS_CS + (ks * 1792 + ct * 112) * 4);
            f32x4 c4 = z4;
#pragma unroll
            for (int ks = 0; ks < 10; ++ks) c4 += __builtin_bit_cast(f32x4, c[ks]);
#pragma unroll
            for (int i = 0; i < 4; ++i) c4[i] += upper(c4[i]);
            if (lane < 28) *(LAS f32x4*)(size_t)(L + L_MISC + lane * 16) = c4;
        }
        LD2(7, 63); LD2(8, 63);
        cnt_wait<LONGNAP>(CN2(P_C1), 4);
        wocopy(0, 42);
        poll(ws, FL_MK, hw * 64 + lane, tag);
        cnt_add(CN2(E_MK));
        cnt_wait<LONGNAP>(CN2(P_C2), 4);
        wocopy(42, 100);
#pragma unroll
        for (int G = 0; G < 7; ++G) {
            const int p = (tb & 3) * 8 + hw * 2;
            glds_dev(lane * 16, ws + g2[G] + min(p, 28) * 1024, L + XCH + hw * 1024);
            glds_dev(lane * 16, ws + g2[G] + min(p + 1, 29) * 1024, L + XCH + hw * 1024);
        }
        wait_vm<0>();
        STAMP(7);
        __builtin_amdgcn_s_setprio(TAILPRIO);
        bar_raw();
#pragma unroll
        for (int i = 0; i < 10; ++i) ldsw16(lp0 + i * 4096, r1[i < 4 ? 25 + i : i - 4]);
        SBAR();
        LD2(6, THROT);
        bar();
        cnt_wait(CN2(M_LDS), 4);
#pragma unroll
        for (int i = 10; i < 35; ++i) {
            ldsw16((i < 15 ? lp0 : i < 30 ? lp1 : lp2) + (i % 15) * 4096, r1[i < 29 ? i - 4 : i]);
            SBAR();
            if (i == 17) cnt_add(CN2(M_W1A));
            if (i >= 18 && i - 18 < NEARLY) {
                const int e = i - 18, G = 3 + (e >> 3), j = e & 7;
                r2[G][j] = bld16(rs, j < 7 ? vp : vq, g2[G] + (j < 7 ? j * 4096 : 0));
                SBAR();
            }
        }
        STAMP(8);
        cnt_add(CN2(M_W1));
#pragma unroll
        for (int e = NEARLY; e < 24; ++e) {
            const int G = 3 + (e >> 3), j = e & 7;
            r2[G][j] = bld16(rs, j < 7 ? vp : vq, g2[G] + (j < 7 ? j * 4096 : 0));
            wait_vm<THROT>();
        }
        cnt_wait<3>(CN2(M_K8), 4);
#pragma unroll 1
        for (int p = hw; p < 60; p += 4) { glds_dev(lane * 16, ws + (p < 30 ? g2[0] + p * 1024 : g2[1] + (p - 30) * 1024), L + p * 1024); wait_vm<THROT>(); }
        cnt_wait<3>(CN2(M_T4), 4);
        copy_dev(ws + g2[2], L + 61440, 30, hw, lane);
        wait_vm<7>();
        STAMP(9);
        bar_raw();
#pragma unroll
        for (int iv = 0; iv < 7; ++iv) {
#pragma unroll
            for (int e = iv < 3 ? iv * 8 - (iv == 2) * 0 : 24 + (iv - 3) * 6; e < (iv < 3 ? iv * 8 + 8 : 24 + (iv - 2) * 6); ++e) {
                if (!(NOST && iv > 0)) switch (e >> 3) {
                    case 0: ST2E(3, e & 7); break;
                    case 1: ST2E(4, e & 7); break;
                    case 2: ST2E(5, e & 7); break;
                    case 3: ST2E(6, e & 7); break;
                    case 4: ST2E(7, e & 7); break;
                    default: ST2E(8, e & 7); break;
                }
                SBAR();
            }
            wait_vm<0>();
            bar();
        }
        asm volatile(".Lpf_end2:");
        STAMP_OUT(1);
        __builtin_amdgcn_endpgm();
    }

    u32x4 xr0, xr1; u32x2 xr2;
    u32x4 qa1, qb1; u32x2 qa2, qb2;
    const int kh = w & 1;
    {
        LANE_VARS();
        LOAD_TAG();
        {
            const int wi = idx * 4 + w;
            touch(lane * 128, x + xg * 327680 + wi * 2560, L + L_JUNK, 0xfffffull);
            touch(lane * 128, ((wi & 1) ? wv : wk) + min(wi >> 1, 49) * 512, L + L_JUNK, 0xfull);
            const char* pc; int l1, l2;
            asm volatile("s_getpc_b64 %0\n.Lpf_pc:\n\ts_mov_b32 %1, .Lpf_end1-.Lpf_pc\n\ts_mov_b32 %2, .Lpf_end2-.Lpf_pc" : "=s"(pc), "=s"(l1), "=s"(l2));
            touch(lane * 128, pc + max(min(wi * 512, max(l1, l2) - 512), 0), L + L_JUNK, 0xfull);
        }
        if (w == 0 && lane < C_NUM) *(LAS unsigned*)(size_t)CNT(lane) = 0u;
        bar();
        u32x4 xf[20], xo[10], kf[20];
        const unsigned xrow = L + L_X + w * 20480 + (c16 >> 3) * 10240 + (c16 & 7) * 128;
        const int sw = c16 >> 1, t32 = (w & 1) * 16 + c16;
        unsigned xa = xrow + (((kh * 4 + g) ^ sw) << 4), xb = xrow + ((((kh ^ 1) * 4 + g) ^ sw) << 4), xc = xa + ((w ^ 1) - w) * 20480, xra[3], qw[5], kvo;
        {
            const int c0 = m * 10 + g, c1 = c0 + 4, c2 = m * 10 + 8 + (g >> 1);
            xra[0] = xrow + (c0 >> 3) * 1024 + (((c0 & 7) ^ sw) << 4);
            xra[1] = xrow + (c1 >> 3) * 1024 + (((c1 & 7) ^ sw) << 4);
            xra[2] = xrow + (c2 >> 3) * 1024 + (((c2 & 7) ^ sw) << 4) + (g & 1) * 8;
            const int d = (m & 1) * 16 + c16;
            kvo = m == 0 ? ((g >> 1) * 32 + t32) * 16 + (g & 1) * 8
                : m == 1 ? (g == 0 ? 1024 + t32 * 8 : 0x80000000u)
                         : (d <= 21 ? 1280 + (((w & 1) * 2 + (g & 1)) * 22 + d) * 16 + (g >> 1) * 8 : 0x80000000u);
            PIN(xa); PIN(xb); PIN(xc); PIN(xra[0]); PIN(xra[1]); PIN(xra[2]); PIN(kvo);
        }
#pragma unroll
        for (int s = 0; s < 5; ++s) {
            const int f0 = s * 16 + 4 * g, hh = f0 / 20, d0 = f0 - hh * 20;
            qw[s] = L + L_QX + (hh * 2 + (w >> 1)) * 1280 + (d0 < 16 ? ((d0 >> 3) * 32 + t32) * 16 + (d0 & 4) * 2 : 1024 + t32 * 8);
            PIN(qw[s]);
        }
        WQ_ADDR();
        unsigned b0 = wo0 + L_WQ1 + kh * 5120, b1 = wo1 + L_WQ1 + kh * 5120;
        PIN(b0); PIN(b1);
        wait_vm<3>();
        MAKE_TAG();
        if (m >= 4) {
            __builtin_amdgcn_s_sleep(PFDELAY);
            const char* wp = wq + (((idx >> 3) * 4 + (m & 3)) * 4 + w) * 12800;
            touch(lane * 128, wp, L + L_JUNK, 0x3ffffffffffffull);
            touch(lane * 128, wp + 6400, L + L_JUNK, 0x3ffffffffffffull);
        }
        f32x4 ka = z4, kc = z4, ssm = z4, ssq = z4, acc[2][5];
        u32x4 one = ones;
        PIN(ssm); PIN(ssq); PIN(one);
        if (m < 4) {
            cnt_wait(CNT(C_WKV), 4);
            const unsigned a0 = L + L_WKV + (8 * g + (c16 >> 2)) * 40 + (m & 1) * 32 + (lane & 3) * 8;
            const unsigned a1 = a0 + kh * 1280, a2 = a0 + (kh ^ 1) * 1280;
#pragma unroll
            for (int j = 0; j < 10; ++j) { kf[j] = ldstr2(a1 + j * 2560, a1 + j * 2560 + 160); kf[10 + j] = ldstr2(a2 + j * 2560, a2 + j * 2560 + 160); }
            if (m < 2) {
#pragma unroll
                for (int j = 0; j < 20; ++j) kc = mfma16(kf[j], ones, kc);
            } else {
#pragma unroll
                for (int j = 0; j < 20; ++j) kc = mfma16(ones, kf[j], kc);
            }
            PIN(kc);
        }
        cnt_add(CNT(C_KFUSED));
        cnt_spin(CNT(C_X), 4);
#pragma unroll
        for (int j = 0; j < 10; ++j) xf[j] = lds16(xa + j * 1024);
#pragma unroll
        for (int j = 0; j < 10; ++j) xf[10 + j] = lds16(xb + j * 1024);
#pragma unroll
        for (int j = 0; j < 10; ++j) xo[j] = lds16(xc + j * 1024);
        xr0 = lds16(xra[0]); xr1 = lds16(xra[1]); xr2 = lds8(xra[2]);
        SBAR();
        if (m < 2) {
#pragma unroll
            for (int j = 0; j < 20; ++j) ka = mfma16(kf[j], xf[j], ka);
        } else if (m < 4) {
#pragma unroll
            for (int j = 0; j < 20; ++j) ka = mfma16(xf[j], kf[j], ka);
        }
        PIN(ka);
#pragma unroll
        for (int j = 0; j < 20; ++j) {
            mma16(ssm, one, xf[j]); mma16(ssq, xf[j], xf[j]);
            if (j == 4) {
#pragma unroll
                for (int i = 0; i < 10; ++i) { PIN(xf[i]); PIN(xf[10 + i]); PIN(xo[i]); }
                PIN(xr0); PIN(xr1); PIN(xr2);
                cnt_add(CNT(C_XUSED));
            }
        }
        mma_done();
        STAMP(1);
        const float sd = (c16 & 2) ? ((c16 & 1) ? ssq[3] : ssq[2]) : ((c16 & 1) ? ssq[1] : ssq[0]);
        const float mean = ssm[0] * (1.f / D), rstd = __builtin_amdgcn_rsqf(fmaxf(__shfl(sd, (c16 >> 2) * 16 + c16) * (1.f / D) - mean * mean, 0.f) + LN_EPS);
        if (m < 4) {
            u32x2 pv;
            if (m < 2) {
                pv = u32x2{pk2((ka[0] - mean * kc[0]) * rstd, (ka[1] - mean * kc[1]) * rstd), pk2((ka[2] - mean * kc[2]) * rstd, (ka[3] - mean * kc[3]) * rstd)};
            } else {
                float v[4];
#pragma unroll
                for (int r = 0; r < 4; ++r) v[r] = (ka[r] - __shfl(mean, 4 * g + r) * kc[0]) * __shfl(rstd, 4 * g + r);
                pv = u32x2{pk2(v[0], v[1]), pk2(v[2], v[3])};
                if (m == 3 && c16 >= 4) pv = c16 == 4 ? u32x2{ONES2, ONES2} : u32x2{0u, 0u};
            }
            bst8(rs, pv, kvo, WS_KV + (tb * 2 + (w >> 1)) * KV_TILE);
        }
        SBAR();
        STAMP(2);
        u32x2 f[2][5][2];
        unsigned xs, xv;
        float sc = rstd * QSCALE, ms = -mean * sc;
        PIN(sc); PIN(ms);
        unsigned b2 = b0 + (L_WQ2 - L_WQ1), b3 = b1 + (L_WQ2 - L_WQ1), cq = CNT(0), cv = 0;
        PIN(b2); PIN(b3); PIN(cq);
        auto qhalf = [&](int j, int s, int e) { return ldstr((j < 4 ? (e ? b1 : b0) + j * 10240 : (e ? b3 : b2) + j % 6 * 10240) + s * 32); };
        auto kvflag = [&] {
            if (m < 4) {
                wait_vm<0>();
                if (cnt_arrive(CNT(C_KVST)) == 3 && lane == 0) set_flag(ws, FL_KV + (tb * 4 + m) * 128, tag);
            }
        };
        if (FLAGAT < 0) kvflag();
        cnt_spin(CNT(C_WQ), 4);
#pragma unroll
        for (int s = 0; s < 5; ++s) { f[0][s][0] = qhalf(0, s, 0); f[0][s][1] = qhalf(0, s, 1); }
        SBAR();
#pragma unroll
        for (int j = 0; j < 10; ++j) {
            if (j < 8 && !(j & 1)) cv = *(volatile LAS unsigned*)(size_t)(cq + (C_WQ + j / 2 + 1) * 4);
            if (j < 9 && (j & 1) && __builtin_expect((unsigned)__builtin_amdgcn_readfirstlane(cv) < 4u, 0)) cnt_spin(CNT(C_WQ + j / 2 + 1), 4);
            asm volatile("" ::: "memory");
            SBAR();
#pragma unroll
            for (int s = 0; s < 5; ++s) {
                const u32x4 fr = {f[j & 1][s][0][0], f[j & 1][s][0][1], f[j & 1][s][1][0], f[j & 1][s][1][1]};
                if (j == 0) mma16z(acc[0][s], fr, xf[j]); else mma16(acc[0][s], fr, xf[j]);
                SBAR();
                if (j < 9) f[(j + 1) & 1][s][0] = qhalf(j + 1, s, 0);
                SBAR();
                if (j == 0) mma16z(acc[1][s], fr, xo[j]); else mma16(acc[1][s], fr, xo[j]);
                SBAR();
                if (j < 9) f[(j + 1) & 1][s][1] = qhalf(j + 1, s, 1);
                SBAR();
            }
            if (j == FLAGAT) {   }
            if (j == FLAGAT) kvflag();
            if (j == 3) asm volatile("s_mov_b64 exec, 1\n\tds_add_u32 %0, %1 offset:%2\n\ts_mov_b64 exec, -1" ::"v"(cq), "v"(1u), "i"(C_R1 * 4) : "memory");
            if (j == FLAGAT) {   }
            if (j == 5) { xs = L + L_WQ1 + (w ^ 1) * 5120 + lane * 16; xv = L + L_WQ1 + w * 5120 + lane * 16; PIN(xs); PIN(xv); }
            if (j == 7) cnt_spin(CNT(C_R1), 4);
        }
        STAMP(3);
        if (m >= 4) wait_vm<0>();
        mma_done();
        send_half(acc[1], xs);
        cnt_add(CNT(C_XCH + (w >> 1)));
        if (w == 0 && lane < M_NUM) *(LAS unsigned*)(size_t)CN2(lane) = 0u;
        f32x4 c5[5], p5[5];
#pragma unroll 1
        for (;;) {
            const unsigned n1 = *(volatile LAS unsigned*)(size_t)CNT(C_CS), n2 = *(volatile LAS unsigned*)(size_t)CNT(C_XCH + (w >> 1));
#pragma unroll
            for (int s = 0; s < 5; ++s) {
                c5[s] = *(volatile LAS f32x4*)(size_t)(L + L_CSX + 1280 + s * 64 + g * 16);
                p5[s] = *(volatile LAS f32x4*)(size_t)(xv + s * 1024);
            }
            if (__builtin_amdgcn_readfirstlane(n1 + n2) == 3) break;
        }
#pragma unroll
        for (int s = 0; s < 5; ++s) {
            const f32x4 v = (acc[0][s] + p5[s]) * sc + c5[s] * ms;
            *(LAS u32x2*)(size_t)qw[s] = u32x2{pk2(v[0], v[1]), pk2(v[2], v[3])};
        }
        bar();
        const int r32 = lane & 31, h = lane >> 5;
        const unsigned qb = L + L_QX + ((w & 1) * 4 + (w >> 1)) * 1280;
        qa1 = lds16(qb + lane * 16); qb1 = lds16(qb + 2560 + lane * 16);
        qa2 = u32x2{lds4(qb + 1024 + r32 * 8 + h * 4), 0u}; qb2 = u32x2{lds4(qb + 2560 + 1024 + r32 * 8 + h * 4), 0u};
    }
    STAMP(4);
    {
        LANE_VARS();
        const int r32 = lane & 31, h = lane >> 5, vrow = min(r32, 21);
        const unsigned kconst = h == 0 ? 0x00003f80u : 0u;
        unsigned ak1 = L + lane * 16, ak2 = L + 1024 + r32 * 8 + h * 4, av = L + 1280 + (h * 22 + vrow) * 16;
        const f32x16 zero16 = {0, 0, 0, 0, 0, 0, 0, 0, 0, 0, 0, 0, 0, 0, 0, 0};
        f32x16 sa, sb, oa = zero16, ob = zero16;
        u32x4 ka1 = lds16(ak1), kb1 = lds16(ak1 + KV_TILE), va0 = lds16(av), va1 = lds16(av + VS), vb0 = va0, vb1 = va0;
        u32x2 ka2 = {lds4(ak2), kconst}, kb2 = {lds4(ak2 + KV_TILE), kconst};
        sa = mfma32(ka1, qa1, mfma32k8(ka2, qa2, zero16));
        sb = mfma32(ka1, qb1, mfma32k8(ka2, qb2, zero16));
        {
            const float ma = halfmax(max16(sa)), mb = halfmax(max16(sb));
            qa2[1] = h == 0 ? (pk2(-ma, 0.f) & 0xffffu) : 0u;
            qb2[1] = h == 0 ? (pk2(-mb, 0.f) & 0xffffu) : 0u;
        }
        sa = mfma32(ka1, qa1, mfma32k8(ka2, qa2, zero16));
        u32x4 pa0 = {0u, 0u, 0u, 0u}, pa1 = pa0, pb0 = pa0, pb1 = pa0;
        float e2 = 0.f, e3 = 0.f;
        unsigned vo0 = (r32 >> 4) * 20480 + h * 256 + (r32 & 15) * 16, vo1 = h ? 0x80000000u : vo0;
        PIN(pa0); PIN(pa1); PIN(pb0); PIN(pb1); PIN(ka2); PIN(kb2); PIN(vo0); PIN(vo1);
#define SLOT(R, MF, X, S, i, PP, jp, P, j) do { R = MF; PIN(R); SBAR(); X; SBAR(); \
        float e0 = EX2(S[i]), e1 = EX2(S[i + 1]); PIN(e0); PIN(e1); SBAR(); \
        PP[jp] = pk2(e2, e3); PIN(PP); SBAR(); \
        e2 = EX2(S[i + 2]); e3 = EX2(S[i + 3]); PIN(e2); PIN(e3); SBAR(); \
        P[j] = pk2(e0, e1); PIN(P); SBAR(); } while (0)
#define STEP(KC1, KC2, KN1, KN2, VC0, VC1, VP0, VP1, OK, OV, X0, X7) do { \
        SLOT(sb, mfma32k8(KC2, qb2, zero16), X0, sa, 0, pb1, 3, pa0, 0); \
        SLOT(sb, mfma32(KC1, qb1, sb), __builtin_amdgcn_s_waitcnt(0xC07F), sa, 4, pa0, 1, pa0, 2); \
        SLOT(ob, mfma32(VP1, pb1, ob), KC1 = lds16(ak1 + (OK) * KV_TILE), sa, 8, pa0, 3, pa1, 0); \
        SLOT(oa, mfma32(VC0, pa0, oa), KC2[0] = lds4(ak2 + (OK) * KV_TILE), sa, 12, pa1, 1, pa1, 2); \
        SLOT(sa, mfma32k8(KN2, qa2, zero16), (void)0, sb, 0, pa1, 3, pb0, 0); \
        SLOT(sa, mfma32(KN1, qa1, sa), VP0 = lds16(av + (OV) * KV_TILE), sb, 4, pb0, 1, pb0, 2); \
        SLOT(oa, mfma32(VC1, pa1, oa), VP1 = lds16(av + (OV) * KV_TILE + VS), sb, 8, pb0, 3, pb1, 0); \
        SLOT(ob, mfma32(VC0, pb0, ob), X7, sb, 12, pb1, 1, pb1, 2); } while (0)
        unsigned so1 = WS_O + (tb * 4 + (w & 2)) * 20480, so0 = so1 + (2 * m + (w & 1)) * 1024;
        so1 += 16384 + (2 * m + (w & 1)) * 256;
        asm volatile("" : "+s"(so0), "+s"(so1));
        unsigned ea = CN2(E_C1), e0a = CN2(E_C0), ev = 0, one = 1;
        PIN(ea); PIN(e0a); PIN(one); PIN(ak1); PIN(ak2); PIN(av);
        auto hook0 = [&](int j, int dn) {
            if (j == 1) ev = *(volatile LAS unsigned*)(size_t)e0a;
            if (j == 14) { ak1 += dn; ak2 += dn; PIN(ak1); PIN(ak2); }
            if (j == 15) { av += dn; ea += 4; PIN(av); PIN(ea); }
        };
        auto hook7 = [&](int j) {
            if (j == 0)
                asm volatile("s_mov_b64 exec, 1\n\tds_add_u32 %0, %1 offset:%2\n\ts_mov_b64 exec, -1" ::"v"(ea), "v"(one), "i"((P_NONE - E_C1) * 4) : "memory");
            if (j == 1 && __builtin_expect((unsigned)__builtin_amdgcn_readfirstlane(ev) < 4u, 0)) cnt_spin(e0a, 4);
            if (j == 9) ev = *(volatile LAS unsigned*)(size_t)ea;
            if (j == 10 && __builtin_expect((unsigned)__builtin_amdgcn_readfirstlane(ev) < 4u, 0))
                cnt_wait(ea, 4);
        };
        STAMP(5);
#pragma unroll 1
        for (int c = 0; c < 4; ++c) {
            const int dn = c == 2 ? -2 * CHUNK : CHUNK;
#pragma unroll
            for (int j = 0; j < 16; j += 2) {
                STEP(ka1, ka2, kb1, kb2, va0, va1, vb0, vb1, (j + 2) & 15, j + 1, hook0(j, dn), hook7(j));
                STEP(kb1, kb2, ka1, ka2, vb0, vb1, va0, va1, (j + 3) & 15, (j + 2) & 15, hook0(j + 1, dn), hook7(j + 1));
            }
        }
        pb1[3] = pk2(e2, e3);
        ob = mfma32(vb1, pb1, ob);
        STAMP(6);
        u32x4 r0, r1, r2;
        auto finish = [&](const f32x16& pa, const f32x16& pb, float la, float lb) {
            const float ra = __builtin_amdgcn_rcpf(la), rb = __builtin_amdgcn_rcpf(lb);
            r0 = u32x4{keep(pk2(pa[0] * ra, pa[1] * ra)), pk2(pa[2] * ra, pa[3] * ra), keep(pk2(pa[4] * ra, pa[5] * ra)), pk2(pa[6] * ra, pa[7] * ra)};
            r1 = u32x4{keep(pk2(pb[0] * rb, pb[1] * rb)), pk2(pb[2] * rb, pb[3] * rb), keep(pk2(pb[4] * rb, pb[5] * rb)), pk2(pb[6] * rb, pb[7] * rb)};
            r2 = u32x4{keep(pk2(pa[8] * ra, pa[9] * ra)), pk2(pa[10] * ra, pa[11] * ra), keep(pk2(pb[8] * rb, pb[9] * rb)), pk2(pb[10] * rb, pb[11] * rb)};
        };
        {
            float am = 0.f;
#pragma unroll
            for (int i = 0; i < 12; i += 2) am = amax3(am, oa[i], oa[i + 1]);
            const float la = upper(oa[8]);
            asm volatile("s_nop 7" : "+v"(ob));
            const float lb = upper(ob[8]);
#pragma unroll
            for (int i = 0; i < 12; i += 2) am = amax3(am, ob[i], ob[i + 1]);
            finish(oa, ob, la, lb);
            const bool ok = (fminf(la, lb) >= 1e-18f) & (fmaxf(la, lb) <= 1e18f) & (am <= 1e37f);
            if (__builtin_expect(__ballot(!ok) != 0, 0)) {
                __builtin_amdgcn_fence(__ATOMIC_ACQUIRE, "agent");
                f32x16 xa, xb;
#pragma unroll 1
                for (int qt = 0; qt < 2; ++qt) {
                    xb = attn_exact(ws + WS_KV, qt ? qb1 : qa1, qt ? qb2[0] : qa2[0], lane, vrow);
                    if (qt == 0) xa = xb;
                }
                finish(xa, xb, upper(xa[8]), upper(xb[8]));
            }
        }
        bst16(rs, r0, vo0, so0);
        bst16(rs, r1, vo0 + 512, so0);
        bst16(rs, r2, vo1, so1);
        STAMP(7);
    }
    u32x4 wd[12], ae[RE][2], we[PE];
    unsigned ge[9], e0, e1, ehv, eprobe, a0;
    const unsigned ehs = WS_F + (tb * 4 + (w & 2)) * 55296 + 16384;
    {
        LANE_VARS();
        const unsigned wl = L + lane * 16;
        f32x4 acc[5];
        u32x4 act[10][2], wf[12];
        unsigned wa = wl + L_WO, wb = wa + 51200, gb[5];
        rot_bases(gb, WS_O + (tb * 4 + w) * 20480, rotc, 4096);
        unsigned v0 = lane * 16, v1 = v0 + 1024, hv = c16 * 256;
        unsigned sv = g == 0 ? c16 * 8 : 0x80000000u, h2v = (g & 1) ? 0x80000000u : (g >> 1) * 256 + c16 * 16;
        PIN(v0); PIN(v1); PIN(hv);
        auto sub = [](int n) { return n < 60 ? (n % 3 == 2 ? 4 : n % 3) : 2 + (n & 1); };
        auto kst = [](int n) { return n < 60 ? n / 3 : (n - 60) / 2; };
        auto wac = [&](int n) { return (kst(n) < 10 ? wa : wb) + (kst(n) % 10) * 5120 + sub(n) * 1024; };
        kstart<RC, 2, 2048>(act, rs, ws, v0, v1, gb,
                           [&] { return __builtin_amdgcn_raw_buffer_load_b32(rs, hv, WS_O + (tb * 4 + w) * 20480 + 16384, SC1) == EMPTY; }, [&] {
            bar();
            PIN(wa); PIN(wb); PIN(sv); PIN(h2v);
#pragma unroll
            for (int n = 0; n < 12; ++n) { wf[n] = lds16(wac(n)); SBAR(); }
            res_init(acc, xr0, xr1, xr2);
            wait_vm<0>();
        }, [] {});
        STAMP(8);
        const unsigned hb = WS_H + (tb * 4 + w) * 20480;
        unsigned t = 0;
        u64 bad = 0;
        static_for<0, 100>([&](auto nn) {
            constexpr int n = nn.v, j = n < 54 ? n / 6 : 0, i = n % 6, q0 = areq(j, RC, 10), q1 = areq(j + 1, RC, 10);
            if (n % 3 == 0) { lgkm<9>(); SBAR(); }
            mma16(acc[sub(n)], wf[n % 12], act[kst(n) >> 1][kst(n) & 1]);
            SBAR();
            if (n + 12 < 100) wf[n % 12] = lds16(wac(n + 12));
            if (n < 54) {
                if (i < 4 && q0 + i / 2 < q1) act[(q0 + i / 2) % 10][i & 1] = aload<2, 2048>(rs, (i & 1) ? v1 : v0, gb, (q0 + i / 2) % 10);
                if (i == 3) { t = max(max(act[j + 1][1][0], act[j + 1][1][2]), max(act[j + 1][0][0], act[j + 1][0][2])); PIN(t); }
                if (i == 4) { bad = __ballot(t == EMPTY); asm volatile("" : "+s"(bad)); }
                if (i == 5 && __builtin_expect(bad != 0, 0))
                    refetch(act[j + 1][0], act[j + 1][1], ws + gb[(j + 1) / 2] + ((j + 1) & 1) * 2048, v0, v1);
            }
            if (n == 68) {
                xr0 = u32x4{keep(pk2(acc[0][0], acc[0][1])), pk2(acc[0][2], acc[0][3]), keep(pk2(acc[1][0], acc[1][1])), pk2(acc[1][2], acc[1][3])};
                bst16(rs, xr0, v0, hb + m * 1024);
            }
            if (n == 70) {
                xr2 = u32x2{keep(pk2(acc[4][0], acc[4][1])), pk2(acc[4][2], acc[4][3])};
                bst16(rs, rowpair(xr2, xr2), h2v, hb + 8192 + m * 512);
            }
            if (n == 92) { lgkm<0>(); bar_raw(); }
            if constexpr (n >= 93 && n < 99) {
#pragma unroll
                for (int e = 0; e < 2; ++e) {
                    const int k = (n - 93) * 2 + e;
                    wd[k] = lds16(wl + (k % 7 < 5 ? 16384 + (k / 7) * 5120 + (k % 7) * 1024 : (k / 7) * 2048 + (k % 7 - 5) * 1024));
                }
            }
            SBAR();
        });
        float sm = 0.f, sq = 0.f, sm2 = 0.f, sq2 = 0.f;
#pragma unroll
        for (int i = 0; i < 4; i += 2) { sm = dot2(xr0[i], ONES2, sm); sq = dot2(xr0[i], xr0[i], sq); sm2 = dot2(xr0[i + 1], ONES2, sm2); sq2 = dot2(xr0[i + 1], xr0[i + 1], sq2); }
        sm = dot2(xr2[0], ONES2, sm); sq = dot2(xr2[0], xr2[0], sq); sm2 = dot2(xr2[1], ONES2, sm2); sq2 = dot2(xr2[1], xr2[1], sq2);
        PIN(sm); PIN(sq); PIN(sm2); PIN(sq2);
        SBAR();
#pragma unroll
        for (int n = 0; n < 12; ++n) PIN(wd[n]);
        cnt_add(CN2(M_LDS));
        xr1 = u32x4{keep(pk2(acc[2][0], acc[2][1])), pk2(acc[2][2], acc[2][3]), keep(pk2(acc[3][0], acc[3][1])), pk2(acc[3][2], acc[3][3])};
        bst16(rs, xr1, v0, hb + (12 + m) * 1024);
#pragma unroll
        for (int i = 0; i < 4; i += 2) { sm = dot2(xr1[i], ONES2, sm); sq = dot2(xr1[i], xr1[i], sq); sm2 = dot2(xr1[i + 1], ONES2, sm2); sq2 = dot2(xr1[i + 1], xr1[i + 1], sq2); }
        sm = xsum16_32(sm + sm2); sq = xsum16_32(sq + sq2);
        bst8(rs, u32x2{keep(__float_as_uint(sm)), __float_as_uint(sq)}, sv, WS_ST + (tb * 4 + w) * 1024 + m * 128);
        SBAR();
        STAMP(9);
    }
    {
        f32x4 acc[2][7], c4[7];
        u32x4 act[12][2];
        u32x2 st[4];
        float mu[2], rp[2];
        const int t16 = (tb >> 1) * 8 + 2 * w;
        unsigned v0 = lane_id() * 16, v1 = v0 + 20480, sv = v0 >> 1, wa = L + v0, wb = wa + 16384 + 46080, gb[5];
        PIN(sv); PIN(wa); PIN(wb); PIN(v0); PIN(v1);
        d_bases(gb, WS_H + t16 * 20480, ct, 4096);
        unsigned fs[7], wc, fw, f2v, ca;
        auto late = [&] {
        zero_acc(acc[0]); zero_acc(acc[1]);
        e_bases(ge, WS_F + tb * 221184 + kh * 1024, m, 6144);
        {
            const unsigned fb = WS_F + t16 * 55296 + ct * 1024;
            fs[0] = fb; fs[1] = fb + 55296; fs[2] = fb + 16384 - ct * 512; fs[3] = fb + 24576; fs[4] = fb + 24576 + 55296; fs[5] = fb + 39936; fs[6] = fb + 39936 + 55296;
#pragma unroll
            for (int i = 0; i < 7; ++i) asm volatile("" : "+s"(fs[i]));
            const int lane = v0 >> 4, g = lane >> 4, c16 = lane & 15;
            f2v = (g & 1) * 55296 + (g >> 1) * 256 + c16 * 16; ca = L + L_MISC + g * 16;
            ehv = (g & 1) * 55296 + c16 * 512;
            wc = wa + 118784;
            PIN(f2v); PIN(ca); PIN(ehv); PIN(wc);
        }
        };
        unsigned hv = ((v0 >> 8) & 1) * 20480 + ((v0 >> 4) & 7) * 512;
        PIN(hv);
        kstart<RD, 4, 1024>(act, rs, ws, v0, v1, gb, [&] { return __builtin_amdgcn_raw_buffer_load_b32(rs, hv, WS_H + t16 * 20480 + 8192, SC1) == EMPTY; },
                            [&] { if (DRD < 0) wait_vm<0>(); else __builtin_amdgcn_s_sleep(DRD < 0 ? 0 : DRD); }, late);
        auto stload = [&] {
#pragma unroll
            for (int i = 0; i < 4; ++i) st[i] = __builtin_amdgcn_raw_buffer_load_b64(rs, sv + i * 512, WS_ST + t16 * 1024, SC1);
        };
        float sa[4];
        unsigned w1v = 0;
        auto dside = [&](int j, int i) {
            if ((j == 0 || j == 6) && i == 12) w1v = *(volatile LAS unsigned*)(size_t)(ca + 512 + (j ? M_W1 : M_W1A) * 4);
            if ((j == 1 || j == 7) && i == 12) {
                if (__builtin_expect((unsigned)__builtin_amdgcn_readfirstlane(w1v) < 4u, 0)) cnt_spin(CN2(j == 7 ? M_W1 : M_W1A), 4);
                asm volatile("" ::: "memory");
            }
            if (j == 5 && i == 4) stload();
            if (j == 9 && i == 6 && __builtin_expect(__any(max(max(st[0][0], st[1][0]), max(st[2][0], st[3][0])) == EMPTY), 0)) {
#pragma unroll 1
                for (int spin = 0; spin < (1 << 16); ++spin) {
                    asm volatile("" ::: "memory");
                    stload();
                    if (!__any(max(max(st[0][0], st[1][0]), max(st[2][0], st[3][0])) == EMPTY)) break;
                }
            }
            if ((j == 10 || j == 11) && (i == 4 || i == 8)) {
                const int k = (j - 10) * 2 + (i == 8);
                sa[k] = xsum16_32(__uint_as_float(st[k & 2][k & 1]) + __uint_as_float(st[(k & 2) + 1][k & 1])); PIN(sa[k]);
            }
            if (j == 12 && (i == 4 || i == 8)) {
                const int t = i == 8;
                mu[t] = sa[2 * t] * (-1.f / D);
                rp[t] = __builtin_amdgcn_rsqf(fmaxf(sa[2 * t + 1] * (1.f / D) - mu[t] * mu[t], 0.f) + LN_EPS) * LOG2E;
                PIN(mu[t]); PIN(rp[t]);
            }
        };
        STAMP(10);
        auto wad = [&](int n) {
            const int j = n < 56 ? n / 7 : n < 86 ? 8 + (n - 56) / 5 : n < 98 ? 14 + (n - 86) / 2 : 14 + (n - 98) / 3;
            const int s = n < 56 ? n % 7 : n < 86 ? (n - 56) % 5 : n < 98 ? (n - 86) % 2 : 2 + (n - 98) % 3;
            return n >= 116 ? wc + (n - 116) * 1024 : s >= 5 ? wa + j * 2048 + (s - 5) * 1024 : (j < 9 ? wa + 16384 + j * 5120 : wb + (j - 9) * 5120) + s * 1024; };
        kloop<0, 8, 20, 0, 140, 7, 5, RD, 4, 1024>(acc, act, wd, wad, rs, ws, v0, v1, gb, dside);
        kloop<8, 1, 20, 56, 140, 5, 5, RD, 4, 1024>(acc, act, wd, wad, rs, ws, v0, v1, gb, dside);
        asm volatile("s_mov_b64 exec, 1\n\tds_add_u32 %0, %1\n\ts_mov_b64 exec, -1" ::"v"(CN2(M_K8)), "v"(1u) : "memory");
        kloop<9, 5, 20, 61, 140, 5, 2, RD, 4, 1024>(acc, act, wd, wad, rs, ws, v0, v1, gb, dside);
        c4[0] = ldsf4(ca); c4[1] = ldsf4(ca + 64);
        SBAR();
        kloop<14, 6, 20, 86, 140, 2, 4, RD, 4, 1024>(acc, act, wd, wad, rs, ws, v0, v1, gb, dside);
        STAMP(11);
        u32x2 lo, hi;
        auto silu = [&](const f32x4& a, const f32x4& c, int t, u32x2& r, auto side) {
            f32x4 y = (a + mu[t] * c) * rp[t], e;
            PIN(y); SBAR();
#pragma unroll
            for (int i = 0; i < 4; ++i) { side(i); SBAR(); e[i] = EX2(-y[i]); PIN(e); SBAR(); }
            e = e * LOG2E + LOG2E; PIN(e); SBAR();
#pragma unroll
            for (int i = 0; i < 4; ++i) { side(4 + i); SBAR(); e[i] = __builtin_amdgcn_rcpf(e[i]); PIN(e); SBAR(); }
            e *= y;
            r = u32x2{keep(pk2(e[0], e[1])), pk2(e[2], e[3])};
            PIN(r); SBAR();
        };
        auto rest = [&](int q) {
            const int n = 98 + (q >> 1), j = n < 116 ? 14 + (n - 98) / 3 : 8 + (n - 116) / 2, s = n < 116 ? 2 + (n - 98) % 3 : 5 + (n - 116) % 2;
            mma16(acc[q & 1][s], wd[n % 12], act[j % 12][q & 1]);
            SBAR();
            if ((q & 1) && n + 12 < 140) wd[n % 12] = lds16(wad(n + 12));
            if (q >= 24 && q < 74 && q % 10 == 4) c4[q == 24 ? 6 : q / 10 - 1] = ldsf4(ca + (q == 24 ? 6 : q / 10 - 1) * 64);
        };
        auto early = [&](int q) {
            if (q == 0) eprobe = __builtin_amdgcn_raw_buffer_load_b32(rs, ehv, ehs, SC1);
            ae[q >> 1][q & 1] = aload<3, 2048>(rs, (q & 1) ? e1 : e0, ge, q >> 1);
        };
#pragma unroll
        for (int q = 0; q < 8; ++q) { rest(q); SBAR(); }
#pragma unroll
        for (int b = 0; b < 10; ++b) {
            const int ut = b < 4 ? b >> 1 : b < 6 ? b & 1 : (b - 6) >> 1, ui = b < 4 ? b & 1 : b < 6 ? 2 : 3 + (b & 1), us = ui < 2 ? ui : ui == 2 ? 6 : ui - 1;
            silu(acc[ut][ui], c4[us], ut, (b & 1) ? hi : lo, [&](int i) { if (8 + b * 8 + i < 84) rest(8 + b * 8 + i); });
            if (b == 5) bst16(rs, rowpair(lo, hi), f2v, fs[2]);
            else if (b & 1) bst16(rs, u32x4{lo[0], lo[1], hi[0], hi[1]}, b < 5 ? v0 : fw, fs[b >> 1]);
            if (b == 1) cnt_add(CN2(M_T4));
            if (b == 1) {   } if (b == 3) {   } if (b == 5) {   } if (b == 7) {   } if (b == 9) {   }
            if (b == 5) {
                PIN(v0);
                fw = ct < 15 ? v0 : 0x80000000u; e0 = v0 + w * 55296; e1 = v0 + (w ^ 1) * 55296; a0 = L + v0 + kh * 5120;
                PIN(fw); PIN(e0); PIN(e1); PIN(a0);
            }
            SBAR();
        }
        bar_raw();
        mma_done();
#pragma unroll
        for (int n = 0; n < PE; ++n) we[n] = lds16(a0 + (n / 5) * 10240 + (n % 5) * 1024);
#pragma unroll
        for (int t = 0; t < 2; ++t) {
            u32x4 r;
#pragma unroll
            for (int u = 0; u < 2; ++u) {
                const f32x4 y = (acc[t][5 + u] + mu[t] * c4[4 + u]) * rp[t];
                f32x4 e = f32x4{EX2(-y[0]), EX2(-y[1]), EX2(-y[2]), EX2(-y[3])} * LOG2E + LOG2E;
                e = f32x4{__builtin_amdgcn_rcpf(e[0]), __builtin_amdgcn_rcpf(e[1]), __builtin_amdgcn_rcpf(e[2]), __builtin_amdgcn_rcpf(e[3])} * y;
                r[2 * u] = keep(pk2(e[0], e[1])); r[2 * u + 1] = pk2(e[2], e[3]);
            }
            bst16(rs, r, fw, fs[5 + t]);
        }
        SBAR();
#pragma unroll
        for (int q = 0; q < 2 * FIRSTFILL; ++q) { early(q); SBAR(); }
        STAMP(12);
    }
    {
        f32x4 acc[2][5];
        unsigned a1 = a0 + 61440, a2 = a0 + 122880;
        PIN(a1); PIN(a2);
        res_init(acc[0], xr0, xr1, xr2); zero_acc(acc[1]);
        if (__any(eprobe == EMPTY))
            kstart<RE, 3, 2048>(ae, rs, ws, e0, e1, ge, [&] { return __builtin_amdgcn_raw_buffer_load_b32(rs, ehv, ehs, SC1) == EMPTY; }, [] {}, [] {});
        if (__builtin_expect(missing(ae[0][0], ae[0][1]), 0)) refetch(ae[0][0], ae[0][1], ws + ge[0], e0, e1);
        SBAR();
        STAMP(13);
        auto wae = [&](int n) { const int q = n / 5 % 15; return (q < 6 ? a0 : q < 12 ? a1 : a2) + (q % 6) * 10240 + (n % 5) * 1024; };
#define GRP(G, WA, WN) kloop<3 * G, 3, 27, 15 * G, 135, 5, 5, RE, 3, 2048>(acc, ae, we, wae, rs, ws, e0, e1, ge, [](int, int) {})
        GRP(0, a0, a0 + 30720);
        bar_raw();

        GRP(1, a0 + 30720, a1);
        bar_raw();
        GRP(2, a1, a1 + 30720);
        bar_raw();
        GRP(3, a1 + 30720, a2); bar_raw();
        GRP(4, a2, a0); bar_raw();

        GRP(5, a0, a0 + 30720); bar_raw();
        GRP(6, a0 + 30720, a1); bar_raw();
        GRP(7, a1, a1 + 30720);
        GRP(8, a1 + 30720, a1);
        mma_done();
        LANE_VARS();
        const unsigned wl = L + lane * 16;
        send_half(acc[1], wl + 122880 + (w ^ 1) * 5120);
        bar();
        recv_half(acc[0], wl + 122880 + w * 5120);
        STAMP(14);
        res_pack(xr0, xr1, xr2, acc[0]);
        char* ob = out + (size_t)(tb * 64 + w * 16 + c16) * 1280 + m * 160;
        asm volatile("global_store_dwordx4 %0, %1, off nt\n\tglobal_store_dwordx4 %0, %2, off offset:64 nt\n\t"
                     "global_store_dwordx2 %3, %4, off offset:128 nt\n\ts_nop 1" ::"v"(ob + g * 16), "v"(xr0), "v"(xr1), "v"(ob + g * 8), "v"(xr2) : "memory");
    }
    asm volatile(".Lpf_end1:");
    STAMP_OUT(0);
    __builtin_amdgcn_endpgm();
}

extern "C" void kernel_launch(void* const* d_in, const int* in_sizes, int n_in, void* d_out, int out_size, void* d_ws, size_t ws_size,
                              hipStream_t stream) {
    (void)hipFuncSetAttribute((const void*)k_block, hipFuncAttributeMaxDynamicSharedMemorySize, LDS_BYTES);
    k_block<<<256, 512, LDS_BYTES, stream>>>((const char*)d_in[0], (const char*)d_in[1], (const char*)d_in[2], (const char*)d_in[3],
                                             (const char*)d_in[4], (const char*)d_in[6], (const char*)d_in[7], (char*)d_ws, (char*)d_out);
}
```

```cpp
#include <hip/hip_runtime.h>
#include <hip/hip_bf16.h>
#include <stdint.h>

typedef __attribute__((ext_vector_type(8))) __bf16 bf16x8;
typedef __attribute__((ext_vector_type(2))) __bf16 bf16x2;
typedef __attribute__((ext_vector_type(4))) short s16x4;
typedef __attribute__((ext_vector_type(4))) float f32x4;
typedef __attribute__((ext_vector_type(2))) float f32x2;
typedef __attribute__((ext_vector_type(16))) float f32x16;
typedef __attribute__((ext_vector_type(4))) unsigned u32x4;
typedef __attribute__((ext_vector_type(2))) unsigned u32x2;
typedef unsigned long long u64;
#define LAS __attribute__((address_space(3)))
#define GAS __attribute__((address_space(1)))
#define DI __device__ __forceinline__

constexpr int D = 640;
constexpr float LN_EPS = 1e-5f;
constexpr float LOG2E = 1.4426950408889634f;
constexpr float QSCALE = 0.22360679774997896f * LOG2E;

constexpr int KV_TILE = 2688;
constexpr int VS = 704;
constexpr unsigned WS_FL = 0;
constexpr unsigned FL_KV = 32768, FL_PR = 2 * 32768, FL_MK = 3 * 32768;
constexpr unsigned WS_KV = 4 * 32768;
constexpr unsigned WS_WO = WS_KV + 64 * KV_TILE;
constexpr unsigned WS_W1 = WS_WO + 8 * 20 * 5 * 1024;
constexpr unsigned WS_W2 = WS_W1 + 16 * 20 * 7 * 1024;
constexpr unsigned WS_CS = WS_W2 + 8 * 54 * 5 * 1024;
constexpr unsigned WS_O  = WS_CS + 20 * 1792 * 4;
constexpr unsigned WS_H  = WS_O + 128 * 20 * 1024;
constexpr unsigned WS_F  = WS_H + 128 * 20 * 1024;
constexpr unsigned WS_ST = WS_F + 128 * 54 * 1024;
constexpr unsigned WS_END = WS_ST + 2048 * 64;
constexpr unsigned EMPTY = 0xffffffffu;
static_assert((WS_END - WS_O) == 256 * 48640, "fill share per workgroup");

constexpr int CHUNK = 16 * KV_TILE;
constexpr int L_X = 0, L_WQ2 = 43008, L_WKV = 83968, L_WQ1 = 122880,
               L_CSX = 111104, L_QX = 152576, L_MISC = 162816;
constexpr int L_AUX = 3 * CHUNK, L_WO = CHUNK, L_CNT = 110592, L_JUNK = 113664, XCH = L_WO + 102400, LDS_BYTES = 163840;

#define STAMP_INIT()
#define STAMP(I)
#define STAMP_OUT(K)

#define THROT 3
#define THROT2 3
#define RC 6
#define RD 7
#define RE 7
#define TAILPRIO 0
#define FIRSTFILL 4
#define ONEWAY 1
#define NEARLY 12
#define PFDELAY 6
#define FLAGAT 0
#define P1PRIO 1
#define DRC -1
#define DRD 0
#define PE 10
#define NOST 0
#define LONGNAP 10
DI void glds(unsigned voff, const void* sbase, unsigned lds) {
    asm volatile("s_mov_b32 m0, %2\n\ts_nop 0\n\tglobal_load_lds_dwordx4 %0, %1" ::"v"(voff), "s"(sbase), "s"(lds) : "memory");
}
DI void glds_dev(unsigned voff, const void* sbase, unsigned lds) {
    asm volatile("s_mov_b32 m0, %2\n\ts_nop 0\n\tglobal_load_lds_dwordx4 %0, %1 sc1" ::"v"(voff), "s"(sbase), "s"(lds) : "memory");
}
DI void touch(unsigned voff, const void* sbase, unsigned lds, u64 mask) {
    asm volatile("s_mov_b32 m0, %2\n\ts_mov_b64 exec, %3\n\tglobal_load_lds_dword %0, %1\n\ts_mov_b64 exec, -1" ::"v"(voff), "s"(sbase), "s"(lds), "s"(mask) : "memory");
}
template <int N> DI void wait_vm() { asm volatile("s_waitcnt vmcnt(%0)" ::"i"(N) : "memory"); }
DI void bar() { asm volatile("s_waitcnt lgkmcnt(0)\n\ts_barrier" ::: "memory"); }
DI void bar_raw() { asm volatile("s_barrier" ::: "memory"); }
#define PIN(x) asm volatile("" : "+v"(x))
#define SBAR() __builtin_amdgcn_sched_barrier(0)
DI unsigned pk2(float a, float b) { bf16x2 v = {(__bf16)a, (__bf16)b}; return __builtin_bit_cast(unsigned, v); }
DI float bflo(unsigned u) { return __uint_as_float(u << 16); }
DI float bfhi(unsigned u) { return __uint_as_float(u & 0xffff0000u); }
DI f32x4 mfma16(u32x4 a, u32x4 b, f32x4 c) {
    return __builtin_amdgcn_mfma_f32_16x16x32_bf16(__builtin_bit_cast(bf16x8, a), __builtin_bit_cast(bf16x8, b), c, 0, 0, 0);
}
DI f32x16 mfma32(u32x4 a, u32x4 b, f32x16 c) {
    return __builtin_amdgcn_mfma_f32_32x32x16_bf16(__builtin_bit_cast(bf16x8, a), __builtin_bit_cast(bf16x8, b), c, 0, 0, 0);
}
DI f32x16 mfma32k8(u32x2 a, u32x2 b, f32x16 c) {
    return __builtin_amdgcn_mfma_f32_32x32x8bf16_1k(__builtin_bit_cast(s16x4, a), __builtin_bit_cast(s16x4, b), c, 0, 0, 0);
}
DI u32x4 lds16(unsigned a) { return *(const LAS u32x4*)(size_t)a; }
DI u32x2 lds8(unsigned a) { return *(const LAS u32x2*)(size_t)a; }
DI unsigned lds4(unsigned a) { return *(const LAS unsigned*)(size_t)a; }
DI f32x4 ldsf4(unsigned a) { return *(const LAS f32x4*)(size_t)a; }
DI u32x2 ldstr(unsigned a) { return __builtin_bit_cast(u32x2, __builtin_amdgcn_ds_read_tr16_b64_v4i16((LAS s16x4*)(size_t)a)); }
DI u32x4 ldstr2(unsigned a0, unsigned a1) { u32x2 lo = ldstr(a0), hi = ldstr(a1); return u32x4{lo[0], lo[1], hi[0], hi[1]}; }
DI float dot2(unsigned a, unsigned b, float c) {
    return __builtin_amdgcn_fdot2_f32_bf16(__builtin_bit_cast(bf16x2, a), __builtin_bit_cast(bf16x2, b), c, false);
}
DI float xsum16_32(float v) {
    const auto a = __builtin_amdgcn_permlane16_swap(__float_as_uint(v), __float_as_uint(v), false, false);
    v = __uint_as_float(a[0]) + __uint_as_float(a[1]);
    const auto b = __builtin_amdgcn_permlane32_swap(__float_as_uint(v), __float_as_uint(v), false, false);
    return __uint_as_float(b[0]) + __uint_as_float(b[1]);
}
constexpr unsigned ONES2 = 0x3f803f80u;
#define EX2(v) __builtin_amdgcn_exp2f(v)
typedef __amdgpu_buffer_rsrc_t rsrc_t;
constexpr int SC1 = 16;
DI u32x4 bld16(rsrc_t r, unsigned v, unsigned s) { return __builtin_amdgcn_raw_buffer_load_b128(r, v, s, SC1); }
DI void bst16(rsrc_t r, u32x4 d, unsigned v, unsigned s) {
    __builtin_amdgcn_raw_buffer_store_b128(d, r, v, s, SC1);
    asm volatile("s_nop 1" ::"v"(d) : "memory");
}
DI void bst8(rsrc_t r, u32x2 d, unsigned v, unsigned s) { __builtin_amdgcn_raw_buffer_store_b64(d, r, v, s, SC1); }

DI void set_flag(char* ws, unsigned off, u64 tag) { __hip_atomic_store((GAS u64*)(ws + off), tag, __ATOMIC_RELAXED, __HIP_MEMORY_SCOPE_AGENT); }
DI void poll(char* ws, unsigned arr, int index, u64 tag) {
    const GAS u64* p = (const GAS u64*)(ws + arr + index * 128);
    bool ok = index < 0;
#pragma unroll 1
    for (int spin = 0; spin < (1 << 18); ++spin) {
        if (!ok) ok = __hip_atomic_load(p, __ATOMIC_RELAXED, __HIP_MEMORY_SCOPE_AGENT) == tag;
        if (__all(ok)) break;
        __builtin_amdgcn_s_sleep(1);
    }
    asm volatile("" ::: "memory");
}
DI void copy_dev(const char* src, unsigned lds, int n, int hw, int lane) {
#pragma unroll 1
    for (int p = hw; p < n; p += 4) glds_dev(lane * 16, src + p * 1024, lds + p * 1024);
}

DI void cnt_add(unsigned a) {
    asm volatile("s_waitcnt lgkmcnt(0)\n\ts_mov_b64 exec, 1\n\tds_add_u32 %0, %1\n\ts_mov_b64 exec, -1" ::"v"(a), "v"(1u) : "memory");
}
DI unsigned cnt_arrive(unsigned a) {
    unsigned r;
    asm volatile("s_waitcnt lgkmcnt(0)\n\ts_mov_b64 exec, 1\n\tds_add_rtn_u32 %0, %1, %2\n\ts_mov_b64 exec, -1\n\ts_waitcnt lgkmcnt(0)"
                 : "=&v"(r) : "v"(a), "v"(1u) : "memory");
    return __builtin_amdgcn_readfirstlane(r);
}
DI void cnt_spin(unsigned a, unsigned n) {
#pragma unroll 1
    while ((unsigned)__builtin_amdgcn_readfirstlane(*(volatile LAS unsigned*)(size_t)a) < n) {}
    asm volatile("" ::: "memory");
}
template <int NAP = 1> DI void cnt_wait(unsigned a, unsigned n) {
#pragma unroll 1
    while ((unsigned)__builtin_amdgcn_readfirstlane(*(volatile LAS unsigned*)(size_t)a) < n) __builtin_amdgcn_s_sleep(NAP);
    asm volatile("" ::: "memory");
}
DI unsigned keep(unsigned v) { return min(v, EMPTY - 1); }
DI bool missing(const u32x4& a, const u32x4& b) { return __any(max(max(a[0], a[2]), max(b[0], b[2])) == EMPTY); }
DI void refetch(u32x4& a, u32x4& b, const char* base, unsigned v0, unsigned v1) {
#pragma unroll 1
    for (int spin = 0; spin < (1 << 16); ++spin) {
        asm volatile("global_load_dwordx4 %0, %2, %4 sc1\n\tglobal_load_dwordx4 %1, %3, %4 sc1\n\ts_waitcnt vmcnt(0)"
                     : "+v"(a), "+v"(b) : "v"(v0), "v"(v1), "s"(base) : "memory");
        if (!missing(a, b)) break;
    }
}
DI u32x4 rowpair(const u32x2& a, const u32x2& b) {
    const auto s0 = __builtin_amdgcn_permlane16_swap(a[0], b[0], false, false), s1 = __builtin_amdgcn_permlane16_swap(a[1], b[1], false, false);
    return u32x4{s0[0], s1[0], s0[1], s1[1]};
}
DI int lane_id() {
    int l;
    asm volatile("v_mbcnt_lo_u32_b32 %0, -1, 0\n\tv_mbcnt_hi_u32_b32 %0, -1, %0" : "=v"(l));
    return l;
}
#define LANE_VARS() const int lane = lane_id(), g = lane >> 4, c16 = lane & 15; (void)g; (void)c16
DI float halfmax(float m) {
    auto r = __builtin_amdgcn_permlane32_swap(__float_as_uint(m), __float_as_uint(m), false, false);
    return fmaxf(__uint_as_float(r[0]), __uint_as_float(r[1]));
}
DI float upper(float v) {
    auto r = __builtin_amdgcn_permlane32_swap(__float_as_uint(v), __float_as_uint(v), false, false);
    return __uint_as_float(r[1]);
}
DI float max16(const f32x16& s) {
    float a = fmaxf(fmaxf(s[0], s[1]), s[2]), b = fmaxf(fmaxf(s[3], s[4]), s[5]);
    a = fmaxf(fmaxf(a, s[6]), s[7]); b = fmaxf(fmaxf(b, s[8]), s[9]);
    a = fmaxf(fmaxf(a, s[10]), s[11]); b = fmaxf(fmaxf(b, s[12]), s[13]);
    a = fmaxf(fmaxf(a, s[14]), s[15]);
    return fmaxf(a, b);
}
DI float amax3(float m, float a, float b) {
    float r;
    asm("v_maximum3_f32 %0, %1, |%2|, |%3|" : "=v"(r) : "v"(m), "v"(a), "v"(b));
    return r;
}
DI f32x16 attn_exact(const char* kv, u32x4 q1, unsigned q2lo, int lane, int vrow) {
    const int r32 = lane & 31, h = lane >> 5;
    const f32x16 zero16 = {0, 0, 0, 0, 0, 0, 0, 0, 0, 0, 0, 0, 0, 0, 0, 0};
    const u32x2 q2 = {q2lo, 0u};
    float m = -INFINITY;
    f32x16 o = zero16;
#pragma unroll 1
    for (int t = 0; t < 64; ++t) {
        const GAS char* tp = (const GAS char*)kv + t * KV_TILE;
        const u32x4 fk1 = *(const GAS u32x4*)(tp + lane * 16);
        const u32x2 fk2 = {*(const GAS unsigned*)(tp + 1024 + r32 * 8 + h * 4), 0u};
        const u32x4 fv0 = *(const GAS u32x4*)(tp + 1280 + (h * 22 + vrow) * 16), fv1 = *(const GAS u32x4*)(tp + 1280 + VS + (h * 22 + vrow) * 16);
        f32x16 s = mfma32(fk1, q1, mfma32k8(fk2, q2, zero16));
        const float mn = fmaxf(m, halfmax(max16(s))), al = EX2(m - mn);
        m = mn;
        u32x4 p0, p1;
#pragma unroll
        for (int i = 0; i < 4; ++i) { p0[i] = pk2(EX2(s[2 * i] - m), EX2(s[2 * i + 1] - m)); p1[i] = pk2(EX2(s[8 + 2 * i] - m), EX2(s[9 + 2 * i] - m)); }
#pragma unroll
        for (int i = 0; i < 16; ++i) o[i] *= al;
        o = mfma32(fv1, p1, mfma32(fv0, p0, o));
    }
    return o;
}

DI void mma16(f32x4& c, const u32x4& a, const u32x4& b) {
    asm volatile("v_mfma_f32_16x16x32_bf16 %0, %1, %2, %0" : "+v"(c) : "v"(a), "v"(b));
}
DI void mma16z(f32x4& c, const u32x4& a, const u32x4& b) { asm volatile("v_mfma_f32_16x16x32_bf16 %0, %1, %2, 0" : "=&v"(c) : "v"(a), "v"(b)); }
DI void mma_done() { SBAR(); asm volatile("s_nop 7\n\ts_nop 1"); SBAR(); }
template <int NS> DI void zero_acc(f32x4 (&a)[NS]) {
#pragma unroll
    for (int s = 0; s < NS; ++s) { a[s] = f32x4{0, 0, 0, 0}; PIN(a[s]); }
}
template <int GS, int ASTR, int NG> DI u32x4 aload(rsrc_t rs, unsigned& v, const unsigned (&gb)[NG], int j) {
    const int o = (j % GS) * ASTR;
    PIN(v);
    return bld16(rs, v + (o & 4095), gb[j / GS] + (o & ~4095));
}
constexpr int areq(int j, int PD, int KT) {
    const int a = FIRSTFILL + 2 * j, b = PD + (j > 0 ? j - 1 : 0), c = a < b ? a : b;
    return c < KT ? c : KT;
}
template <int N> struct Int { static constexpr int v = N; };
template <int A, int B, class Fn> DI void static_for(Fn fn) { if constexpr (A < B) { fn(Int<A>{}); static_for<A + 1, B>(fn); } }
template <int N> DI void lgkm() { __builtin_amdgcn_s_waitcnt(0xC07F | (N << 8)); }
template <int K0, int NK, int KT, int F0, int FT, int NS, int NSN, int PD, int GS, int ASTR, int NA, int RING, int POOL, int NG, class WAddr, class Side>
DI void kloop(f32x4 (&acc)[2][NA], u32x4 (&act)[RING][2], u32x4 (&wf)[POOL], WAddr waddr, rsrc_t rs, const char* ws, unsigned& v0,
              unsigned& v1, const unsigned (&gb)[NG], Side side) {
    if constexpr (NK > 0) {
        constexpr int j = K0, q0 = areq(j, PD, KT), q1 = areq(j + 1, PD, KT), jc = j + 1 < KT ? j + 1 : 0, nn = NK > 1 ? NS : NSN;
        constexpr int issued = F0 + NS + POOL < FT ? F0 + NS + POOL : FT, need = F0 + NS + nn < FT ? F0 + NS + nn : FT;
        unsigned t = 0;
        u64 bad = 0;
        if (j == 0) { lgkm<POOL - NS>(); SBAR(); }
#pragma unroll
        for (int i = 0; i < 2 * NS; ++i) {
            const int n = F0 + (i >> 1);
            mma16(acc[i & 1][i >> 1], wf[n % POOL], act[j % RING][i & 1]);
            SBAR();
            if ((i & 1) && n + POOL < FT) wf[n % POOL] = lds16(waddr(n + POOL));
            if (!(i & 1) && q0 + i / 4 < q1) act[(q0 + i / 4) % RING][i / 2 & 1] = aload<GS, ASTR>(rs, (i & 2) ? v1 : v0, gb, q0 + i / 4);
            if (jc) {
                if (i == (NS > 2 ? 2 * NS - 6 : 0)) {
                    t = max(act[jc % RING][1][0], act[jc % RING][1][2]);
                    t = max(max(t, act[jc % RING][0][0]), act[jc % RING][0][2]); PIN(t);
                }
                if (i == (NS > 2 ? 2 * NS - 4 : 1)) { bad = __ballot(t == EMPTY); asm volatile("" : "+s"(bad)); }
                if (i == 2 * NS - 2 && __builtin_expect(bad != 0, 0))
                    refetch(act[jc % RING][0], act[jc % RING][1], ws + gb[jc / GS] + (jc % GS) * ASTR, v0, v1);
            }
            side(j, i);
            if (i == 2 * NS - 1 && need > F0 + NS) { SBAR(); lgkm<issued - need>(); }
            SBAR();
        }
        kloop<K0 + 1, NK - 1, KT, F0 + NS, FT, NS, NSN, PD, GS, ASTR>(acc, act, wf, waddr, rs, ws, v0, v1, gb, side);
    }
}
template <int PD, int GS, int ASTR, int RING, int NG, class Probe, class Before, class After>
DI void kstart(u32x4 (&act)[RING][2], rsrc_t rs, const char* ws, unsigned& v0, unsigned& v1, const unsigned (&gb)[NG], Probe probe,
               Before before, After after) {
    before();
    SBAR();
#pragma unroll 1
    for (int spin = 0; spin < (1 << 16); ++spin) {
        const bool miss = probe();
        SBAR();
#pragma unroll
        for (int k = 0; k < FIRSTFILL; ++k) {
            act[k][0] = aload<GS, ASTR>(rs, v0, gb, k); SBAR();
            act[k][1] = aload<GS, ASTR>(rs, v1, gb, k); SBAR();
        }
        if (!__any(miss)) break;
        asm volatile("" ::: "memory");
    }
    SBAR();
    after();
    SBAR();
    if (__builtin_expect(missing(act[0][0], act[0][1]), 0)) refetch(act[0][0], act[0][1], ws + gb[0], v0, v1);
    SBAR();
}
DI void e_bases(unsigned (&gb)[9], unsigned base, int m, int stride) {
    const int m3 = m % 3;
#pragma unroll
    for (int i = 0; i < 9; ++i) {
        const int q = i < 4 ? (i + m) & 3 : i < 6 ? 4 + ((i + (m >> 2)) & 1) : 6 + (i - 6 + m3 >= 3 ? i - 9 + m3 : i - 6 + m3);
        gb[i] = base + q * stride;
        asm volatile("" : "+s"(gb[i]));
    }
}
DI void d_bases(unsigned (&gb)[5], unsigned base, int ct, int stride) {
    const int c3 = ct % 3;
#pragma unroll
    for (int i = 0; i < 5; ++i) {
        gb[i] = base + (i < 3 ? (i + c3 >= 3 ? i + c3 - 3 : i + c3) : 3 + ((i + 1 + ct) & 1)) * stride;
        asm volatile("" : "+s"(gb[i]));
    }
}
template <int NG> DI void rot_bases(unsigned (&gb)[NG], unsigned base, int rot, int stride) {
#pragma unroll
    for (int i = 0; i < NG; ++i) { const int q = i + rot; gb[i] = base + (q >= NG ? q - NG : q) * stride; asm volatile("" : "+s"(gb[i])); }
}
DI void send_half(const f32x4 (&a)[5], unsigned dst) {
#pragma unroll
    for (int s = 0; s < 5; ++s) *(LAS f32x4*)(size_t)(dst + s * 1024) = a[s];
}
DI void recv_half(f32x4 (&a)[5], unsigned src) {
#pragma unroll
    for (int s = 0; s < 5; ++s) a[s] += ldsf4(src + s * 1024);
}
DI void ldsw16(unsigned a, const u32x4& v) { *(LAS u32x4*)(size_t)a = v; }
DI f32x4 unpk(unsigned r0, unsigned r1) { return f32x4{bflo(r0), bfhi(r0), bflo(r1), bfhi(r1)}; }
DI void res_init(f32x4 (&a)[5], const u32x4& x0, const u32x4& x1, const u32x2& x2) {
    a[0] = unpk(x0[0], x0[1]); a[1] = unpk(x0[2], x0[3]); a[2] = unpk(x1[0], x1[1]); a[3] = unpk(x1[2], x1[3]); a[4] = unpk(x2[0], x2[1]);
#pragma unroll
    for (int s = 0; s < 5; ++s) PIN(a[s]);
}
DI void res_pack(u32x4& x0, u32x4& x1, u32x2& x2, const f32x4 (&a)[5]) {
    x0 = u32x4{pk2(a[0][0], a[0][1]), pk2(a[0][2], a[0][3]), pk2(a[1][0], a[1][1]), pk2(a[1][2], a[1][3])};
    x1 = u32x4{pk2(a[2][0], a[2][1]), pk2(a[2][2], a[2][3]), pk2(a[3][0], a[3][1]), pk2(a[3][2], a[3][3])};
    x2 = u32x2{pk2(a[4][0], a[4][1]), pk2(a[4][2], a[4][3])};
}
enum { C_WKV, C_KFUSED, C_X, C_XUSED, C_WQ, C_CSP = C_WQ + 5, C_CS, C_KVST, C_R1, C_XCH, C_NUM = C_XCH + 2 };
enum { E_C1, E_C2, E_C3, E_MK, P_NONE, P_C0, P_C1, P_C2, E_C0, H_PREP, H_FILL, M_LDS, M_W1A, M_W1, M_K8, M_T4, M_NUM };

__global__ __launch_bounds__(512, 2) void k_block(const char* __restrict__ x, const char* __restrict__ wq, const char* __restrict__ wk,
                                                  const char* __restrict__ wv, const char* __restrict__ wo, const char* __restrict__ w1,
                                                  const char* __restrict__ w2, char* ws, char* __restrict__ out) {
    extern __shared__ __attribute__((aligned(16))) char smem[];
    const unsigned L = (unsigned)(size_t)(LAS char*)smem;
    const int w = __builtin_amdgcn_readfirstlane(threadIdx.x >> 6);
    const int xg = blockIdx.x & 7, idx = blockIdx.x >> 3;
    const int tb = ONEWAY ? xg * 4 + (idx >> 3) : (xg >> 1) * 8 + (idx >> 2), m = ONEWAY ? (idx & 7) : (xg & 1) * 4 + (idx & 3), cid = tb * 8 + m;
    const int ct = (m >> 2) * 8 + (tb & 1) * 4 + (m & 3);
    const rsrc_t rs = __builtin_amdgcn_make_buffer_rsrc(ws, 0, WS_END, 0x00020000);
    const u32x4 ones = {ONES2, ONES2, ONES2, ONES2};
    const f32x4 z4 = {0, 0, 0, 0};
    STAMP_INIT();

#define LOAD_TAG() u64 tag; asm volatile("global_load_dwordx2 %0, %1, %2 sc1" : "=v"(tag) : "v"(0u), "s"(ws + WS_FL) : "memory")
#define MAKE_TAG() do { PIN(tag); \
        tag = ((u64)(unsigned)__builtin_amdgcn_readfirstlane(tag >> 32) << 32) | (unsigned)__builtin_amdgcn_readfirstlane((unsigned)tag); \
        tag = (tag ^ 0x9E3779B97F4A7C15ull) * 0xBF58476D1CE4E5B9ull + 0x632BE59BD9B4E019ull; asm volatile("" : "+s"(tag)); } while (0)

#define CNT(i) (L + L_CNT + (i) * 4)
#define CN2(i) (L + L_MISC + 512 + (i) * 4)
#define WQ_AT(r) ((r) < 2 ? L_WQ1 + (r) * 20480 : L_WQ2 + (r) % 3 * 20480)
#define WQ_ADDR() const unsigned wrow = L + (8 * g + (c16 >> 2)) * 160 + (lane & 3) * 8, wo0 = wrow + (g & 1) * 640, wo1 = wrow + ((g & 1) ^ 1) * 640
    const int rotc = ONEWAY ? m % 5 : m & 3;

#define WQ_SRC() unsigned vw[5]; _Pragma("unroll") for (int i = 0; i < 5; ++i) { \
        const int ci = i * 64 + lane, pos = (ci * 6554) >> 16, ch = ci - pos * 10, k = pos ^ (((pos >> 3) & 1) << 2); \
        vw[i] = k * 1280 + ch * 16; PIN(vw[i]); } \
    auto wqload = [&](int r) {     \
        _Pragma("unroll") for (int i = 0; i < 5; ++i) glds(vw[i], wq + m * 160 + hw * 40960 + r * 163840, L + WQ_AT(r) + hw * 5120 + i * 1024); }
    if (w < 4) {
        const int lane = lane_id();
        {
            const int wi = idx * 4 + w;
            touch(lane * 128, x + xg * 327680 + wi * 2560, L + L_JUNK, 0xfffffull);
            touch(lane * 128, ((wi & 1) ? wv : wk) + min(wi >> 1, 49) * 512, L + L_JUNK, 0xfull);
            const char* pc; int l1, l2;
            asm volatile("s_getpc_b64 %0\n.Lpf_pc:\n\ts_mov_b32 %1, .Lpf_end1-.Lpf_pc\n\ts_mov_b32 %2, .Lpf_end2-.Lpf_pc" : "=s"(pc), "=s"(l1), "=s"(l2));
            touch(lane * 128, pc + max(min(wi * 512, max(l1, l2) - 512), 0), L + L_JUNK, 0xfull);
        }
        if (w == 0 && lane < C_NUM) *(LAS unsigned*)(size_t)CNT(lane) = 0u;
        bar();
    } else {
        const int hw = w - 4;
        LANE_VARS();
        __builtin_amdgcn_s_setprio(P1PRIO);
        if (m < 4) {
            const char* src = m < 2 ? wk : wv;
#pragma unroll 1
            for (int p = hw; p < 25; p += 4) glds(lane * 16, src + p * 1024, L + L_WKV + p * 1024);
        }
        const unsigned vx0 = (lane >> 3) * 1280 + (((lane & 7) ^ g) << 4), vx1 = (8 + (lane >> 3)) * 1280 + (((lane & 7) ^ (4 + g)) << 4);
        const char* xb = x + (size_t)(tb * 64 + hw * 16) * 1280;
        int sg = m;
        auto xload = [&](int n) {
#pragma unroll 1
            for (int i = 0; i < n; ++i) {
                glds(vx0, xb + sg * 128, L + L_X + (hw * 20 + sg) * 1024);
                glds(vx1, xb + sg * 128, L + L_X + (hw * 20 + 10 + sg) * 1024);
                sg = sg == 9 ? 0 : sg + 1;
            }
        };
        xload(3);
        WQ_SRC();
        bar_raw();
        wait_vm<6>();
        cnt_add(CNT(C_WKV));
        xload(7);
        wqload(0);
        wait_vm<5>();
        cnt_add(CNT(C_X));
        wqload(1);
    }
    int role = w;
    asm volatile("" : "+s"(role));
    if (role >= 4) {
        const int hw = w - 4;
        LANE_VARS();
        u64 f0, tag;
        const int c0 = tb >> 3;
        {
            WQ_SRC();
            WQ_ADDR();
            f32x4 cs[5];
            u32x4 one = ones;
            auto wqsum = [&](int r) {
                u32x4 f[5];
#pragma unroll
                for (int s = 0; s < 5; ++s) f[s] = ldstr2(wo0 + WQ_AT(r) + hw * 5120 + s * 32, wo1 + WQ_AT(r) + hw * 5120 + s * 32);
#pragma unroll
                for (int s = 0; s < 5; ++s) mma16(cs[s], f[s], one);
            };
            STAMP(1);
            PIN(one);
            zero_acc(cs);
            cnt_spin(CNT(C_KFUSED), 4);
            wqload(2); wait_vm<10>(); cnt_add(CNT(C_WQ));
            cnt_spin(CNT(C_XUSED), 4);
            wqload(3); wait_vm<10>(); cnt_add(CNT(C_WQ + 1));
            asm volatile("global_load_dwordx2 %0, %1, %2 sc1" : "=v"(tag) : "v"(0u), "s"(ws + WS_FL) : "memory");
            wqload(4);
            wqsum(0); wqsum(1);
            wait_vm<11>(); cnt_add(CNT(C_WQ + 2)); wqsum(2);
            wait_vm<6>(); cnt_add(CNT(C_WQ + 3)); wqsum(3);
            wait_vm<0>(); cnt_add(CNT(C_WQ + 4));
            MAKE_TAG();
            f0 = __hip_atomic_load((const GAS u64*)(ws + FL_KV + (c0 * 32 + (lane & 31)) * 128), __ATOMIC_RELAXED, __HIP_MEMORY_SCOPE_AGENT);
            wqsum(4);
            mma_done();
            if (c16 == 0) {
#pragma unroll
                for (int s = 0; s < 5; ++s) *(LAS f32x4*)(size_t)(L + L_CSX + hw * 320 + s * 64 + g * 16) = cs[s];
            }
            if (cnt_arrive(CNT(C_CSP)) == 3) {
                const unsigned ca = L + L_CSX + (lane < 20 ? lane : 0) * 16;
                const f32x4 c4 = (ldsf4(ca) + ldsf4(ca + 320)) + (ldsf4(ca + 640) + ldsf4(ca + 960));
                *(LAS f32x4*)(size_t)(ca + 1280) = c4;
                cnt_add(CNT(C_CS));
            }
        }
        const char* kv = ws + WS_KV;
        STAMP(2);
        poll(ws, FL_KV, lane < 32 && f0 != tag ? c0 * 32 + lane : -1, tag);
        STAMP(3);
        copy_dev(kv + c0 * CHUNK, L, 42, hw, lane);
        u64 f1;
        asm volatile("global_load_dwordx2 %0, %1, %2 sc1" : "=v"(f1) : "v"(((c0 * 32 + 32 + lane) & 127) * 128), "s"(ws + FL_KV) : "memory");
        wait_vm<8>();
        STAMP(4);
        __builtin_amdgcn_s_setprio(2);
        bar_raw();
        wait_vm<0>();
        cnt_add(CN2(E_C0));
        PIN(f1);
        poll(ws, FL_KV, f1 != tag ? (c0 * 32 + 32 + lane) & 127 : -1, tag);
        copy_dev(kv + ((c0 + 1) & 3) * CHUNK, L + CHUNK, 42, hw, lane);
        {
            int u = blockIdx.x * 4 + hw;
            const int pmat = u < 160 ? 0 : (u < 480 ? 1 : (u < 912 ? 2 : 3));
            u = pmat == 0 ? u : pmat == 1 ? u - 160 : pmat == 2 ? u - 480 : 0;
            const int pc = pmat < 2 ? u / 20 : u / 54, ks = u - pc * (pmat < 2 ? 20 : 54);
            int rb, a3, a2, a1;
            if (pmat == 0) { if (ks < 16) { rb = 40 * ks; a3 = 20; a2 = 4; a1 = 8; } else { rb = 160 * (ks - 16) + 16; a3 = 80; a2 = 40; a1 = 20; } }
            else if (pmat == 1) { rb = ks < 8 ? 80 * ks : ks < 12 ? 160 * (ks - 8) + 64 : 80 * (ks - 12) + 32; a3 = ks >= 8 && ks < 12 ? 80 : 16; a2 = 8; a1 = 4; }
            else if (ks >= 16 && ks < 24) { rb = 224 * (ks - 16) + 96; a3 = ks < 23 ? 112 : 48; a2 = 8; a1 = 4; }
            else { rb = ks < 16 ? 112 * ks : (ks < 39 ? 112 * (ks - 24) + 32 : 112 * (ks - 39) + 64); a3 = 8; a2 = 4; a1 = 16; }
            const int ld = pmat == 1 ? 3456 : 1280, chmax = pmat != 1 ? 9 : (pc == 15 ? 5 : 13);
            const char* psrc = pmat == 0 ? wo + pc * 160 : pmat == 1 ? w1 + pc * 224 : w2 + pc * 160;
            const unsigned pb = L + 3 * CHUNK + hw * 8192, pv = g * ld + min(c16, chmax) * 16;
            cnt_wait(CN2(P_NONE), 4);
#pragma unroll 1
            for (int i = 0; i < 8; ++i) glds(pv, psrc + (rb + a3 * (i >> 2) + a2 * ((i >> 1) & 1) + a1 * (i & 1)) * ld, pb + i * 1024);
            wait_vm<8>();
            cnt_add(CN2(E_C1));
            copy_dev(kv + ((c0 + 2) & 3) * CHUNK, L + 2 * CHUNK, 42, hw, lane);
            wait_vm<10>();
            const unsigned a0 = pb + (8 * g + (c16 >> 2)) * 256 + (lane & 3) * 8;
            if (pmat == 1) {
                const unsigned pdst = WS_W1 + pc * 143360 + ks * 5120, pd2 = WS_W1 + pc * 143360 + 102400 + ks * 2048;
                const unsigned cv = c16 == 0 ? g * 16 : 0x80000000u;
#pragma unroll
                for (int s = 0; s < 7; ++s) {
                    const unsigned a1 = a0 + (s == 6 ? (pc == 15 ? 64 : 192) : s * 32);
                    const u32x4 f = ldstr2(a1, a1 + 1024);
                    bst16(rs, f, lane * 16, s < 2 ? pdst + s * 1024 : s < 4 ? pdst + (s + 1) * 1024 : s < 6 ? pd2 + (s - 4) * 1024 : pdst + 2048);
                    bst16(rs, __builtin_bit_cast(u32x4, mfma16(f, ones, z4)), cv, WS_CS + (ks * 1792 + pc * 112) * 4 + s * 64);
                }
            } else if (pmat < 3) {
                const unsigned a1 = a0 + (lane & 3) * 8, pdst = (pmat == 0 ? WS_WO : WS_W2) + u * 5120;
#pragma unroll
                for (int s = 0; s < 5; ++s) {
                    const unsigned a2 = s < 4 ? a1 + (s >> 1) * 64 + (s & 1) * 8 : a0 + 128;
                    bst16(rs, ldstr2(a2, a2 + 1024), lane * 16, pdst + s * 1024);
                }
            }
        }
        wait_vm<0>();
        cnt_add(CN2(E_C2));
        if (cnt_arrive(CN2(H_PREP)) == 3 && lane == 0) set_flag(ws, FL_PR + cid * 128, tag);
        STAMP(5);
        cnt_wait<3>(CN2(H_PREP), 4);
        {
            const u32x4 e = {EMPTY, EMPTY, EMPTY, EMPTY};
#pragma unroll 1
            for (int p = hw; p < 44; p += 4) bst16(rs, e, lane * 16, WS_O + blockIdx.x * 48640 + p * 1024);
            bst16(rs, e, hw < 3 || lane < 32 ? lane * 16 : 0x80000000u, WS_O + blockIdx.x * 48640 + (44 + hw) * 1024);
        }
        poll(ws, FL_KV, (c0 * 32 + 96 + (lane & 31)) & 127, tag);
        wait_vm<0>();
        if (cnt_arrive(CN2(H_FILL)) == 3 && lane == 0) set_flag(ws, FL_MK + cid * 128, tag);
        cnt_wait<LONGNAP>(CN2(P_C0), 4);
        copy_dev(kv + ((c0 + 3) & 3) * CHUNK, L, 42, hw, lane);
        poll(ws, FL_PR, hw * 64 + lane, tag);
        wait_vm<0>();
        cnt_add(CN2(E_C3));
        cnt_wait<3>(CN2(E_C3), 4);
        STAMP(6);
        if (blockIdx.x == 0 && hw == 1 && lane == 0) set_flag(ws, WS_FL, tag);
        auto wocopy = [&](int p0, int p1) {
#pragma unroll 1
            for (int p = p0 + hw; p < p1; p += 4) {
                const int q = p + rotc * 20;
                glds_dev(lane * 16, ws + WS_WO + m * 102400 + (q >= 100 ? q - 100 : q) * 1024, L + L_WO + p * 1024);
            }
        };
        u32x4 r1[35], r2[9][8];
        unsigned g1[5], g1b[5], g2[9];
        d_bases(g1, WS_W1 + ct * 143360, ct, 20480);
        d_bases(g1b, WS_W1 + ct * 143360 + 102400, ct, 8192);
        e_bases(g2, WS_W2 + m * 276480, m, 30720);
        const unsigned vp = lane * 16 + hw * 1024, vq = lane * 16 + min(28 + hw, 29) * 1024;
        unsigned lp0 = L + vp, lp1 = lp0 + 61440, lp2 = lp0 + 122880, lq0 = L + vq, lq1 = lq0 + 61440, lq2 = lq0 + 122880;
        PIN(lp0); PIN(lp1); PIN(lp2); PIN(lq0); PIN(lq1); PIN(lq2);
#define LD2(G, THR) do { _Pragma("unroll") for (int j_ = 0; j_ < 7; ++j_) { r2[G][j_] = bld16(rs, vp, g2[G] + j_ * 4096); wait_vm<THR>(); } \
        r2[G][7] = bld16(rs, vq, g2[G]); wait_vm<THR>(); } while (0)
#define ST2E(G, j) do { constexpr int sl_ = (G) % 5; \
        ldsw16((j) < 7 ? (sl_ < 2 ? lp0 : sl_ < 4 ? lp1 : lp2) + (sl_ & 1) * 30720 + (j) * 4096 : (sl_ < 2 ? lq0 : sl_ < 4 ? lq1 : lq2) + (sl_ & 1) * 30720, r2[G][j]); } while (0)
#pragma unroll
        for (int i = 0; i < 35; ++i) r1[i] = bld16(rs, vp, i < 25 ? g1[i / 5] + (i % 5) * 4096 : g1b[(i - 25) / 2] + ((i - 25) & 1) * 4096);
        SBAR();
        if (hw == 3) {
            u32x4 c[10];
            const unsigned cv = min(lane & 31, 27) * 16 + (lane >> 5) * 71680;
#pragma unroll
            for (int ks = 0; ks < 10; ++ks) c[ks] = bld16(rs, cv, WS_CS + (ks * 1792 + ct * 112) * 4);
            f32x4 c4 = z4;
#pragma unroll
            for (int ks = 0; ks < 10; ++ks) c4 += __builtin_bit_cast(f32x4, c[ks]);
#pragma unroll
            for (int i = 0; i < 4; ++i) c4[i] += upper(c4[i]);
            if (lane < 28) *(LAS f32x4*)(size_t)(L + L_MISC + lane * 16) = c4;
        }
        LD2(7, 63); LD2(8, 63);
        cnt_wait<LONGNAP>(CN2(P_C1), 4);
        wocopy(0, 42);
        poll(ws, FL_MK, hw * 64 + lane, tag);
        cnt_add(CN2(E_MK));
        cnt_wait<LONGNAP>(CN2(P_C2), 4);
        wocopy(42, 100);
#pragma unroll
        for (int G = 0; G < 7; ++G) {
            const int p = (tb & 3) * 8 + hw * 2;
            glds_dev(lane * 16, ws + g2[G] + min(p, 28) * 1024, L + XCH + hw * 1024);
            glds_dev(lane * 16, ws + g2[G] + min(p + 1, 29) * 1024, L + XCH + hw * 1024);
        }
        wait_vm<0>();
        STAMP(7);
        __builtin_amdgcn_s_setprio(TAILPRIO);
        bar_raw();
#pragma unroll
        for (int i = 0; i < 10; ++i) ldsw16(lp0 + i * 4096, r1[i < 4 ? 25 + i : i - 4]);
        SBAR();
        LD2(6, THROT);
        bar();
        cnt_wait(CN2(M_LDS), 4);
#pragma unroll
        for (int i = 10; i < 35; ++i) {
            ldsw16((i < 15 ? lp0 : i < 30 ? lp1 : lp2) + (i % 15) * 4096, r1[i < 29 ? i - 4 : i]);
            SBAR();
            if (i == 17) cnt_add(CN2(M_W1A));
            if (i >= 18 && i - 18 < NEARLY) {
                const int e = i - 18, G = 3 + (e >> 3), j = e & 7;
                r2[G][j] = bld16(rs, j < 7 ? vp : vq, g2[G] + (j < 7 ? j * 4096 : 0));
                SBAR();
            }
        }
        STAMP(8);
        cnt_add(CN2(M_W1));
#pragma unroll
        for (int e = NEARLY; e < 24; ++e) {
            const int G = 3 + (e >> 3), j = e & 7;
            r2[G][j] = bld16(rs, j < 7 ? vp : vq, g2[G] + (j < 7 ? j * 4096 : 0));
            wait_vm<THROT>();
        }
        cnt_wait<3>(CN2(M_K8), 4);
#pragma unroll 1
        for (int p = hw; p < 60; p += 4) { glds_dev(lane * 16, ws + (p < 30 ? g2[0] + p * 1024 : g2[1] + (p - 30) * 1024), L + p * 1024); wait_vm<THROT>(); }
        cnt_wait<3>(CN2(M_T4), 4);
        copy_dev(ws + g2[2], L + 61440, 30, hw, lane);
        wait_vm<7>();
        STAMP(9);
        bar_raw();
#pragma unroll
        for (int iv = 0; iv < 7; ++iv) {
#pragma unroll
            for (int e = iv < 3 ? iv * 8 - (iv == 2) * 0 : 24 + (iv - 3) * 6; e < (iv < 3 ? iv * 8 + 8 : 24 + (iv - 2) * 6); ++e) {
                if (!(NOST && iv > 0)) switch (e >> 3) {
                    case 0: ST2E(3, e & 7); break;
                    case 1: ST2E(4, e & 7); break;
                    case 2: ST2E(5, e & 7); break;
                    case 3: ST2E(6, e & 7); break;
                    case 4: ST2E(7, e & 7); break;
                    default: ST2E(8, e & 7); break;
                }
                SBAR();
            }
            wait_vm<0>();
            bar();
        }
        asm volatile(".Lpf_end2:");
        STAMP_OUT(1);
        __builtin_amdgcn_endpgm();
    }

    u32x4 xr0, xr1; u32x2 xr2;
    u32x4 qa1, qb1; u32x2 qa2, qb2;
    const int kh = w & 1;
    {
        LANE_VARS();
        LOAD_TAG();
        u32x4 xf[20], xo[10], kf[20];
        const unsigned xrow = L + L_X + w * 20480 + (c16 >> 3) * 10240 + (c16 & 7) * 128;
        const int sw = c16 >> 1, t32 = (w & 1) * 16 + c16;
        unsigned xa = xrow + (((kh * 4 + g) ^ sw) << 4), xb = xrow + ((((kh ^ 1) * 4 + g) ^ sw) << 4), xc = xa + ((w ^ 1) - w) * 20480, xra[3], qw[5], kvo;
        {
            const int c0 = m * 10 + g, c1 = c0 + 4, c2 = m * 10 + 8 + (g >> 1);
            xra[0] = xrow + (c0 >> 3) * 1024 + (((c0 & 7) ^ sw) << 4);
            xra[1] = xrow + (c1 >> 3) * 1024 + (((c1 & 7) ^ sw) << 4);
            xra[2] = xrow + (c2 >> 3) * 1024 + (((c2 & 7) ^ sw) << 4) + (g & 1) * 8;
            const int d = (m & 1) * 16 + c16;
            kvo = m == 0 ? ((g >> 1) * 32 + t32) * 16 + (g & 1) * 8
                : m == 1 ? (g == 0 ? 1024 + t32 * 8 : 0x80000000u)
                         : (d <= 21 ? 1280 + (((w & 1) * 2 + (g & 1)) * 22 + d) * 16 + (g >> 1) * 8 : 0x80000000u);
            PIN(xa); PIN(xb); PIN(xc); PIN(xra[0]); PIN(xra[1]); PIN(xra[2]); PIN(kvo);
        }
#pragma unroll
        for (int s = 0; s < 5; ++s) {
            const int f0 = s * 16 + 4 * g, hh = f0 / 20, d0 = f0 - hh * 20;
            qw[s] = L + L_QX + (hh * 2 + (w >> 1)) * 1280 + (d0 < 16 ? ((d0 >> 3) * 32 + t32) * 16 + (d0 & 4) * 2 : 1024 + t32 * 8);
            PIN(qw[s]);
        }
        WQ_ADDR();
        unsigned b0 = wo0 + L_WQ1 + kh * 5120, b1 = wo1 + L_WQ1 + kh * 5120;
        PIN(b0); PIN(b1);
        wait_vm<0>();
        MAKE_TAG();
        if (m >= 4) {
            __builtin_amdgcn_s_sleep(PFDELAY);
            const char* wp = wq + (((idx >> 3) * 4 + (m & 3)) * 4 + w) * 12800;
            touch(lane * 128, wp, L + L_JUNK, 0x3ffffffffffffull);
            touch(lane * 128, wp + 6400, L + L_JUNK, 0x3ffffffffffffull);
        }
        f32x4 ka = z4, kc = z4, ssm = z4, ssq = z4, acc[2][5];
        u32x4 one = ones;
        PIN(ssm); PIN(ssq); PIN(one);
        if (m < 4) {
            cnt_wait(CNT(C_WKV), 4);
            const unsigned a0 = L + L_WKV + (8 * g + (c16 >> 2)) * 40 + (m & 1) * 32 + (lane & 3) * 8;
            const unsigned a1 = a0 + kh * 1280, a2 = a0 + (kh ^ 1) * 1280;
#pragma unroll
            for (int j = 0; j < 10; ++j) { kf[j] = ldstr2(a1 + j * 2560, a1 + j * 2560 + 160); kf[10 + j] = ldstr2(a2 + j * 2560, a2 + j * 2560 + 160); }
            if (m < 2) {
#pragma unroll
                for (int j = 0; j < 20; ++j) kc = mfma16(kf[j], ones, kc);
            } else {
#pragma unroll
                for (int j = 0; j < 20; ++j) kc = mfma16(ones, kf[j], kc);
            }
            PIN(kc);
        }
        cnt_add(CNT(C_KFUSED));
        cnt_spin(CNT(C_X), 4);
#pragma unroll
        for (int j = 0; j < 10; ++j) xf[j] = lds16(xa + j * 1024);
#pragma unroll
        for (int j = 0; j < 10; ++j) xf[10 + j] = lds16(xb + j * 1024);
#pragma unroll
        for (int j = 0; j < 10; ++j) xo[j] = lds16(xc + j * 1024);
        xr0 = lds16(xra[0]); xr1 = lds16(xra[1]); xr2 = lds8(xra[2]);
        SBAR();
        if (m < 2) {
#pragma unroll
            for (int j = 0; j < 20; ++j) ka = mfma16(kf[j], xf[j], ka);
        } else if (m < 4) {
#pragma unroll
            for (int j = 0; j < 20; ++j) ka = mfma16(xf[j], kf[j], ka);
        }
        PIN(ka);
#pragma unroll
        for (int j = 0; j < 20; ++j) {
            mma16(ssm, one, xf[j]); mma16(ssq, xf[j], xf[j]);
            if (j == 4) {
#pragma unroll
                for (int i = 0; i < 10; ++i) { PIN(xf[i]); PIN(xf[10 + i]); PIN(xo[i]); }
                PIN(xr0); PIN(xr1); PIN(xr2);
                cnt_add(CNT(C_XUSED));
            }
        }
        mma_done();
        STAMP(1);
        const float sd = (c16 & 2) ? ((c16 & 1) ? ssq[3] : ssq[2]) : ((c16 & 1) ? ssq[1] : ssq[0]);
        const float mean = ssm[0] * (1.f / D), rstd = __builtin_amdgcn_rsqf(fmaxf(__shfl(sd, (c16 >> 2) * 16 + c16) * (1.f / D) - mean * mean, 0.f) + LN_EPS);
        if (m < 4) {
            u32x2 pv;
            if (m < 2) {
                pv = u32x2{pk2((ka[0] - mean * kc[0]) * rstd, (ka[1] - mean * kc[1]) * rstd), pk2((ka[2] - mean * kc[2]) * rstd, (ka[3] - mean * kc[3]) * rstd)};
            } else {
                float v[4];
#pragma unroll
                for (int r = 0; r < 4; ++r) v[r] = (ka[r] - __shfl(mean, 4 * g + r) * kc[0]) * __shfl(rstd, 4 * g + r);
                pv = u32x2{pk2(v[0], v[1]), pk2(v[2], v[3])};
                if (m == 3 && c16 >= 4) pv = c16 == 4 ? u32x2{ONES2, ONES2} : u32x2{0u, 0u};
            }
            bst8(rs, pv, kvo, WS_KV + (tb * 2 + (w >> 1)) * KV_TILE);
        }
        SBAR();
        STAMP(2);
        u32x2 f[2][5][2];
        unsigned xs, xv;
        float sc = rstd * QSCALE, ms = -mean * sc;
        PIN(sc); PIN(ms);
        unsigned b2 = b0 + (L_WQ2 - L_WQ1), b3 = b1 + (L_WQ2 - L_WQ1), cq = CNT(0), cv = 0;
        PIN(b2); PIN(b3); PIN(cq);
        auto qhalf = [&](int j, int s, int e) { return ldstr((j < 4 ? (e ? b1 : b0) + j * 10240 : (e ? b3 : b2) + j % 6 * 10240) + s * 32); };
        auto kvflag = [&] {
            if (m < 4) {
                wait_vm<0>();
                if (cnt_arrive(CNT(C_KVST)) == 3 && lane == 0) set_flag(ws, FL_KV + (tb * 4 + m) * 128, tag);
            }
        };
        if (FLAGAT < 0) kvflag();
        cnt_spin(CNT(C_WQ), 4);
#pragma unroll
        for (int s = 0; s < 5; ++s) { f[0][s][0] = qhalf(0, s, 0); f[0][s][1] = qhalf(0, s, 1); }
        SBAR();
#pragma unroll
        for (int j = 0; j < 10; ++j) {
            if (j < 8 && !(j & 1)) cv = *(volatile LAS unsigned*)(size_t)(cq + (C_WQ + j / 2 + 1) * 4);
            if (j < 9 && (j & 1) && __builtin_expect((unsigned)__builtin_amdgcn_readfirstlane(cv) < 4u, 0)) cnt_spin(CNT(C_WQ + j / 2 + 1), 4);
            asm volatile("" ::: "memory");
            SBAR();
#pragma unroll
            for (int s = 0; s < 5; ++s) {
                const u32x4 fr = {f[j & 1][s][0][0], f[j & 1][s][0][1], f[j & 1][s][1][0], f[j & 1][s][1][1]};
                if (j == 0) mma16z(acc[0][s], fr, xf[j]); else mma16(acc[0][s], fr, xf[j]);
                SBAR();
                if (j < 9) f[(j + 1) & 1][s][0] = qhalf(j + 1, s, 0);
                SBAR();
                if (j == 0) mma16z(acc[1][s], fr, xo[j]); else mma16(acc[1][s], fr, xo[j]);
                SBAR();
                if (j < 9) f[(j + 1) & 1][s][1] = qhalf(j + 1, s, 1);
                SBAR();
            }
            if (j == FLAGAT) {   }
            if (j == FLAGAT) kvflag();
            if (j == 3) asm volatile("s_mov_b64 exec, 1\n\tds_add_u32 %0, %1 offset:%2\n\ts_mov_b64 exec, -1" ::"v"(cq), "v"(1u), "i"(C_R1 * 4) : "memory");
            if (j == FLAGAT) {   }
            if (j == 5) { xs = L + L_WQ1 + (w ^ 1) * 5120 + lane * 16; xv = L + L_WQ1 + w * 5120 + lane * 16; PIN(xs); PIN(xv); }
            if (j == 7) cnt_spin(CNT(C_R1), 4);
        }
        STAMP(3);
        if (m >= 4) wait_vm<0>();
        mma_done();
        send_half(acc[1], xs);
        cnt_add(CNT(C_XCH + (w >> 1)));
        if (w == 0 && lane < M_NUM) *(LAS unsigned*)(size_t)CN2(lane) = 0u;
        f32x4 c5[5], p5[5];
#pragma unroll 1
        for (;;) {
            const unsigned n1 = *(volatile LAS unsigned*)(size_t)CNT(C_CS), n2 = *(volatile LAS unsigned*)(size_t)CNT(C_XCH + (w >> 1));
#pragma unroll
            for (int s = 0; s < 5; ++s) {
                c5[s] = *(volatile LAS f32x4*)(size_t)(L + L_CSX + 1280 + s * 64 + g * 16);
                p5[s] = *(volatile LAS f32x4*)(size_t)(xv + s * 1024);
            }
            if (__builtin_amdgcn_readfirstlane(n1 + n2) == 3) break;
        }
#pragma unroll
        for (int s = 0; s < 5; ++s) {
            const f32x4 v = (acc[0][s] + p5[s]) * sc + c5[s] * ms;
            *(LAS u32x2*)(size_t)qw[s] = u32x2{pk2(v[0], v[1]), pk2(v[2], v[3])};
        }
        bar();
        const int r32 = lane & 31, h = lane >> 5;
        const unsigned qb = L + L_QX + ((w & 1) * 4 + (w >> 1)) * 1280;
        qa1 = lds16(qb + lane * 16); qb1 = lds16(qb + 2560 + lane * 16);
        qa2 = u32x2{lds4(qb + 1024 + r32 * 8 + h * 4), 0u}; qb2 = u32x2{lds4(qb + 2560 + 1024 + r32 * 8 + h * 4), 0u};
    }
    STAMP(4);
    {
        LANE_VARS();
        const int r32 = lane & 31, h = lane >> 5, vrow = min(r32, 21);
        const unsigned kconst = h == 0 ? 0x00003f80u : 0u;
        unsigned ak1 = L + lane * 16, ak2 = L + 1024 + r32 * 8 + h * 4, av = L + 1280 + (h * 22 + vrow) * 16;
        const f32x16 zero16 = {0, 0, 0, 0, 0, 0, 0, 0, 0, 0, 0, 0, 0, 0, 0, 0};
        f32x16 sa, sb, oa = zero16, ob = zero16;
        u32x4 ka1 = lds16(ak1), kb1 = lds16(ak1 + KV_TILE), va0 = lds16(av), va1 = lds16(av + VS), vb0 = va0, vb1 = va0;
        u32x2 ka2 = {lds4(ak2), kconst}, kb2 = {lds4(ak2 + KV_TILE), kconst};
        sa = mfma32(ka1, qa1, mfma32k8(ka2, qa2, zero16));
        sb = mfma32(ka1, qb1, mfma32k8(ka2, qb2, zero16));
        {
            const float ma = halfmax(max16(sa)), mb = halfmax(max16(sb));
            qa2[1] = h == 0 ? (pk2(-ma, 0.f) & 0xffffu) : 0u;
            qb2[1] = h == 0 ? (pk2(-mb, 0.f) & 0xffffu) : 0u;
        }
        sa = mfma32(ka1, qa1, mfma32k8(ka2, qa2, zero16));
        u32x4 pa0 = {0u, 0u, 0u, 0u}, pa1 = pa0, pb0 = pa0, pb1 = pa0;
        float e2 = 0.f, e3 = 0.f;
        unsigned vo0 = (r32 >> 4) * 20480 + h * 256 + (r32 & 15) * 16, vo1 = h ? 0x80000000u : vo0;
        PIN(pa0); PIN(pa1); PIN(pb0); PIN(pb1); PIN(ka2); PIN(kb2); PIN(vo0); PIN(vo1);
#define SLOT(R, MF, X, S, i, PP, jp, P, j) do { R = MF; PIN(R); SBAR(); X; SBAR(); \
        float e0 = EX2(S[i]), e1 = EX2(S[i + 1]); PIN(e0); PIN(e1); SBAR(); \
        PP[jp] = pk2(e2, e3); PIN(PP); SBAR(); \
        e2 = EX2(S[i + 2]); e3 = EX2(S[i + 3]); PIN(e2); PIN(e3); SBAR(); \
        P[j] = pk2(e0, e1); PIN(P); SBAR(); } while (0)
#define STEP(KC1, KC2, KN1, KN2, VC0, VC1, VP0, VP1, OK, OV, X0, X7) do { \
        SLOT(sb, mfma32k8(KC2, qb2, zero16), X0, sa, 0, pb1, 3, pa0, 0); \
        SLOT(sb, mfma32(KC1, qb1, sb), __builtin_amdgcn_s_waitcnt(0xC07F), sa, 4, pa0, 1, pa0, 2); \
        SLOT(ob, mfma32(VP1, pb1, ob), KC1 = lds16(ak1 + (OK) * KV_TILE), sa, 8, pa0, 3, pa1, 0); \
        SLOT(oa, mfma32(VC0, pa0, oa), KC2[0] = lds4(ak2 + (OK) * KV_TILE), sa, 12, pa1, 1, pa1, 2); \
        SLOT(sa, mfma32k8(KN2, qa2, zero16), (void)0, sb, 0, pa1, 3, pb0, 0); \
        SLOT(sa, mfma32(KN1, qa1, sa), VP0 = lds16(av + (OV) * KV_TILE), sb, 4, pb0, 1, pb0, 2); \
        SLOT(oa, mfma32(VC1, pa1, oa), VP1 = lds16(av + (OV) * KV_TILE + VS), sb, 8, pb0, 3, pb1, 0); \
        SLOT(ob, mfma32(VC0, pb0, ob), X7, sb, 12, pb1, 1, pb1, 2); } while (0)
        unsigned so1 = WS_O + (tb * 4 + (w & 2)) * 20480, so0 = so1 + (2 * m + (w & 1)) * 1024;
        so1 += 16384 + (2 * m + (w & 1)) * 256;
        asm volatile("" : "+s"(so0), "+s"(so1));
        unsigned ea = CN2(E_C1), e0a = CN2(E_C0), ev = 0, one = 1;
        PIN(ea); PIN(e0a); PIN(one); PIN(ak1); PIN(ak2); PIN(av);
        auto hook0 = [&](int j, int dn) {
            if (j == 1) ev = *(volatile LAS unsigned*)(size_t)e0a;
            if (j == 14) { ak1 += dn; ak2 += dn; PIN(ak1); PIN(ak2); }
            if (j == 15) { av += dn; ea += 4; PIN(av); PIN(ea); }
        };
        auto hook7 = [&](int j) {
            if (j == 0)
                asm volatile("s_mov_b64 exec, 1\n\tds_add_u32 %0, %1 offset:%2\n\ts_mov_b64 exec, -1" ::"v"(ea), "v"(one), "i"((P_NONE - E_C1) * 4) : "memory");
            if (j == 1 && __builtin_expect((unsigned)__builtin_amdgcn_readfirstlane(ev) < 4u, 0)) cnt_spin(e0a, 4);
            if (j == 9) ev = *(volatile LAS unsigned*)(size_t)ea;
            if (j == 10 && __builtin_expect((unsigned)__builtin_amdgcn_readfirstlane(ev) < 4u, 0))
                cnt_wait(ea, 4);
        };
        STAMP(5);
#pragma unroll 1
        for (int c = 0; c < 4; ++c) {
            const int dn = c == 2 ? -2 * CHUNK : CHUNK;
#pragma unroll
            for (int j = 0; j < 16; j += 2) {
                STEP(ka1, ka2, kb1, kb2, va0, va1, vb0, vb1, (j + 2) & 15, j + 1, hook0(j, dn), hook7(j));
                STEP(kb1, kb2, ka1, ka2, vb0, vb1, va0, va1, (j + 3) & 15, (j + 2) & 15, hook0(j + 1, dn), hook7(j + 1));
            }
        }
        pb1[3] = pk2(e2, e3);
        ob = mfma32(vb1, pb1, ob);
        STAMP(6);
        u32x4 r0, r1, r2;
        auto finish = [&](const f32x16& pa, const f32x16& pb, float la, float lb) {
            const float ra = __builtin_amdgcn_rcpf(la), rb = __builtin_amdgcn_rcpf(lb);
            r0 = u32x4{keep(pk2(pa[0] * ra, pa[1] * ra)), pk2(pa[2] * ra, pa[3] * ra), keep(pk2(pa[4] * ra, pa[5] * ra)), pk2(pa[6] * ra, pa[7] * ra)};
            r1 = u32x4{keep(pk2(pb[0] * rb, pb[1] * rb)), pk2(pb[2] * rb, pb[3] * rb), keep(pk2(pb[4] * rb, pb[5] * rb)), pk2(pb[6] * rb, pb[7] * rb)};
            r2 = u32x4{keep(pk2(pa[8] * ra, pa[9] * ra)), pk2(pa[10] * ra, pa[11] * ra), keep(pk2(pb[8] * rb, pb[9] * rb)), pk2(pb[10] * rb, pb[11] * rb)};
        };
        {
            float am = 0.f;
#pragma unroll
            for (int i = 0; i < 12; i += 2) am = amax3(am, oa[i], oa[i + 1]);
            const float la = upper(oa[8]);
            asm volatile("s_nop 7" : "+v"(ob));
            const float lb = upper(ob[8]);
#pragma unroll
            for (int i = 0; i < 12; i += 2) am = amax3(am, ob[i], ob[i + 1]);
            finish(oa, ob, la, lb);
            const bool ok = (fminf(la, lb) >= 1e-18f) & (fmaxf(la, lb) <= 1e18f) & (am <= 1e37f);
            if (__builtin_expect(__ballot(!ok) != 0, 0)) {
                __builtin_amdgcn_fence(__ATOMIC_ACQUIRE, "agent");
                f32x16 xa, xb;
#pragma unroll 1
                for (int qt = 0; qt < 2; ++qt) {
                    xb = attn_exact(ws + WS_KV, qt ? qb1 : qa1, qt ? qb2[0] : qa2[0], lane, vrow);
                    if (qt == 0) xa = xb;
                }
                finish(xa, xb, upper(xa[8]), upper(xb[8]));
            }
        }
        bst16(rs, r0, vo0, so0);
        bst16(rs, r1, vo0 + 512, so0);
        bst16(rs, r2, vo1, so1);
        STAMP(7);
    }
    u32x4 wd[12], ae[RE][2], we[PE];
    unsigned ge[9], e0, e1, ehv, eprobe, a0;
    const unsigned ehs = WS_F + (tb * 4 + (w & 2)) * 55296 + 16384;
    {
        LANE_VARS();
        const unsigned wl = L + lane * 16;
        f32x4 acc[5];
        u32x4 act[10][2], wf[12];
        unsigned wa = wl + L_WO, wb = wa + 51200, gb[5];
        rot_bases(gb, WS_O + (tb * 4 + w) * 20480, rotc, 4096);
        unsigned v0 = lane * 16, v1 = v0 + 1024, hv = c16 * 256;
        unsigned sv = g == 0 ? c16 * 8 : 0x80000000u, h2v = (g & 1) ? 0x80000000u : (g >> 1) * 256 + c16 * 16;
        PIN(v0); PIN(v1); PIN(hv);
        auto sub = [](int n) { return n < 60 ? (n % 3 == 2 ? 4 : n % 3) : 2 + (n & 1); };
        auto kst = [](int n) { return n < 60 ? n / 3 : (n - 60) / 2; };
        auto wac = [&](int n) { return (kst(n) < 10 ? wa : wb) + (kst(n) % 10) * 5120 + sub(n) * 1024; };
        kstart<RC, 2, 2048>(act, rs, ws, v0, v1, gb,
                           [&] { return __builtin_amdgcn_raw_buffer_load_b32(rs, hv, WS_O + (tb * 4 + w) * 20480 + 16384, SC1) == EMPTY; }, [&] {
            bar();
            PIN(wa); PIN(wb); PIN(sv); PIN(h2v);
#pragma unroll
            for (int n = 0; n < 12; ++n) { wf[n] = lds16(wac(n)); SBAR(); }
            res_init(acc, xr0, xr1, xr2);
            wait_vm<0>();
        }, [] {});
        STAMP(8);
        const unsigned hb = WS_H + (tb * 4 + w) * 20480;
        unsigned t = 0;
        u64 bad = 0;
        static_for<0, 100>([&](auto nn) {
            constexpr int n = nn.v, j = n < 54 ? n / 6 : 0, i = n % 6, q0 = areq(j, RC, 10), q1 = areq(j + 1, RC, 10);
            if (n % 3 == 0) { lgkm<9>(); SBAR(); }
            mma16(acc[sub(n)], wf[n % 12], act[kst(n) >> 1][kst(n) & 1]);
            SBAR();
            if (n + 12 < 100) wf[n % 12] = lds16(wac(n + 12));
            if (n < 54) {
                if (i < 4 && q0 + i / 2 < q1) act[(q0 + i / 2) % 10][i & 1] = aload<2, 2048>(rs, (i & 1) ? v1 : v0, gb, (q0 + i / 2) % 10);
                if (i == 3) { t = max(max(act[j + 1][1][0], act[j + 1][1][2]), max(act[j + 1][0][0], act[j + 1][0][2])); PIN(t); }
                if (i == 4) { bad = __ballot(t == EMPTY); asm volatile("" : "+s"(bad)); }
                if (i == 5 && __builtin_expect(bad != 0, 0))
                    refetch(act[j + 1][0], act[j + 1][1], ws + gb[(j + 1) / 2] + ((j + 1) & 1) * 2048, v0, v1);
            }
            if (n == 68) {
                xr0 = u32x4{keep(pk2(acc[0][0], acc[0][1])), pk2(acc[0][2], acc[0][3]), keep(pk2(acc[1][0], acc[1][1])), pk2(acc[1][2], acc[1][3])};
                bst16(rs, xr0, v0, hb + m * 1024);
            }
            if (n == 70) {
                xr2 = u32x2{keep(pk2(acc[4][0], acc[4][1])), pk2(acc[4][2], acc[4][3])};
                bst16(rs, rowpair(xr2, xr2), h2v, hb + 8192 + m * 512);
            }
            if (n == 92) { lgkm<0>(); bar_raw(); }
            if constexpr (n >= 93 && n < 99) {
#pragma unroll
                for (int e = 0; e < 2; ++e) {
                    const int k = (n - 93) * 2 + e;
                    wd[k] = lds16(wl + (k % 7 < 5 ? 16384 + (k / 7) * 5120 + (k % 7) * 1024 : (k / 7) * 2048 + (k % 7 - 5) * 1024));
                }
            }
            SBAR();
        });
        float sm = 0.f, sq = 0.f, sm2 = 0.f, sq2 = 0.f;
#pragma unroll
        for (int i = 0; i < 4; i += 2) { sm = dot2(xr0[i], ONES2, sm); sq = dot2(xr0[i], xr0[i], sq); sm2 = dot2(xr0[i + 1], ONES2, sm2); sq2 = dot2(xr0[i + 1], xr0[i + 1], sq2); }
        sm = dot2(xr2[0], ONES2, sm); sq = dot2(xr2[0], xr2[0], sq); sm2 = dot2(xr2[1], ONES2, sm2); sq2 = dot2(xr2[1], xr2[1], sq2);
        PIN(sm); PIN(sq); PIN(sm2); PIN(sq2);
        SBAR();
#pragma unroll
        for (int n = 0; n < 12; ++n) PIN(wd[n]);
        cnt_add(CN2(M_LDS));
        xr1 = u32x4{keep(pk2(acc[2][0], acc[2][1])), pk2(acc[2][2], acc[2][3]), keep(pk2(acc[3][0], acc[3][1])), pk2(acc[3][2], acc[3][3])};
        bst16(rs, xr1, v0, hb + (12 + m) * 1024);
#pragma unroll
        for (int i = 0; i < 4; i += 2) { sm = dot2(xr1[i], ONES2, sm); sq = dot2(xr1[i], xr1[i], sq); sm2 = dot2(xr1[i + 1], ONES2, sm2); sq2 = dot2(xr1[i + 1], xr1[i + 1], sq2); }
        sm = xsum16_32(sm + sm2); sq = xsum16_32(sq + sq2);
        bst8(rs, u32x2{keep(__float_as_uint(sm)), __float_as_uint(sq)}, sv, WS_ST + (tb * 4 + w) * 1024 + m * 128);
        SBAR();
        STAMP(9);
    }
    {
        f32x4 acc[2][7], c4[7];
        u32x4 act[12][2];
        u32x2 st[4];
        float mu[2], rp[2];
        const int t16 = (tb >> 1) * 8 + 2 * w;
        unsigned v0 = lane_id() * 16, v1 = v0 + 20480, sv = v0 >> 1, wa = L + v0, wb = wa + 16384 + 46080, gb[5];
        PIN(sv); PIN(wa); PIN(wb); PIN(v0); PIN(v1);
        d_bases(gb, WS_H + t16 * 20480, ct, 4096);
        unsigned fs[7], wc, fw, f2v, ca;
        auto late = [&] {
        zero_acc(acc[0]); zero_acc(acc[1]);
        e_bases(ge, WS_F + tb * 221184 + kh * 1024, m, 6144);
        {
            const unsigned fb = WS_F + t16 * 55296 + ct * 1024;
            fs[0] = fb; fs[1] = fb + 55296; fs[2] = fb + 16384 - ct * 512; fs[3] = fb + 24576; fs[4] = fb + 24576 + 55296; fs[5] = fb + 39936; fs[6] = fb + 39936 + 55296;
#pragma unroll
            for (int i = 0; i < 7; ++i) asm volatile("" : "+s"(fs[i]));
            const int lane = v0 >> 4, g = lane >> 4, c16 = lane & 15;
            f2v = (g & 1) * 55296 + (g >> 1) * 256 + c16 * 16; ca = L + L_MISC + g * 16;
            ehv = (g & 1) * 55296 + c16 * 512;
            wc = wa + 118784;
            PIN(f2v); PIN(ca); PIN(ehv); PIN(wc);
        }
        };
        unsigned hv = ((v0 >> 8) & 1) * 20480 + ((v0 >> 4) & 7) * 512;
        PIN(hv);
        kstart<RD, 4, 1024>(act, rs, ws, v0, v1, gb, [&] { return __builtin_amdgcn_raw_buffer_load_b32(rs, hv, WS_H + t16 * 20480 + 8192, SC1) == EMPTY; },
                            [&] { if (DRD < 0) wait_vm<0>(); else __builtin_amdgcn_s_sleep(DRD < 0 ? 0 : DRD); }, late);
        auto stload = [&] {
#pragma unroll
            for (int i = 0; i < 4; ++i) st[i] = __builtin_amdgcn_raw_buffer_load_b64(rs, sv + i * 512, WS_ST + t16 * 1024, SC1);
        };
        float sa[4];
        unsigned w1v = 0;
        auto dside = [&](int j, int i) {
            if ((j == 0 || j == 6) && i == 12) w1v = *(volatile LAS unsigned*)(size_t)(ca + 512 + (j ? M_W1 : M_W1A) * 4);
            if ((j == 1 || j == 7) && i == 12) {
                if (__builtin_expect((unsigned)__builtin_amdgcn_readfirstlane(w1v) < 4u, 0)) cnt_spin(CN2(j == 7 ? M_W1 : M_W1A), 4);
                asm volatile("" ::: "memory");
            }
            if (j == 5 && i == 4) stload();
            if (j == 9 && i == 6 && __builtin_expect(__any(max(max(st[0][0], st[1][0]), max(st[2][0], st[3][0])) == EMPTY), 0)) {
#pragma unroll 1
                for (int spin = 0; spin < (1 << 16); ++spin) {
                    asm volatile("" ::: "memory");
                    stload();
                    if (!__any(max(max(st[0][0], st[1][0]), max(st[2][0], st[3][0])) == EMPTY)) break;
                }
            }
            if ((j == 10 || j == 11) && (i == 4 || i == 8)) {
                const int k = (j - 10) * 2 + (i == 8);
                sa[k] = xsum16_32(__uint_as_float(st[k & 2][k & 1]) + __uint_as_float(st[(k & 2) + 1][k & 1])); PIN(sa[k]);
            }
            if (j == 12 && (i == 4 || i == 8)) {
                const int t = i == 8;
                mu[t] = sa[2 * t] * (-1.f / D);
                rp[t] = __builtin_amdgcn_rsqf(fmaxf(sa[2 * t + 1] * (1.f / D) - mu[t] * mu[t], 0.f) + LN_EPS) * LOG2E;
                PIN(mu[t]); PIN(rp[t]);
            }
        };
        STAMP(10);
        auto wad = [&](int n) {
            const int j = n < 56 ? n / 7 : n < 86 ? 8 + (n - 56) / 5 : n < 98 ? 14 + (n - 86) / 2 : 14 + (n - 98) / 3;
            const int s = n < 56 ? n % 7 : n < 86 ? (n - 56) % 5 : n < 98 ? (n - 86) % 2 : 2 + (n - 98) % 3;
            return n >= 116 ? wc + (n - 116) * 1024 : s >= 5 ? wa + j * 2048 + (s - 5) * 1024 : (j < 9 ? wa + 16384 + j * 5120 : wb + (j - 9) * 5120) + s * 1024; };
        kloop<0, 8, 20, 0, 140, 7, 5, RD, 4, 1024>(acc, act, wd, wad, rs, ws, v0, v1, gb, dside);
        kloop<8, 1, 20, 56, 140, 5, 5, RD, 4, 1024>(acc, act, wd, wad, rs, ws, v0, v1, gb, dside);
        asm volatile("s_mov_b64 exec, 1\n\tds_add_u32 %0, %1\n\ts_mov_b64 exec, -1" ::"v"(CN2(M_K8)), "v"(1u) : "memory");
        kloop<9, 5, 20, 61, 140, 5, 2, RD, 4, 1024>(acc, act, wd, wad, rs, ws, v0, v1, gb, dside);
        c4[0] = ldsf4(ca); c4[1] = ldsf4(ca + 64);
        SBAR();
        kloop<14, 6, 20, 86, 140, 2, 4, RD, 4, 1024>(acc, act, wd, wad, rs, ws, v0, v1, gb, dside);
        STAMP(11);
        u32x2 lo, hi;
        auto silu = [&](const f32x4& a, const f32x4& c, int t, u32x2& r, auto side) {
            f32x4 y = (a + mu[t] * c) * rp[t], e;
            PIN(y); SBAR();
#pragma unroll
            for (int i = 0; i < 4; ++i) { side(i); SBAR(); e[i] = EX2(-y[i]); PIN(e); SBAR(); }
            e = e * LOG2E + LOG2E; PIN(e); SBAR();
#pragma unroll
            for (int i = 0; i < 4; ++i) { side(4 + i); SBAR(); e[i] = __builtin_amdgcn_rcpf(e[i]); PIN(e); SBAR(); }
            e *= y;
            r = u32x2{keep(pk2(e[0], e[1])), pk2(e[2], e[3])};
            PIN(r); SBAR();
        };
        auto rest = [&](int q) {
            const int n = 98 + (q >> 1), j = n < 116 ? 14 + (n - 98) / 3 : 8 + (n - 116) / 2, s = n < 116 ? 2 + (n - 98) % 3 : 5 + (n - 116) % 2;
            mma16(acc[q & 1][s], wd[n % 12], act[j % 12][q & 1]);
            SBAR();
            if ((q & 1) && n + 12 < 140) wd[n % 12] = lds16(wad(n + 12));
            if (q >= 24 && q < 74 && q % 10 == 4) c4[q == 24 ? 6 : q / 10 - 1] = ldsf4(ca + (q == 24 ? 6 : q / 10 - 1) * 64);
        };
        auto early = [&](int q) {
            if (q == 0) eprobe = __builtin_amdgcn_raw_buffer_load_b32(rs, ehv, ehs, SC1);
            ae[q >> 1][q & 1] = aload<3, 2048>(rs, (q & 1) ? e1 : e0, ge, q >> 1);
        };
#pragma unroll
        for (int q = 0; q < 8; ++q) { rest(q); SBAR(); }
#pragma unroll
        for (int b = 0; b < 10; ++b) {
            const int ut = b < 4 ? b >> 1 : b < 6 ? b & 1 : (b - 6) >> 1, ui = b < 4 ? b & 1 : b < 6 ? 2 : 3 + (b & 1), us = ui < 2 ? ui : ui == 2 ? 6 : ui - 1;
            silu(acc[ut][ui], c4[us], ut, (b & 1) ? hi : lo, [&](int i) { if (8 + b * 8 + i < 84) rest(8 + b * 8 + i); });
            if (b == 5) bst16(rs, rowpair(lo, hi), f2v, fs[2]);
            else if (b & 1) bst16(rs, u32x4{lo[0], lo[1], hi[0], hi[1]}, b < 5 ? v0 : fw, fs[b >> 1]);
            if (b == 1) cnt_add(CN2(M_T4));
            if (b == 1) {   } if (b == 3) {   } if (b == 5) {   } if (b == 7) {   } if (b == 9) {   }
            if (b == 5) {
                PIN(v0);
                fw = ct < 15 ? v0 : 0x80000000u; e0 = v0 + w * 55296; e1 = v0 + (w ^ 1) * 55296; a0 = L + v0 + kh * 5120;
                PIN(fw); PIN(e0); PIN(e1); PIN(a0);
            }
            SBAR();
        }
        bar_raw();
        mma_done();
#pragma unroll
        for (int n = 0; n < PE; ++n) we[n] = lds16(a0 + (n / 5) * 10240 + (n % 5) * 1024);
#pragma unroll
        for (int t = 0; t < 2; ++t) {
            u32x4 r;
#pragma unroll
            for (int u = 0; u < 2; ++u) {
                const f32x4 y = (acc[t][5 + u] + mu[t] * c4[4 + u]) * rp[t];
                f32x4 e = f32x4{EX2(-y[0]), EX2(-y[1]), EX2(-y[2]), EX2(-y[3])} * LOG2E + LOG2E;
                e = f32x4{__builtin_amdgcn_rcpf(e[0]), __builtin_amdgcn_rcpf(e[1]), __builtin_amdgcn_rcpf(e[2]), __builtin_amdgcn_rcpf(e[3])} * y;
                r[2 * u] = keep(pk2(e[0], e[1])); r[2 * u + 1] = pk2(e[2], e[3]);
            }
            bst16(rs, r, fw, fs[5 + t]);
        }
        SBAR();
#pragma unroll
        for (int q = 0; q < 2 * FIRSTFILL; ++q) { early(q); SBAR(); }
        STAMP(12);
    }
    {
        f32x4 acc[2][5];
        unsigned a1 = a0 + 61440, a2 = a0 + 122880;
        PIN(a1); PIN(a2);
        res_init(acc[0], xr0, xr1, xr2); zero_acc(acc[1]);
        if (__any(eprobe == EMPTY))
            kstart<RE, 3, 2048>(ae, rs, ws, e0, e1, ge, [&] { return __builtin_amdgcn_raw_buffer_load_b32(rs, ehv, ehs, SC1) == EMPTY; }, [] {}, [] {});
        if (__builtin_expect(missing(ae[0][0], ae[0][1]), 0)) refetch(ae[0][0], ae[0][1], ws + ge[0], e0, e1);
        SBAR();
        STAMP(13);
        auto wae = [&](int n) { const int q = n / 5 % 15; return (q < 6 ? a0 : q < 12 ? a1 : a2) + (q % 6) * 10240 + (n % 5) * 1024; };
#define GRP(G, WA, WN) kloop<3 * G, 3, 27, 15 * G, 135, 5, 5, RE, 3, 2048>(acc, ae, we, wae, rs, ws, e0, e1, ge, [](int, int) {})
        GRP(0, a0, a0 + 30720);
        bar_raw();

        GRP(1, a0 + 30720, a1);
        bar_raw();
        GRP(2, a1, a1 + 30720);
        bar_raw();
        GRP(3, a1 + 30720, a2); bar_raw();
        GRP(4, a2, a0); bar_raw();

        GRP(5, a0, a0 + 30720); bar_raw();
        GRP(6, a0 + 30720, a1); bar_raw();
        GRP(7, a1, a1 + 30720);
        GRP(8, a1 + 30720, a1);
        mma_done();
        LANE_VARS();
        const unsigned wl = L + lane * 16;
        send_half(acc[1], wl + 122880 + (w ^ 1) * 5120);
        bar();
        recv_half(acc[0], wl + 122880 + w * 5120);
        STAMP(14);
        res_pack(xr0, xr1, xr2, acc[0]);
        char* ob = out + (size_t)(tb * 64 + w * 16 + c16) * 1280 + m * 160;
        asm volatile("global_store_dwordx4 %0, %1, off nt\n\tglobal_store_dwordx4 %0, %2, off offset:64 nt\n\t"
                     "global_store_dwordx2 %3, %4, off offset:128 nt\n\ts_nop 1" ::"v"(ob + g * 16), "v"(xr0), "v"(xr1), "v"(ob + g * 8), "v"(xr2) : "memory");
    }
    asm volatile(".Lpf_end1:");
    STAMP_OUT(0);
    __builtin_amdgcn_endpgm();
}

extern "C" void kernel_launch(void* const* d_in, const int* in_sizes, int n_in, void* d_out, int out_size, void* d_ws, size_t ws_size,
                              hipStream_t stream) {
    (void)hipFuncSetAttribute((const void*)k_block, hipFuncAttributeMaxDynamicSharedMemorySize, LDS_BYTES);
    k_block<<<256, 512, LDS_BYTES, stream>>>((const char*)d_in[0], (const char*)d_in[1], (const char*)d_in[2], (const char*)d_in[3],
                                             (const char*)d_in[4], (const char*)d_in[6], (const char*)d_in[7], (char*)d_ws, (char*)d_out);
}
```

```cpp
#include <hip/hip_runtime.h>
#include <hip/hip_bf16.h>
#include <stdint.h>

typedef __attribute__((ext_vector_type(8))) __bf16 bf16x8;
typedef __attribute__((ext_vector_type(2))) __bf16 bf16x2;
typedef __attribute__((ext_vector_type(4))) short s16x4;
typedef __attribute__((ext_vector_type(4))) float f32x4;
typedef __attribute__((ext_vector_type(2))) float f32x2;
typedef __attribute__((ext_vector_type(16))) float f32x16;
typedef __attribute__((ext_vector_type(4))) unsigned u32x4;
typedef __attribute__((ext_vector_type(2))) unsigned u32x2;
typedef unsigned long long u64;
#define LAS __attribute__((address_space(3)))
#define GAS __attribute__((address_space(1)))
#define DI __device__ __forceinline__

constexpr int D = 640;
constexpr float LN_EPS = 1e-5f;
constexpr float LOG2E = 1.4426950408889634f;
constexpr float QSCALE = 0.22360679774997896f * LOG2E;

constexpr int KV_TILE = 2688;
constexpr int VS = 704;
constexpr unsigned WS_FL = 0;
constexpr unsigned FL_KV = 32768, FL_PR = 2 * 32768, FL_MK = 3 * 32768;
constexpr unsigned WS_KV = 4 * 32768;
constexpr unsigned WS_WO = WS_KV + 64 * KV_TILE;
constexpr unsigned WS_W1 = WS_WO + 8 * 20 * 5 * 1024;
constexpr unsigned WS_W2 = WS_W1 + 16 * 20 * 7 * 1024;
constexpr unsigned WS_CS = WS_W2 + 8 * 54 * 5 * 1024;
constexpr unsigned WS_O  = WS_CS + 20 * 1792 * 4;
constexpr unsigned WS_H  = WS_O + 128 * 20 * 1024;
constexpr unsigned WS_F  = WS_H + 128 * 20 * 1024;
constexpr unsigned WS_ST = WS_F + 128 * 54 * 1024;
constexpr unsigned WS_END = WS_ST + 2048 * 64;
constexpr unsigned EMPTY = 0xffffffffu;
static_assert((WS_END - WS_O) == 256 * 48640, "fill share per workgroup");

constexpr int CHUNK = 16 * KV_TILE;
constexpr int L_X = 0, L_WQ2 = 43008, L_WKV = 83968, L_WQ1 = 122880,
               L_CSX = 111104, L_QX = 152576, L_MISC = 162816;
constexpr int L_AUX = 3 * CHUNK, L_WO = CHUNK, L_CNT = 110592, L_JUNK = 113664, XCH = L_WO + 102400, LDS_BYTES = 163840;

#define STAMP_INIT()
#define STAMP(I)
#define STAMP_OUT(K)

#define THROT 3
#define THROT2 3
#define RC 6
#define RD 7
#define RE 7
#define TAILPRIO 0
#define FIRSTFILL 4
#define ONEWAY 1
#define NEARLY 12
#define PFDELAY 6
#define FLAGAT 0
#define P1PRIO 1
#define DRC -1
#define DRD 0
#define PE 10
#define NOST 0
#define LONGNAP 10
#define QROT (tb & 3)
#define WQT 0
DI void glds(unsigned voff, const void* sbase, unsigned lds) {
    asm volatile("s_mov_b32 m0, %2\n\ts_nop 0\n\tglobal_load_lds_dwordx4 %0, %1" ::"v"(voff), "s"(sbase), "s"(lds) : "memory");
}
DI void glds_dev(unsigned voff, const void* sbase, unsigned lds) {
    asm volatile("s_mov_b32 m0, %2\n\ts_nop 0\n\tglobal_load_lds_dwordx4 %0, %1 sc1" ::"v"(voff), "s"(sbase), "s"(lds) : "memory");
}
DI void touch(unsigned voff, const void* sbase, unsigned lds, u64 mask) {
    asm volatile("s_mov_b32 m0, %2\n\ts_mov_b64 exec, %3\n\tglobal_load_lds_dword %0, %1\n\ts_mov_b64 exec, -1" ::"v"(voff), "s"(sbase), "s"(lds), "s"(mask) : "memory");
}
template <int N> DI void wait_vm() { asm volatile("s_waitcnt vmcnt(%0)" ::"i"(N) : "memory"); }
DI void bar() { asm volatile("s_waitcnt lgkmcnt(0)\n\ts_barrier" ::: "memory"); }
DI void bar_raw() { asm volatile("s_barrier" ::: "memory"); }
#define PIN(x) asm volatile("" : "+v"(x))
#define SBAR() __builtin_amdgcn_sched_barrier(0)
DI unsigned pk2(float a, float b) { bf16x2 v = {(__bf16)a, (__bf16)b}; return __builtin_bit_cast(unsigned, v); }
DI float bflo(unsigned u) { return __uint_as_float(u << 16); }
DI float bfhi(unsigned u) { return __uint_as_float(u & 0xffff0000u); }
DI f32x4 mfma16(u32x4 a, u32x4 b, f32x4 c) {
    return __builtin_amdgcn_mfma_f32_16x16x32_bf16(__builtin_bit_cast(bf16x8, a), __builtin_bit_cast(bf16x8, b), c, 0, 0, 0);
}
DI f32x16 mfma32(u32x4 a, u32x4 b, f32x16 c) {
    return __builtin_amdgcn_mfma_f32_32x32x16_bf16(__builtin_bit_cast(bf16x8, a), __builtin_bit_cast(bf16x8, b), c, 0, 0, 0);
}
DI f32x16 mfma32k8(u32x2 a, u32x2 b, f32x16 c) {
    return __builtin_amdgcn_mfma_f32_32x32x8bf16_1k(__builtin_bit_cast(s16x4, a), __builtin_bit_cast(s16x4, b), c, 0, 0, 0);
}
DI u32x4 lds16(unsigned a) { return *(const LAS u32x4*)(size_t)a; }
DI u32x2 lds8(unsigned a) { return *(const LAS u32x2*)(size_t)a; }
DI unsigned lds4(unsigned a) { return *(const LAS unsigned*)(size_t)a; }
DI f32x4 ldsf4(unsigned a) { return *(const LAS f32x4*)(size_t)a; }
DI u32x2 ldstr(unsigned a) { return __builtin_bit_cast(u32x2, __builtin_amdgcn_ds_read_tr16_b64_v4i16((LAS s16x4*)(size_t)a)); }
DI u32x4 ldstr2(unsigned a0, unsigned a1) { u32x2 lo = ldstr(a0), hi = ldstr(a1); return u32x4{lo[0], lo[1], hi[0], hi[1]}; }
DI float dot2(unsigned a, unsigned b, float c) {
    return __builtin_amdgcn_fdot2_f32_bf16(__builtin_bit_cast(bf16x2, a), __builtin_bit_cast(bf16x2, b), c, false);
}
DI float xsum16_32(float v) {
    const auto a = __builtin_amdgcn_permlane16_swap(__float_as_uint(v), __float_as_uint(v), false, false);
    v = __uint_as_float(a[0]) + __uint_as_float(a[1]);
    const auto b = __builtin_amdgcn_permlane32_swap(__float_as_uint(v), __float_as_uint(v), false, false);
    return __uint_as_float(b[0]) + __uint_as_float(b[1]);
}
constexpr unsigned ONES2 = 0x3f803f80u;
#define EX2(v) __builtin_amdgcn_exp2f(v)
typedef __amdgpu_buffer_rsrc_t rsrc_t;
constexpr int SC1 = 16;
DI u32x4 bld16(rsrc_t r, unsigned v, unsigned s) { return __builtin_amdgcn_raw_buffer_load_b128(r, v, s, SC1); }
DI void bst16(rsrc_t r, u32x4 d, unsigned v, unsigned s) {
    __builtin_amdgcn_raw_buffer_store_b128(d, r, v, s, SC1);
    asm volatile("s_nop 1" ::"v"(d) : "memory");
}
DI void bst8(rsrc_t r, u32x2 d, unsigned v, unsigned s) { __builtin_amdgcn_raw_buffer_store_b64(d, r, v, s, SC1); }

DI void set_flag(char* ws, unsigned off, u64 tag) { __hip_atomic_store((GAS u64*)(ws + off), tag, __ATOMIC_RELAXED, __HIP_MEMORY_SCOPE_AGENT); }
DI void poll(char* ws, unsigned arr, int index, u64 tag) {
    const GAS u64* p = (const GAS u64*)(ws + arr + index * 128);
    bool ok = index < 0;
#pragma unroll 1
    for (int spin = 0; spin < (1 << 18); ++spin) {
        if (!ok) ok = __hip_atomic_load(p, __ATOMIC_RELAXED, __HIP_MEMORY_SCOPE_AGENT) == tag;
        if (__all(ok)) break;
        __builtin_amdgcn_s_sleep(1);
    }
    asm volatile("" ::: "memory");
}
DI void copy_dev(const char* src, unsigned lds, int n, int hw, int lane) {
#pragma unroll 1
    for (int p = hw; p < n; p += 4) glds_dev(lane * 16, src + p * 1024, lds + p * 1024);
}

DI void cnt_add(unsigned a) {
    asm volatile("s_waitcnt lgkmcnt(0)\n\ts_mov_b64 exec, 1\n\tds_add_u32 %0, %1\n\ts_mov_b64 exec, -1" ::"v"(a), "v"(1u) : "memory");
}
DI unsigned cnt_arrive(unsigned a) {
    unsigned r;
    asm volatile("s_waitcnt lgkmcnt(0)\n\ts_mov_b64 exec, 1\n\tds_add_rtn_u32 %0, %1, %2\n\ts_mov_b64 exec, -1\n\ts_waitcnt lgkmcnt(0)"
                 : "=&v"(r) : "v"(a), "v"(1u) : "memory");
    return __builtin_amdgcn_readfirstlane(r);
}
DI void cnt_spin(unsigned a, unsigned n) {
#pragma unroll 1
    while ((unsigned)__builtin_amdgcn_readfirstlane(*(volatile LAS unsigned*)(size_t)a) < n) {}
    asm volatile("" ::: "memory");
}
template <int NAP = 1> DI void cnt_wait(unsigned a, unsigned n) {
#pragma unroll 1
    while ((unsigned)__builtin_amdgcn_readfirstlane(*(volatile LAS unsigned*)(size_t)a) < n) __builtin_amdgcn_s_sleep(NAP);
    asm volatile("" ::: "memory");
}
DI unsigned keep(unsigned v) { return min(v, EMPTY - 1); }
DI bool missing(const u32x4& a, const u32x4& b) { return __any(max(max(a[0], a[2]), max(b[0], b[2])) == EMPTY); }
DI void refetch(u32x4& a, u32x4& b, const char* base, unsigned v0, unsigned v1) {
#pragma unroll 1
    for (int spin = 0; spin < (1 << 16); ++spin) {
        asm volatile("global_load_dwordx4 %0, %2, %4 sc1\n\tglobal_load_dwordx4 %1, %3, %4 sc1\n\ts_waitcnt vmcnt(0)"
                     : "+v"(a), "+v"(b) : "v"(v0), "v"(v1), "s"(base) : "memory");
        if (!missing(a, b)) break;
    }
}
DI u32x4 rowpair(const u32x2& a, const u32x2& b) {
    const auto s0 = __builtin_amdgcn_permlane16_swap(a[0], b[0], false, false), s1 = __builtin_amdgcn_permlane16_swap(a[1], b[1], false, false);
    return u32x4{s0[0], s1[0], s0[1], s1[1]};
}
DI int lane_id() {
    int l;
    asm volatile("v_mbcnt_lo_u32_b32 %0, -1, 0\n\tv_mbcnt_hi_u32_b32 %0, -1, %0" : "=v"(l));
    return l;
}
#define LANE_VARS() const int lane = lane_id(), g = lane >> 4, c16 = lane & 15; (void)g; (void)c16
DI float halfmax(float m) {
    auto r = __builtin_amdgcn_permlane32_swap(__float_as_uint(m), __float_as_uint(m), false, false);
    return fmaxf(__uint_as_float(r[0]), __uint_as_float(r[1]));
}
DI float upper(float v) {
    auto r = __builtin_amdgcn_permlane32_swap(__float_as_uint(v), __float_as_uint(v), false, false);
    return __uint_as_float(r[1]);
}
DI float max16(const f32x16& s) {
    float a = fmaxf(fmaxf(s[0], s[1]), s[2]), b = fmaxf(fmaxf(s[3], s[4]), s[5]);
    a = fmaxf(fmaxf(a, s[6]), s[7]); b = fmaxf(fmaxf(b, s[8]), s[9]);
    a = fmaxf(fmaxf(a, s[10]), s[11]); b = fmaxf(fmaxf(b, s[12]), s[13]);
    a = fmaxf(fmaxf(a, s[14]), s[15]);
    return fmaxf(a, b);
}
DI float amax3(float m, float a, float b) {
    float r;
    asm("v_maximum3_f32 %0, %1, |%2|, |%3|" : "=v"(r) : "v"(m), "v"(a), "v"(b));
    return r;
}
DI f32x16 attn_exact(const char* kv, u32x4 q1, unsigned q2lo, int lane, int vrow) {
    const int r32 = lane & 31, h = lane >> 5;
    const f32x16 zero16 = {0, 0, 0, 0, 0, 0, 0, 0, 0, 0, 0, 0, 0, 0, 0, 0};
    const u32x2 q2 = {q2lo, 0u};
    float m = -INFINITY;
    f32x16 o = zero16;
#pragma unroll 1
    for (int t = 0; t < 64; ++t) {
        const GAS char* tp = (const GAS char*)kv + t * KV_TILE;
        const u32x4 fk1 = *(const GAS u32x4*)(tp + lane * 16);
        const u32x2 fk2 = {*(const GAS unsigned*)(tp + 1024 + r32 * 8 + h * 4), 0u};
        const u32x4 fv0 = *(const GAS u32x4*)(tp + 1280 + (h * 22 + vrow) * 16), fv1 = *(const GAS u32x4*)(tp + 1280 + VS + (h * 22 + vrow) * 16);
        f32x16 s = mfma32(fk1, q1, mfma32k8(fk2, q2, zero16));
        const float mn = fmaxf(m, halfmax(max16(s))), al = EX2(m - mn);
        m = mn;
        u32x4 p0, p1;
#pragma unroll
        for (int i = 0; i < 4; ++i) { p0[i] = pk2(EX2(s[2 * i] - m), EX2(s[2 * i + 1] - m)); p1[i] = pk2(EX2(s[8 + 2 * i] - m), EX2(s[9 + 2 * i] - m)); }
#pragma unroll
        for (int i = 0; i < 16; ++i) o[i] *= al;
        o = mfma32(fv1, p1, mfma32(fv0, p0, o));
    }
    return o;
}

DI void mma16(f32x4& c, const u32x4& a, const u32x4& b) {
    asm volatile("v_mfma_f32_16x16x32_bf16 %0, %1, %2, %0" : "+v"(c) : "v"(a), "v"(b));
}
DI void mma16z(f32x4& c, const u32x4& a, const u32x4& b) { asm volatile("v_mfma_f32_16x16x32_bf16 %0, %1, %2, 0" : "=&v"(c) : "v"(a), "v"(b)); }
DI void mma_done() { SBAR(); asm volatile("s_nop 7\n\ts_nop 1"); SBAR(); }
template <int NS> DI void zero_acc(f32x4 (&a)[NS]) {
#pragma unroll
    for (int s = 0; s < NS; ++s) { a[s] = f32x4{0, 0, 0, 0}; PIN(a[s]); }
}
template <int GS, int ASTR, int NG> DI u32x4 aload(rsrc_t rs, unsigned& v, const unsigned (&gb)[NG], int j) {
    const int o = (j % GS) * ASTR;
    PIN(v);
    return bld16(rs, v + (o & 4095), gb[j / GS] + (o & ~4095));
}
constexpr int areq(int j, int PD, int KT) {
    const int a = FIRSTFILL + 2 * j, b = PD + (j > 0 ? j - 1 : 0), c = a < b ? a : b;
    return c < KT ? c : KT;
}
template <int N> struct Int { static constexpr int v = N; };
template <int A, int B, class Fn> DI void static_for(Fn fn) { if constexpr (A < B) { fn(Int<A>{}); static_for<A + 1, B>(fn); } }
template <int N> DI void lgkm() { __builtin_amdgcn_s_waitcnt(0xC07F | (N << 8)); }
template <int K0, int NK, int KT, int F0, int FT, int NS, int NSN, int PD, int GS, int ASTR, int NA, int RING, int POOL, int NG, class WAddr, class Side>
DI void kloop(f32x4 (&acc)[2][NA], u32x4 (&act)[RING][2], u32x4 (&wf)[POOL], WAddr waddr, rsrc_t rs, const char* ws, unsigned& v0,
              unsigned& v1, const unsigned (&gb)[NG], Side side) {
    if constexpr (NK > 0) {
        constexpr int j = K0, q0 = areq(j, PD, KT), q1 = areq(j + 1, PD, KT), jc = j + 1 < KT ? j + 1 : 0, nn = NK > 1 ? NS : NSN;
        constexpr int issued = F0 + NS + POOL < FT ? F0 + NS + POOL : FT, need = F0 + NS + nn < FT ? F0 + NS + nn : FT;
        unsigned t = 0;
        u64 bad = 0;
        if (j == 0) { lgkm<POOL - NS>(); SBAR(); }
#pragma unroll
        for (int i = 0; i < 2 * NS; ++i) {
            const int n = F0 + (i >> 1);
            mma16(acc[i & 1][i >> 1], wf[n % POOL], act[j % RING][i & 1]);
            SBAR();
            if ((i & 1) && n + POOL < FT) wf[n % POOL] = lds16(waddr(n + POOL));
            if (!(i & 1) && q0 + i / 4 < q1) act[(q0 + i / 4) % RING][i / 2 & 1] = aload<GS, ASTR>(rs, (i & 2) ? v1 : v0, gb, q0 + i / 4);
            if (jc) {
                if (i == (NS > 2 ? 2 * NS - 6 : 0)) {
                    t = max(act[jc % RING][1][0], act[jc % RING][1][2]);
                    t = max(max(t, act[jc % RING][0][0]), act[jc % RING][0][2]); PIN(t);
                }
                if (i == (NS > 2 ? 2 * NS - 4 : 1)) { bad = __ballot(t == EMPTY); asm volatile("" : "+s"(bad)); }
                if (i == 2 * NS - 2 && __builtin_expect(bad != 0, 0))
                    refetch(act[jc % RING][0], act[jc % RING][1], ws + gb[jc / GS] + (jc % GS) * ASTR, v0, v1);
            }
            side(j, i);
            if (i == 2 * NS - 1 && need > F0 + NS) { SBAR(); lgkm<issued - need>(); }
            SBAR();
        }
        kloop<K0 + 1, NK - 1, KT, F0 + NS, FT, NS, NSN, PD, GS, ASTR>(acc, act, wf, waddr, rs, ws, v0, v1, gb, side);
    }
}
template <int PD, int GS, int ASTR, int RING, int NG, class Probe, class Before, class After>
DI void kstart(u32x4 (&act)[RING][2], rsrc_t rs, const char* ws, unsigned& v0, unsigned& v1, const unsigned (&gb)[NG], Probe probe,
               Before before, After after) {
    before();
    SBAR();
#pragma unroll 1
    for (int spin = 0; spin < (1 << 16); ++spin) {
        const bool miss = probe();
        SBAR();
#pragma unroll
        for (int k = 0; k < FIRSTFILL; ++k) {
            act[k][0] = aload<GS, ASTR>(rs, v0, gb, k); SBAR();
            act[k][1] = aload<GS, ASTR>(rs, v1, gb, k); SBAR();
        }
        if (!__any(miss)) break;
        asm volatile("" ::: "memory");
    }
    SBAR();
    after();
    SBAR();
    if (__builtin_expect(missing(act[0][0], act[0][1]), 0)) refetch(act[0][0], act[0][1], ws + gb[0], v0, v1);
    SBAR();
}
DI void e_bases(unsigned (&gb)[9], unsigned base, int m, int stride) {
    const int m3 = m % 3;
#pragma unroll
    for (int i = 0; i < 9; ++i) {
        const int q = i < 4 ? (i + m) & 3 : i < 6 ? 4 + ((i + (m >> 2)) & 1) : 6 + (i - 6 + m3 >= 3 ? i - 9 + m3 : i - 6 + m3);
        gb[i] = base + q * stride;
        asm volatile("" : "+s"(gb[i]));
    }
}
DI void d_bases(unsigned (&gb)[5], unsigned base, int ct, int stride) {
    const int c3 = ct % 3;
#pragma unroll
    for (int i = 0; i < 5; ++i) {
        gb[i] = base + (i < 3 ? (i + c3 >= 3 ? i + c3 - 3 : i + c3) : 3 + ((i + 1 + ct) & 1)) * stride;
        asm volatile("" : "+s"(gb[i]));
    }
}
template <int NG> DI void rot_bases(unsigned (&gb)[NG], unsigned base, int rot, int stride) {
#pragma unroll
    for (int i = 0; i < NG; ++i) { const int q = i + rot; gb[i] = base + (q >= NG ? q - NG : q) * stride; asm volatile("" : "+s"(gb[i])); }
}
DI void send_half(const f32x4 (&a)[5], unsigned dst) {
#pragma unroll
    for (int s = 0; s < 5; ++s) *(LAS f32x4*)(size_t)(dst + s * 1024) = a[s];
}
DI void recv_half(f32x4 (&a)[5], unsigned src) {
#pragma unroll
    for (int s = 0; s < 5; ++s) a[s] += ldsf4(src + s * 1024);
}
DI void ldsw16(unsigned a, const u32x4& v) { *(LAS u32x4*)(size_t)a = v; }
DI f32x4 unpk(unsigned r0, unsigned r1) { return f32x4{bflo(r0), bfhi(r0), bflo(r1), bfhi(r1)}; }
DI void res_init(f32x4 (&a)[5], const u32x4& x0, const u32x4& x1, const u32x2& x2) {
    a[0] = unpk(x0[0], x0[1]); a[1] = unpk(x0[2], x0[3]); a[2] = unpk(x1[0], x1[1]); a[3] = unpk(x1[2], x1[3]); a[4] = unpk(x2[0], x2[1]);
#pragma unroll
    for (int s = 0; s < 5; ++s) PIN(a[s]);
}
DI void res_pack(u32x4& x0, u32x4& x1, u32x2& x2, const f32x4 (&a)[5]) {
    x0 = u32x4{pk2(a[0][0], a[0][1]), pk2(a[0][2], a[0][3]), pk2(a[1][0], a[1][1]), pk2(a[1][2], a[1][3])};
    x1 = u32x4{pk2(a[2][0], a[2][1]), pk2(a[2][2], a[2][3]), pk2(a[3][0], a[3][1]), pk2(a[3][2], a[3][3])};
    x2 = u32x2{pk2(a[4][0], a[4][1]), pk2(a[4][2], a[4][3])};
}
enum { C_WKV, C_KFUSED, C_X, C_XUSED, C_WQ, C_CSP = C_WQ + 5, C_CS, C_KVST, C_R1, C_XCH, C_NUM = C_XCH + 2 };
enum { E_C1, E_C2, E_C3, E_MK, P_NONE, P_C0, P_C1, P_C2, E_C0, H_PREP, H_FILL, M_LDS, M_W1A, M_W1, M_K8, M_T4, M_NUM };

__global__ __launch_bounds__(512, 2) void k_block(const char* __restrict__ x, const char* __restrict__ wq, const char* __restrict__ wk,
                                                  const char* __restrict__ wv, const char* __restrict__ wo, const char* __restrict__ w1,
                                                  const char* __restrict__ w2, char* ws, char* __restrict__ out) {
    extern __shared__ __attribute__((aligned(16))) char smem[];
    const unsigned L = (unsigned)(size_t)(LAS char*)smem;
    const int w = __builtin_amdgcn_readfirstlane(threadIdx.x >> 6);
    const int xg = blockIdx.x & 7, idx = blockIdx.x >> 3;
    const int tb = ONEWAY ? xg * 4 + (idx >> 3) : (xg >> 1) * 8 + (idx >> 2), m = ONEWAY ? (idx & 7) : (xg & 1) * 4 + (idx & 3), cid = tb * 8 + m;
    const int ct = (m >> 2) * 8 + (tb & 1) * 4 + (m & 3);
    const rsrc_t rs = __builtin_amdgcn_make_buffer_rsrc(ws, 0, WS_END, 0x00020000);
    const u32x4 ones = {ONES2, ONES2, ONES2, ONES2};
    const f32x4 z4 = {0, 0, 0, 0};
    STAMP_INIT();

#define LOAD_TAG() u64 tag; asm volatile("global_load_dwordx2 %0, %1, %2 sc1" : "=v"(tag) : "v"(0u), "s"(ws + WS_FL) : "memory")
#define MAKE_TAG() do { PIN(tag); \
        tag = ((u64)(unsigned)__builtin_amdgcn_readfirstlane(tag >> 32) << 32) | (unsigned)__builtin_amdgcn_readfirstlane((unsigned)tag); \
        tag = (tag ^ 0x9E3779B97F4A7C15ull) * 0xBF58476D1CE4E5B9ull + 0x632BE59BD9B4E019ull; asm volatile("" : "+s"(tag)); } while (0)

#define CNT(i) (L + L_CNT + (i) * 4)
#define CN2(i) (L + L_MISC + 512 + (i) * 4)
#define WQ_AT(r) ((r) < 2 ? L_WQ1 + (r) * 20480 : L_WQ2 + (r) % 3 * 20480)
#define WQ_ADDR() const unsigned wrow = L + (8 * g + (c16 >> 2)) * 160 + (lane & 3) * 8, wo0 = wrow + (g & 1) * 640, wo1 = wrow + ((g & 1) ^ 1) * 640
    const int rq = QROT;
    const int rotc = ONEWAY ? m % 5 : m & 3;

#define WQ_SRC() unsigned vw[5]; _Pragma("unroll") for (int i = 0; i < 5; ++i) { \
        const int ci = i * 64 + lane, pos = (ci * 6554) >> 16, ch = ci - pos * 10, k = pos ^ (((pos >> 3) & 1) << 2); \
        vw[i] = k * 1280 + ch * 16; PIN(vw[i]); } \
    auto wqload = [&](int r) {     \
        _Pragma("unroll") for (int i = 0; i < 5; ++i) glds(vw[i], wq + m * 160 + hw * 40960 + (r + rq >= 5 ? r + rq - 5 : r + rq) * 163840, L + WQ_AT(r) + hw * 5120 + i * 1024); }
    if (w < 4) {
        const int lane = lane_id();
        {
            const int wi = idx * 4 + w;
            touch(lane * 128, x + xg * 327680 + wi * 2560, L + L_JUNK, 0xfffffull);
            touch(lane * 128, ((wi & 1) ? wv : wk) + min(wi >> 1, 49) * 512, L + L_JUNK, 0xfull);
            const char* pc; int l1, l2;
            asm volatile("s_getpc_b64 %0\n.Lpf_pc:\n\ts_mov_b32 %1, .Lpf_end1-.Lpf_pc\n\ts_mov_b32 %2, .Lpf_end2-.Lpf_pc" : "=s"(pc), "=s"(l1), "=s"(l2));
            touch(lane * 128, pc + max(min(wi * 512, max(l1, l2) - 512), 0), L + L_JUNK, 0xfull);
        }
        if (w == 0 && lane < C_NUM) *(LAS unsigned*)(size_t)CNT(lane) = 0u;
        bar();
    } else {
        const int hw = w - 4;
        LANE_VARS();
        __builtin_amdgcn_s_setprio(P1PRIO);
        if (m < 4) {
            const char* src = m < 2 ? wk : wv;
#pragma unroll 1
            for (int p = hw; p < 25; p += 4) glds(lane * 16, src + (p + 5 * rq >= 25 ? p + 5 * rq - 25 : p + 5 * rq) * 1024, L + L_WKV + p * 1024);
        }
        const unsigned vx0 = (lane >> 3) * 1280 + (((lane & 7) ^ g) << 4), vx1 = (8 + (lane >> 3)) * 1280 + (((lane & 7) ^ (4 + g)) << 4);
        const char* xb = x + (size_t)(tb * 64 + hw * 16) * 1280;
        int sg = m;
        auto xload = [&](int n) {
#pragma unroll 1
            for (int i = 0; i < n; ++i) {
                const int sp = sg - 2 * rq < 0 ? sg - 2 * rq + 10 : sg - 2 * rq;
                glds(vx0, xb + sg * 128, L + L_X + (hw * 20 + sp) * 1024);
                glds(vx1, xb + sg * 128, L + L_X + (hw * 20 + 10 + sp) * 1024);
                sg = sg == 9 ? 0 : sg + 1;
            }
        };
        xload(3);
        WQ_SRC();
        bar_raw();
        wait_vm<6>();
        cnt_add(CNT(C_WKV));
        xload(7);
        wqload(0);
        wait_vm<5>();
        cnt_add(CNT(C_X));
        wqload(1);
    }
    int role = w;
    asm volatile("" : "+s"(role));
    if (role >= 4) {
        const int hw = w - 4;
        LANE_VARS();
        u64 f0, tag;
        const int c0 = tb >> 3;
        {
            WQ_SRC();
            WQ_ADDR();
            f32x4 cs[5];
            u32x4 one = ones;
            auto wqsum = [&](int r) {
                u32x4 f[5];
#pragma unroll
                for (int s = 0; s < 5; ++s) f[s] = ldstr2(wo0 + WQ_AT(r) + hw * 5120 + s * 32, wo1 + WQ_AT(r) + hw * 5120 + s * 32);
#pragma unroll
                for (int s = 0; s < 5; ++s) mma16(cs[s], f[s], one);
            };
            STAMP(1);
            PIN(one);
            zero_acc(cs);
            cnt_spin(CNT(C_KFUSED), 4);
            wqload(2); wait_vm<10>(); cnt_add(CNT(C_WQ));
            cnt_spin(CNT(C_XUSED), 4);
            wqload(3); wait_vm<10>(); cnt_add(CNT(C_WQ + 1));
            asm volatile("global_load_dwordx2 %0, %1, %2 sc1" : "=v"(tag) : "v"(0u), "s"(ws + WS_FL) : "memory");
            wqload(4);
            wqsum(0); wqsum(1);
            wait_vm<11>(); cnt_add(CNT(C_WQ + 2)); wqsum(2);
            wait_vm<6>(); cnt_add(CNT(C_WQ + 3)); wqsum(3);
            wait_vm<0>(); cnt_add(CNT(C_WQ + 4));
            MAKE_TAG();
            f0 = __hip_atomic_load((const GAS u64*)(ws + FL_KV + (c0 * 32 + (lane & 31)) * 128), __ATOMIC_RELAXED, __HIP_MEMORY_SCOPE_AGENT);
            wqsum(4);
            mma_done();
            if (c16 == 0) {
#pragma unroll
                for (int s = 0; s < 5; ++s) *(LAS f32x4*)(size_t)(L + L_CSX + hw * 320 + s * 64 + g * 16) = cs[s];
            }
            if (cnt_arrive(CNT(C_CSP)) == 3) {
                const unsigned ca = L + L_CSX + (lane < 20 ? lane : 0) * 16;
                const f32x4 c4 = (ldsf4(ca) + ldsf4(ca + 320)) + (ldsf4(ca + 640) + ldsf4(ca + 960));
                *(LAS f32x4*)(size_t)(ca + 1280) = c4;
                cnt_add(CNT(C_CS));
            }
        }
        const char* kv = ws + WS_KV;
        STAMP(2);
        poll(ws, FL_KV, lane < 32 && f0 != tag ? c0 * 32 + lane : -1, tag);
        STAMP(3);
        copy_dev(kv + c0 * CHUNK, L, 42, hw, lane);
        u64 f1;
        asm volatile("global_load_dwordx2 %0, %1, %2 sc1" : "=v"(f1) : "v"(((c0 * 32 + 32 + lane) & 127) * 128), "s"(ws + FL_KV) : "memory");
        wait_vm<8>();
        STAMP(4);
        __builtin_amdgcn_s_setprio(2);
        bar_raw();
        wait_vm<0>();
        cnt_add(CN2(E_C0));
        PIN(f1);
        poll(ws, FL_KV, f1 != tag ? (c0 * 32 + 32 + lane) & 127 : -1, tag);
        copy_dev(kv + ((c0 + 1) & 3) * CHUNK, L + CHUNK, 42, hw, lane);
        {
            int u = blockIdx.x * 4 + hw;
            const int pmat = u < 160 ? 0 : (u < 480 ? 1 : (u < 912 ? 2 : 3));
            u = pmat == 0 ? u : pmat == 1 ? u - 160 : pmat == 2 ? u - 480 : 0;
            const int pc = pmat < 2 ? u / 20 : u / 54, ks = u - pc * (pmat < 2 ? 20 : 54);
            int rb, a3, a2, a1;
            if (pmat == 0) { if (ks < 16) { rb = 40 * ks; a3 = 20; a2 = 4; a1 = 8; } else { rb = 160 * (ks - 16) + 16; a3 = 80; a2 = 40; a1 = 20; } }
            else if (pmat == 1) { rb = ks < 8 ? 80 * ks : ks < 12 ? 160 * (ks - 8) + 64 : 80 * (ks - 12) + 32; a3 = ks >= 8 && ks < 12 ? 80 : 16; a2 = 8; a1 = 4; }
            else if (ks >= 16 && ks < 24) { rb = 224 * (ks - 16) + 96; a3 = ks < 23 ? 112 : 48; a2 = 8; a1 = 4; }
            else { rb = ks < 16 ? 112 * ks : (ks < 39 ? 112 * (ks - 24) + 32 : 112 * (ks - 39) + 64); a3 = 8; a2 = 4; a1 = 16; }
            const int ld = pmat == 1 ? 3456 : 1280, chmax = pmat != 1 ? 9 : (pc == 15 ? 5 : 13);
            const char* psrc = pmat == 0 ? wo + pc * 160 : pmat == 1 ? w1 + pc * 224 : w2 + pc * 160;
            const unsigned pb = L + 3 * CHUNK + hw * 8192, pv = g * ld + min(c16, chmax) * 16;
            cnt_wait(CN2(P_NONE), 4);
#pragma unroll 1
            for (int i = 0; i < 8; ++i) glds(pv, psrc + (rb + a3 * (i >> 2) + a2 * ((i >> 1) & 1) + a1 * (i & 1)) * ld, pb + i * 1024);
            wait_vm<8>();
            cnt_add(CN2(E_C1));
            copy_dev(kv + ((c0 + 2) & 3) * CHUNK, L + 2 * CHUNK, 42, hw, lane);
            wait_vm<10>();
            const unsigned a0 = pb + (8 * g + (c16 >> 2)) * 256 + (lane & 3) * 8;
            if (pmat == 1) {
                const unsigned pdst = WS_W1 + pc * 143360 + ks * 5120, pd2 = WS_W1 + pc * 143360 + 102400 + ks * 2048;
                const unsigned cv = c16 == 0 ? g * 16 : 0x80000000u;
#pragma unroll
                for (int s = 0; s < 7; ++s) {
                    const unsigned a1 = a0 + (s == 6 ? (pc == 15 ? 64 : 192) : s * 32);
                    const u32x4 f = ldstr2(a1, a1 + 1024);
                    bst16(rs, f, lane * 16, s < 2 ? pdst + s * 1024 : s < 4 ? pdst + (s + 1) * 1024 : s < 6 ? pd2 + (s - 4) * 1024 : pdst + 2048);
                    bst16(rs, __builtin_bit_cast(u32x4, mfma16(f, ones, z4)), cv, WS_CS + (ks * 1792 + pc * 112) * 4 + s * 64);
                }
            } else if (pmat < 3) {
                const unsigned a1 = a0 + (lane & 3) * 8, pdst = (pmat == 0 ? WS_WO : WS_W2) + u * 5120;
#pragma unroll
                for (int s = 0; s < 5; ++s) {
                    const unsigned a2 = s < 4 ? a1 + (s >> 1) * 64 + (s & 1) * 8 : a0 + 128;
                    bst16(rs, ldstr2(a2, a2 + 1024), lane * 16, pdst + s * 1024);
                }
            }
        }
        wait_vm<0>();
        cnt_add(CN2(E_C2));
        if (cnt_arrive(CN2(H_PREP)) == 3 && lane == 0) set_flag(ws, FL_PR + cid * 128, tag);
        STAMP(5);
        cnt_wait<3>(CN2(H_PREP), 4);
        {
            const u32x4 e = {EMPTY, EMPTY, EMPTY, EMPTY};
#pragma unroll 1
            for (int p = hw; p < 44; p += 4) bst16(rs, e, lane * 16, WS_O + blockIdx.x * 48640 + p * 1024);
            bst16(rs, e, hw < 3 || lane < 32 ? lane * 16 : 0x80000000u, WS_O + blockIdx.x * 48640 + (44 + hw) * 1024);
        }
        poll(ws, FL_KV, (c0 * 32 + 96 + (lane & 31)) & 127, tag);
        wait_vm<0>();
        if (cnt_arrive(CN2(H_FILL)) == 3 && lane == 0) set_flag(ws, FL_MK + cid * 128, tag);
        cnt_wait<LONGNAP>(CN2(P_C0), 4);
        copy_dev(kv + ((c0 + 3) & 3) * CHUNK, L, 42, hw, lane);
        poll(ws, FL_PR, hw * 64 + lane, tag);
        wait_vm<0>();
        cnt_add(CN2(E_C3));
        cnt_wait<3>(CN2(E_C3), 4);
        STAMP(6);
        if (blockIdx.x == 0 && hw == 1 && lane == 0) set_flag(ws, WS_FL, tag);
        auto wocopy = [&](int p0, int p1) {
#pragma unroll 1
            for (int p = p0 + hw; p < p1; p += 4) {
                const int q = p + rotc * 20;
                glds_dev(lane * 16, ws + WS_WO + m * 102400 + (q >= 100 ? q - 100 : q) * 1024, L + L_WO + p * 1024);
            }
        };
        u32x4 r1[35], r2[9][8];
        unsigned g1[5], g1b[5], g2[9];
        d_bases(g1, WS_W1 + ct * 143360, ct, 20480);
        d_bases(g1b, WS_W1 + ct * 143360 + 102400, ct, 8192);
        e_bases(g2, WS_W2 + m * 276480, m, 30720);
        const unsigned vp = lane * 16 + hw * 1024, vq = lane * 16 + min(28 + hw, 29) * 1024;
        unsigned lp0 = L + vp, lp1 = lp0 + 61440, lp2 = lp0 + 122880, lq0 = L + vq, lq1 = lq0 + 61440, lq2 = lq0 + 122880;
        PIN(lp0); PIN(lp1); PIN(lp2); PIN(lq0); PIN(lq1); PIN(lq2);
#define LD2(G, THR) do { _Pragma("unroll") for (int j_ = 0; j_ < 7; ++j_) { r2[G][j_] = bld16(rs, vp, g2[G] + j_ * 4096); wait_vm<THR>(); } \
        r2[G][7] = bld16(rs, vq, g2[G]); wait_vm<THR>(); } while (0)
#define ST2E(G, j) do { constexpr int sl_ = (G) % 5; \
        ldsw16((j) < 7 ? (sl_ < 2 ? lp0 : sl_ < 4 ? lp1 : lp2) + (sl_ & 1) * 30720 + (j) * 4096 : (sl_ < 2 ? lq0 : sl_ < 4 ? lq1 : lq2) + (sl_ & 1) * 30720, r2[G][j]); } while (0)
#pragma unroll
        for (int i = 0; i < 35; ++i) r1[i] = bld16(rs, vp, i < 25 ? g1[i / 5] + (i % 5) * 4096 : g1b[(i - 25) / 2] + ((i - 25) & 1) * 4096);
        SBAR();
        if (hw == 3) {
            u32x4 c[10];
            const unsigned cv = min(lane & 31, 27) * 16 + (lane >> 5) * 71680;
#pragma unroll
            for (int ks = 0; ks < 10; ++ks) c[ks] = bld16(rs, cv, WS_CS + (ks * 1792 + ct * 112) * 4);
            f32x4 c4 = z4;
#pragma unroll
            for (int ks = 0; ks < 10; ++ks) c4 += __builtin_bit_cast(f32x4, c[ks]);
#pragma unroll
            for (int i = 0; i < 4; ++i) c4[i] += upper(c4[i]);
            if (lane < 28) *(LAS f32x4*)(size_t)(L + L_MISC + lane * 16) = c4;
        }
        LD2(7, 63); LD2(8, 63);
        cnt_wait<LONGNAP>(CN2(P_C1), 4);
        wocopy(0, 42);
        poll(ws, FL_MK, hw * 64 + lane, tag);
        cnt_add(CN2(E_MK));
        cnt_wait<LONGNAP>(CN2(P_C2), 4);
        wocopy(42, 100);
#pragma unroll
        for (int G = 0; G < 7; ++G) {
            const int p = (tb & 3) * 8 + hw * 2;
            glds_dev(lane * 16, ws + g2[G] + min(p, 28) * 1024, L + XCH + hw * 1024);
            glds_dev(lane * 16, ws + g2[G] + min(p + 1, 29) * 1024, L + XCH + hw * 1024);
        }
        wait_vm<0>();
        STAMP(7);
        __builtin_amdgcn_s_setprio(TAILPRIO);
        bar_raw();
#pragma unroll
        for (int i = 0; i < 10; ++i) ldsw16(lp0 + i * 4096, r1[i < 4 ? 25 + i : i - 4]);
        SBAR();
        LD2(6, THROT);
        bar();
        cnt_wait(CN2(M_LDS), 4);
#pragma unroll
        for (int i = 10; i < 35; ++i) {
            ldsw16((i < 15 ? lp0 : i < 30 ? lp1 : lp2) + (i % 15) * 4096, r1[i < 29 ? i - 4 : i]);
            SBAR();
            if (i == 17) cnt_add(CN2(M_W1A));
            if (i >= 18 && i - 18 < NEARLY) {
                const int e = i - 18, G = 3 + (e >> 3), j = e & 7;
                r2[G][j] = bld16(rs, j < 7 ? vp : vq, g2[G] + (j < 7 ? j * 4096 : 0));
                SBAR();
            }
        }
        STAMP(8);
        cnt_add(CN2(M_W1));
#pragma unroll
        for (int e = NEARLY; e < 24; ++e) {
            const int G = 3 + (e >> 3), j = e & 7;
            r2[G][j] = bld16(rs, j < 7 ? vp : vq, g2[G] + (j < 7 ? j * 4096 : 0));
            wait_vm<THROT>();
        }
        cnt_wait<3>(CN2(M_K8), 4);
#pragma unroll 1
        for (int p = hw; p < 60; p += 4) { glds_dev(lane * 16, ws + (p < 30 ? g2[0] + p * 1024 : g2[1] + (p - 30) * 1024), L + p * 1024); wait_vm<THROT>(); }
        cnt_wait<3>(CN2(M_T4), 4);
        copy_dev(ws + g2[2], L + 61440, 30, hw, lane);
        wait_vm<7>();
        STAMP(9);
        bar_raw();
#pragma unroll
        for (int iv = 0; iv < 7; ++iv) {
#pragma unroll
            for (int e = iv < 3 ? iv * 8 - (iv == 2) * 0 : 24 + (iv - 3) * 6; e < (iv < 3 ? iv * 8 + 8 : 24 + (iv - 2) * 6); ++e) {
                if (!(NOST && iv > 0)) switch (e >> 3) {
                    case 0: ST2E(3, e & 7); break;
                    case 1: ST2E(4, e & 7); break;
                    case 2: ST2E(5, e & 7); break;
                    case 3: ST2E(6, e & 7); break;
                    case 4: ST2E(7, e & 7); break;
                    default: ST2E(8, e & 7); break;
                }
                SBAR();
            }
            wait_vm<0>();
            bar();
        }
        asm volatile(".Lpf_end2:");
        STAMP_OUT(1);
        __builtin_amdgcn_endpgm();
    }

    u32x4 xr0, xr1; u32x2 xr2;
    u32x4 qa1, qb1; u32x2 qa2, qb2;
    const int kh = w & 1;
    {
        LANE_VARS();
        LOAD_TAG();
        u32x4 xf[20], xo[10], kf[20];
        const unsigned xrow = L + L_X + w * 20480 + (c16 >> 3) * 10240 + (c16 & 7) * 128;
        const int sw = c16 >> 1, t32 = (w & 1) * 16 + c16;
        unsigned xa = xrow + (((kh * 4 + g) ^ sw) << 4), xb = xrow + ((((kh ^ 1) * 4 + g) ^ sw) << 4), xc = xa + ((w ^ 1) - w) * 20480, xra[3], qw[5], kvo;
        {
            auto rotx = [&](int c) { c -= 16 * rq; return c < 0 ? c + 80 : c; };
            const int c0 = rotx(m * 10 + g), c1 = rotx(m * 10 + 4 + g), c2 = rotx(m * 10 + 8 + (g >> 1));
            xra[0] = xrow + (c0 >> 3) * 1024 + (((c0 & 7) ^ sw) << 4);
            xra[1] = xrow + (c1 >> 3) * 1024 + (((c1 & 7) ^ sw) << 4);
            xra[2] = xrow + (c2 >> 3) * 1024 + (((c2 & 7) ^ sw) << 4) + (g & 1) * 8;
            const int d = (m & 1) * 16 + c16;
            kvo = m == 0 ? ((g >> 1) * 32 + t32) * 16 + (g & 1) * 8
                : m == 1 ? (g == 0 ? 1024 + t32 * 8 : 0x80000000u)
                         : (d <= 21 ? 1280 + (((w & 1) * 2 + (g & 1)) * 22 + d) * 16 + (g >> 1) * 8 : 0x80000000u);
            PIN(xa); PIN(xb); PIN(xc); PIN(xra[0]); PIN(xra[1]); PIN(xra[2]); PIN(kvo);
        }
#pragma unroll
        for (int s = 0; s < 5; ++s) {
            const int f0 = s * 16 + 4 * g, hh = f0 / 20, d0 = f0 - hh * 20;
            qw[s] = L + L_QX + (hh * 2 + (w >> 1)) * 1280 + (d0 < 16 ? ((d0 >> 3) * 32 + t32) * 16 + (d0 & 4) * 2 : 1024 + t32 * 8);
            PIN(qw[s]);
        }
        WQ_ADDR();
        unsigned b0 = wo0 + L_WQ1 + kh * 5120, b1 = wo1 + L_WQ1 + kh * 5120;
        PIN(b0); PIN(b1);
        wait_vm<0>();
        MAKE_TAG();
        if (WQT && m >= 4) {
            __builtin_amdgcn_s_sleep(PFDELAY);
            const char* wp = wq + (((idx >> 3) * 4 + (m & 3)) * 4 + w) * 12800;
            touch(lane * 128, wp, L + L_JUNK, 0x3ffffffffffffull);
            touch(lane * 128, wp + 6400, L + L_JUNK, 0x3ffffffffffffull);
        }
        f32x4 ka = z4, kc = z4, ssm = z4, ssq = z4, acc[2][5];
        u32x4 one = ones;
        PIN(ssm); PIN(ssq); PIN(one);
        if (m < 4) {
            cnt_wait(CNT(C_WKV), 4);
            const unsigned a0 = L + L_WKV + (8 * g + (c16 >> 2)) * 40 + (m & 1) * 32 + (lane & 3) * 8;
            const unsigned a1 = a0 + kh * 1280, a2 = a0 + (kh ^ 1) * 1280;
#pragma unroll
            for (int j = 0; j < 10; ++j) { kf[j] = ldstr2(a1 + j * 2560, a1 + j * 2560 + 160); kf[10 + j] = ldstr2(a2 + j * 2560, a2 + j * 2560 + 160); }
            if (m < 2) {
#pragma unroll
                for (int j = 0; j < 20; ++j) kc = mfma16(kf[j], ones, kc);
            } else {
#pragma unroll
                for (int j = 0; j < 20; ++j) kc = mfma16(ones, kf[j], kc);
            }
            PIN(kc);
        }
        cnt_add(CNT(C_KFUSED));
        cnt_spin(CNT(C_X), 4);
#pragma unroll
        for (int j = 0; j < 10; ++j) xf[j] = lds16(xa + j * 1024);
#pragma unroll
        for (int j = 0; j < 10; ++j) xf[10 + j] = lds16(xb + j * 1024);
#pragma unroll
        for (int j = 0; j < 10; ++j) xo[j] = lds16(xc + j * 1024);
        xr0 = lds16(xra[0]); xr1 = lds16(xra[1]); xr2 = lds8(xra[2]);
        SBAR();
        if (m < 2) {
#pragma unroll
            for (int j = 0; j < 20; ++j) ka = mfma16(kf[j], xf[j], ka);
        } else if (m < 4) {
#pragma unroll
            for (int j = 0; j < 20; ++j) ka = mfma16(xf[j], kf[j], ka);
        }
        PIN(ka);
#pragma unroll
        for (int j = 0; j < 20; ++j) {
            mma16(ssm, one, xf[j]); mma16(ssq, xf[j], xf[j]);
            if (j == 4) {
#pragma unroll
                for (int i = 0; i < 10; ++i) { PIN(xf[i]); PIN(xf[10 + i]); PIN(xo[i]); }
                PIN(xr0); PIN(xr1); PIN(xr2);
                cnt_add(CNT(C_XUSED));
            }
        }
        mma_done();
        STAMP(1);
        const float sd = (c16 & 2) ? ((c16 & 1) ? ssq[3] : ssq[2]) : ((c16 & 1) ? ssq[1] : ssq[0]);
        const float mean = ssm[0] * (1.f / D), rstd = __builtin_amdgcn_rsqf(fmaxf(__shfl(sd, (c16 >> 2) * 16 + c16) * (1.f / D) - mean * mean, 0.f) + LN_EPS);
        if (m < 4) {
            u32x2 pv;
            if (m < 2) {
                pv = u32x2{pk2((ka[0] - mean * kc[0]) * rstd, (ka[1] - mean * kc[1]) * rstd), pk2((ka[2] - mean * kc[2]) * rstd, (ka[3] - mean * kc[3]) * rstd)};
            } else {
                float v[4];
#pragma unroll
                for (int r = 0; r < 4; ++r) v[r] = (ka[r] - __shfl(mean, 4 * g + r) * kc[0]) * __shfl(rstd, 4 * g + r);
                pv = u32x2{pk2(v[0], v[1]), pk2(v[2], v[3])};
                if (m == 3 && c16 >= 4) pv = c16 == 4 ? u32x2{ONES2, ONES2} : u32x2{0u, 0u};
            }
            bst8(rs, pv, kvo, WS_KV + (tb * 2 + (w >> 1)) * KV_TILE);
        }
        SBAR();
        STAMP(2);
        u32x2 f[2][5][2];
        unsigned xs, xv;
        float sc = rstd * QSCALE, ms = -mean * sc;
        PIN(sc); PIN(ms);
        unsigned b2 = b0 + (L_WQ2 - L_WQ1), b3 = b1 + (L_WQ2 - L_WQ1), cq = CNT(0), cv = 0;
        PIN(b2); PIN(b3); PIN(cq);
        auto qhalf = [&](int j, int s, int e) { return ldstr((j < 4 ? (e ? b1 : b0) + j * 10240 : (e ? b3 : b2) + j % 6 * 10240) + s * 32); };
        auto kvflag = [&] {
            if (m < 4) {
                wait_vm<0>();
                if (cnt_arrive(CNT(C_KVST)) == 3 && lane == 0) set_flag(ws, FL_KV + (tb * 4 + m) * 128, tag);
            }
        };
        if (FLAGAT < 0) kvflag();
        cnt_spin(CNT(C_WQ), 4);
#pragma unroll
        for (int s = 0; s < 5; ++s) { f[0][s][0] = qhalf(0, s, 0); f[0][s][1] = qhalf(0, s, 1); }
        SBAR();
#pragma unroll
        for (int j = 0; j < 10; ++j) {
            if (j < 8 && !(j & 1)) cv = *(volatile LAS unsigned*)(size_t)(cq + (C_WQ + j / 2 + 1) * 4);
            if (j < 9 && (j & 1) && __builtin_expect((unsigned)__builtin_amdgcn_readfirstlane(cv) < 4u, 0)) cnt_spin(CNT(C_WQ + j / 2 + 1), 4);
            asm volatile("" ::: "memory");
            SBAR();
#pragma unroll
            for (int s = 0; s < 5; ++s) {
                const u32x4 fr = {f[j & 1][s][0][0], f[j & 1][s][0][1], f[j & 1][s][1][0], f[j & 1][s][1][1]};
                if (j == 0) mma16z(acc[0][s], fr, xf[j]); else mma16(acc[0][s], fr, xf[j]);
                SBAR();
                if (j < 9) f[(j + 1) & 1][s][0] = qhalf(j + 1, s, 0);
                SBAR();
                if (j == 0) mma16z(acc[1][s], fr, xo[j]); else mma16(acc[1][s], fr, xo[j]);
                SBAR();
                if (j < 9) f[(j + 1) & 1][s][1] = qhalf(j + 1, s, 1);
                SBAR();
            }
            if (j == FLAGAT) {   }
            if (j == FLAGAT) kvflag();
            if (j == 3) asm volatile("s_mov_b64 exec, 1\n\tds_add_u32 %0, %1 offset:%2\n\ts_mov_b64 exec, -1" ::"v"(cq), "v"(1u), "i"(C_R1 * 4) : "memory");
            if (j == FLAGAT) {   }
            if (j == 5) { xs = L + L_WQ1 + (w ^ 1) * 5120 + lane * 16; xv = L + L_WQ1 + w * 5120 + lane * 16; PIN(xs); PIN(xv); }
            if (j == 7) cnt_spin(CNT(C_R1), 4);
        }
        STAMP(3);
        if (m >= 4) wait_vm<0>();
        mma_done();
        send_half(acc[1], xs);
        cnt_add(CNT(C_XCH + (w >> 1)));
        if (w == 0 && lane < M_NUM) *(LAS unsigned*)(size_t)CN2(lane) = 0u;
        f32x4 c5[5], p5[5];
#pragma unroll 1
        for (;;) {
            const unsigned n1 = *(volatile LAS unsigned*)(size_t)CNT(C_CS), n2 = *(volatile LAS unsigned*)(size_t)CNT(C_XCH + (w >> 1));
#pragma unroll
            for (int s = 0; s < 5; ++s) {
                c5[s] = *(volatile LAS f32x4*)(size_t)(L + L_CSX + 1280 + s * 64 + g * 16);
                p5[s] = *(volatile LAS f32x4*)(size_t)(xv + s * 1024);
            }
            if (__builtin_amdgcn_readfirstlane(n1 + n2) == 3) break;
        }
#pragma unroll
        for (int s = 0; s < 5; ++s) {
            const f32x4 v = (acc[0][s] + p5[s]) * sc + c5[s] * ms;
            *(LAS u32x2*)(size_t)qw[s] = u32x2{pk2(v[0], v[1]), pk2(v[2], v[3])};
        }
        bar();
        const int r32 = lane & 31, h = lane >> 5;
        const unsigned qb = L + L_QX + ((w & 1) * 4 + (w >> 1)) * 1280;
        qa1 = lds16(qb + lane * 16); qb1 = lds16(qb + 2560 + lane * 16);
        qa2 = u32x2{lds4(qb + 1024 + r32 * 8 + h * 4), 0u}; qb2 = u32x2{lds4(qb + 2560 + 1024 + r32 * 8 + h * 4), 0u};
    }
    STAMP(4);
    {
        LANE_VARS();
        const int r32 = lane & 31, h = lane >> 5, vrow = min(r32, 21);
        const unsigned kconst = h == 0 ? 0x00003f80u : 0u;
        unsigned ak1 = L + lane * 16, ak2 = L + 1024 + r32 * 8 + h * 4, av = L + 1280 + (h * 22 + vrow) * 16;
        const f32x16 zero16 = {0, 0, 0, 0, 0, 0, 0, 0, 0, 0, 0, 0, 0, 0, 0, 0};
        f32x16 sa, sb, oa = zero16, ob = zero16;
        u32x4 ka1 = lds16(ak1), kb1 = lds16(ak1 + KV_TILE), va0 = lds16(av), va1 = lds16(av + VS), vb0 = va0, vb1 = va0;
        u32x2 ka2 = {lds4(ak2), kconst}, kb2 = {lds4(ak2 + KV_TILE), kconst};
        sa = mfma32(ka1, qa1, mfma32k8(ka2, qa2, zero16));
        sb = mfma32(ka1, qb1, mfma32k8(ka2, qb2, zero16));
        {
            const float ma = halfmax(max16(sa)), mb = halfmax(max16(sb));
            qa2[1] = h == 0 ? (pk2(-ma, 0.f) & 0xffffu) : 0u;
            qb2[1] = h == 0 ? (pk2(-mb, 0.f) & 0xffffu) : 0u;
        }
        sa = mfma32(ka1, qa1, mfma32k8(ka2, qa2, zero16));
        u32x4 pa0 = {0u, 0u, 0u, 0u}, pa1 = pa0, pb0 = pa0, pb1 = pa0;
        float e2 = 0.f, e3 = 0.f;
        unsigned vo0 = (r32 >> 4) * 20480 + h * 256 + (r32 & 15) * 16, vo1 = h ? 0x80000000u : vo0;
        PIN(pa0); PIN(pa1); PIN(pb0); PIN(pb1); PIN(ka2); PIN(kb2); PIN(vo0); PIN(vo1);
#define SLOT(R, MF, X, S, i, PP, jp, P, j) do { R = MF; PIN(R); SBAR(); X; SBAR(); \
        float e0 = EX2(S[i]), e1 = EX2(S[i + 1]); PIN(e0); PIN(e1); SBAR(); \
        PP[jp] = pk2(e2, e3); PIN(PP); SBAR(); \
        e2 = EX2(S[i + 2]); e3 = EX2(S[i + 3]); PIN(e2); PIN(e3); SBAR(); \
        P[j] = pk2(e0, e1); PIN(P); SBAR(); } while (0)
#define STEP(KC1, KC2, KN1, KN2, VC0, VC1, VP0, VP1, OK, OV, X0, X7) do { \
        SLOT(sb, mfma32k8(KC2, qb2, zero16), X0, sa, 0, pb1, 3, pa0, 0); \
        SLOT(sb, mfma32(KC1, qb1, sb), __builtin_amdgcn_s_waitcnt(0xC07F), sa, 4, pa0, 1, pa0, 2); \
        SLOT(ob, mfma32(VP1, pb1, ob), KC1 = lds16(ak1 + (OK) * KV_TILE), sa, 8, pa0, 3, pa1, 0); \
        SLOT(oa, mfma32(VC0, pa0, oa), KC2[0] = lds4(ak2 + (OK) * KV_TILE), sa, 12, pa1, 1, pa1, 2); \
        SLOT(sa, mfma32k8(KN2, qa2, zero16), (void)0, sb, 0, pa1, 3, pb0, 0); \
        SLOT(sa, mfma32(KN1, qa1, sa), VP0 = lds16(av + (OV) * KV_TILE), sb, 4, pb0, 1, pb0, 2); \
        SLOT(oa, mfma32(VC1, pa1, oa), VP1 = lds16(av + (OV) * KV_TILE + VS), sb, 8, pb0, 3, pb1, 0); \
        SLOT(ob, mfma32(VC0, pb0, ob), X7, sb, 12, pb1, 1, pb1, 2); } while (0)
        unsigned so1 = WS_O + (tb * 4 + (w & 2)) * 20480, so0 = so1 + (2 * m + (w & 1)) * 1024;
        so1 += 16384 + (2 * m + (w & 1)) * 256;
        asm volatile("" : "+s"(so0), "+s"(so1));
        unsigned ea = CN2(E_C1), e0a = CN2(E_C0), ev = 0, one = 1;
        PIN(ea); PIN(e0a); PIN(one); PIN(ak1); PIN(ak2); PIN(av);
        auto hook0 = [&](int j, int dn) {
            if (j == 1) ev = *(volatile LAS unsigned*)(size_t)e0a;
            if (j == 14) { ak1 += dn; ak2 += dn; PIN(ak1); PIN(ak2); }
            if (j == 15) { av += dn; ea += 4; PIN(av); PIN(ea); }
        };
        auto hook7 = [&](int j) {
            if (j == 0)
                asm volatile("s_mov_b64 exec, 1\n\tds_add_u32 %0, %1 offset:%2\n\ts_mov_b64 exec, -1" ::"v"(ea), "v"(one), "i"((P_NONE - E_C1) * 4) : "memory");
            if (j == 1 && __builtin_expect((unsigned)__builtin_amdgcn_readfirstlane(ev) < 4u, 0)) cnt_spin(e0a, 4);
            if (j == 9) ev = *(volatile LAS unsigned*)(size_t)ea;
            if (j == 10 && __builtin_expect((unsigned)__builtin_amdgcn_readfirstlane(ev) < 4u, 0))
                cnt_wait(ea, 4);
        };
        STAMP(5);
#pragma unroll 1
        for (int c = 0; c < 4; ++c) {
            const int dn = c == 2 ? -2 * CHUNK : CHUNK;
#pragma unroll
            for (int j = 0; j < 16; j += 2) {
                STEP(ka1, ka2, kb1, kb2, va0, va1, vb0, vb1, (j + 2) & 15, j + 1, hook0(j, dn), hook7(j));
                STEP(kb1, kb2, ka1, ka2, vb0, vb1, va0, va1, (j + 3) & 15, (j + 2) & 15, hook0(j + 1, dn), hook7(j + 1));
            }
        }
        pb1[3] = pk2(e2, e3);
        ob = mfma32(vb1, pb1, ob);
        STAMP(6);
        u32x4 r0, r1, r2;
        auto finish = [&](const f32x16& pa, const f32x16& pb, float la, float lb) {
            const float ra = __builtin_amdgcn_rcpf(la), rb = __builtin_amdgcn_rcpf(lb);
            r0 = u32x4{keep(pk2(pa[0] * ra, pa[1] * ra)), pk2(pa[2] * ra, pa[3] * ra), keep(pk2(pa[4] * ra, pa[5] * ra)), pk2(pa[6] * ra, pa[7] * ra)};
            r1 = u32x4{keep(pk2(pb[0] * rb, pb[1] * rb)), pk2(pb[2] * rb, pb[3] * rb), keep(pk2(pb[4] * rb, pb[5] * rb)), pk2(pb[6] * rb, pb[7] * rb)};
            r2 = u32x4{keep(pk2(pa[8] * ra, pa[9] * ra)), pk2(pa[10] * ra, pa[11] * ra), keep(pk2(pb[8] * rb, pb[9] * rb)), pk2(pb[10] * rb, pb[11] * rb)};
        };
        {
            float am = 0.f;
#pragma unroll
            for (int i = 0; i < 12; i += 2) am = amax3(am, oa[i], oa[i + 1]);
            const float la = upper(oa[8]);
            asm volatile("s_nop 7" : "+v"(ob));
            const float lb = upper(ob[8]);
#pragma unroll
            for (int i = 0; i < 12; i += 2) am = amax3(am, ob[i], ob[i + 1]);
            finish(oa, ob, la, lb);
            const bool ok = (fminf(la, lb) >= 1e-18f) & (fmaxf(la, lb) <= 1e18f) & (am <= 1e37f);
            if (__builtin_expect(__ballot(!ok) != 0, 0)) {
                __builtin_amdgcn_fence(__ATOMIC_ACQUIRE, "agent");
                f32x16 xa, xb;
#pragma unroll 1
                for (int qt = 0; qt < 2; ++qt) {
                    xb = attn_exact(ws + WS_KV, qt ? qb1 : qa1, qt ? qb2[0] : qa2[0], lane, vrow);
                    if (qt == 0) xa = xb;
                }
                finish(xa, xb, upper(xa[8]), upper(xb[8]));
            }
        }
        bst16(rs, r0, vo0, so0);
        bst16(rs, r1, vo0 + 512, so0);
        bst16(rs, r2, vo1, so1);
        STAMP(7);
    }
    u32x4 wd[12], ae[RE][2], we[PE];
    unsigned ge[9], e0, e1, ehv, eprobe, a0;
    const unsigned ehs = WS_F + (tb * 4 + (w & 2)) * 55296 + 16384;
    {
        LANE_VARS();
        const unsigned wl = L + lane * 16;
        f32x4 acc[5];
        u32x4 act[10][2], wf[12];
        unsigned wa = wl + L_WO, wb = wa + 51200, gb[5];
        rot_bases(gb, WS_O + (tb * 4 + w) * 20480, rotc, 4096);
        unsigned v0 = lane * 16, v1 = v0 + 1024, hv = c16 * 256;
        unsigned sv = g == 0 ? c16 * 8 : 0x80000000u, h2v = (g & 1) ? 0x80000000u : (g >> 1) * 256 + c16 * 16;
        PIN(v0); PIN(v1); PIN(hv);
        auto sub = [](int n) { return n < 60 ? (n % 3 == 2 ? 4 : n % 3) : 2 + (n & 1); };
        auto kst = [](int n) { return n < 60 ? n / 3 : (n - 60) / 2; };
        auto wac = [&](int n) { return (kst(n) < 10 ? wa : wb) + (kst(n) % 10) * 5120 + sub(n) * 1024; };
        kstart<RC, 2, 2048>(act, rs, ws, v0, v1, gb,
                           [&] { return __builtin_amdgcn_raw_buffer_load_b32(rs, hv, WS_O + (tb * 4 + w) * 20480 + 16384, SC1) == EMPTY; }, [&] {
            bar();
            PIN(wa); PIN(wb); PIN(sv); PIN(h2v);
#pragma unroll
            for (int n = 0; n < 12; ++n) { wf[n] = lds16(wac(n)); SBAR(); }
            res_init(acc, xr0, xr1, xr2);
            wait_vm<0>();
        }, [] {});
        STAMP(8);
        const unsigned hb = WS_H + (tb * 4 + w) * 20480;
        unsigned t = 0;
        u64 bad = 0;
        static_for<0, 100>([&](auto nn) {
            constexpr int n = nn.v, j = n < 54 ? n / 6 : 0, i = n % 6, q0 = areq(j, RC, 10), q1 = areq(j + 1, RC, 10);
            if (n == 0) {   } if (n == 18) {   } if (n == 36) {   } if (n == 60) {   } if (n == 72) {   } if (n == 92) {   } if (n == 93) {   }
            if (n % 3 == 0) { lgkm<9>(); SBAR(); }
            mma16(acc[sub(n)], wf[n % 12], act[kst(n) >> 1][kst(n) & 1]);
            SBAR();
            if (n + 12 < 100) wf[n % 12] = lds16(wac(n + 12));
            if (n < 54) {
                if (i < 4 && q0 + i / 2 < q1) act[(q0 + i / 2) % 10][i & 1] = aload<2, 2048>(rs, (i & 1) ? v1 : v0, gb, (q0 + i / 2) % 10);
                if (i == 3) { t = max(max(act[j + 1][1][0], act[j + 1][1][2]), max(act[j + 1][0][0], act[j + 1][0][2])); PIN(t); }
                if (i == 4) { bad = __ballot(t == EMPTY); asm volatile("" : "+s"(bad)); }
                if (i == 5 && __builtin_expect(bad != 0, 0))
                    refetch(act[j + 1][0], act[j + 1][1], ws + gb[(j + 1) / 2] + ((j + 1) & 1) * 2048, v0, v1);
            }
            if (n == 68) {
                xr0 = u32x4{keep(pk2(acc[0][0], acc[0][1])), pk2(acc[0][2], acc[0][3]), keep(pk2(acc[1][0], acc[1][1])), pk2(acc[1][2], acc[1][3])};
                bst16(rs, xr0, v0, hb + m * 1024);
            }
            if (n == 70) {
                xr2 = u32x2{keep(pk2(acc[4][0], acc[4][1])), pk2(acc[4][2], acc[4][3])};
                bst16(rs, rowpair(xr2, xr2), h2v, hb + 8192 + m * 512);
            }
            if (n == 92) { lgkm<0>(); bar_raw(); }
            if constexpr (n >= 93 && n < 99) {
#pragma unroll
                for (int e = 0; e < 2; ++e) {
                    const int k = (n - 93) * 2 + e;
                    wd[k] = lds16(wl + (k % 7 < 5 ? 16384 + (k / 7) * 5120 + (k % 7) * 1024 : (k / 7) * 2048 + (k % 7 - 5) * 1024));
                }
            }
            SBAR();
        });
        float sm = 0.f, sq = 0.f, sm2 = 0.f, sq2 = 0.f;
#pragma unroll
        for (int i = 0; i < 4; i += 2) { sm = dot2(xr0[i], ONES2, sm); sq = dot2(xr0[i], xr0[i], sq); sm2 = dot2(xr0[i + 1], ONES2, sm2); sq2 = dot2(xr0[i + 1], xr0[i + 1], sq2); }
        sm = dot2(xr2[0], ONES2, sm); sq = dot2(xr2[0], xr2[0], sq); sm2 = dot2(xr2[1], ONES2, sm2); sq2 = dot2(xr2[1], xr2[1], sq2);
        PIN(sm); PIN(sq); PIN(sm2); PIN(sq2);
        SBAR();
#pragma unroll
        for (int n = 0; n < 12; ++n) PIN(wd[n]);
        cnt_add(CN2(M_LDS));
        xr1 = u32x4{keep(pk2(acc[2][0], acc[2][1])), pk2(acc[2][2], acc[2][3]), keep(pk2(acc[3][0], acc[3][1])), pk2(acc[3][2], acc[3][3])};
        bst16(rs, xr1, v0, hb + (12 + m) * 1024);
#pragma unroll
        for (int i = 0; i < 4; i += 2) { sm = dot2(xr1[i], ONES2, sm); sq = dot2(xr1[i], xr1[i], sq); sm2 = dot2(xr1[i + 1], ONES2, sm2); sq2 = dot2(xr1[i + 1], xr1[i + 1], sq2); }
        sm = xsum16_32(sm + sm2); sq = xsum16_32(sq + sq2);
        bst8(rs, u32x2{keep(__float_as_uint(sm)), __float_as_uint(sq)}, sv, WS_ST + (tb * 4 + w) * 1024 + m * 128);
        SBAR();
        STAMP(9);
    }
    {
        f32x4 acc[2][7], c4[7];
        u32x4 act[12][2];
        u32x2 st[4];
        float mu[2], rp[2];
        const int t16 = (tb >> 1) * 8 + 2 * w;
        unsigned v0 = lane_id() * 16, v1 = v0 + 20480, sv = v0 >> 1, wa = L + v0, wb = wa + 16384 + 46080, gb[5];
        PIN(sv); PIN(wa); PIN(wb); PIN(v0); PIN(v1);
        d_bases(gb, WS_H + t16 * 20480, ct, 4096);
        unsigned fs[7], wc, fw, f2v, ca;
        auto late = [&] {
        zero_acc(acc[0]); zero_acc(acc[1]);
        e_bases(ge, WS_F + tb * 221184 + kh * 1024, m, 6144);
        {
            const unsigned fb = WS_F + t16 * 55296 + ct * 1024;
            fs[0] = fb; fs[1] = fb + 55296; fs[2] = fb + 16384 - ct * 512; fs[3] = fb + 24576; fs[4] = fb + 24576 + 55296; fs[5] = fb + 39936; fs[6] = fb + 39936 + 55296;
#pragma unroll
            for (int i = 0; i < 7; ++i) asm volatile("" : "+s"(fs[i]));
            const int lane = v0 >> 4, g = lane >> 4, c16 = lane & 15;
            f2v = (g & 1) * 55296 + (g >> 1) * 256 + c16 * 16; ca = L + L_MISC + g * 16;
            ehv = (g & 1) * 55296 + c16 * 512;
            wc = wa + 118784;
            PIN(f2v); PIN(ca); PIN(ehv); PIN(wc);
        }
        };
        unsigned hv = ((v0 >> 8) & 1) * 20480 + ((v0 >> 4) & 7) * 512;
        PIN(hv);
        kstart<RD, 4, 1024>(act, rs, ws, v0, v1, gb, [&] { return __builtin_amdgcn_raw_buffer_load_b32(rs, hv, WS_H + t16 * 20480 + 8192, SC1) == EMPTY; },
                            [&] { if (DRD < 0) wait_vm<0>(); else __builtin_amdgcn_s_sleep(DRD < 0 ? 0 : DRD); }, late);
        auto stload = [&] {
#pragma unroll
            for (int i = 0; i < 4; ++i) st[i] = __builtin_amdgcn_raw_buffer_load_b64(rs, sv + i * 512, WS_ST + t16 * 1024, SC1);
        };
        float sa[4];
        unsigned w1v = 0;
        auto dside = [&](int j, int i) {
            if ((j == 0 || j == 6) && i == 12) w1v = *(volatile LAS unsigned*)(size_t)(ca + 512 + (j ? M_W1 : M_W1A) * 4);
            if ((j == 1 || j == 7) && i == 12) {
                if (__builtin_expect((unsigned)__builtin_amdgcn_readfirstlane(w1v) < 4u, 0)) cnt_spin(CN2(j == 7 ? M_W1 : M_W1A), 4);
                asm volatile("" ::: "memory");
            }
            if (j == 5 && i == 4) stload();
            if (j == 9 && i == 6 && __builtin_expect(__any(max(max(st[0][0], st[1][0]), max(st[2][0], st[3][0])) == EMPTY), 0)) {
#pragma unroll 1
                for (int spin = 0; spin < (1 << 16); ++spin) {
                    asm volatile("" ::: "memory");
                    stload();
                    if (!__any(max(max(st[0][0], st[1][0]), max(st[2][0], st[3][0])) == EMPTY)) break;
                }
            }
            if ((j == 10 || j == 11) && (i == 4 || i == 8)) {
                const int k = (j - 10) * 2 + (i == 8);
                sa[k] = xsum16_32(__uint_as_float(st[k & 2][k & 1]) + __uint_as_float(st[(k & 2) + 1][k & 1])); PIN(sa[k]);
            }
            if (j == 12 && (i == 4 || i == 8)) {
                const int t = i == 8;
                mu[t] = sa[2 * t] * (-1.f / D);
                rp[t] = __builtin_amdgcn_rsqf(fmaxf(sa[2 * t + 1] * (1.f / D) - mu[t] * mu[t], 0.f) + LN_EPS) * LOG2E;
                PIN(mu[t]); PIN(rp[t]);
            }
        };
        STAMP(10);
        auto wad = [&](int n) {
            const int j = n < 56 ? n / 7 : n < 86 ? 8 + (n - 56) / 5 : n < 98 ? 14 + (n - 86) / 2 : 14 + (n - 98) / 3;
            const int s = n < 56 ? n % 7 : n < 86 ? (n - 56) % 5 : n < 98 ? (n - 86) % 2 : 2 + (n - 98) % 3;
            return n >= 116 ? wc + (n - 116) * 1024 : s >= 5 ? wa + j * 2048 + (s - 5) * 1024 : (j < 9 ? wa + 16384 + j * 5120 : wb + (j - 9) * 5120) + s * 1024; };
        kloop<0, 8, 20, 0, 140, 7, 5, RD, 4, 1024>(acc, act, wd, wad, rs, ws, v0, v1, gb, dside);
        kloop<8, 1, 20, 56, 140, 5, 5, RD, 4, 1024>(acc, act, wd, wad, rs, ws, v0, v1, gb, dside);
        asm volatile("s_mov_b64 exec, 1\n\tds_add_u32 %0, %1\n\ts_mov_b64 exec, -1" ::"v"(CN2(M_K8)), "v"(1u) : "memory");
        kloop<9, 5, 20, 61, 140, 5, 2, RD, 4, 1024>(acc, act, wd, wad, rs, ws, v0, v1, gb, dside);
        c4[0] = ldsf4(ca); c4[1] = ldsf4(ca + 64);
        SBAR();
        kloop<14, 6, 20, 86, 140, 2, 4, RD, 4, 1024>(acc, act, wd, wad, rs, ws, v0, v1, gb, dside);
        STAMP(11);
        u32x2 lo, hi;
        auto silu = [&](const f32x4& a, const f32x4& c, int t, u32x2& r, auto side) {
            f32x4 y = (a + mu[t] * c) * rp[t], e;
            PIN(y); SBAR();
#pragma unroll
            for (int i = 0; i < 4; ++i) { side(i); SBAR(); e[i] = EX2(-y[i]); PIN(e); SBAR(); }
            e = e * LOG2E + LOG2E; PIN(e); SBAR();
#pragma unroll
            for (int i = 0; i < 4; ++i) { side(4 + i); SBAR(); e[i] = __builtin_amdgcn_rcpf(e[i]); PIN(e); SBAR(); }
            e *= y;
            r = u32x2{keep(pk2(e[0], e[1])), pk2(e[2], e[3])};
            PIN(r); SBAR();
        };
        auto rest = [&](int q) {
            const int n = 98 + (q >> 1), j = n < 116 ? 14 + (n - 98) / 3 : 8 + (n - 116) / 2, s = n < 116 ? 2 + (n - 98) % 3 : 5 + (n - 116) % 2;
            mma16(acc[q & 1][s], wd[n % 12], act[j % 12][q & 1]);
            SBAR();
            if ((q & 1) && n + 12 < 140) wd[n % 12] = lds16(wad(n + 12));
            if (q >= 24 && q < 74 && q % 10 == 4) c4[q == 24 ? 6 : q / 10 - 1] = ldsf4(ca + (q == 24 ? 6 : q / 10 - 1) * 64);
        };
        auto early = [&](int q) {
            if (q == 0) eprobe = __builtin_amdgcn_raw_buffer_load_b32(rs, ehv, ehs, SC1);
            ae[q >> 1][q & 1] = aload<3, 2048>(rs, (q & 1) ? e1 : e0, ge, q >> 1);
        };
#pragma unroll
        for (int q = 0; q < 8; ++q) { rest(q); SBAR(); }
#pragma unroll
        for (int b = 0; b < 10; ++b) {
            const int ut = b < 4 ? b >> 1 : b < 6 ? b & 1 : (b - 6) >> 1, ui = b < 4 ? b & 1 : b < 6 ? 2 : 3 + (b & 1), us = ui < 2 ? ui : ui == 2 ? 6 : ui - 1;
            silu(acc[ut][ui], c4[us], ut, (b & 1) ? hi : lo, [&](int i) { if (8 + b * 8 + i < 84) rest(8 + b * 8 + i); });
            if (b == 5) bst16(rs, rowpair(lo, hi), f2v, fs[2]);
            else if (b & 1) bst16(rs, u32x4{lo[0], lo[1], hi[0], hi[1]}, b < 5 ? v0 : fw, fs[b >> 1]);
            if (b == 1) cnt_add(CN2(M_T4));
            if (b == 1) {   } if (b == 3) {   } if (b == 5) {   } if (b == 7) {   } if (b == 9) {   }
            if (b == 5) {
                PIN(v0);
                fw = ct < 15 ? v0 : 0x80000000u; e0 = v0 + w * 55296; e1 = v0 + (w ^ 1) * 55296; a0 = L + v0 + kh * 5120;
                PIN(fw); PIN(e0); PIN(e1); PIN(a0);
            }
            SBAR();
        }
        bar_raw();
        mma_done();
#pragma unroll
        for (int n = 0; n < PE; ++n) we[n] = lds16(a0 + (n / 5) * 10240 + (n % 5) * 1024);
#pragma unroll
        for (int t = 0; t < 2; ++t) {
            u32x4 r;
#pragma unroll
            for (int u = 0; u < 2; ++u) {
                const f32x4 y = (acc[t][5 + u] + mu[t] * c4[4 + u]) * rp[t];
                f32x4 e = f32x4{EX2(-y[0]), EX2(-y[1]), EX2(-y[2]), EX2(-y[3])} * LOG2E + LOG2E;
                e = f32x4{__builtin_amdgcn_rcpf(e[0]), __builtin_amdgcn_rcpf(e[1]), __builtin_amdgcn_rcpf(e[2]), __builtin_amdgcn_rcpf(e[3])} * y;
                r[2 * u] = keep(pk2(e[0], e[1])); r[2 * u + 1] = pk2(e[2], e[3]);
            }
            bst16(rs, r, fw, fs[5 + t]);
        }
        SBAR();
#pragma unroll
        for (int q = 0; q < 2 * FIRSTFILL; ++q) { early(q); SBAR(); }
        STAMP(12);
    }
    {
        f32x4 acc[2][5];
        unsigned a1 = a0 + 61440, a2 = a0 + 122880;
        PIN(a1); PIN(a2);
        res_init(acc[0], xr0, xr1, xr2); zero_acc(acc[1]);
        if (__any(eprobe == EMPTY))
            kstart<RE, 3, 2048>(ae, rs, ws, e0, e1, ge, [&] { return __builtin_amdgcn_raw_buffer_load_b32(rs, ehv, ehs, SC1) == EMPTY; }, [] {}, [] {});
        if (__builtin_expect(missing(ae[0][0], ae[0][1]), 0)) refetch(ae[0][0], ae[0][1], ws + ge[0], e0, e1);
        SBAR();
        STAMP(13);
        auto wae = [&](int n) { const int q = n / 5 % 15; return (q < 6 ? a0 : q < 12 ? a1 : a2) + (q % 6) * 10240 + (n % 5) * 1024; };
#define GRP(G, WA, WN) kloop<3 * G, 3, 27, 15 * G, 135, 5, 5, RE, 3, 2048>(acc, ae, we, wae, rs, ws, e0, e1, ge, [](int, int) {})
        GRP(0, a0, a0 + 30720);
        bar_raw();

        GRP(1, a0 + 30720, a1);
        bar_raw();
        GRP(2, a1, a1 + 30720);
        bar_raw();
        GRP(3, a1 + 30720, a2); bar_raw();
        GRP(4, a2, a0); bar_raw();

        GRP(5, a0, a0 + 30720); bar_raw();
        GRP(6, a0 + 30720, a1); bar_raw();
        GRP(7, a1, a1 + 30720);
        GRP(8, a1 + 30720, a1);
        mma_done();
        LANE_VARS();
        const unsigned wl = L + lane * 16;
        send_half(acc[1], wl + 122880 + (w ^ 1) * 5120);
        bar();
        recv_half(acc[0], wl + 122880 + w * 5120);
        STAMP(14);
        res_pack(xr0, xr1, xr2, acc[0]);
        char* ob = out + (size_t)(tb * 64 + w * 16 + c16) * 1280 + m * 160;
        asm volatile("global_store_dwordx4 %0, %1, off nt\n\tglobal_store_dwordx4 %0, %2, off offset:64 nt\n\t"
                     "global_store_dwordx2 %3, %4, off offset:128 nt\n\ts_nop 1" ::"v"(ob + g * 16), "v"(xr0), "v"(xr1), "v"(ob + g * 8), "v"(xr2) : "memory");
    }
    asm volatile(".Lpf_end1:");
    STAMP_OUT(0);
    __builtin_amdgcn_endpgm();
}

extern "C" void kernel_launch(void* const* d_in, const int* in_sizes, int n_in, void* d_out, int out_size, void* d_ws, size_t ws_size,
                              hipStream_t stream) {
    (void)hipFuncSetAttribute((const void*)k_block, hipFuncAttributeMaxDynamicSharedMemorySize, LDS_BYTES);
    k_block<<<256, 512, LDS_BYTES, stream>>>((const char*)d_in[0], (const char*)d_in[1], (const char*)d_in[2], (const char*)d_in[3],
                                             (const char*)d_in[4], (const char*)d_in[6], (const char*)d_in[7], (char*)d_ws, (char*)d_out);
}
```

```cpp
#include <hip/hip_runtime.h>
#include <hip/hip_bf16.h>
#include <stdint.h>

typedef __attribute__((ext_vector_type(8))) __bf16 bf16x8;
typedef __attribute__((ext_vector_type(2))) __bf16 bf16x2;
typedef __attribute__((ext_vector_type(4))) short s16x4;
typedef __attribute__((ext_vector_type(4))) float f32x4;
typedef __attribute__((ext_vector_type(2))) float f32x2;
typedef __attribute__((ext_vector_type(16))) float f32x16;
typedef __attribute__((ext_vector_type(4))) unsigned u32x4;
typedef __attribute__((ext_vector_type(2))) unsigned u32x2;
typedef unsigned long long u64;
#define LAS __attribute__((address_space(3)))
#define GAS __attribute__((address_space(1)))
#define DI __device__ __forceinline__

constexpr int D = 640;
constexpr float LN_EPS = 1e-5f;
constexpr float LOG2E = 1.4426950408889634f;
constexpr float QSCALE = 0.22360679774997896f * LOG2E;

constexpr int KV_TILE = 2688;
constexpr int VS = 704;
constexpr unsigned WS_FL = 0;
constexpr unsigned FL_KV = 32768, FL_PR = 2 * 32768, FL_MK = 3 * 32768;
constexpr unsigned WS_KV = 4 * 32768;
constexpr unsigned WS_WO = WS_KV + 64 * KV_TILE;
constexpr unsigned WS_W1 = WS_WO + 8 * 20 * 5 * 1024;
constexpr unsigned WS_W2 = WS_W1 + 16 * 20 * 7 * 1024;
constexpr unsigned WS_CS = WS_W2 + 8 * 54 * 5 * 1024;
constexpr unsigned WS_O  = WS_CS + 20 * 1792 * 4;
constexpr unsigned WS_H  = WS_O + 128 * 20 * 1024;
constexpr unsigned WS_F  = WS_H + 128 * 20 * 1024;
constexpr unsigned WS_ST = WS_F + 128 * 54 * 1024;
constexpr unsigned WS_END = WS_ST + 2048 * 64;
constexpr unsigned EMPTY = 0xffffffffu;
static_assert((WS_END - WS_O) == 256 * 48640, "fill share per workgroup");

constexpr int CHUNK = 16 * KV_TILE;
constexpr int L_X = 0, L_WQ2 = 43008, L_WKV = 83968, L_WQ1 = 122880,
               L_CSX = 111104, L_QX = 152576, L_MISC = 162816;
constexpr int L_AUX = 3 * CHUNK, L_WO = CHUNK, L_CNT = 110592, L_JUNK = 113664, XCH = L_WO + 102400, LDS_BYTES = 163840;

#define STAMP_INIT() const u64 rt0_ = __builtin_amdgcn_s_memrealtime()
#define STAMP(I)
#define STAMP_OUT(K) do { if ((K) == 0) { if (blockIdx.x == 0) while (__builtin_amdgcn_s_memrealtime() - rt0_ < 6000) __builtin_amdgcn_s_sleep(2); } } while (0)

#define THROT 3
#define THROT2 3
#define RC 6
#define RD 8
#define RE 8
#define TAILPRIO 0
#define FIRSTFILL 4
#define ONEWAY 1
#define NEARLY 12
#define PFDELAY 6
#define FLAGAT 0
#define P1PRIO 1
#define DRC -1
#define DRD 0
#define PE 10
#define LONGNAP 10
#define QROT (tb & 3)
#define WQT 0
#define PC 24
#define GAP1 0
DI void glds(unsigned voff, const void* sbase, unsigned lds) {
    asm volatile("s_mov_b32 m0, %2\n\ts_nop 0\n\tglobal_load_lds_dwordx4 %0, %1" ::"v"(voff), "s"(sbase), "s"(lds) : "memory");
}
DI void glds_dev(unsigned voff, const void* sbase, unsigned lds) {
    asm volatile("s_mov_b32 m0, %2\n\ts_nop 0\n\tglobal_load_lds_dwordx4 %0, %1 sc1" ::"v"(voff), "s"(sbase), "s"(lds) : "memory");
}
DI void touch(unsigned voff, const void* sbase, unsigned lds, u64 mask) {
    asm volatile("s_mov_b32 m0, %2\n\ts_mov_b64 exec, %3\n\tglobal_load_lds_dword %0, %1\n\ts_mov_b64 exec, -1" ::"v"(voff), "s"(sbase), "s"(lds), "s"(mask) : "memory");
}
template <int N> DI void wait_vm() { asm volatile("s_waitcnt vmcnt(%0)" ::"i"(N) : "memory"); }
DI void bar() { asm volatile("s_waitcnt lgkmcnt(0)\n\ts_barrier" ::: "memory"); }
DI void bar_raw() { asm volatile("s_barrier" ::: "memory"); }
#define PIN(x) asm volatile("" : "+v"(x))
#define SBAR() __builtin_amdgcn_sched_barrier(0)
DI unsigned pk2(float a, float b) { bf16x2 v = {(__bf16)a, (__bf16)b}; return __builtin_bit_cast(unsigned, v); }
DI float bflo(unsigned u) { return __uint_as_float(u << 16); }
DI float bfhi(unsigned u) { return __uint_as_float(u & 0xffff0000u); }
DI f32x4 mfma16(u32x4 a, u32x4 b, f32x4 c) {
    return __builtin_amdgcn_mfma_f32_16x16x32_bf16(__builtin_bit_cast(bf16x8, a), __builtin_bit_cast(bf16x8, b), c, 0, 0, 0);
}
DI f32x16 mfma32(u32x4 a, u32x4 b, f32x16 c) {
    return __builtin_amdgcn_mfma_f32_32x32x16_bf16(__builtin_bit_cast(bf16x8, a), __builtin_bit_cast(bf16x8, b), c, 0, 0, 0);
}
DI f32x16 mfma32k8(u32x2 a, u32x2 b, f32x16 c) {
    return __builtin_amdgcn_mfma_f32_32x32x8bf16_1k(__builtin_bit_cast(s16x4, a), __builtin_bit_cast(s16x4, b), c, 0, 0, 0);
}
DI u32x4 lds16(unsigned a) { return *(const LAS u32x4*)(size_t)a; }
DI u32x2 lds8(unsigned a) { return *(const LAS u32x2*)(size_t)a; }
DI unsigned lds4(unsigned a) { return *(const LAS unsigned*)(size_t)a; }
DI f32x4 ldsf4(unsigned a) { return *(const LAS f32x4*)(size_t)a; }
DI u32x2 ldstr(unsigned a) { return __builtin_bit_cast(u32x2, __builtin_amdgcn_ds_read_tr16_b64_v4i16((LAS s16x4*)(size_t)a)); }
DI u32x4 ldstr2(unsigned a0, unsigned a1) { u32x2 lo = ldstr(a0), hi = ldstr(a1); return u32x4{lo[0], lo[1], hi[0], hi[1]}; }
DI float dot2(unsigned a, unsigned b, float c) {
    return __builtin_amdgcn_fdot2_f32_bf16(__builtin_bit_cast(bf16x2, a), __builtin_bit_cast(bf16x2, b), c, false);
}
DI float xsum16_32(float v) {
    const auto a = __builtin_amdgcn_permlane16_swap(__float_as_uint(v), __float_as_uint(v), false, false);
    v = __uint_as_float(a[0]) + __uint_as_float(a[1]);
    const auto b = __builtin_amdgcn_permlane32_swap(__float_as_uint(v), __float_as_uint(v), false, false);
    return __uint_as_float(b[0]) + __uint_as_float(b[1]);
}
constexpr unsigned ONES2 = 0x3f803f80u;
#define EX2(v) __builtin_amdgcn_exp2f(v)
typedef __amdgpu_buffer_rsrc_t rsrc_t;
constexpr int SC1 = 16;
DI u32x4 bld16(rsrc_t r, unsigned v, unsigned s) { return __builtin_amdgcn_raw_buffer_load_b128(r, v, s, SC1); }
DI void bst16(rsrc_t r, u32x4 d, unsigned v, unsigned s) {
    __builtin_amdgcn_raw_buffer_store_b128(d, r, v, s, SC1);
    asm volatile("s_nop 1" ::"v"(d) : "memory");
}
DI void bst8(rsrc_t r, u32x2 d, unsigned v, unsigned s) { __builtin_amdgcn_raw_buffer_store_b64(d, r, v, s, SC1); }

DI void set_flag(char* ws, unsigned off, u64 tag) { __hip_atomic_store((GAS u64*)(ws + off), tag, __ATOMIC_RELAXED, __HIP_MEMORY_SCOPE_AGENT); }
DI void poll(char* ws, unsigned arr, int index, u64 tag) {
    const GAS u64* p = (const GAS u64*)(ws + arr + index * 128);
    bool ok = index < 0;
#pragma unroll 1
    for (int spin = 0; spin < (1 << 18); ++spin) {
        if (!ok) ok = __hip_atomic_load(p, __ATOMIC_RELAXED, __HIP_MEMORY_SCOPE_AGENT) == tag;
        if (__all(ok)) break;
        __builtin_amdgcn_s_sleep(1);
    }
    asm volatile("" ::: "memory");
}
DI void copy_dev(const char* src, unsigned lds, int n, int hw, int lane) {
#pragma unroll 1
    for (int p = hw; p < n; p += 4) glds_dev(lane * 16, src + p * 1024, lds + p * 1024);
}

DI void cnt_add(unsigned a) {
    asm volatile("s_waitcnt lgkmcnt(0)\n\ts_mov_b64 exec, 1\n\tds_add_u32 %0, %1\n\ts_mov_b64 exec, -1" ::"v"(a), "v"(1u) : "memory");
}
DI void cnt_add_now(unsigned a) {
    asm volatile("s_mov_b64 exec, 1\n\tds_add_u32 %0, %1\n\ts_mov_b64 exec, -1" ::"v"(a), "v"(1u) : "memory");
}
DI unsigned cnt_arrive(unsigned a) {
    unsigned r;
    asm volatile("s_waitcnt lgkmcnt(0)\n\ts_mov_b64 exec, 1\n\tds_add_rtn_u32 %0, %1, %2\n\ts_mov_b64 exec, -1\n\ts_waitcnt lgkmcnt(0)"
                 : "=&v"(r) : "v"(a), "v"(1u) : "memory");
    return __builtin_amdgcn_readfirstlane(r);
}
DI void cnt_spin(unsigned a, unsigned n) {
#pragma unroll 1
    while ((unsigned)__builtin_amdgcn_readfirstlane(*(volatile LAS unsigned*)(size_t)a) < n) {}
    asm volatile("" ::: "memory");
}
template <int NAP = 1> DI void cnt_wait(unsigned a, unsigned n) {
#pragma unroll 1
    while ((unsigned)__builtin_amdgcn_readfirstlane(*(volatile LAS unsigned*)(size_t)a) < n) __builtin_amdgcn_s_sleep(NAP);
    asm volatile("" ::: "memory");
}
DI unsigned keep(unsigned v) { return min(v, EMPTY - 1); }
DI bool missing(const u32x4& a, const u32x4& b) { return __any(max(max(a[0], a[2]), max(b[0], b[2])) == EMPTY); }
DI void refetch(u32x4& a, u32x4& b, const char* base, unsigned v0, unsigned v1) {
#pragma unroll 1
    for (int spin = 0; spin < (1 << 16); ++spin) {
        asm volatile("global_load_dwordx4 %0, %2, %4 sc1\n\tglobal_load_dwordx4 %1, %3, %4 sc1\n\ts_waitcnt vmcnt(0)"
                     : "+v"(a), "+v"(b) : "v"(v0), "v"(v1), "s"(base) : "memory");
        if (!missing(a, b)) break;
    }
}
DI u32x4 rowpair(const u32x2& a, const u32x2& b) {
    const auto s0 = __builtin_amdgcn_permlane16_swap(a[0], b[0], false, false), s1 = __builtin_amdgcn_permlane16_swap(a[1], b[1], false, false);
    return u32x4{s0[0], s1[0], s0[1], s1[1]};
}
DI int lane_id() {
    int l;
    asm volatile("v_mbcnt_lo_u32_b32 %0, -1, 0\n\tv_mbcnt_hi_u32_b32 %0, -1, %0" : "=v"(l));
    return l;
}
#define LANE_VARS() const int lane = lane_id(), g = lane >> 4, c16 = lane & 15; (void)g; (void)c16
DI float halfmax(float m) {
    auto r = __builtin_amdgcn_permlane32_swap(__float_as_uint(m), __float_as_uint(m), false, false);
    return fmaxf(__uint_as_float(r[0]), __uint_as_float(r[1]));
}
DI float upper(float v) {
    auto r = __builtin_amdgcn_permlane32_swap(__float_as_uint(v), __float_as_uint(v), false, false);
    return __uint_as_float(r[1]);
}
DI float max16(const f32x16& s) {
    float a = fmaxf(fmaxf(s[0], s[1]), s[2]), b = fmaxf(fmaxf(s[3], s[4]), s[5]);
    a = fmaxf(fmaxf(a, s[6]), s[7]); b = fmaxf(fmaxf(b, s[8]), s[9]);
    a = fmaxf(fmaxf(a, s[10]), s[11]); b = fmaxf(fmaxf(b, s[12]), s[13]);
    a = fmaxf(fmaxf(a, s[14]), s[15]);
    return fmaxf(a, b);
}
DI float amax3(float m, float a, float b) {
    float r;
    asm("v_maximum3_f32 %0, %1, |%2|, |%3|" : "=v"(r) : "v"(m), "v"(a), "v"(b));
    return r;
}
DI f32x16 attn_exact(const char* kv, u32x4 q1, unsigned q2lo, int lane, int vrow) {
    const int r32 = lane & 31, h = lane >> 5;
    const f32x16 zero16 = {0, 0, 0, 0, 0, 0, 0, 0, 0, 0, 0, 0, 0, 0, 0, 0};
    const u32x2 q2 = {q2lo, 0u};
    float m = -INFINITY;
    f32x16 o = zero16;
#pragma unroll 1
    for (int t = 0; t < 64; ++t) {
        const GAS char* tp = (const GAS char*)kv + t * KV_TILE;
        const u32x4 fk1 = *(const GAS u32x4*)(tp + lane * 16);
        const u32x2 fk2 = {*(const GAS unsigned*)(tp + 1024 + r32 * 8 + h * 4), 0u};
        const u32x4 fv0 = *(const GAS u32x4*)(tp + 1280 + (h * 22 + vrow) * 16), fv1 = *(const GAS u32x4*)(tp + 1280 + VS + (h * 22 + vrow) * 16);
        f32x16 s = mfma32(fk1, q1, mfma32k8(fk2, q2, zero16));
        const float mn = fmaxf(m, halfmax(max16(s))), al = EX2(m - mn);
        m = mn;
        u32x4 p0, p1;
#pragma unroll
        for (int i = 0; i < 4; ++i) { p0[i] = pk2(EX2(s[2 * i] - m), EX2(s[2 * i + 1] - m)); p1[i] = pk2(EX2(s[8 + 2 * i] - m), EX2(s[9 + 2 * i] - m)); }
#pragma unroll
        for (int i = 0; i < 16; ++i) o[i] *= al;
        o = mfma32(fv1, p1, mfma32(fv0, p0, o));
    }
    return o;
}

DI void mma16(f32x4& c, const u32x4& a, const u32x4& b) {
    asm volatile("v_mfma_f32_16x16x32_bf16 %0, %1, %2, %0" : "+v"(c) : "v"(a), "v"(b));
}
DI void mma16z(f32x4& c, const u32x4& a, const u32x4& b) { asm volatile("v_mfma_f32_16x16x32_bf16 %0, %1, %2, 0" : "=&v"(c) : "v"(a), "v"(b)); }
DI void mma_done() { SBAR(); asm volatile("s_nop 7\n\ts_nop 1"); SBAR(); }
template <int NS> DI void zero_acc(f32x4 (&a)[NS]) {
#pragma unroll
    for (int s = 0; s < NS; ++s) { a[s] = f32x4{0, 0, 0, 0}; PIN(a[s]); }
}
template <int GS, int ASTR, int NG> DI u32x4 aload(rsrc_t rs, unsigned& v, const unsigned (&gb)[NG], int j) {
    const int o = (j % GS) * ASTR;
    PIN(v);
    return bld16(rs, v + (o & 4095), gb[j / GS] + (o & ~4095));
}
constexpr int areq(int j, int PD, int KT) {
    const int a = FIRSTFILL + 2 * j, b = PD + (j > 0 ? j - 1 : 0), c = a < b ? a : b;
    return c < KT ? c : KT;
}
template <int N> struct Int { static constexpr int v = N; };
template <int A, int B, class Fn> DI void static_for(Fn fn) { if constexpr (A < B) { fn(Int<A>{}); static_for<A + 1, B>(fn); } }
template <int N> DI void lgkm() { __builtin_amdgcn_s_waitcnt(0xC07F | (N << 8)); }
template <int K0, int NK, int KT, int F0, int FT, int NS, int NSN, int PD, int GS, int ASTR, int NA, int RING, int POOL, int NG, class WAddr, class Side>
DI void kloop(f32x4 (&acc)[2][NA], u32x4 (&act)[RING][2], u32x4 (&wf)[POOL], WAddr waddr, rsrc_t rs, const char* ws, unsigned& v0,
              unsigned& v1, const unsigned (&gb)[NG], Side side) {
    if constexpr (NK > 0) {
        constexpr int j = K0, q0 = areq(j, PD, KT), q1 = areq(j + 1, PD, KT), jc = j + 1 < KT ? j + 1 : 0, nn = NK > 1 ? NS : NSN;
        constexpr int issued = F0 + NS + POOL < FT ? F0 + NS + POOL : FT, need = F0 + NS + nn < FT ? F0 + NS + nn : FT;
        unsigned t = 0;
        u64 bad = 0;
        if (j == 0) { lgkm<POOL - NS>(); SBAR(); }
#pragma unroll
        for (int i = 0; i < 2 * NS; ++i) {
            const int n = F0 + (i >> 1);
            mma16(acc[i & 1][i >> 1], wf[n % POOL], act[j % RING][i & 1]);
            SBAR();
            if ((i & 1) && n + POOL < FT) wf[n % POOL] = lds16(waddr(n + POOL));
            if (!(i & 1) && q0 + i / 4 < q1) act[(q0 + i / 4) % RING][i / 2 & 1] = aload<GS, ASTR>(rs, (i & 2) ? v1 : v0, gb, q0 + i / 4);
            if (jc && !(j & 1)) {
                constexpr int jd = j + 2 < KT ? j + 2 : jc;
                if (i == (NS > 2 ? 2 * NS - 6 : 0)) {
                    t = max(max(act[jd % RING][1][0], act[jd % RING][1][2]), act[jd % RING][0][0]);
                    t = max(max(t, act[jd % RING][0][2]), act[jc % RING][1][0]); PIN(t);
                }
                if (i == (NS > 2 ? 2 * NS - 5 : 0)) {
                    t = max(max(t, act[jc % RING][1][2]), act[jc % RING][0][0]);
                    t = max(t, act[jc % RING][0][2]); PIN(t);
                }
                if (i == (NS > 2 ? 2 * NS - 4 : 1)) { bad = __ballot(t == EMPTY); asm volatile("" : "+s"(bad)); }
                if (i == 2 * NS - 2 && __builtin_expect(bad != 0, 0)) {
                    refetch(act[jc % RING][0], act[jc % RING][1], ws + gb[jc / GS] + (jc % GS) * ASTR, v0, v1);
                    refetch(act[jd % RING][0], act[jd % RING][1], ws + gb[jd / GS] + (jd % GS) * ASTR, v0, v1);
                }
            }
            side(j, i);
            if (i == 2 * NS - 1 && need > F0 + NS) { SBAR(); lgkm<issued - need>(); }
            SBAR();
        }
        kloop<K0 + 1, NK - 1, KT, F0 + NS, FT, NS, NSN, PD, GS, ASTR>(acc, act, wf, waddr, rs, ws, v0, v1, gb, side);
    }
}
template <int PD, int GS, int ASTR, int RING, int NG, class Probe, class Before, class After>
DI void kstart(u32x4 (&act)[RING][2], rsrc_t rs, const char* ws, unsigned& v0, unsigned& v1, const unsigned (&gb)[NG], Probe probe,
               Before before, After after) {
    before();
    SBAR();
#pragma unroll 1
    for (int spin = 0; spin < (1 << 16); ++spin) {
        const bool miss = probe();
        SBAR();
#pragma unroll
        for (int k = 0; k < FIRSTFILL; ++k) {
            act[k][0] = aload<GS, ASTR>(rs, v0, gb, k); SBAR();
            act[k][1] = aload<GS, ASTR>(rs, v1, gb, k); SBAR();
        }
        if (spin == 0) after();
        if (!__any(miss)) break;
        asm volatile("" ::: "memory");
    }
    SBAR();
    if (__builtin_expect(missing(act[0][0], act[0][1]), 0)) refetch(act[0][0], act[0][1], ws + gb[0], v0, v1);
    SBAR();
}
DI void e_bases(unsigned (&gb)[9], unsigned base, int m, int stride) {
    const int m3 = m % 3;
#pragma unroll
    for (int i = 0; i < 9; ++i) {
        const int q = i < 4 ? (i + m) & 3 : i < 6 ? 4 + ((i + (m >> 2)) & 1) : 6 + (i - 6 + m3 >= 3 ? i - 9 + m3 : i - 6 + m3);
        gb[i] = base + q * stride;
        asm volatile("" : "+s"(gb[i]));
    }
}
DI void d_bases(unsigned (&gb)[5], unsigned base, int ct, int stride) {
    const int c3 = ct % 3;
#pragma unroll
    for (int i = 0; i < 5; ++i) {
        gb[i] = base + (i < 3 ? (i + c3 >= 3 ? i + c3 - 3 : i + c3) : 3 + ((i + 1 + ct) & 1)) * stride;
        asm volatile("" : "+s"(gb[i]));
    }
}
template <int NG> DI void rot_bases(unsigned (&gb)[NG], unsigned base, int rot, int stride) {
#pragma unroll
    for (int i = 0; i < NG; ++i) { const int q = i + rot; gb[i] = base + (q >= NG ? q - NG : q) * stride; asm volatile("" : "+s"(gb[i])); }
}
DI void send_half(const f32x4 (&a)[5], unsigned dst) {
#pragma unroll
    for (int s = 0; s < 5; ++s) *(LAS f32x4*)(size_t)(dst + s * 1024) = a[s];
}
DI void recv_half(f32x4 (&a)[5], unsigned src) {
#pragma unroll
    for (int s = 0; s < 5; ++s) a[s] += ldsf4(src + s * 1024);
}
DI void ldsw16(unsigned a, const u32x4& v) { *(LAS u32x4*)(size_t)a = v; }
DI f32x4 unpk(unsigned r0, unsigned r1) { return f32x4{bflo(r0), bfhi(r0), bflo(r1), bfhi(r1)}; }
DI void res_init(f32x4 (&a)[5], const u32x4& x0, const u32x4& x1, const u32x2& x2) {
    a[0] = unpk(x0[0], x0[1]); a[1] = unpk(x0[2], x0[3]); a[2] = unpk(x1[0], x1[1]); a[3] = unpk(x1[2], x1[3]); a[4] = unpk(x2[0], x2[1]);
#pragma unroll
    for (int s = 0; s < 5; ++s) PIN(a[s]);
}
DI void res_pack(u32x4& x0, u32x4& x1, u32x2& x2, const f32x4 (&a)[5]) {
    x0 = u32x4{pk2(a[0][0], a[0][1]), pk2(a[0][2], a[0][3]), pk2(a[1][0], a[1][1]), pk2(a[1][2], a[1][3])};
    x1 = u32x4{pk2(a[2][0], a[2][1]), pk2(a[2][2], a[2][3]), pk2(a[3][0], a[3][1]), pk2(a[3][2], a[3][3])};
    x2 = u32x2{pk2(a[4][0], a[4][1]), pk2(a[4][2], a[4][3])};
}
enum { C_WKV, C_KFUSED, C_X, C_XUSED, C_WQ, C_CSP = C_WQ + 5, C_CS, C_KVST, C_R1, C_XCH, C_NUM = C_XCH + 2 };
enum { E_C1, E_C2, E_C3, E_MK, P_NONE, P_C0, P_C1, P_C2, E_C0, H_PREP, H_FILL, M_LDS, M_P1, M_W0, M_W1A, M_W1, M_K8, M_T4, M_T5, G_RDY, G_USED = G_RDY + 9, M_NUM = G_USED + 5 };

__global__ __launch_bounds__(512, 2) void k_block(const char* __restrict__ x, const char* __restrict__ wq, const char* __restrict__ wk,
                                                  const char* __restrict__ wv, const char* __restrict__ wo, const char* __restrict__ w1,
                                                  const char* __restrict__ w2, char* ws, char* __restrict__ out) {
    extern __shared__ __attribute__((aligned(16))) char smem[];
    const unsigned L = (unsigned)(size_t)(LAS char*)smem;
    const int w = __builtin_amdgcn_readfirstlane(threadIdx.x >> 6);
    const int xg = blockIdx.x & 7, idx = blockIdx.x >> 3;
    const int tb = ONEWAY ? xg * 4 + (idx >> 3) : (xg >> 1) * 8 + (idx >> 2), m = ONEWAY ? (idx & 7) : (xg & 1) * 4 + (idx & 3), cid = tb * 8 + m;
    const int ct = (m >> 2) * 8 + (tb & 1) * 4 + (m & 3);
    const rsrc_t rs = __builtin_amdgcn_make_buffer_rsrc(ws, 0, WS_END, 0x00020000);
    const u32x4 ones = {ONES2, ONES2, ONES2, ONES2};
    const f32x4 z4 = {0, 0, 0, 0};
    STAMP_INIT();

#define LOAD_TAG() u64 tag; asm volatile("global_load_dwordx2 %0, %1, %2 sc1" : "=v"(tag) : "v"(0u), "s"(ws + WS_FL) : "memory")
#define MAKE_TAG() do { PIN(tag); \
        tag = ((u64)(unsigned)__builtin_amdgcn_readfirstlane(tag >> 32) << 32) | (unsigned)__builtin_amdgcn_readfirstlane((unsigned)tag); \
        tag = (tag ^ 0x9E3779B97F4A7C15ull) * 0xBF58476D1CE4E5B9ull + 0x632BE59BD9B4E019ull; asm volatile("" : "+s"(tag)); } while (0)

#define CNT(i) (L + L_CNT + (i) * 4)
#define CN2(i) (L + L_MISC + 512 + (i) * 4)
#define WQ_AT(r) ((r) < 2 ? L_WQ1 + (r) * 20480 : L_WQ2 + (r) % 3 * 20480)
#define WQ_ADDR() const unsigned wrow = L + (8 * g + (c16 >> 2)) * 160 + (lane & 3) * 8, wo0 = wrow + (g & 1) * 640, wo1 = wrow + ((g & 1) ^ 1) * 640
    const int rq = QROT;
    const int rotc = (m + tb) % 5, rotd = ct + (tb >> 1), rote = m + tb;

#define WQ_SRC() unsigned vw[5]; _Pragma("unroll") for (int i = 0; i < 5; ++i) { \
        const int ci = i * 64 + lane, pos = (ci * 6554) >> 16, ch = ci - pos * 10, k = pos ^ (((pos >> 3) & 1) << 2); \
        vw[i] = k * 1280 + ch * 16; PIN(vw[i]); } \
    auto wqload = [&](int r) {     \
        _Pragma("unroll") for (int i = 0; i < 5; ++i) glds(vw[i], wq + m * 160 + hw * 40960 + (r + rq >= 5 ? r + rq - 5 : r + rq) * 163840, L + WQ_AT(r) + hw * 5120 + i * 1024); }
    if (w < 4) {
        const int lane = lane_id();
        {
            const int wi = idx * 4 + w;
            touch(lane * 128, x + xg * 327680 + wi * 2560, L + L_JUNK, 0xfffffull);
            touch(lane * 128, ((wi & 1) ? wv : wk) + min(wi >> 1, 49) * 512, L + L_JUNK, 0xfull);
            const char* pc; int l1, l2;
            asm volatile("s_getpc_b64 %0\n.Lpf_pc:\n\ts_mov_b32 %1, .Lpf_end1-.Lpf_pc\n\ts_mov_b32 %2, .Lpf_end2-.Lpf_pc" : "=s"(pc), "=s"(l1), "=s"(l2));
            touch(lane * 128, pc + max(min(wi * 512, max(l1, l2) - 512), 0), L + L_JUNK, 0xfull);
        }
        if (w == 0 && lane < C_NUM) *(LAS unsigned*)(size_t)CNT(lane) = 0u;
        bar();
    } else {
        const int hw = w - 4;
        LANE_VARS();
        __builtin_amdgcn_s_setprio(P1PRIO);
        if (m < 4) {
            const char* src = m < 2 ? wk : wv;
#pragma unroll 1
            for (int p = hw; p < 25; p += 4) glds(lane * 16, src + (p + 5 * rq >= 25 ? p + 5 * rq - 25 : p + 5 * rq) * 1024, L + L_WKV + p * 1024);
        }
        const unsigned vx0 = (lane >> 3) * 1280 + (((lane & 7) ^ g) << 4), vx1 = (8 + (lane >> 3)) * 1280 + (((lane & 7) ^ (4 + g)) << 4);
        const char* xb = x + (size_t)(tb * 64 + hw * 16) * 1280;
        int sg = m;
        auto xload = [&](int n) {
#pragma unroll 1
            for (int i = 0; i < n; ++i) {
                const int sp = sg - 2 * rq < 0 ? sg - 2 * rq + 10 : sg - 2 * rq;
                glds(vx0, xb + sg * 128, L + L_X + (hw * 20 + sp) * 1024);
                glds(vx1, xb + sg * 128, L + L_X + (hw * 20 + 10 + sp) * 1024);
                sg = sg == 9 ? 0 : sg + 1;
            }
        };
        xload(3);
        WQ_SRC();
        bar_raw();
        wait_vm<6>();
        cnt_add(CNT(C_WKV));
        xload(7);
        wqload(0);
        wait_vm<5>();
        cnt_add(CNT(C_X));
        wqload(1);
    }
    int role = w;
    asm volatile("" : "+s"(role));
    if (role >= 4) {
        const int hw = w - 4;
        LANE_VARS();
        u64 f0, tag;
        const int c0 = tb >> 3;
        {
            WQ_SRC();
            WQ_ADDR();
            f32x4 cs[5];
            u32x4 one = ones;
            auto wqsum = [&](int r) {
                u32x4 f[5];
#pragma unroll
                for (int s = 0; s < 5; ++s) f[s] = ldstr2(wo0 + WQ_AT(r) + hw * 5120 + s * 32, wo1 + WQ_AT(r) + hw * 5120 + s * 32);
#pragma unroll
                for (int s = 0; s < 5; ++s) mma16(cs[s], f[s], one);
            };
            STAMP(1);
            PIN(one);
            zero_acc(cs);
            cnt_spin(CNT(C_KFUSED), 4);
            wqload(2); wait_vm<10>(); cnt_add(CNT(C_WQ));
            cnt_spin(CNT(C_XUSED), 4);
            wqload(3); wait_vm<10>(); cnt_add(CNT(C_WQ + 1));
            asm volatile("global_load_dwordx2 %0, %1, %2 sc1" : "=v"(tag) : "v"(0u), "s"(ws + WS_FL) : "memory");
            wqload(4);
            wqsum(0); wqsum(1);
            wait_vm<11>(); cnt_add(CNT(C_WQ + 2)); wqsum(2);
            wait_vm<6>(); cnt_add(CNT(C_WQ + 3)); wqsum(3);
            wait_vm<0>(); cnt_add(CNT(C_WQ + 4));
            MAKE_TAG();
            f0 = __hip_atomic_load((const GAS u64*)(ws + FL_KV + (c0 * 32 + (lane & 31)) * 128), __ATOMIC_RELAXED, __HIP_MEMORY_SCOPE_AGENT);
            wqsum(4);
            mma_done();
            if (c16 == 0) {
#pragma unroll
                for (int s = 0; s < 5; ++s) *(LAS f32x4*)(size_t)(L + L_CSX + hw * 320 + s * 64 + g * 16) = cs[s];
            }
            if (cnt_arrive(CNT(C_CSP)) == 3) {
                const unsigned ca = L + L_CSX + (lane < 20 ? lane : 0) * 16;
                const f32x4 c4 = (ldsf4(ca) + ldsf4(ca + 320)) + (ldsf4(ca + 640) + ldsf4(ca + 960));
                *(LAS f32x4*)(size_t)(ca + 1280) = c4;
                cnt_add(CNT(C_CS));
            }
        }
        const char* kv = ws + WS_KV;
        STAMP(2);
        poll(ws, FL_KV, lane < 32 && f0 != tag ? c0 * 32 + lane : -1, tag);
        STAMP(3);
        copy_dev(kv + c0 * CHUNK, L, 42, hw, lane);
        u64 f1;
        asm volatile("global_load_dwordx2 %0, %1, %2 sc1" : "=v"(f1) : "v"(((c0 * 32 + 32 + lane) & 127) * 128), "s"(ws + FL_KV) : "memory");
        wait_vm<8>();
        STAMP(4);
        __builtin_amdgcn_s_setprio(2);
        bar_raw();
        wait_vm<0>();
        cnt_add(CN2(E_C0));
        PIN(f1);
        poll(ws, FL_KV, f1 != tag ? (c0 * 32 + 32 + lane) & 127 : -1, tag);
        copy_dev(kv + ((c0 + 1) & 3) * CHUNK, L + CHUNK, 42, hw, lane);
        {
            int u = blockIdx.x * 4 + hw;
            const int pmat = u < 160 ? 0 : (u < 480 ? 1 : (u < 912 ? 2 : 3));
            u = pmat == 0 ? u : pmat == 1 ? u - 160 : pmat == 2 ? u - 480 : 0;
            const int pc = pmat < 2 ? u / 20 : u / 54, ks = u - pc * (pmat < 2 ? 20 : 54);
            int rb, a3, a2, a1;
            if (pmat == 0) { if (ks < 16) { rb = 40 * ks; a3 = 20; a2 = 4; a1 = 8; } else { rb = 160 * (ks - 16) + 16; a3 = 80; a2 = 40; a1 = 20; } }
            else if (pmat == 1) { rb = ks < 8 ? 80 * ks : ks < 12 ? 160 * (ks - 8) + 64 : 80 * (ks - 12) + 32; a3 = ks >= 8 && ks < 12 ? 80 : 16; a2 = 8; a1 = 4; }
            else if (ks >= 16 && ks < 24) { rb = 224 * (ks - 16) + 96; a3 = ks < 23 ? 112 : 48; a2 = 8; a1 = 4; }
            else { rb = ks < 16 ? 112 * ks : (ks < 39 ? 112 * (ks - 24) + 32 : 112 * (ks - 39) + 64); a3 = 8; a2 = 4; a1 = 16; }
            const int ld = pmat == 1 ? 3456 : 1280, chmax = pmat != 1 ? 9 : (pc == 15 ? 5 : 13);
            const char* psrc = pmat == 0 ? wo + pc * 160 : pmat == 1 ? w1 + pc * 224 : w2 + pc * 160;
            const unsigned pb = L + 3 * CHUNK + hw * 8192, pv = g * ld + min(c16, chmax) * 16;
            cnt_wait(CN2(P_NONE), 4);
#pragma unroll 1
            for (int i = 0; i < 8; ++i) glds(pv, psrc + (rb + a3 * (i >> 2) + a2 * ((i >> 1) & 1) + a1 * (i & 1)) * ld, pb + i * 1024);
            wait_vm<8>();
            cnt_add(CN2(E_C1));
            copy_dev(kv + ((c0 + 2) & 3) * CHUNK, L + 2 * CHUNK, 42, hw, lane);
            wait_vm<10>();
            const unsigned a0 = pb + (8 * g + (c16 >> 2)) * 256 + (lane & 3) * 8;
            if (pmat == 1) {
                const unsigned pdst = WS_W1 + pc * 143360 + ks * 5120, pd2 = WS_W1 + pc * 143360 + 102400 + ks * 2048;
                const unsigned cv = c16 == 0 ? g * 16 : 0x80000000u;
#pragma unroll
                for (int s = 0; s < 7; ++s) {
                    const unsigned a1 = a0 + (s == 6 ? (pc == 15 ? 64 : 192) : s * 32);
                    const u32x4 f = ldstr2(a1, a1 + 1024);
                    bst16(rs, f, lane * 16, s < 2 ? pdst + s * 1024 : s < 4 ? pdst + (s + 1) * 1024 : s < 6 ? pd2 + (s - 4) * 1024 : pdst + 2048);
                    bst16(rs, __builtin_bit_cast(u32x4, mfma16(f, ones, z4)), cv, WS_CS + (ks * 1792 + pc * 112) * 4 + s * 64);
                }
            } else if (pmat < 3) {
                const unsigned a1 = a0 + (lane & 3) * 8, pdst = pmat == 0 ? WS_WO + pc * 102400 + ks * 3072 : WS_W2 + u * 5120;
                const unsigned pd2 = pmat == 0 ? WS_WO + pc * 102400 + 61440 + ks * 2048 : pdst + 2048, pd4 = pdst + (pmat == 0 ? 2048 : 4096);
#pragma unroll
                for (int s = 0; s < 5; ++s) {
                    const unsigned a2 = s < 4 ? a1 + (s >> 1) * 64 + (s & 1) * 8 : a0 + 128;
                    bst16(rs, ldstr2(a2, a2 + 1024), lane * 16, s < 2 ? pdst + s * 1024 : s < 4 ? pd2 + (s - 2) * 1024 : pd4);
                }
            }
        }
        wait_vm<0>();
        cnt_add(CN2(E_C2));
        if (cnt_arrive(CN2(H_PREP)) == 3 && lane == 0) set_flag(ws, FL_PR + cid * 128, tag);
        STAMP(5);
        cnt_wait<3>(CN2(H_PREP), 4);
        {
            const u32x4 e = {EMPTY, EMPTY, EMPTY, EMPTY};
#pragma unroll 1
            for (int p = hw; p < 44; p += 4) bst16(rs, e, lane * 16, WS_O + blockIdx.x * 48640 + p * 1024);
            bst16(rs, e, hw < 3 || lane < 32 ? lane * 16 : 0x80000000u, WS_O + blockIdx.x * 48640 + (44 + hw) * 1024);
        }
        poll(ws, FL_KV, (c0 * 32 + 96 + (lane & 31)) & 127, tag);
        wait_vm<0>();
        if (cnt_arrive(CN2(H_FILL)) == 3 && lane == 0) set_flag(ws, FL_MK + cid * 128, tag);
        cnt_wait<LONGNAP>(CN2(P_C0), 4);
        copy_dev(kv + ((c0 + 3) & 3) * CHUNK, L, 42, hw, lane);
        poll(ws, FL_PR, hw * 64 + lane, tag);
        wait_vm<0>();
        cnt_add(CN2(E_C3));
        cnt_wait<3>(CN2(E_C3), 4);
        STAMP(6);
        if (blockIdx.x == 0 && hw == 1 && lane == 0) set_flag(ws, WS_FL, tag);
        auto wocopy = [&](int p0, int p1) {
#pragma unroll 1
            for (int p = p0 + hw; p < p1; p += 4) {
                const int q = p + rotc * (p < 60 ? 12 : 8), r = p < 60 ? (q >= 60 ? q - 60 : q) : (q >= 100 ? q - 40 : q);
                glds_dev(lane * 16, ws + WS_WO + m * 102400 + r * 1024, L + L_WO + p * 1024);
            }
        };
        u32x4 r1[35], r2[9][8];
        unsigned g1[5], g1b[5], g2[9];
        d_bases(g1, WS_W1 + ct * 143360, rotd, 20480);
        d_bases(g1b, WS_W1 + ct * 143360 + 102400, rotd, 8192);
        e_bases(g2, WS_W2 + m * 276480, rote, 30720);
        const unsigned vp = lane * 16 + hw * 1024, vq = lane * 16 + min(28 + hw, 29) * 1024;
        unsigned lp0 = L + vp, lp1 = lp0 + 61440, lp2 = lp0 + 122880, lq0 = L + vq, lq1 = lq0 + 61440, lq2 = lq0 + 122880;
        PIN(lp0); PIN(lp1); PIN(lp2); PIN(lq0); PIN(lq1); PIN(lq2);
#define LD2(G, THR) do { _Pragma("unroll") for (int j_ = 0; j_ < 7; ++j_) { r2[G][j_] = bld16(rs, vp, g2[G] + j_ * 4096); wait_vm<THR>(); } \
        r2[G][7] = bld16(rs, vq, g2[G]); wait_vm<THR>(); } while (0)
#define ST2E(G, j) do { constexpr int sl_ = (G) % 5; \
        ldsw16((j) < 7 ? (sl_ < 2 ? lp0 : sl_ < 4 ? lp1 : lp2) + (sl_ & 1) * 30720 + (j) * 4096 : (sl_ < 2 ? lq0 : sl_ < 4 ? lq1 : lq2) + (sl_ & 1) * 30720, r2[G][j]); } while (0)
#pragma unroll
        for (int i = 0; i < 35; ++i) r1[i] = bld16(rs, vp, i < 25 ? g1[i / 5] + (i % 5) * 4096 : g1b[(i - 25) / 2] + ((i - 25) & 1) * 4096);
        SBAR();
        if (hw == 3) {
            u32x4 c[10];
            const unsigned cv = min(lane & 31, 27) * 16 + (lane >> 5) * 71680;
#pragma unroll
            for (int ks = 0; ks < 10; ++ks) c[ks] = bld16(rs, cv, WS_CS + (ks * 1792 + ct * 112) * 4);
            f32x4 c4 = z4;
#pragma unroll
            for (int ks = 0; ks < 10; ++ks) c4 += __builtin_bit_cast(f32x4, c[ks]);
#pragma unroll
            for (int i = 0; i < 4; ++i) c4[i] += upper(c4[i]);
            if (lane < 28) *(LAS f32x4*)(size_t)(L + L_MISC + lane * 16) = c4;
        }
        LD2(6, 63); LD2(7, 63); LD2(8, 63);
        cnt_wait<LONGNAP>(CN2(P_C1), 4);
        wocopy(0, 42);
        poll(ws, FL_MK, hw * 64 + lane, tag);
        cnt_add(CN2(E_MK));
        cnt_wait<LONGNAP>(CN2(P_C2), 4);
        wocopy(42, 100);
#pragma unroll
        for (int i = 0; i < 12; ++i) glds_dev(lane * 16, ws + WS_W2 + m * 276480 + (((tb & 3) * 4 + hw) * 12 + i) * 1024, L + XCH + hw * 1024);
        wait_vm<12>();
        STAMP(7);
        __builtin_amdgcn_s_setprio(TAILPRIO);
        bar_raw();
#pragma unroll
        for (int i = 0; i < 10; ++i) ldsw16(lp0 + i * 4096, r1[i < 4 ? 25 + i : i - 4]);
        cnt_add(CN2(M_W0));
#pragma unroll
        for (int j = 0; j < GAP1; ++j) r2[3][j] = bld16(rs, j < 7 ? vp : vq, g2[3] + (j < 7 ? j * 4096 : 0));
        SBAR();
        cnt_wait(CN2(M_P1), 4);
#pragma unroll
        for (int i = 10; i < 35; ++i) {
            if (i == 25) cnt_wait(CN2(M_LDS), 4);
            ldsw16((i < 15 ? lp0 : i < 30 ? lp1 : lp2) + (i % 15) * 4096, r1[i < 29 ? i - 4 : i]);
            SBAR();
            if (i == 17) cnt_add(CN2(M_W1A));
            if (i >= 18 && i - 18 + GAP1 < NEARLY) {
                const int e = i - 18 + GAP1, G = 3 + (e >> 3), j = e & 7;
                r2[G][j] = bld16(rs, j < 7 ? vp : vq, g2[G] + (j < 7 ? j * 4096 : 0));
                SBAR();
            }
        }
        STAMP(8);
        cnt_add(CN2(M_W1));
#pragma unroll
        for (int e = NEARLY; e < 24; ++e) {
            const int G = 3 + (e >> 3), j = e & 7;
            r2[G][j] = bld16(rs, j < 7 ? vp : vq, g2[G] + (j < 7 ? j * 4096 : 0));
            wait_vm<THROT>();
        }
        cnt_wait<3>(CN2(M_K8), 4);
#pragma unroll 1
        for (int p = hw; p < 60; p += 4) { glds_dev(lane * 16, ws + (p < 30 ? g2[0] + p * 1024 : g2[1] + (p - 30) * 1024), L + p * 1024); wait_vm<THROT>(); }
        wait_vm<0>();
        cnt_add(CN2(G_RDY + 1));
        cnt_wait<3>(CN2(M_T4), 4);
        copy_dev(ws + g2[2], L + 61440, 30, hw, lane);
        wait_vm<0>();
        cnt_add(CN2(G_RDY + 2));
        STAMP(9);
        static_for<3, 9>([&](auto gg) {
            constexpr int G = gg.v;
            if (G == 3) cnt_wait<2>(CN2(M_T5), 4);
            if (G >= 5) cnt_wait<2>(CN2(G_USED + G - 5), 4);
#pragma unroll
            for (int j = 0; j < 8; ++j) { ST2E(G, j); SBAR(); }
            cnt_add(CN2(G_RDY + G));
        });
        asm volatile(".Lpf_end2:");
        STAMP_OUT(1);
        __builtin_amdgcn_endpgm();
    }

    u32x4 xr0, xr1; u32x2 xr2;
    u32x4 qa1, qb1; u32x2 qa2, qb2;
    const int kh = w & 1;
    {
        LANE_VARS();
        LOAD_TAG();
        u32x4 xf[20], xo[10], kf[20];
        const unsigned xrow = L + L_X + w * 20480 + (c16 >> 3) * 10240 + (c16 & 7) * 128;
        const int sw = c16 >> 1, t32 = (w & 1) * 16 + c16;
        unsigned xa = xrow + (((kh * 4 + g) ^ sw) << 4), xb = xrow + ((((kh ^ 1) * 4 + g) ^ sw) << 4), xc = xa + ((w ^ 1) - w) * 20480, xra[3], qw[5], kvo;
        {
            auto rotx = [&](int c) { c -= 16 * rq; return c < 0 ? c + 80 : c; };
            const int c0 = rotx(m * 10 + g), c1 = rotx(m * 10 + 4 + g), c2 = rotx(m * 10 + 8 + (g >> 1));
            xra[0] = xrow + (c0 >> 3) * 1024 + (((c0 & 7) ^ sw) << 4);
            xra[1] = xrow + (c1 >> 3) * 1024 + (((c1 & 7) ^ sw) << 4);
            xra[2] = xrow + (c2 >> 3) * 1024 + (((c2 & 7) ^ sw) << 4) + (g & 1) * 8;
            const int d = (m & 1) * 16 + c16;
            kvo = m == 0 ? ((g >> 1) * 32 + t32) * 16 + (g & 1) * 8
                : m == 1 ? (g == 0 ? 1024 + t32 * 8 : 0x80000000u)
                         : (d <= 21 ? 1280 + (((w & 1) * 2 + (g & 1)) * 22 + d) * 16 + (g >> 1) * 8 : 0x80000000u);
            PIN(xa); PIN(xb); PIN(xc); PIN(xra[0]); PIN(xra[1]); PIN(xra[2]); PIN(kvo);
        }
#pragma unroll
        for (int s = 0; s < 5; ++s) {
            const int f0 = s * 16 + 4 * g, hh = f0 / 20, d0 = f0 - hh * 20;
            qw[s] = L + L_QX + (hh * 2 + (w >> 1)) * 1280 + (d0 < 16 ? ((d0 >> 3) * 32 + t32) * 16 + (d0 & 4) * 2 : 1024 + t32 * 8);
            PIN(qw[s]);
        }
        WQ_ADDR();
        unsigned b0 = wo0 + L_WQ1 + kh * 5120, b1 = wo1 + L_WQ1 + kh * 5120;
        PIN(b0); PIN(b1);
        wait_vm<0>();
        MAKE_TAG();
        if (WQT && m >= 4) {
            __builtin_amdgcn_s_sleep(PFDELAY);
            const char* wp = wq + (((idx >> 3) * 4 + (m & 3)) * 4 + w) * 12800;
            touch(lane * 128, wp, L + L_JUNK, 0x3ffffffffffffull);
            touch(lane * 128, wp + 6400, L + L_JUNK, 0x3ffffffffffffull);
        }
        f32x4 ka = z4, kc = z4, ssm = z4, ssq = z4, acc[2][5];
        u32x4 one = ones;
        PIN(ssm); PIN(ssq); PIN(one);
        if (m < 4) {
            cnt_wait(CNT(C_WKV), 4);
            const unsigned a0 = L + L_WKV + (8 * g + (c16 >> 2)) * 40 + (m & 1) * 32 + (lane & 3) * 8;
            const unsigned a1 = a0 + kh * 1280, a2 = a0 + (kh ^ 1) * 1280;
#pragma unroll
            for (int j = 0; j < 10; ++j) { kf[j] = ldstr2(a1 + j * 2560, a1 + j * 2560 + 160); kf[10 + j] = ldstr2(a2 + j * 2560, a2 + j * 2560 + 160); }
            if (m < 2) {
#pragma unroll
                for (int j = 0; j < 20; ++j) kc = mfma16(kf[j], ones, kc);
            } else {
#pragma unroll
                for (int j = 0; j < 20; ++j) kc = mfma16(ones, kf[j], kc);
            }
            PIN(kc);
        }
        cnt_add(CNT(C_KFUSED));
        cnt_spin(CNT(C_X), 4);
#pragma unroll
        for (int j = 0; j < 10; ++j) xf[j] = lds16(xa + j * 1024);
#pragma unroll
        for (int j = 0; j < 10; ++j) xf[10 + j] = lds16(xb + j * 1024);
#pragma unroll
        for (int j = 0; j < 10; ++j) xo[j] = lds16(xc + j * 1024);
        xr0 = lds16(xra[0]); xr1 = lds16(xra[1]); xr2 = lds8(xra[2]);
        SBAR();
        if (m < 2) {
#pragma unroll
            for (int j = 0; j < 20; ++j) ka = mfma16(kf[j], xf[j], ka);
        } else if (m < 4) {
#pragma unroll
            for (int j = 0; j < 20; ++j) ka = mfma16(xf[j], kf[j], ka);
        }
        PIN(ka);
#pragma unroll
        for (int j = 0; j < 20; ++j) {
            mma16(ssm, one, xf[j]); mma16(ssq, xf[j], xf[j]);
            if (j == 4) {
#pragma unroll
                for (int i = 0; i < 10; ++i) { PIN(xf[i]); PIN(xf[10 + i]); PIN(xo[i]); }
                PIN(xr0); PIN(xr1); PIN(xr2);
                cnt_add(CNT(C_XUSED));
            }
        }
        mma_done();
        STAMP(1);
        const float sd = (c16 & 2) ? ((c16 & 1) ? ssq[3] : ssq[2]) : ((c16 & 1) ? ssq[1] : ssq[0]);
        const float mean = ssm[0] * (1.f / D), rstd = __builtin_amdgcn_rsqf(fmaxf(__shfl(sd, (c16 >> 2) * 16 + c16) * (1.f / D) - mean * mean, 0.f) + LN_EPS);
        if (m < 4) {
            u32x2 pv;
            if (m < 2) {
                pv = u32x2{pk2((ka[0] - mean * kc[0]) * rstd, (ka[1] - mean * kc[1]) * rstd), pk2((ka[2] - mean * kc[2]) * rstd, (ka[3] - mean * kc[3]) * rstd)};
            } else {
                float v[4];
#pragma unroll
                for (int r = 0; r < 4; ++r) v[r] = (ka[r] - __shfl(mean, 4 * g + r) * kc[0]) * __shfl(rstd, 4 * g + r);
                pv = u32x2{pk2(v[0], v[1]), pk2(v[2], v[3])};
                if (m == 3 && c16 >= 4) pv = c16 == 4 ? u32x2{ONES2, ONES2} : u32x2{0u, 0u};
            }
            bst8(rs, pv, kvo, WS_KV + (tb * 2 + (w >> 1)) * KV_TILE);
        }
        SBAR();
        STAMP(2);
        u32x2 f[2][5][2];
        unsigned xs, xv;
        float sc = rstd * QSCALE, ms = -mean * sc;
        PIN(sc); PIN(ms);
        unsigned b2 = b0 + (L_WQ2 - L_WQ1), b3 = b1 + (L_WQ2 - L_WQ1), cq = CNT(0), cv = 0;
        PIN(b2); PIN(b3); PIN(cq);
        auto qhalf = [&](int j, int s, int e) { return ldstr((j < 4 ? (e ? b1 : b0) + j * 10240 : (e ? b3 : b2) + j % 6 * 10240) + s * 32); };
        auto kvflag = [&] {
            if (m < 4) {
                wait_vm<0>();
                if (cnt_arrive(CNT(C_KVST)) == 3 && lane == 0) set_flag(ws, FL_KV + (tb * 4 + m) * 128, tag);
            }
        };
        if (FLAGAT < 0) kvflag();
        cnt_spin(CNT(C_WQ), 4);
#pragma unroll
        for (int s = 0; s < 5; ++s) { f[0][s][0] = qhalf(0, s, 0); f[0][s][1] = qhalf(0, s, 1); }
        SBAR();
#pragma unroll
        for (int j = 0; j < 10; ++j) {
            if (j < 8 && !(j & 1)) cv = *(volatile LAS unsigned*)(size_t)(cq + (C_WQ + j / 2 + 1) * 4);
            if (j < 9 && (j & 1) && __builtin_expect((unsigned)__builtin_amdgcn_readfirstlane(cv) < 4u, 0)) cnt_spin(CNT(C_WQ + j / 2 + 1), 4);
            asm volatile("" ::: "memory");
            SBAR();
#pragma unroll
            for (int s = 0; s < 5; ++s) {
                const u32x4 fr = {f[j & 1][s][0][0], f[j & 1][s][0][1], f[j & 1][s][1][0], f[j & 1][s][1][1]};
                if (j == 0) mma16z(acc[0][s], fr, xf[j]); else mma16(acc[0][s], fr, xf[j]);
                SBAR();
                if (j < 9) f[(j + 1) & 1][s][0] = qhalf(j + 1, s, 0);
                SBAR();
                if (j == 0) mma16z(acc[1][s], fr, xo[j]); else mma16(acc[1][s], fr, xo[j]);
                SBAR();
                if (j < 9) f[(j + 1) & 1][s][1] = qhalf(j + 1, s, 1);
                SBAR();
            }
            if (j == FLAGAT) {   }
            if (j == FLAGAT) kvflag();
            if (j == 3) asm volatile("s_mov_b64 exec, 1\n\tds_add_u32 %0, %1 offset:%2\n\ts_mov_b64 exec, -1" ::"v"(cq), "v"(1u), "i"(C_R1 * 4) : "memory");
            if (j == FLAGAT) {   }
            if (j == 5) { xs = L + L_WQ1 + (w ^ 1) * 5120 + lane * 16; xv = L + L_WQ1 + w * 5120 + lane * 16; PIN(xs); PIN(xv); }
            if (j == 7) cnt_spin(CNT(C_R1), 4);
        }
        STAMP(3);
        if (m >= 4) wait_vm<0>();
        mma_done();
        send_half(acc[1], xs);
        cnt_add(CNT(C_XCH + (w >> 1)));
        if (w == 0 && lane < M_NUM) *(LAS unsigned*)(size_t)CN2(lane) = 0u;
        f32x4 c5[5], p5[5];
#pragma unroll 1
        for (;;) {
            const unsigned n1 = *(volatile LAS unsigned*)(size_t)CNT(C_CS), n2 = *(volatile LAS unsigned*)(size_t)CNT(C_XCH + (w >> 1));
#pragma unroll
            for (int s = 0; s < 5; ++s) {
                c5[s] = *(volatile LAS f32x4*)(size_t)(L + L_CSX + 1280 + s * 64 + g * 16);
                p5[s] = *(volatile LAS f32x4*)(size_t)(xv + s * 1024);
            }
            if (__builtin_amdgcn_readfirstlane(n1 + n2) == 3) break;
        }
#pragma unroll
        for (int s = 0; s < 5; ++s) {
            const f32x4 v = (acc[0][s] + p5[s]) * sc + c5[s] * ms;
            *(LAS u32x2*)(size_t)qw[s] = u32x2{pk2(v[0], v[1]), pk2(v[2], v[3])};
        }
        bar();
        const int r32 = lane & 31, h = lane >> 5;
        const unsigned qb = L + L_QX + ((w & 1) * 4 + (w >> 1)) * 1280;
        qa1 = lds16(qb + lane * 16); qb1 = lds16(qb + 2560 + lane * 16);
        qa2 = u32x2{lds4(qb + 1024 + r32 * 8 + h * 4), 0u}; qb2 = u32x2{lds4(qb + 2560 + 1024 + r32 * 8 + h * 4), 0u};
    }
    STAMP(4);
    {
        LANE_VARS();
        const int r32 = lane & 31, h = lane >> 5, vrow = min(r32, 21);
        const unsigned kconst = h == 0 ? 0x00003f80u : 0u;
        unsigned ak1 = L + lane * 16, ak2 = L + 1024 + r32 * 8 + h * 4, av = L + 1280 + (h * 22 + vrow) * 16;
        const f32x16 zero16 = {0, 0, 0, 0, 0, 0, 0, 0, 0, 0, 0, 0, 0, 0, 0, 0};
        f32x16 sa, sb, oa = zero16, ob = zero16;
        u32x4 ka1 = lds16(ak1), kb1 = lds16(ak1 + KV_TILE), va0 = lds16(av), va1 = lds16(av + VS), vb0 = va0, vb1 = va0;
        u32x2 ka2 = {lds4(ak2), kconst}, kb2 = {lds4(ak2 + KV_TILE), kconst};
        sa = mfma32(ka1, qa1, mfma32k8(ka2, qa2, zero16));
        sb = mfma32(ka1, qb1, mfma32k8(ka2, qb2, zero16));
        {
            const float ma = halfmax(max16(sa)), mb = halfmax(max16(sb));
            qa2[1] = h == 0 ? (pk2(-ma, 0.f) & 0xffffu) : 0u;
            qb2[1] = h == 0 ? (pk2(-mb, 0.f) & 0xffffu) : 0u;
        }
        sa = mfma32(ka1, qa1, mfma32k8(ka2, qa2, zero16));
        u32x4 pa0 = {0u, 0u, 0u, 0u}, pa1 = pa0, pb0 = pa0, pb1 = pa0;
        float e2 = 0.f, e3 = 0.f;
        unsigned vo0 = (r32 >> 4) * 20480 + h * 256 + (r32 & 15) * 16, vo1 = h ? 0x80000000u : vo0;
        PIN(pa0); PIN(pa1); PIN(pb0); PIN(pb1); PIN(ka2); PIN(kb2); PIN(vo0); PIN(vo1);
#define SLOT(R, MF, X, S, i, PP, jp, P, j) do { R = MF; PIN(R); SBAR(); X; SBAR(); \
        float e0 = EX2(S[i]), e1 = EX2(S[i + 1]); PIN(e0); PIN(e1); SBAR(); \
        PP[jp] = pk2(e2, e3); PIN(PP); SBAR(); \
        e2 = EX2(S[i + 2]); e3 = EX2(S[i + 3]); PIN(e2); PIN(e3); SBAR(); \
        P[j] = pk2(e0, e1); PIN(P); SBAR(); } while (0)
#define STEP(KC1, KC2, KN1, KN2, VC0, VC1, VP0, VP1, OK, OV, X0, X7) do { \
        SLOT(sb, mfma32k8(KC2, qb2, zero16), X0, sa, 0, pb1, 3, pa0, 0); \
        SLOT(sb, mfma32(KC1, qb1, sb), __builtin_amdgcn_s_waitcnt(0xC07F), sa, 4, pa0, 1, pa0, 2); \
        SLOT(ob, mfma32(VP1, pb1, ob), KC1 = lds16(ak1 + (OK) * KV_TILE), sa, 8, pa0, 3, pa1, 0); \
        SLOT(oa, mfma32(VC0, pa0, oa), KC2[0] = lds4(ak2 + (OK) * KV_TILE), sa, 12, pa1, 1, pa1, 2); \
        SLOT(sa, mfma32k8(KN2, qa2, zero16), (void)0, sb, 0, pa1, 3, pb0, 0); \
        SLOT(sa, mfma32(KN1, qa1, sa), VP0 = lds16(av + (OV) * KV_TILE), sb, 4, pb0, 1, pb0, 2); \
        SLOT(oa, mfma32(VC1, pa1, oa), VP1 = lds16(av + (OV) * KV_TILE + VS), sb, 8, pb0, 3, pb1, 0); \
        SLOT(ob, mfma32(VC0, pb0, ob), X7, sb, 12, pb1, 1, pb1, 2); } while (0)
        unsigned so1 = WS_O + (tb * 4 + (w & 2)) * 20480, so0 = so1 + (2 * m + (w & 1)) * 1024;
        so1 += 16384 + (2 * m + (w & 1)) * 256;
        asm volatile("" : "+s"(so0), "+s"(so1));
        unsigned ea = CN2(E_C1), e0a = CN2(E_C0), ev = 0, one = 1;
        PIN(ea); PIN(e0a); PIN(one); PIN(ak1); PIN(ak2); PIN(av);
        auto hook0 = [&](int j, int dn) {
            if (j == 1) ev = *(volatile LAS unsigned*)(size_t)e0a;
            if (j == 14) { ak1 += dn; ak2 += dn; PIN(ak1); PIN(ak2); }
            if (j == 15) { av += dn; ea += 4; PIN(av); PIN(ea); }
        };
        auto hook7 = [&](int j) {
            if (j == 0)
                asm volatile("s_mov_b64 exec, 1\n\tds_add_u32 %0, %1 offset:%2\n\ts_mov_b64 exec, -1" ::"v"(ea), "v"(one), "i"((P_NONE - E_C1) * 4) : "memory");
            if (j == 1 && __builtin_expect((unsigned)__builtin_amdgcn_readfirstlane(ev) < 4u, 0)) cnt_spin(e0a, 4);
            if (j == 9) ev = *(volatile LAS unsigned*)(size_t)ea;
            if (j == 10 && __builtin_expect((unsigned)__builtin_amdgcn_readfirstlane(ev) < 4u, 0))
                cnt_wait(ea, 4);
        };
        STAMP(5);
#pragma unroll 1
        for (int c = 0; c < 4; ++c) {
            const int dn = c == 2 ? -2 * CHUNK : CHUNK;
#pragma unroll
            for (int j = 0; j < 16; j += 2) {
                STEP(ka1, ka2, kb1, kb2, va0, va1, vb0, vb1, (j + 2) & 15, j + 1, hook0(j, dn), hook7(j));
                STEP(kb1, kb2, ka1, ka2, vb0, vb1, va0, va1, (j + 3) & 15, (j + 2) & 15, hook0(j + 1, dn), hook7(j + 1));
            }
        }
        pb1[3] = pk2(e2, e3);
        ob = mfma32(vb1, pb1, ob);
        STAMP(6);
        u32x4 r0, r1, r2;
        auto finish = [&](const f32x16& pa, const f32x16& pb, float la, float lb) {
            const float ra = __builtin_amdgcn_rcpf(la), rb = __builtin_amdgcn_rcpf(lb);
            r0 = u32x4{keep(pk2(pa[0] * ra, pa[1] * ra)), pk2(pa[2] * ra, pa[3] * ra), keep(pk2(pa[4] * ra, pa[5] * ra)), pk2(pa[6] * ra, pa[7] * ra)};
            r1 = u32x4{keep(pk2(pb[0] * rb, pb[1] * rb)), pk2(pb[2] * rb, pb[3] * rb), keep(pk2(pb[4] * rb, pb[5] * rb)), pk2(pb[6] * rb, pb[7] * rb)};
            r2 = u32x4{keep(pk2(pa[8] * ra, pa[9] * ra)), pk2(pa[10] * ra, pa[11] * ra), keep(pk2(pb[8] * rb, pb[9] * rb)), pk2(pb[10] * rb, pb[11] * rb)};
        };
        {
            float am = 0.f;
#pragma unroll
            for (int i = 0; i < 12; i += 2) am = amax3(am, oa[i], oa[i + 1]);
            const float la = upper(oa[8]);
            asm volatile("s_nop 7" : "+v"(ob));
            const float lb = upper(ob[8]);
#pragma unroll
            for (int i = 0; i < 12; i += 2) am = amax3(am, ob[i], ob[i + 1]);
            finish(oa, ob, la, lb);
            const bool ok = (fminf(la, lb) >= 1e-18f) & (fmaxf(la, lb) <= 1e18f) & (am <= 1e37f);
            if (__builtin_expect(__ballot(!ok) != 0, 0)) {
                __builtin_amdgcn_fence(__ATOMIC_ACQUIRE, "agent");
                f32x16 xa, xb;
#pragma unroll 1
                for (int qt = 0; qt < 2; ++qt) {
                    xb = attn_exact(ws + WS_KV, qt ? qb1 : qa1, qt ? qb2[0] : qa2[0], lane, vrow);
                    if (qt == 0) xa = xb;
                }
                finish(xa, xb, upper(xa[8]), upper(xb[8]));
            }
        }
        bst16(rs, r0, vo0, so0);
        bst16(rs, r1, vo0 + 512, so0);
        bst16(rs, r2, vo1, so1);
        STAMP(7);
    }
    u32x4 wd[12], ae[RE][2], we[PE];
    unsigned ge[9], e0, e1, ehv, eprobe, a0;
    const unsigned ehs = WS_F + (tb * 4 + (w & 2)) * 55296 + 16384;
    {
        LANE_VARS();
        const unsigned wl = L + lane * 16;
        f32x4 acc[5];
        u32x4 act[10][2], wf[PC];
        unsigned wa = wl + L_WO, wb = wa + 51200, gb[5];
        rot_bases(gb, WS_O + (tb * 4 + w) * 20480, rotc, 4096);
        unsigned v0 = lane * 16, v1 = v0 + 1024, hv = c16 * 256;
        unsigned sv = g == 0 ? c16 * 8 : 0x80000000u, h2v = (g & 1) ? 0x80000000u : (g >> 1) * 256 + c16 * 16;
        PIN(v0); PIN(v1); PIN(hv);
        auto sub = [](int n) { return n < 60 ? (n % 3 == 2 ? 4 : n % 3) : 2 + (n & 1); };
        auto kst = [](int n) { return n < 60 ? n / 3 : (n - 60) / 2; };
        auto wac = [&](int n) { return (n < 50 ? wa : wb) + (n % 50) * 1024; };
        kstart<RC, 2, 2048>(act, rs, ws, v0, v1, gb,
                           [&] { return __builtin_amdgcn_raw_buffer_load_b32(rs, hv, WS_O + (tb * 4 + w) * 20480 + 16384, SC1) == EMPTY; }, [&] {
            bar();
            PIN(wa); PIN(wb); PIN(sv); PIN(h2v);
#pragma unroll
            for (int n = 0; n < PC; ++n) { wf[n] = lds16(wac(n)); SBAR(); }
            res_init(acc, xr0, xr1, xr2);
            wait_vm<0>();
        }, [] {});
        STAMP(8);
        const unsigned hb = WS_H + (tb * 4 + w) * 20480;
        unsigned t = 0;
        u64 bad = 0;
        static_for<0, 100>([&](auto nn) {
            constexpr int n = nn.v, j = n < 54 ? n / 6 : 0, i = n % 6, q0 = areq(j, RC, 10), q1 = areq(j + 1, RC, 10);
            if (n == 0) {   } if (n == 18) {   } if (n == 36) {   } if (n == 60) {   } if (n == 72) {   } if (n == 92) {   } if (n == 93) {   }
            if (n % 3 == 0) { lgkm<(PC - 3 < 15 ? PC - 3 : 15)>(); SBAR(); }
            mma16(acc[sub(n)], wf[n % PC], act[kst(n) >> 1][kst(n) & 1]);
            SBAR();
            if (n + PC < 100) wf[n % PC] = lds16(wac(n + PC));
            if (n == 59) asm volatile("s_mov_b64 exec, 1\n\tds_add_u32 %0, %1\n\ts_mov_b64 exec, -1" ::"v"(CN2(M_P1)), "v"(1u) : "memory");
            if (n < 54) {
                if (i < 4 && q0 + i / 2 < q1) act[(q0 + i / 2) % 10][i & 1] = aload<2, 2048>(rs, (i & 1) ? v1 : v0, gb, (q0 + i / 2) % 10);
                if (i == 3) { t = max(max(act[j + 1][1][0], act[j + 1][1][2]), max(act[j + 1][0][0], act[j + 1][0][2])); PIN(t); }
                if (i == 4) { bad = __ballot(t == EMPTY); asm volatile("" : "+s"(bad)); }
                if (i == 5 && __builtin_expect(bad != 0, 0))
                    refetch(act[j + 1][0], act[j + 1][1], ws + gb[(j + 1) / 2] + ((j + 1) & 1) * 2048, v0, v1);
            }
            if (n == 68) {
                xr0 = u32x4{keep(pk2(acc[0][0], acc[0][1])), pk2(acc[0][2], acc[0][3]), keep(pk2(acc[1][0], acc[1][1])), pk2(acc[1][2], acc[1][3])};
                bst16(rs, xr0, v0, hb + m * 1024);
            }
            if (n == 70) {
                xr2 = u32x2{keep(pk2(acc[4][0], acc[4][1])), pk2(acc[4][2], acc[4][3])};
                bst16(rs, rowpair(xr2, xr2), h2v, hb + 8192 + m * 512);
            }
            if (n == 74) t = *(volatile LAS unsigned*)(size_t)CN2(M_W0);
            if (n == 86) {
                if (__builtin_expect((unsigned)__builtin_amdgcn_readfirstlane(t) < 4u, 0)) cnt_spin(CN2(M_W0), 4);
                asm volatile("" ::: "memory");
            }
            if constexpr (n >= 88) {
                constexpr int k = n - 88;
                wd[k] = lds16(wl + (k % 7 < 5 ? 16384 + (k / 7) * 5120 + (k % 7) * 1024 : (k / 7) * 2048 + (k % 7 - 5) * 1024));
            }
            SBAR();
        });
        float sm = 0.f, sq = 0.f, sm2 = 0.f, sq2 = 0.f;
#pragma unroll
        for (int i = 0; i < 4; i += 2) { sm = dot2(xr0[i], ONES2, sm); sq = dot2(xr0[i], xr0[i], sq); sm2 = dot2(xr0[i + 1], ONES2, sm2); sq2 = dot2(xr0[i + 1], xr0[i + 1], sq2); }
        sm = dot2(xr2[0], ONES2, sm); sq = dot2(xr2[0], xr2[0], sq); sm2 = dot2(xr2[1], ONES2, sm2); sq2 = dot2(xr2[1], xr2[1], sq2);
        PIN(sm); PIN(sq); PIN(sm2); PIN(sq2);
        SBAR();
#pragma unroll
        for (int n = 0; n < 12; ++n) PIN(wd[n]);
        cnt_add(CN2(M_LDS));
        xr1 = u32x4{keep(pk2(acc[2][0], acc[2][1])), pk2(acc[2][2], acc[2][3]), keep(pk2(acc[3][0], acc[3][1])), pk2(acc[3][2], acc[3][3])};
        bst16(rs, xr1, v0, hb + (12 + m) * 1024);
#pragma unroll
        for (int i = 0; i < 4; i += 2) { sm = dot2(xr1[i], ONES2, sm); sq = dot2(xr1[i], xr1[i], sq); sm2 = dot2(xr1[i + 1], ONES2, sm2); sq2 = dot2(xr1[i + 1], xr1[i + 1], sq2); }
        sm = xsum16_32(sm + sm2); sq = xsum16_32(sq + sq2);
        bst8(rs, u32x2{keep(__float_as_uint(sm)), __float_as_uint(sq)}, sv, WS_ST + (tb * 4 + w) * 1024 + m * 128);
        SBAR();
        STAMP(9);
    }
    {
        f32x4 acc[2][7], c4[7];
        u32x4 act[12][2];
        u32x2 st[4];
        float mu[2], rp[2];
        const int t16 = (tb >> 1) * 8 + 2 * w;
        unsigned v0 = lane_id() * 16, v1 = v0 + 20480, sv = v0 >> 1, wa = L + v0, wb = wa + 16384 + 46080, gb[5];
        PIN(sv); PIN(wa); PIN(wb); PIN(v0); PIN(v1);
        d_bases(gb, WS_H + t16 * 20480, rotd, 4096);
        unsigned fs[7], wc, fw, f2v, ca;
        auto late = [&] {
        zero_acc(acc[0]); zero_acc(acc[1]);
        e_bases(ge, WS_F + tb * 221184 + kh * 1024, rote, 6144);
        {
            const unsigned fb = WS_F + t16 * 55296 + ct * 1024;
            fs[0] = fb; fs[1] = fb + 55296; fs[2] = fb + 16384 - ct * 512; fs[3] = fb + 24576; fs[4] = fb + 24576 + 55296; fs[5] = fb + 39936; fs[6] = fb + 39936 + 55296;
#pragma unroll
            for (int i = 0; i < 7; ++i) asm volatile("" : "+s"(fs[i]));
            const int lane = v0 >> 4, g = lane >> 4, c16 = lane & 15;
            f2v = (g & 1) * 55296 + (g >> 1) * 256 + c16 * 16; ca = L + L_MISC + g * 16;
            ehv = (g & 1) * 55296 + c16 * 512;
            wc = wa + 118784;
            PIN(f2v); PIN(ca); PIN(ehv); PIN(wc);
        }
        };
        unsigned hv = ((v0 >> 8) & 1) * 20480 + ((v0 >> 4) & 7) * 512;
        PIN(hv);
        kstart<RD, 4, 1024>(act, rs, ws, v0, v1, gb, [&] { return __builtin_amdgcn_raw_buffer_load_b32(rs, hv, WS_H + t16 * 20480 + 8192, SC1) == EMPTY; },
                            [&] { if (DRD < 0) wait_vm<0>(); else __builtin_amdgcn_s_sleep(DRD < 0 ? 0 : DRD); }, late);
        auto stload = [&] {
#pragma unroll
            for (int i = 0; i < 4; ++i) st[i] = __builtin_amdgcn_raw_buffer_load_b64(rs, sv + i * 512, WS_ST + t16 * 1024, SC1);
        };
        float sa[4];
        unsigned w1v = 0;
        auto dside = [&](int j, int i) {
            if ((j == 0 || j == 6) && i == 12) w1v = *(volatile LAS unsigned*)(size_t)(ca + 512 + (j ? M_W1 : M_W1A) * 4);
            if ((j == 1 || j == 7) && i == 12) {
                if (__builtin_expect((unsigned)__builtin_amdgcn_readfirstlane(w1v) < 4u, 0)) cnt_spin(CN2(j == 7 ? M_W1 : M_W1A), 4);
                asm volatile("" ::: "memory");
            }
            if (j == 5 && i == 4) stload();
            if (j == 9 && i == 6 && __builtin_expect(__any(max(max(st[0][0], st[1][0]), max(st[2][0], st[3][0])) == EMPTY), 0)) {
#pragma unroll 1
                for (int spin = 0; spin < (1 << 16); ++spin) {
                    asm volatile("" ::: "memory");
                    stload();
                    if (!__any(max(max(st[0][0], st[1][0]), max(st[2][0], st[3][0])) == EMPTY)) break;
                }
            }
            if ((j == 10 || j == 11) && (i == 4 || i == 8)) {
                const int k = (j - 10) * 2 + (i == 8);
                sa[k] = xsum16_32(__uint_as_float(st[k & 2][k & 1]) + __uint_as_float(st[(k & 2) + 1][k & 1])); PIN(sa[k]);
            }
            if (j == 12 && (i == 4 || i == 8)) {
                const int t = i == 8;
                mu[t] = sa[2 * t] * (-1.f / D);
                rp[t] = __builtin_amdgcn_rsqf(fmaxf(sa[2 * t + 1] * (1.f / D) - mu[t] * mu[t], 0.f) + LN_EPS) * LOG2E;
                PIN(mu[t]); PIN(rp[t]);
            }
        };
        STAMP(10);
        auto wad = [&](int n) {
            const int j = n < 56 ? n / 7 : n < 86 ? 8 + (n - 56) / 5 : n < 98 ? 14 + (n - 86) / 2 : 14 + (n - 98) / 3;
            const int s = n < 56 ? n % 7 : n < 86 ? (n - 56) % 5 : n < 98 ? (n - 86) % 2 : 2 + (n - 98) % 3;
            return n >= 116 ? wc + (n - 116) * 1024 : s >= 5 ? wa + j * 2048 + (s - 5) * 1024 : (j < 9 ? wa + 16384 + j * 5120 : wb + (j - 9) * 5120) + s * 1024; };
        kloop<0, 8, 20, 0, 140, 7, 5, RD, 4, 1024>(acc, act, wd, wad, rs, ws, v0, v1, gb, dside);
        kloop<8, 1, 20, 56, 140, 5, 5, RD, 4, 1024>(acc, act, wd, wad, rs, ws, v0, v1, gb, dside);
        asm volatile("s_mov_b64 exec, 1\n\tds_add_u32 %0, %1\n\ts_mov_b64 exec, -1" ::"v"(CN2(M_K8)), "v"(1u) : "memory");
        kloop<9, 5, 20, 61, 140, 5, 2, RD, 4, 1024>(acc, act, wd, wad, rs, ws, v0, v1, gb, dside);
        c4[0] = ldsf4(ca); c4[1] = ldsf4(ca + 64);
        SBAR();
        kloop<14, 6, 20, 86, 140, 2, 4, RD, 4, 1024>(acc, act, wd, wad, rs, ws, v0, v1, gb, dside);
        STAMP(11);
        u32x2 lo, hi;
        auto silu = [&](const f32x4& a, const f32x4& c, int t, u32x2& r, auto side) {
            f32x4 y = (a + mu[t] * c) * rp[t], e;
            PIN(y); SBAR();
#pragma unroll
            for (int i = 0; i < 4; ++i) { side(i); SBAR(); e[i] = EX2(-y[i]); PIN(e); SBAR(); }
            e = e * LOG2E + LOG2E; PIN(e); SBAR();
#pragma unroll
            for (int i = 0; i < 4; ++i) { side(4 + i); SBAR(); e[i] = __builtin_amdgcn_rcpf(e[i]); PIN(e); SBAR(); }
            e *= y;
            r = u32x2{keep(pk2(e[0], e[1])), pk2(e[2], e[3])};
            PIN(r); SBAR();
        };
        auto rest = [&](int q) {
            const int n = 98 + (q >> 1), j = n < 116 ? 14 + (n - 98) / 3 : 8 + (n - 116) / 2, s = n < 116 ? 2 + (n - 98) % 3 : 5 + (n - 116) % 2;
            mma16(acc[q & 1][s], wd[n % 12], act[j % 12][q & 1]);
            SBAR();
            if ((q & 1) && n + 12 < 140) wd[n % 12] = lds16(wad(n + 12));
            if (q >= 24 && q < 74 && q % 10 == 4) c4[q == 24 ? 6 : q / 10 - 1] = ldsf4(ca + (q == 24 ? 6 : q / 10 - 1) * 64);
        };
        auto early = [&](int q) {
            if (q == 0) eprobe = __builtin_amdgcn_raw_buffer_load_b32(rs, ehv, ehs, SC1);
            ae[q >> 1][q & 1] = aload<3, 2048>(rs, (q & 1) ? e1 : e0, ge, q >> 1);
        };
#pragma unroll
        for (int q = 0; q < 8; ++q) { rest(q); SBAR(); }
#pragma unroll
        for (int b = 0; b < 10; ++b) {
            const int ut = b < 4 ? b >> 1 : b < 6 ? b & 1 : (b - 6) >> 1, ui = b < 4 ? b & 1 : b < 6 ? 2 : 3 + (b & 1), us = ui < 2 ? ui : ui == 2 ? 6 : ui - 1;
            silu(acc[ut][ui], c4[us], ut, (b & 1) ? hi : lo, [&](int i) { if (8 + b * 8 + i < 84) rest(8 + b * 8 + i); });
            if (b == 5) bst16(rs, rowpair(lo, hi), f2v, fs[2]);
            else if (b & 1) bst16(rs, u32x4{lo[0], lo[1], hi[0], hi[1]}, b < 5 ? v0 : fw, fs[b >> 1]);
            if (b == 1) cnt_add(CN2(M_T4));
            if (b == 7) cnt_add(CN2(M_T5));
            if (b == 8) w1v = *(volatile LAS unsigned*)(size_t)(ca + 512 + (G_RDY + 1) * 4);
            if (b == 1) {   } if (b == 3) {   } if (b == 5) {   } if (b == 7) {   } if (b == 9) {   }
            if (b == 5) {
                PIN(v0);
                fw = ct < 15 ? v0 : 0x80000000u; e0 = v0 + w * 55296; e1 = v0 + (w ^ 1) * 55296; a0 = L + v0 + kh * 5120;
                PIN(fw); PIN(e0); PIN(e1); PIN(a0);
            }
            SBAR();
        }
        if (__builtin_expect((unsigned)__builtin_amdgcn_readfirstlane(w1v) < 4u, 0)) cnt_spin(CN2(G_RDY + 1), 4);
        asm volatile("" ::: "memory");
        mma_done();
#pragma unroll
        for (int n = 0; n < PE; ++n) we[n] = lds16(a0 + (n / 5) * 10240 + (n % 5) * 1024);
#pragma unroll
        for (int t = 0; t < 2; ++t) {
            u32x4 r;
#pragma unroll
            for (int u = 0; u < 2; ++u) {
                const f32x4 y = (acc[t][5 + u] + mu[t] * c4[4 + u]) * rp[t];
                f32x4 e = f32x4{EX2(-y[0]), EX2(-y[1]), EX2(-y[2]), EX2(-y[3])} * LOG2E + LOG2E;
                e = f32x4{__builtin_amdgcn_rcpf(e[0]), __builtin_amdgcn_rcpf(e[1]), __builtin_amdgcn_rcpf(e[2]), __builtin_amdgcn_rcpf(e[3])} * y;
                r[2 * u] = keep(pk2(e[0], e[1])); r[2 * u + 1] = pk2(e[2], e[3]);
            }
            bst16(rs, r, fw, fs[5 + t]);
        }
        SBAR();
#pragma unroll
        for (int q = 0; q < 2 * FIRSTFILL; ++q) { early(q); SBAR(); }
        STAMP(12);
    }
    {
        f32x4 acc[2][5];
        unsigned a1 = a0 + 61440, a2 = a0 + 122880;
        PIN(a1); PIN(a2);
        res_init(acc[0], xr0, xr1, xr2); zero_acc(acc[1]);
        if (__any(eprobe == EMPTY))
            kstart<RE, 3, 2048>(ae, rs, ws, e0, e1, ge, [&] { return __builtin_amdgcn_raw_buffer_load_b32(rs, ehv, ehs, SC1) == EMPTY; }, [] {}, [] {});
        if (__builtin_expect(missing(ae[0][0], ae[0][1]), 0)) refetch(ae[0][0], ae[0][1], ws + ge[0], e0, e1);
        SBAR();
        STAMP(13);
        auto wae = [&](int n) { const int q = n / 5 % 15; return (q < 6 ? a0 : q < 12 ? a1 : a2) + (q % 6) * 10240 + (n % 5) * 1024; };
        unsigned cg = CN2(0), gv = 0;
        PIN(cg);
        kloop<0, 27, 27, 0, 135, 5, 5, RE, 3, 2048>(acc, ae, we, wae, rs, ws, e0, e1, ge, [&](int j, int i) {
            if (j % 3 == 1 && i == 8 && j / 3 + 2 < 9) gv = *(volatile LAS unsigned*)(size_t)(cg + (G_RDY + j / 3 + 2) * 4);
            if (j % 3 == 2 && i == 8 && j / 3 + 2 < 9) {
                if (__builtin_expect((unsigned)__builtin_amdgcn_readfirstlane(gv) < 4u, 0)) cnt_spin(cg + (G_RDY + j / 3 + 2) * 4, 4);
                asm volatile("" ::: "memory");
            }
            if (j % 3 == 2 && i == 4 && j / 3 < 5)
                asm volatile("s_mov_b64 exec, 1\n\tds_add_u32 %0, %1 offset:%2\n\ts_mov_b64 exec, -1" ::"v"(cg), "v"(1u), "i"((G_USED + j / 3) * 4) : "memory");
            if (j == 24 && i == 8) gv = *(volatile LAS unsigned*)(size_t)(cg + (G_USED + 4) * 4);
        });
        if (__builtin_expect((unsigned)__builtin_amdgcn_readfirstlane(gv) < 4u, 0)) cnt_spin(cg + (G_USED + 4) * 4, 4);
        mma_done();
        LANE_VARS();
        const unsigned wl = L + lane * 16;
        send_half(acc[1], wl + 122880 + (w ^ 1) * 5120);
        bar();
        recv_half(acc[0], wl + 122880 + w * 5120);
        STAMP(14);
        res_pack(xr0, xr1, xr2, acc[0]);
        char* ob = out + (size_t)(tb * 64 + w * 16 + c16) * 1280 + m * 160;
        asm volatile("global_store_dwordx4 %0, %1, off sc0 sc1\n\tglobal_store_dwordx4 %0, %2, off offset:64 sc0 sc1\n\t"
                     "global_store_dwordx2 %3, %4, off offset:128 sc0 sc1\n\ts_nop 1" ::"v"(ob + g * 16), "v"(xr0), "v"(xr1), "v"(ob + g * 8), "v"(xr2) : "memory");
    }
    asm volatile(".Lpf_end1:");
    STAMP_OUT(0);
    __builtin_amdgcn_endpgm();
}

extern "C" void kernel_launch(void* const* d_in, const int* in_sizes, int n_in, void* d_out, int out_size, void* d_ws, size_t ws_size,
                              hipStream_t stream) {
    (void)hipFuncSetAttribute((const void*)k_block, hipFuncAttributeMaxDynamicSharedMemorySize, LDS_BYTES);
    k_block<<<256, 512, LDS_BYTES, stream>>>((const char*)d_in[0], (const char*)d_in[1], (const char*)d_in[2], (const char*)d_in[3],
                                             (const char*)d_in[4], (const char*)d_in[6], (const char*)d_in[7], (char*)d_ws, (char*)d_out);
}
```

```cpp
#include <hip/hip_runtime.h>
#include <hip/hip_bf16.h>
#include <stdint.h>

typedef __attribute__((ext_vector_type(8))) __bf16 bf16x8;
typedef __attribute__((ext_vector_type(2))) __bf16 bf16x2;
typedef __attribute__((ext_vector_type(4))) short s16x4;
typedef __attribute__((ext_vector_type(4))) float f32x4;
typedef __attribute__((ext_vector_type(2))) float f32x2;
typedef __attribute__((ext_vector_type(16))) float f32x16;
typedef __attribute__((ext_vector_type(4))) unsigned u32x4;
typedef __attribute__((ext_vector_type(2))) unsigned u32x2;
typedef unsigned long long u64;
#define LAS __attribute__((address_space(3)))
#define GAS __attribute__((address_space(1)))
#define DI __device__ __forceinline__

constexpr int D = 640;
constexpr float LN_EPS = 1e-5f;
constexpr float LOG2E = 1.4426950408889634f;
constexpr float QSCALE = 0.22360679774997896f * LOG2E;

constexpr int KV_TILE = 2688;
constexpr int VS = 704;
constexpr unsigned WS_FL = 0;
constexpr unsigned FL_KV = 32768, FL_PR = 2 * 32768, FL_MK = 3 * 32768;
constexpr unsigned WS_KV = 4 * 32768;
constexpr unsigned WS_WO = WS_KV + 64 * KV_TILE;
constexpr unsigned WS_W1 = WS_WO + 8 * 20 * 5 * 1024;
constexpr unsigned WS_W2 = WS_W1 + 16 * 20 * 7 * 1024;
constexpr unsigned WS_CS = WS_W2 + 8 * 54 * 5 * 1024;
constexpr unsigned WS_O  = WS_CS + 20 * 1792 * 4;
constexpr unsigned WS_H  = WS_O + 128 * 20 * 1024;
constexpr unsigned WS_F  = WS_H + 128 * 20 * 1024;
constexpr unsigned WS_ST = WS_F + 128 * 54 * 1024;
constexpr unsigned WS_END = WS_ST + 2048 * 64;
constexpr unsigned EMPTY = 0xffffffffu;
static_assert((WS_END - WS_O) == 256 * 48640, "fill share per workgroup");

constexpr int CHUNK = 16 * KV_TILE;
constexpr int L_X = 0, L_WQ2 = 43008, L_WKV = 83968, L_WQ1 = 122880,
               L_CSX = 111104, L_QX = 152576, L_MISC = 162816;
constexpr int L_WO = CHUNK, L_CNT = 110592, L_JUNK = 113664, L_JUNK2 = 153600, LDS_BYTES = 163840;

constexpr int FIRSTFILL = 4;
constexpr int RC = 6, RD = 8, RE = 8;
constexpr int PC = 24, PE = 10;
constexpr int THROT = 3, NEARLY = 12;

DI void glds(unsigned voff, const void* sbase, unsigned lds) {
    asm volatile("s_mov_b32 m0, %2\n\ts_nop 0\n\tglobal_load_lds_dwordx4 %0, %1" ::"v"(voff), "s"(sbase), "s"(lds) : "memory");
}
DI void glds_dev(unsigned voff, const void* sbase, unsigned lds) {
    asm volatile("s_mov_b32 m0, %2\n\ts_nop 0\n\tglobal_load_lds_dwordx4 %0, %1 sc1" ::"v"(voff), "s"(sbase), "s"(lds) : "memory");
}
DI void touch(unsigned voff, const void* sbase, unsigned lds, u64 mask) {
    asm volatile("s_mov_b32 m0, %2\n\ts_mov_b64 exec, %3\n\tglobal_load_lds_dword %0, %1\n\ts_mov_b64 exec, -1" ::"v"(voff), "s"(sbase), "s"(lds), "s"(mask) : "memory");
}
template <int N> DI void wait_vm() { asm volatile("s_waitcnt vmcnt(%0)" ::"i"(N) : "memory"); }
DI void bar() { asm volatile("s_waitcnt lgkmcnt(0)\n\ts_barrier" ::: "memory"); }
DI void bar_raw() { asm volatile("s_barrier" ::: "memory"); }
#define PIN(x) asm volatile("" : "+v"(x))
#define SBAR() __builtin_amdgcn_sched_barrier(0)
DI unsigned pk2(float a, float b) { bf16x2 v = {(__bf16)a, (__bf16)b}; return __builtin_bit_cast(unsigned, v); }
DI float bflo(unsigned u) { return __uint_as_float(u << 16); }
DI float bfhi(unsigned u) { return __uint_as_float(u & 0xffff0000u); }
DI f32x4 mfma16(u32x4 a, u32x4 b, f32x4 c) {
    return __builtin_amdgcn_mfma_f32_16x16x32_bf16(__builtin_bit_cast(bf16x8, a), __builtin_bit_cast(bf16x8, b), c, 0, 0, 0);
}
DI f32x16 mfma32(u32x4 a, u32x4 b, f32x16 c) {
    return __builtin_amdgcn_mfma_f32_32x32x16_bf16(__builtin_bit_cast(bf16x8, a), __builtin_bit_cast(bf16x8, b), c, 0, 0, 0);
}
DI f32x16 mfma32k8(u32x2 a, u32x2 b, f32x16 c) {
    return __builtin_amdgcn_mfma_f32_32x32x8bf16_1k(__builtin_bit_cast(s16x4, a), __builtin_bit_cast(s16x4, b), c, 0, 0, 0);
}
DI u32x4 lds16(unsigned a) { return *(const LAS u32x4*)(size_t)a; }
DI u32x2 lds8(unsigned a) { return *(const LAS u32x2*)(size_t)a; }
DI unsigned lds4(unsigned a) { return *(const LAS unsigned*)(size_t)a; }
DI f32x4 ldsf4(unsigned a) { return *(const LAS f32x4*)(size_t)a; }
DI u32x2 ldstr(unsigned a) { return __builtin_bit_cast(u32x2, __builtin_amdgcn_ds_read_tr16_b64_v4i16((LAS s16x4*)(size_t)a)); }
DI u32x4 ldstr2(unsigned a0, unsigned a1) { u32x2 lo = ldstr(a0), hi = ldstr(a1); return u32x4{lo[0], lo[1], hi[0], hi[1]}; }
DI float dot2(unsigned a, unsigned b, float c) {
    return __builtin_amdgcn_fdot2_f32_bf16(__builtin_bit_cast(bf16x2, a), __builtin_bit_cast(bf16x2, b), c, false);
}
DI float xsum16_32(float v) {
    const auto a = __builtin_amdgcn_permlane16_swap(__float_as_uint(v), __float_as_uint(v), false, false);
    v = __uint_as_float(a[0]) + __uint_as_float(a[1]);
    const auto b = __builtin_amdgcn_permlane32_swap(__float_as_uint(v), __float_as_uint(v), false, false);
    return __uint_as_float(b[0]) + __uint_as_float(b[1]);
}
constexpr unsigned ONES2 = 0x3f803f80u;
#define EX2(v) __builtin_amdgcn_exp2f(v)
typedef __amdgpu_buffer_rsrc_t rsrc_t;
constexpr int SC1 = 16;
DI u32x4 bld16(rsrc_t r, unsigned v, unsigned s) { return __builtin_amdgcn_raw_buffer_load_b128(r, v, s, SC1); }
DI void bst16(rsrc_t r, u32x4 d, unsigned v, unsigned s) {
    __builtin_amdgcn_raw_buffer_store_b128(d, r, v, s, SC1);
    asm volatile("s_nop 1" ::"v"(d) : "memory");
}
DI void bst8(rsrc_t r, u32x2 d, unsigned v, unsigned s) { __builtin_amdgcn_raw_buffer_store_b64(d, r, v, s, SC1); }

DI void set_flag(char* ws, unsigned off, u64 tag) { __hip_atomic_store((GAS u64*)(ws + off), tag, __ATOMIC_RELAXED, __HIP_MEMORY_SCOPE_AGENT); }
DI void poll(char* ws, unsigned arr, int index, u64 tag) {
    const GAS u64* p = (const GAS u64*)(ws + arr + index * 128);
    bool ok = index < 0;
#pragma unroll 1
    for (int spin = 0; spin < (1 << 18); ++spin) {
        if (!ok) ok = __hip_atomic_load(p, __ATOMIC_RELAXED, __HIP_MEMORY_SCOPE_AGENT) == tag;
        if (__all(ok)) break;
        __builtin_amdgcn_s_sleep(1);
    }
    asm volatile("" ::: "memory");
}
DI void copy_dev(const char* src, unsigned lds, int n, int hw, int lane) {
#pragma unroll 1
    for (int p = hw; p < n; p += 4) glds_dev(lane * 16, src + p * 1024, lds + p * 1024);
}

DI void cnt_add(unsigned a) {
    asm volatile("s_waitcnt lgkmcnt(0)\n\ts_mov_b64 exec, 1\n\tds_add_u32 %0, %1\n\ts_mov_b64 exec, -1" ::"v"(a), "v"(1u) : "memory");
}
DI void cnt_add_now(unsigned a) {
    asm volatile("s_mov_b64 exec, 1\n\tds_add_u32 %0, %1\n\ts_mov_b64 exec, -1" ::"v"(a), "v"(1u) : "memory");
}
DI unsigned cnt_arrive(unsigned a) {
    unsigned r;
    asm volatile("s_waitcnt lgkmcnt(0)\n\ts_mov_b64 exec, 1\n\tds_add_rtn_u32 %0, %1, %2\n\ts_mov_b64 exec, -1\n\ts_waitcnt lgkmcnt(0)"
                 : "=&v"(r) : "v"(a), "v"(1u) : "memory");
    return __builtin_amdgcn_readfirstlane(r);
}
DI void cnt_spin(unsigned a, unsigned n) {
#pragma unroll 1
    while ((unsigned)__builtin_amdgcn_readfirstlane(*(volatile LAS unsigned*)(size_t)a) < n) {}
    asm volatile("" ::: "memory");
}
template <int NAP = 1> DI void cnt_wait(unsigned a, unsigned n) {
#pragma unroll 1
    while ((unsigned)__builtin_amdgcn_readfirstlane(*(volatile LAS unsigned*)(size_t)a) < n) __builtin_amdgcn_s_sleep(NAP);
    asm volatile("" ::: "memory");
}
DI unsigned keep(unsigned v) { return min(v, EMPTY - 1); }
DI bool missing(const u32x4& a, const u32x4& b) { return __any(max(max(a[0], a[2]), max(b[0], b[2])) == EMPTY); }
DI void refetch(u32x4& a, u32x4& b, const char* base, unsigned v0, unsigned v1) {
#pragma unroll 1
    for (int spin = 0; spin < (1 << 16); ++spin) {
        asm volatile("global_load_dwordx4 %0, %2, %4 sc1\n\tglobal_load_dwordx4 %1, %3, %4 sc1\n\ts_waitcnt vmcnt(0)"
                     : "+v"(a), "+v"(b) : "v"(v0), "v"(v1), "s"(base) : "memory");
        if (!missing(a, b)) break;
    }
}
DI u32x4 rowpair(const u32x2& a, const u32x2& b) {
    const auto s0 = __builtin_amdgcn_permlane16_swap(a[0], b[0], false, false), s1 = __builtin_amdgcn_permlane16_swap(a[1], b[1], false, false);
    return u32x4{s0[0], s1[0], s0[1], s1[1]};
}
DI int lane_id() {
    int l;
    asm volatile("v_mbcnt_lo_u32_b32 %0, -1, 0\n\tv_mbcnt_hi_u32_b32 %0, -1, %0" : "=v"(l));
    return l;
}
#define LANE_VARS() const int lane = lane_id(), g = lane >> 4, c16 = lane & 15; (void)g; (void)c16
DI float halfmax(float m) {
    auto r = __builtin_amdgcn_permlane32_swap(__float_as_uint(m), __float_as_uint(m), false, false);
    return fmaxf(__uint_as_float(r[0]), __uint_as_float(r[1]));
}
DI float upper(float v) {
    auto r = __builtin_amdgcn_permlane32_swap(__float_as_uint(v), __float_as_uint(v), false, false);
    return __uint_as_float(r[1]);
}
DI float max16(const f32x16& s) {
    float a = fmaxf(fmaxf(s[0], s[1]), s[2]), b = fmaxf(fmaxf(s[3], s[4]), s[5]);
    a = fmaxf(fmaxf(a, s[6]), s[7]); b = fmaxf(fmaxf(b, s[8]), s[9]);
    a = fmaxf(fmaxf(a, s[10]), s[11]); b = fmaxf(fmaxf(b, s[12]), s[13]);
    a = fmaxf(fmaxf(a, s[14]), s[15]);
    return fmaxf(a, b);
}
DI float amax3(float m, float a, float b) {
    float r;
    asm("v_maximum3_f32 %0, %1, |%2|, |%3|" : "=v"(r) : "v"(m), "v"(a), "v"(b));
    return r;
}
DI f32x16 attn_exact(const char* kv, u32x4 q1, unsigned q2lo, int lane, int vrow) {
    const int r32 = lane & 31, h = lane >> 5;
    const f32x16 zero16 = {0, 0, 0, 0, 0, 0, 0, 0, 0, 0, 0, 0, 0, 0, 0, 0};
    const u32x2 q2 = {q2lo, 0u};
    float m = -INFINITY;
    f32x16 o = zero16;
#pragma unroll 1
    for (int t = 0; t < 64; ++t) {
        const GAS char* tp = (const GAS char*)kv + t * KV_TILE;
        const u32x4 fk1 = *(const GAS u32x4*)(tp + lane * 16);
        const u32x2 fk2 = {*(const GAS unsigned*)(tp + 1024 + r32 * 8 + h * 4), 0u};
        const u32x4 fv0 = *(const GAS u32x4*)(tp + 1280 + (h * 22 + vrow) * 16), fv1 = *(const GAS u32x4*)(tp + 1280 + VS + (h * 22 + vrow) * 16);
        f32x16 s = mfma32(fk1, q1, mfma32k8(fk2, q2, zero16));
        const float mn = fmaxf(m, halfmax(max16(s))), al = EX2(m - mn);
        m = mn;
        u32x4 p0, p1;
#pragma unroll
        for (int i = 0; i < 4; ++i) { p0[i] = pk2(EX2(s[2 * i] - m), EX2(s[2 * i + 1] - m)); p1[i] = pk2(EX2(s[8 + 2 * i] - m), EX2(s[9 + 2 * i] - m)); }
#pragma unroll
        for (int i = 0; i < 16; ++i) o[i] *= al;
        o = mfma32(fv1, p1, mfma32(fv0, p0, o));
    }
    return o;
}

DI void mma16(f32x4& c, const u32x4& a, const u32x4& b) {
    asm volatile("v_mfma_f32_16x16x32_bf16 %0, %1, %2, %0" : "+v"(c) : "v"(a), "v"(b));
}
DI void mma16z(f32x4& c, const u32x4& a, const u32x4& b) { asm volatile("v_mfma_f32_16x16x32_bf16 %0, %1, %2, 0" : "=&v"(c) : "v"(a), "v"(b)); }
DI void mma_done() { SBAR(); asm volatile("s_nop 7\n\ts_nop 1"); SBAR(); }
template <int NS> DI void zero_acc(f32x4 (&a)[NS]) {
#pragma unroll
    for (int s = 0; s < NS; ++s) { a[s] = f32x4{0, 0, 0, 0}; PIN(a[s]); }
}
template <int GS, int ASTR, int NG> DI u32x4 aload(rsrc_t rs, unsigned& v, const unsigned (&gb)[NG], int j) {
    const int o = (j % GS) * ASTR;
    PIN(v);
    return bld16(rs, v + (o & 4095), gb[j / GS] + (o & ~4095));
}
constexpr int areq(int j, int PD, int KT) {
    const int a = FIRSTFILL + 2 * j, b = PD + (j > 0 ? j - 1 : 0), c = a < b ? a : b;
    return c < KT ? c : KT;
}
template <int N> struct Int { static constexpr int v = N; };
template <int A, int B, class Fn> DI void static_for(Fn fn) { if constexpr (A < B) { fn(Int<A>{}); static_for<A + 1, B>(fn); } }
template <int N> DI void lgkm() { __builtin_amdgcn_s_waitcnt(0xC07F | (N << 8)); }
template <int K0, int NK, int KT, int F0, int FT, int NS, int NSN, int PD, int GS, int ASTR, int NA, int RING, int POOL, int NG, class WAddr, class Side>
DI void kloop(f32x4 (&acc)[2][NA], u32x4 (&act)[RING][2], u32x4 (&wf)[POOL], WAddr waddr, rsrc_t rs, const char* ws, unsigned& v0,
              unsigned& v1, const unsigned (&gb)[NG], Side side) {
    if constexpr (NK > 0) {
        constexpr int j = K0, q0 = areq(j, PD, KT), q1 = areq(j + 1, PD, KT), jc = j + 1 < KT ? j + 1 : 0, nn = NK > 1 ? NS : NSN;
        constexpr int issued = F0 + NS + POOL < FT ? F0 + NS + POOL : FT, need = F0 + NS + nn < FT ? F0 + NS + nn : FT;
        unsigned t = 0;
        u64 bad = 0;
        if (j == 0) { lgkm<POOL - NS>(); SBAR(); }
#pragma unroll
        for (int i = 0; i < 2 * NS; ++i) {
            const int n = F0 + (i >> 1);
            mma16(acc[i & 1][i >> 1], wf[n % POOL], act[j % RING][i & 1]);
            SBAR();
            if ((i & 1) && n + POOL < FT) wf[n % POOL] = lds16(waddr(n + POOL));
            if (!(i & 1) && q0 + i / 4 < q1) act[(q0 + i / 4) % RING][i / 2 & 1] = aload<GS, ASTR>(rs, (i & 2) ? v1 : v0, gb, q0 + i / 4);
            if (jc && !(j & 1)) {
                constexpr int jd = j + 2 < KT ? j + 2 : jc;
                if (i == (NS > 2 ? 2 * NS - 6 : 0)) {
                    t = max(max(act[jd % RING][1][0], act[jd % RING][1][2]), act[jd % RING][0][0]);
                    t = max(max(t, act[jd % RING][0][2]), act[jc % RING][1][0]); PIN(t);
                }
                if (i == (NS > 2 ? 2 * NS - 5 : 0)) {
                    t = max(max(t, act[jc % RING][1][2]), act[jc % RING][0][0]);
                    t = max(t, act[jc % RING][0][2]); PIN(t);
                }
                if (i == (NS > 2 ? 2 * NS - 4 : 1)) { bad = __ballot(t == EMPTY); asm volatile("" : "+s"(bad)); }
                if (i == 2 * NS - 2 && __builtin_expect(bad != 0, 0)) {
                    refetch(act[jc % RING][0], act[jc % RING][1], ws + gb[jc / GS] + (jc % GS) * ASTR, v0, v1);
                    refetch(act[jd % RING][0], act[jd % RING][1], ws + gb[jd / GS] + (jd % GS) * ASTR, v0, v1);
                }
            }
            side(j, i);
            if (i == 2 * NS - 1 && need > F0 + NS) { SBAR(); lgkm<issued - need>(); }
            SBAR();
        }
        kloop<K0 + 1, NK - 1, KT, F0 + NS, FT, NS, NSN, PD, GS, ASTR>(acc, act, wf, waddr, rs, ws, v0, v1, gb, side);
    }
}
template <int PD, int GS, int ASTR, int RING, int NG, class Probe, class Before, class After>
DI void kstart(u32x4 (&act)[RING][2], rsrc_t rs, const char* ws, unsigned& v0, unsigned& v1, const unsigned (&gb)[NG], Probe probe,
               Before before, After after) {
    before();
    SBAR();
#pragma unroll 1
    for (int spin = 0; spin < (1 << 16); ++spin) {
        const bool miss = probe();
        SBAR();
#pragma unroll
        for (int k = 0; k < FIRSTFILL; ++k) {
            act[k][0] = aload<GS, ASTR>(rs, v0, gb, k); SBAR();
            act[k][1] = aload<GS, ASTR>(rs, v1, gb, k); SBAR();
        }
        if (spin == 0) after();
        if (!__any(miss)) break;
        asm volatile("" ::: "memory");
    }
    SBAR();
    if (__builtin_expect(missing(act[0][0], act[0][1]), 0)) refetch(act[0][0], act[0][1], ws + gb[0], v0, v1);
    SBAR();
}
DI void e_bases(unsigned (&gb)[9], unsigned base, int m, int stride) {
    const int m3 = m % 3;
#pragma unroll
    for (int i = 0; i < 9; ++i) {
        const int q = i < 4 ? (i + m) & 3 : i < 6 ? 4 + ((i + (m >> 2)) & 1) : 6 + (i - 6 + m3 >= 3 ? i - 9 + m3 : i - 6 + m3);
        gb[i] = base + q * stride;
        asm volatile("" : "+s"(gb[i]));
    }
}
DI void d_bases(unsigned (&gb)[5], unsigned base, int ct, int stride) {
    const int c3 = ct % 3;
#pragma unroll
    for (int i = 0; i < 5; ++i) {
        gb[i] = base + (i < 3 ? (i + c3 >= 3 ? i + c3 - 3 : i + c3) : 3 + ((i + 1 + ct) & 1)) * stride;
        asm volatile("" : "+s"(gb[i]));
    }
}
template <int NG> DI void rot_bases(unsigned (&gb)[NG], unsigned base, int rot, int stride) {
#pragma unroll
    for (int i = 0; i < NG; ++i) { const int q = i + rot; gb[i] = base + (q >= NG ? q - NG : q) * stride; asm volatile("" : "+s"(gb[i])); }
}
DI void send_half(const f32x4 (&a)[5], unsigned dst) {
#pragma unroll
    for (int s = 0; s < 5; ++s) *(LAS f32x4*)(size_t)(dst + s * 1024) = a[s];
}
DI void recv_half(f32x4 (&a)[5], unsigned src) {
#pragma unroll
    for (int s = 0; s < 5; ++s) a[s] += ldsf4(src + s * 1024);
}
DI void ldsw16(unsigned a, const u32x4& v) { *(LAS u32x4*)(size_t)a = v; }
DI f32x4 unpk(unsigned r0, unsigned r1) { return f32x4{bflo(r0), bfhi(r0), bflo(r1), bfhi(r1)}; }
DI void res_init(f32x4 (&a)[5], const u32x4& x0, const u32x4& x1, const u32x2& x2) {
    a[0] = unpk(x0[0], x0[1]); a[1] = unpk(x0[2], x0[3]); a[2] = unpk(x1[0], x1[1]); a[3] = unpk(x1[2], x1[3]); a[4] = unpk(x2[0], x2[1]);
#pragma unroll
    for (int s = 0; s < 5; ++s) PIN(a[s]);
}
DI void res_pack(u32x4& x0, u32x4& x1, u32x2& x2, const f32x4 (&a)[5]) {
    x0 = u32x4{pk2(a[0][0], a[0][1]), pk2(a[0][2], a[0][3]), pk2(a[1][0], a[1][1]), pk2(a[1][2], a[1][3])};
    x1 = u32x4{pk2(a[2][0], a[2][1]), pk2(a[2][2], a[2][3]), pk2(a[3][0], a[3][1]), pk2(a[3][2], a[3][3])};
    x2 = u32x2{pk2(a[4][0], a[4][1]), pk2(a[4][2], a[4][3])};
}
enum { C_WKV, C_KFUSED, C_X, C_XUSED, C_WQ, C_CSP = C_WQ + 5, C_CS, C_KVST, C_R1, C_XCH, C_NUM = C_XCH + 2 };
enum { E_C1, E_C2, E_C3, E_MK, P_NONE, P_C0, P_C1, P_C2, E_C0, H_PREP, H_FILL, M_LDS, M_P1, M_W0, M_W1A, M_W1, M_K8, M_T4, M_T5, G_RDY, G_USED = G_RDY + 9, M_NUM = G_USED + 5 };

__global__ __launch_bounds__(512, 2) void k_block(const char* __restrict__ x, const char* __restrict__ wq, const char* __restrict__ wk,
                                                  const char* __restrict__ wv, const char* __restrict__ wo, const char* __restrict__ w1,
                                                  const char* __restrict__ w2, char* ws, char* __restrict__ out) {
    extern __shared__ __attribute__((aligned(16))) char smem[];
    const unsigned L = (unsigned)(size_t)(LAS char*)smem;
    const int w = __builtin_amdgcn_readfirstlane(threadIdx.x >> 6);
    const int xg = blockIdx.x & 7, idx = blockIdx.x >> 3;
    const int tb = xg * 4 + (idx >> 3), m = idx & 7, cid = tb * 8 + m;
    const int ct = (m >> 2) * 8 + (tb & 1) * 4 + (m & 3);
    const rsrc_t rs = __builtin_amdgcn_make_buffer_rsrc(ws, 0, WS_END, 0x00020000);
    const u32x4 ones = {ONES2, ONES2, ONES2, ONES2};
    const f32x4 z4 = {0, 0, 0, 0};

#define LOAD_TAG() u64 tag; asm volatile("global_load_dwordx2 %0, %1, %2 sc1" : "=v"(tag) : "v"(0u), "s"(ws + WS_FL) : "memory")
#define MAKE_TAG() do { PIN(tag); \
        tag = ((u64)(unsigned)__builtin_amdgcn_readfirstlane(tag >> 32) << 32) | (unsigned)__builtin_amdgcn_readfirstlane((unsigned)tag); \
        tag = (tag ^ 0x9E3779B97F4A7C15ull) * 0xBF58476D1CE4E5B9ull + 0x632BE59BD9B4E019ull; asm volatile("" : "+s"(tag)); } while (0)

#define CNT(i) (L + L_CNT + (i) * 4)
#define CN2(i) (L + L_MISC + 512 + (i) * 4)
#define WQ_AT(r) ((r) < 2 ? L_WQ1 + (r) * 20480 : L_WQ2 + (r) % 3 * 20480)
#define WQ_ADDR() const unsigned wrow = L + (8 * g + (c16 >> 2)) * 160 + (lane & 3) * 8, wo0 = wrow + (g & 1) * 640, wo1 = wrow + ((g & 1) ^ 1) * 640
    const int rq = tb & 3;
    const int rotc = (m + tb) % 5, rotd = ct + (tb >> 1), rote = m + tb;

#define WQ_SRC() unsigned vw[5]; _Pragma("unroll") for (int i = 0; i < 5; ++i) { \
        const int ci = i * 64 + lane, pos = (ci * 6554) >> 16, ch = ci - pos * 10, k = pos ^ (((pos >> 3) & 1) << 2); \
        vw[i] = k * 1280 + ch * 16; PIN(vw[i]); } \
    auto wqload = [&](int r) {     \
        _Pragma("unroll") for (int i = 0; i < 5; ++i) glds(vw[i], wq + m * 160 + hw * 40960 + (r + rq >= 5 ? r + rq - 5 : r + rq) * 163840, L + WQ_AT(r) + hw * 5120 + i * 1024); }
    if (w < 4) {
        const int lane = lane_id();
        {
            const int wi = idx * 4 + w;
            touch(lane * 128, x + xg * 327680 + wi * 2560, L + L_JUNK, 0xfffffull);
            touch(lane * 128, ((wi & 1) ? wv : wk) + min(wi >> 1, 49) * 512, L + L_JUNK, 0xfull);
            const char* pc; int l1, l2;
            asm volatile("s_getpc_b64 %0\n.Lpf_pc:\n\ts_mov_b32 %1, .Lpf_end1-.Lpf_pc\n\ts_mov_b32 %2, .Lpf_end2-.Lpf_pc" : "=s"(pc), "=s"(l1), "=s"(l2));
            touch(lane * 128, pc + max(min(wi * 512, max(l1, l2) - 512), 0), L + L_JUNK, 0xfull);
        }
        if (w == 0 && lane < C_NUM) *(LAS unsigned*)(size_t)CNT(lane) = 0u;
        bar();
    } else {
        const int hw = w - 4;
        LANE_VARS();
        __builtin_amdgcn_s_setprio(1);
        if (m < 4) {
            const char* src = m < 2 ? wk : wv;
#pragma unroll 1
            for (int p = hw; p < 25; p += 4) glds(lane * 16, src + (p + 5 * rq >= 25 ? p + 5 * rq - 25 : p + 5 * rq) * 1024, L + L_WKV + p * 1024);
        }
        const unsigned vx0 = (lane >> 3) * 1280 + (((lane & 7) ^ g) << 4), vx1 = (8 + (lane >> 3)) * 1280 + (((lane & 7) ^ (4 + g)) << 4);
        const char* xb = x + (size_t)(tb * 64 + hw * 16) * 1280;
        int sg = m;
        auto xload = [&](int n) {
#pragma unroll 1
            for (int i = 0; i < n; ++i) {
                const int sp = sg - 2 * rq < 0 ? sg - 2 * rq + 10 : sg - 2 * rq;
                glds(vx0, xb + sg * 128, L + L_X + (hw * 20 + sp) * 1024);
                glds(vx1, xb + sg * 128, L + L_X + (hw * 20 + 10 + sp) * 1024);
                sg = sg == 9 ? 0 : sg + 1;
            }
        };
        xload(3);
        WQ_SRC();
        bar_raw();
        wait_vm<6>();
        cnt_add(CNT(C_WKV));
        xload(7);
        wqload(0);
        wait_vm<5>();
        cnt_add(CNT(C_X));
        wqload(1);
    }
    int role = w;
    asm volatile("" : "+s"(role));
    if (role >= 4) {
        const int hw = w - 4;
        LANE_VARS();
        u64 f0, tag;
        const int c0 = tb >> 3;
        {
            WQ_SRC();
            WQ_ADDR();
            f32x4 cs[5];
            u32x4 one = ones;
            auto wqsum = [&](int r) {
                u32x4 f[5];
#pragma unroll
                for (int s = 0; s < 5; ++s) f[s] = ldstr2(wo0 + WQ_AT(r) + hw * 5120 + s * 32, wo1 + WQ_AT(r) + hw * 5120 + s * 32);
#pragma unroll
                for (int s = 0; s < 5; ++s) mma16(cs[s], f[s], one);
            };
            PIN(one);
            zero_acc(cs);
            cnt_spin(CNT(C_KFUSED), 4);
            wqload(2); wait_vm<10>(); cnt_add(CNT(C_WQ));
            cnt_spin(CNT(C_XUSED), 4);
            wqload(3); wait_vm<10>(); cnt_add(CNT(C_WQ + 1));
            asm volatile("global_load_dwordx2 %0, %1, %2 sc1" : "=v"(tag) : "v"(0u), "s"(ws + WS_FL) : "memory");
            wqload(4);
            wqsum(0); wqsum(1);
            wait_vm<11>(); cnt_add(CNT(C_WQ + 2)); wqsum(2);
            wait_vm<6>(); cnt_add(CNT(C_WQ + 3)); wqsum(3);
            wait_vm<0>(); cnt_add(CNT(C_WQ + 4));
            MAKE_TAG();
            f0 = __hip_atomic_load((const GAS u64*)(ws + FL_KV + (c0 * 32 + (lane & 31)) * 128), __ATOMIC_RELAXED, __HIP_MEMORY_SCOPE_AGENT);
            wqsum(4);
            mma_done();
            if (c16 == 0) {
#pragma unroll
                for (int s = 0; s < 5; ++s) *(LAS f32x4*)(size_t)(L + L_CSX + hw * 320 + s * 64 + g * 16) = cs[s];
            }
            if (cnt_arrive(CNT(C_CSP)) == 3) {
                const unsigned ca = L + L_CSX + (lane < 20 ? lane : 0) * 16;
                const f32x4 c4 = (ldsf4(ca) + ldsf4(ca + 320)) + (ldsf4(ca + 640) + ldsf4(ca + 960));
                *(LAS f32x4*)(size_t)(ca + 1280) = c4;
                cnt_add(CNT(C_CS));
            }
        }
        const char* kv = ws + WS_KV;
        poll(ws, FL_KV, lane < 32 && f0 != tag ? c0 * 32 + lane : -1, tag);
        copy_dev(kv + c0 * CHUNK, L, 42, hw, lane);
        u64 f1;
        asm volatile("global_load_dwordx2 %0, %1, %2 sc1" : "=v"(f1) : "v"(((c0 * 32 + 32 + lane) & 127) * 128), "s"(ws + FL_KV) : "memory");
        wait_vm<8>();
        __builtin_amdgcn_s_setprio(2);
        bar_raw();
        wait_vm<0>();
        cnt_add(CN2(E_C0));
        PIN(f1);
        poll(ws, FL_KV, f1 != tag ? (c0 * 32 + 32 + lane) & 127 : -1, tag);
        copy_dev(kv + ((c0 + 1) & 3) * CHUNK, L + CHUNK, 42, hw, lane);
        {
            int u = blockIdx.x * 4 + hw;
            const int pmat = u < 160 ? 0 : (u < 480 ? 1 : (u < 912 ? 2 : 3));
            u = pmat == 0 ? u : pmat == 1 ? u - 160 : pmat == 2 ? u - 480 : 0;
            const int pc = pmat < 2 ? u / 20 : u / 54, ks = u - pc * (pmat < 2 ? 20 : 54);
            int rb, a3, a2, a1;
            if (pmat == 0) { if (ks < 16) { rb = 40 * ks; a3 = 20; a2 = 4; a1 = 8; } else { rb = 160 * (ks - 16) + 16; a3 = 80; a2 = 40; a1 = 20; } }
            else if (pmat == 1) { rb = ks < 8 ? 80 * ks : ks < 12 ? 160 * (ks - 8) + 64 : 80 * (ks - 12) + 32; a3 = ks >= 8 && ks < 12 ? 80 : 16; a2 = 8; a1 = 4; }
            else if (ks >= 16 && ks < 24) { rb = 224 * (ks - 16) + 96; a3 = ks < 23 ? 112 : 48; a2 = 8; a1 = 4; }
            else { rb = ks < 16 ? 112 * ks : (ks < 39 ? 112 * (ks - 24) + 32 : 112 * (ks - 39) + 64); a3 = 8; a2 = 4; a1 = 16; }
            const int ld = pmat == 1 ? 3456 : 1280, chmax = pmat != 1 ? 9 : (pc == 15 ? 5 : 13);
            const char* psrc = pmat == 0 ? wo + pc * 160 : pmat == 1 ? w1 + pc * 224 : w2 + pc * 160;
            const unsigned pb = L + 3 * CHUNK + hw * 8192, pv = g * ld + min(c16, chmax) * 16;
            cnt_wait(CN2(P_NONE), 4);
#pragma unroll 1
            for (int i = 0; i < 8; ++i) glds(pv, psrc + (rb + a3 * (i >> 2) + a2 * ((i >> 1) & 1) + a1 * (i & 1)) * ld, pb + i * 1024);
            wait_vm<8>();
            cnt_add(CN2(E_C1));
            copy_dev(kv + ((c0 + 2) & 3) * CHUNK, L + 2 * CHUNK, 42, hw, lane);
            wait_vm<10>();
            const unsigned a0 = pb + (8 * g + (c16 >> 2)) * 256 + (lane & 3) * 8;
            if (pmat == 1) {
                const unsigned pdst = WS_W1 + pc * 143360 + ks * 5120, pd2 = WS_W1 + pc * 143360 + 102400 + ks * 2048;
                const unsigned cv = c16 == 0 ? g * 16 : 0x80000000u;
#pragma unroll
                for (int s = 0; s < 7; ++s) {
                    const unsigned a1 = a0 + (s == 6 ? (pc == 15 ? 64 : 192) : s * 32);
                    const u32x4 f = ldstr2(a1, a1 + 1024);
                    bst16(rs, f, lane * 16, s < 2 ? pdst + s * 1024 : s < 4 ? pdst + (s + 1) * 1024 : s < 6 ? pd2 + (s - 4) * 1024 : pdst + 2048);
                    bst16(rs, __builtin_bit_cast(u32x4, mfma16(f, ones, z4)), cv, WS_CS + (ks * 1792 + pc * 112) * 4 + s * 64);
                }
            } else if (pmat < 3) {
                const unsigned a1 = a0 + (lane & 3) * 8, pdst = pmat == 0 ? WS_WO + pc * 102400 + ks * 3072 : WS_W2 + u * 5120;
                const unsigned pd2 = pmat == 0 ? WS_WO + pc * 102400 + 61440 + ks * 2048 : pdst + 2048, pd4 = pdst + (pmat == 0 ? 2048 : 4096);
#pragma unroll
                for (int s = 0; s < 5; ++s) {
                    const unsigned a2 = s < 4 ? a1 + (s >> 1) * 64 + (s & 1) * 8 : a0 + 128;
                    bst16(rs, ldstr2(a2, a2 + 1024), lane * 16, s < 2 ? pdst + s * 1024 : s < 4 ? pd2 + (s - 2) * 1024 : pd4);
                }
            }
        }
        wait_vm<0>();
        cnt_add(CN2(E_C2));
        if (cnt_arrive(CN2(H_PREP)) == 3 && lane == 0) set_flag(ws, FL_PR + cid * 128, tag);
        cnt_wait<3>(CN2(H_PREP), 4);
        {
            const u32x4 e = {EMPTY, EMPTY, EMPTY, EMPTY};
#pragma unroll 1
            for (int p = hw; p < 44; p += 4) bst16(rs, e, lane * 16, WS_O + blockIdx.x * 48640 + p * 1024);
            bst16(rs, e, hw < 3 || lane < 32 ? lane * 16 : 0x80000000u, WS_O + blockIdx.x * 48640 + (44 + hw) * 1024);
        }
        poll(ws, FL_KV, (c0 * 32 + 96 + (lane & 31)) & 127, tag);
        wait_vm<0>();
        if (cnt_arrive(CN2(H_FILL)) == 3 && lane == 0) set_flag(ws, FL_MK + cid * 128, tag);
        cnt_wait<10>(CN2(P_C0), 4);
        copy_dev(kv + ((c0 + 3) & 3) * CHUNK, L, 42, hw, lane);
        poll(ws, FL_PR, hw * 64 + lane, tag);
        wait_vm<0>();
        cnt_add(CN2(E_C3));
        cnt_wait<3>(CN2(E_C3), 4);
        if (blockIdx.x == 0 && hw == 1 && lane == 0) set_flag(ws, WS_FL, tag);
        auto wocopy = [&](int p0, int p1) {
#pragma unroll 1
            for (int p = p0 + hw; p < p1; p += 4) {
                const int q = p + rotc * (p < 60 ? 12 : 8), r = p < 60 ? (q >= 60 ? q - 60 : q) : (q >= 100 ? q - 40 : q);
                glds_dev(lane * 16, ws + WS_WO + m * 102400 + r * 1024, L + L_WO + p * 1024);
            }
        };
        u32x4 r1[35], r2[9][8];
        unsigned g1[5], g1b[5], g2[9];
        d_bases(g1, WS_W1 + ct * 143360, rotd, 20480);
        d_bases(g1b, WS_W1 + ct * 143360 + 102400, rotd, 8192);
        e_bases(g2, WS_W2 + m * 276480, rote, 30720);
        const unsigned vp = lane * 16 + hw * 1024, vq = lane * 16 + min(28 + hw, 29) * 1024;
        unsigned lp0 = L + vp, lp1 = lp0 + 61440, lp2 = lp0 + 122880, lq0 = L + vq, lq1 = lq0 + 61440, lq2 = lq0 + 122880;
        PIN(lp0); PIN(lp1); PIN(lp2); PIN(lq0); PIN(lq1); PIN(lq2);
#define LD2(G, THR) do { _Pragma("unroll") for (int j_ = 0; j_ < 7; ++j_) { r2[G][j_] = bld16(rs, vp, g2[G] + j_ * 4096); wait_vm<THR>(); } \
        r2[G][7] = bld16(rs, vq, g2[G]); wait_vm<THR>(); } while (0)
#define ST2E(G, j) do { constexpr int sl_ = (G) % 5; \
        ldsw16((j) < 7 ? (sl_ < 2 ? lp0 : sl_ < 4 ? lp1 : lp2) + (sl_ & 1) * 30720 + (j) * 4096 : (sl_ < 2 ? lq0 : sl_ < 4 ? lq1 : lq2) + (sl_ & 1) * 30720, r2[G][j]); } while (0)
#pragma unroll
        for (int i = 0; i < 35; ++i) r1[i] = bld16(rs, vp, i < 25 ? g1[i / 5] + (i % 5) * 4096 : g1b[(i - 25) / 2] + ((i - 25) & 1) * 4096);
        SBAR();
        if (hw == 3) {
            u32x4 c[10];
            const unsigned cv = min(lane & 31, 27) * 16 + (lane >> 5) * 71680;
#pragma unroll
            for (int ks = 0; ks < 10; ++ks) c[ks] = bld16(rs, cv, WS_CS + (ks * 1792 + ct * 112) * 4);
            f32x4 c4 = z4;
#pragma unroll
            for (int ks = 0; ks < 10; ++ks) c4 += __builtin_bit_cast(f32x4, c[ks]);
#pragma unroll
            for (int i = 0; i < 4; ++i) c4[i] += upper(c4[i]);
            if (lane < 28) *(LAS f32x4*)(size_t)(L + L_MISC + lane * 16) = c4;
        }
        LD2(6, 63); LD2(7, 63); LD2(8, 63);
        cnt_wait<10>(CN2(P_C1), 4);
        wocopy(0, 42);
        poll(ws, FL_MK, hw * 64 + lane, tag);
        cnt_add(CN2(E_MK));
        cnt_wait<10>(CN2(P_C2), 4);
        wocopy(42, 100);
#pragma unroll
        for (int i = 0; i < 12; ++i) glds_dev(lane * 16, ws + WS_W2 + m * 276480 + (((tb & 3) * 4 + hw) * 12 + i) * 1024, L + L_JUNK2 + hw * 1024);
        wait_vm<12>();
        __builtin_amdgcn_s_setprio(0);
        bar_raw();
#pragma unroll
        for (int i = 0; i < 10; ++i) ldsw16(lp0 + i * 4096, r1[i < 4 ? 25 + i : i - 4]);
        cnt_add(CN2(M_W0));
        cnt_wait(CN2(M_P1), 4);
#pragma unroll
        for (int i = 10; i < 35; ++i) {
            if (i == 25) cnt_wait(CN2(M_LDS), 4);
            ldsw16((i < 15 ? lp0 : i < 30 ? lp1 : lp2) + (i % 15) * 4096, r1[i < 29 ? i - 4 : i]);
            SBAR();
            if (i == 17) cnt_add(CN2(M_W1A));
            if (i >= 18 && i - 18 < NEARLY) {
                const int e = i - 18, G = 3 + (e >> 3), j = e & 7;
                r2[G][j] = bld16(rs, j < 7 ? vp : vq, g2[G] + (j < 7 ? j * 4096 : 0));
                SBAR();
            }
        }
        cnt_add(CN2(M_W1));
#pragma unroll
        for (int e = NEARLY; e < 24; ++e) {
            const int G = 3 + (e >> 3), j = e & 7;
            r2[G][j] = bld16(rs, j < 7 ? vp : vq, g2[G] + (j < 7 ? j * 4096 : 0));
            wait_vm<THROT>();
        }
        cnt_wait<3>(CN2(M_K8), 4);
#pragma unroll 1
        for (int p = hw; p < 60; p += 4) { glds_dev(lane * 16, ws + (p < 30 ? g2[0] + p * 1024 : g2[1] + (p - 30) * 1024), L + p * 1024); wait_vm<THROT>(); }
        wait_vm<0>();
        cnt_add(CN2(G_RDY + 1));
        cnt_wait<3>(CN2(M_T4), 4);
        copy_dev(ws + g2[2], L + 61440, 30, hw, lane);
        wait_vm<0>();
        cnt_add(CN2(G_RDY + 2));
        static_for<3, 9>([&](auto gg) {
            constexpr int G = gg.v;
            if (G == 3) cnt_wait<2>(CN2(M_T5), 4);
            if (G >= 5) cnt_wait<2>(CN2(G_USED + G - 5), 4);
#pragma unroll
            for (int j = 0; j < 8; ++j) { ST2E(G, j); SBAR(); }
            cnt_add(CN2(G_RDY + G));
        });
        asm volatile(".Lpf_end2:");
        __builtin_amdgcn_endpgm();
    }

    u32x4 xr0, xr1; u32x2 xr2;
    u32x4 qa1, qb1; u32x2 qa2, qb2;
    const int kh = w & 1;
    {
        LANE_VARS();
        LOAD_TAG();
        u32x4 xf[20], xo[10], kf[20];
        const unsigned xrow = L + L_X + w * 20480 + (c16 >> 3) * 10240 + (c16 & 7) * 128;
        const int sw = c16 >> 1, t32 = (w & 1) * 16 + c16;
        unsigned xa = xrow + (((kh * 4 + g) ^ sw) << 4), xb = xrow + ((((kh ^ 1) * 4 + g) ^ sw) << 4), xc = xa + ((w ^ 1) - w) * 20480, xra[3], qw[5], kvo;
        {
            auto rotx = [&](int c) { c -= 16 * rq; return c < 0 ? c + 80 : c; };
            const int c0 = rotx(m * 10 + g), c1 = rotx(m * 10 + 4 + g), c2 = rotx(m * 10 + 8 + (g >> 1));
            xra[0] = xrow + (c0 >> 3) * 1024 + (((c0 & 7) ^ sw) << 4);
            xra[1] = xrow + (c1 >> 3) * 1024 + (((c1 & 7) ^ sw) << 4);
            xra[2] = xrow + (c2 >> 3) * 1024 + (((c2 & 7) ^ sw) << 4) + (g & 1) * 8;
            const int d = (m & 1) * 16 + c16;
            kvo = m == 0 ? ((g >> 1) * 32 + t32) * 16 + (g & 1) * 8
                : m == 1 ? (g == 0 ? 1024 + t32 * 8 : 0x80000000u)
                         : (d <= 21 ? 1280 + (((w & 1) * 2 + (g & 1)) * 22 + d) * 16 + (g >> 1) * 8 : 0x80000000u);
            PIN(xa); PIN(xb); PIN(xc); PIN(xra[0]); PIN(xra[1]); PIN(xra[2]); PIN(kvo);
        }
#pragma unroll
        for (int s = 0; s < 5; ++s) {
            const int f0 = s * 16 + 4 * g, hh = f0 / 20, d0 = f0 - hh * 20;
            qw[s] = L + L_QX + (hh * 2 + (w >> 1)) * 1280 + (d0 < 16 ? ((d0 >> 3) * 32 + t32) * 16 + (d0 & 4) * 2 : 1024 + t32 * 8);
            PIN(qw[s]);
        }
        WQ_ADDR();
        unsigned b0 = wo0 + L_WQ1 + kh * 5120, b1 = wo1 + L_WQ1 + kh * 5120;
        PIN(b0); PIN(b1);
        wait_vm<0>();
        MAKE_TAG();
        f32x4 ka = z4, kc = z4, ssm = z4, ssq = z4, acc[2][5];
        u32x4 one = ones;
        PIN(ssm); PIN(ssq); PIN(one);
        if (m < 4) {
            cnt_wait(CNT(C_WKV), 4);
            const unsigned a0 = L + L_WKV + (8 * g + (c16 >> 2)) * 40 + (m & 1) * 32 + (lane & 3) * 8;
            const unsigned a1 = a0 + kh * 1280, a2 = a0 + (kh ^ 1) * 1280;
#pragma unroll
            for (int j = 0; j < 10; ++j) { kf[j] = ldstr2(a1 + j * 2560, a1 + j * 2560 + 160); kf[10 + j] = ldstr2(a2 + j * 2560, a2 + j * 2560 + 160); }
            if (m < 2) {
#pragma unroll
                for (int j = 0; j < 20; ++j) kc = mfma16(kf[j], ones, kc);
            } else {
#pragma unroll
                for (int j = 0; j < 20; ++j) kc = mfma16(ones, kf[j], kc);
            }
            PIN(kc);
        }
        cnt_add(CNT(C_KFUSED));
        cnt_spin(CNT(C_X), 4);
#pragma unroll
        for (int j = 0; j < 10; ++j) xf[j] = lds16(xa + j * 1024);
#pragma unroll
        for (int j = 0; j < 10; ++j) xf[10 + j] = lds16(xb + j * 1024);
#pragma unroll
        for (int j = 0; j < 10; ++j) xo[j] = lds16(xc + j * 1024);
        xr0 = lds16(xra[0]); xr1 = lds16(xra[1]); xr2 = lds8(xra[2]);
        SBAR();
        if (m < 2) {
#pragma unroll
            for (int j = 0; j < 20; ++j) ka = mfma16(kf[j], xf[j], ka);
        } else if (m < 4) {
#pragma unroll
            for (int j = 0; j < 20; ++j) ka = mfma16(xf[j], kf[j], ka);
        }
        PIN(ka);
#pragma unroll
        for (int j = 0; j < 20; ++j) {
            mma16(ssm, one, xf[j]); mma16(ssq, xf[j], xf[j]);
            if (j == 4) {
#pragma unroll
                for (int i = 0; i < 10; ++i) { PIN(xf[i]); PIN(xf[10 + i]); PIN(xo[i]); }
                PIN(xr0); PIN(xr1); PIN(xr2);
                cnt_add(CNT(C_XUSED));
            }
        }
        mma_done();
        const float sd = (c16 & 2) ? ((c16 & 1) ? ssq[3] : ssq[2]) : ((c16 & 1) ? ssq[1] : ssq[0]);
        const float mean = ssm[0] * (1.f / D), rstd = __builtin_amdgcn_rsqf(fmaxf(__shfl(sd, (c16 >> 2) * 16 + c16) * (1.f / D) - mean * mean, 0.f) + LN_EPS);
        if (m < 4) {
            u32x2 pv;
            if (m < 2) {
                pv = u32x2{pk2((ka[0] - mean * kc[0]) * rstd, (ka[1] - mean * kc[1]) * rstd), pk2((ka[2] - mean * kc[2]) * rstd, (ka[3] - mean * kc[3]) * rstd)};
            } else {
                float v[4];
#pragma unroll
                for (int r = 0; r < 4; ++r) v[r] = (ka[r] - __shfl(mean, 4 * g + r) * kc[0]) * __shfl(rstd, 4 * g + r);
                pv = u32x2{pk2(v[0], v[1]), pk2(v[2], v[3])};
                if (m == 3 && c16 >= 4) pv = c16 == 4 ? u32x2{ONES2, ONES2} : u32x2{0u, 0u};
            }
            bst8(rs, pv, kvo, WS_KV + (tb * 2 + (w >> 1)) * KV_TILE);
        }
        SBAR();
        u32x2 f[2][5][2];
        unsigned xs, xv;
        float sc = rstd * QSCALE, ms = -mean * sc;
        PIN(sc); PIN(ms);
        unsigned b2 = b0 + (L_WQ2 - L_WQ1), b3 = b1 + (L_WQ2 - L_WQ1), cq = CNT(0), cv = 0;
        PIN(b2); PIN(b3); PIN(cq);
        auto qhalf = [&](int j, int s, int e) { return ldstr((j < 4 ? (e ? b1 : b0) + j * 10240 : (e ? b3 : b2) + j % 6 * 10240) + s * 32); };
        auto kvflag = [&] {
            if (m < 4) {
                wait_vm<0>();
                if (cnt_arrive(CNT(C_KVST)) == 3 && lane == 0) set_flag(ws, FL_KV + (tb * 4 + m) * 128, tag);
            }
        };
        cnt_spin(CNT(C_WQ), 4);
#pragma unroll
        for (int s = 0; s < 5; ++s) { f[0][s][0] = qhalf(0, s, 0); f[0][s][1] = qhalf(0, s, 1); }
        SBAR();
#pragma unroll
        for (int j = 0; j < 10; ++j) {
            if (j < 8 && !(j & 1)) cv = *(volatile LAS unsigned*)(size_t)(cq + (C_WQ + j / 2 + 1) * 4);
            if (j < 9 && (j & 1) && __builtin_expect((unsigned)__builtin_amdgcn_readfirstlane(cv) < 4u, 0)) cnt_spin(CNT(C_WQ + j / 2 + 1), 4);
            asm volatile("" ::: "memory");
            SBAR();
#pragma unroll
            for (int s = 0; s < 5; ++s) {
                const u32x4 fr = {f[j & 1][s][0][0], f[j & 1][s][0][1], f[j & 1][s][1][0], f[j & 1][s][1][1]};
                if (j == 0) mma16z(acc[0][s], fr, xf[j]); else mma16(acc[0][s], fr, xf[j]);
                SBAR();
                if (j < 9) f[(j + 1) & 1][s][0] = qhalf(j + 1, s, 0);
                SBAR();
                if (j == 0) mma16z(acc[1][s], fr, xo[j]); else mma16(acc[1][s], fr, xo[j]);
                SBAR();
                if (j < 9) f[(j + 1) & 1][s][1] = qhalf(j + 1, s, 1);
                SBAR();
            }
            if (j == 0) kvflag();
            if (j == 3) asm volatile("s_mov_b64 exec, 1\n\tds_add_u32 %0, %1 offset:%2\n\ts_mov_b64 exec, -1" ::"v"(cq), "v"(1u), "i"(C_R1 * 4) : "memory");
            if (j == 5) { xs = L + L_WQ1 + (w ^ 1) * 5120 + lane * 16; xv = L + L_WQ1 + w * 5120 + lane * 16; PIN(xs); PIN(xv); }
            if (j == 7) cnt_spin(CNT(C_R1), 4);
        }
        mma_done();
        send_half(acc[1], xs);
        cnt_add(CNT(C_XCH + (w >> 1)));
        if (w == 0 && lane < M_NUM) *(LAS unsigned*)(size_t)CN2(lane) = 0u;
        f32x4 c5[5], p5[5];
#pragma unroll 1
        for (;;) {
            const unsigned n1 = *(volatile LAS unsigned*)(size_t)CNT(C_CS), n2 = *(volatile LAS unsigned*)(size_t)CNT(C_XCH + (w >> 1));
#pragma unroll
            for (int s = 0; s < 5; ++s) {
                c5[s] = *(volatile LAS f32x4*)(size_t)(L + L_CSX + 1280 + s * 64 + g * 16);
                p5[s] = *(volatile LAS f32x4*)(size_t)(xv + s * 1024);
            }
            if (__builtin_amdgcn_readfirstlane(n1 + n2) == 3) break;
        }
#pragma unroll
        for (int s = 0; s < 5; ++s) {
            const f32x4 v = (acc[0][s] + p5[s]) * sc + c5[s] * ms;
            *(LAS u32x2*)(size_t)qw[s] = u32x2{pk2(v[0], v[1]), pk2(v[2], v[3])};
        }
        bar();
        const int r32 = lane & 31, h = lane >> 5;
        const unsigned qb = L + L_QX + ((w & 1) * 4 + (w >> 1)) * 1280;
        qa1 = lds16(qb + lane * 16); qb1 = lds16(qb + 2560 + lane * 16);
        qa2 = u32x2{lds4(qb + 1024 + r32 * 8 + h * 4), 0u}; qb2 = u32x2{lds4(qb + 2560 + 1024 + r32 * 8 + h * 4), 0u};
    }
    {
        LANE_VARS();
        const int r32 = lane & 31, h = lane >> 5, vrow = min(r32, 21);
        const unsigned kconst = h == 0 ? 0x00003f80u : 0u;
        unsigned ak1 = L + lane * 16, ak2 = L + 1024 + r32 * 8 + h * 4, av = L + 1280 + (h * 22 + vrow) * 16;
        const f32x16 zero16 = {0, 0, 0, 0, 0, 0, 0, 0, 0, 0, 0, 0, 0, 0, 0, 0};
        f32x16 sa, sb, oa = zero16, ob = zero16;
        u32x4 ka1 = lds16(ak1), kb1 = lds16(ak1 + KV_TILE), va0 = lds16(av), va1 = lds16(av + VS), vb0 = va0, vb1 = va0;
        u32x2 ka2 = {lds4(ak2), kconst}, kb2 = {lds4(ak2 + KV_TILE), kconst};
        sa = mfma32(ka1, qa1, mfma32k8(ka2, qa2, zero16));
        sb = mfma32(ka1, qb1, mfma32k8(ka2, qb2, zero16));
        {
            const float ma = halfmax(max16(sa)), mb = halfmax(max16(sb));
            qa2[1] = h == 0 ? (pk2(-ma, 0.f) & 0xffffu) : 0u;
            qb2[1] = h == 0 ? (pk2(-mb, 0.f) & 0xffffu) : 0u;
        }
        sa = mfma32(ka1, qa1, mfma32k8(ka2, qa2, zero16));
        u32x4 pa0 = {0u, 0u, 0u, 0u}, pa1 = pa0, pb0 = pa0, pb1 = pa0;
        float e2 = 0.f, e3 = 0.f;
        unsigned vo0 = (r32 >> 4) * 20480 + h * 256 + (r32 & 15) * 16, vo1 = h ? 0x80000000u : vo0;
        PIN(pa0); PIN(pa1); PIN(pb0); PIN(pb1); PIN(ka2); PIN(kb2); PIN(vo0); PIN(vo1);
#define SLOT(R, MF, X, S, i, PP, jp, P, j) do { R = MF; PIN(R); SBAR(); X; SBAR(); \
        float e0 = EX2(S[i]), e1 = EX2(S[i + 1]); PIN(e0); PIN(e1); SBAR(); \
        PP[jp] = pk2(e2, e3); PIN(PP); SBAR(); \
        e2 = EX2(S[i + 2]); e3 = EX2(S[i + 3]); PIN(e2); PIN(e3); SBAR(); \
        P[j] = pk2(e0, e1); PIN(P); SBAR(); } while (0)
#define STEP(KC1, KC2, KN1, KN2, VC0, VC1, VP0, VP1, OK, OV, X0, X7) do { \
        SLOT(sb, mfma32k8(KC2, qb2, zero16), X0, sa, 0, pb1, 3, pa0, 0); \
        SLOT(sb, mfma32(KC1, qb1, sb), __builtin_amdgcn_s_waitcnt(0xC07F), sa, 4, pa0, 1, pa0, 2); \
        SLOT(ob, mfma32(VP1, pb1, ob), KC1 = lds16(ak1 + (OK) * KV_TILE), sa, 8, pa0, 3, pa1, 0); \
        SLOT(oa, mfma32(VC0, pa0, oa), KC2[0] = lds4(ak2 + (OK) * KV_TILE), sa, 12, pa1, 1, pa1, 2); \
        SLOT(sa, mfma32k8(KN2, qa2, zero16), (void)0, sb, 0, pa1, 3, pb0, 0); \
        SLOT(sa, mfma32(KN1, qa1, sa), VP0 = lds16(av + (OV) * KV_TILE), sb, 4, pb0, 1, pb0, 2); \
        SLOT(oa, mfma32(VC1, pa1, oa), VP1 = lds16(av + (OV) * KV_TILE + VS), sb, 8, pb0, 3, pb1, 0); \
        SLOT(ob, mfma32(VC0, pb0, ob), X7, sb, 12, pb1, 1, pb1, 2); } while (0)
        unsigned so1 = WS_O + (tb * 4 + (w & 2)) * 20480, so0 = so1 + (2 * m + (w & 1)) * 1024;
        so1 += 16384 + (2 * m + (w & 1)) * 256;
        asm volatile("" : "+s"(so0), "+s"(so1));
        unsigned ea = CN2(E_C1), e0a = CN2(E_C0), ev = 0, one = 1;
        PIN(ea); PIN(e0a); PIN(one); PIN(ak1); PIN(ak2); PIN(av);
        auto hook0 = [&](int j, int dn) {
            if (j == 1) ev = *(volatile LAS unsigned*)(size_t)e0a;
            if (j == 14) { ak1 += dn; ak2 += dn; PIN(ak1); PIN(ak2); }
            if (j == 15) { av += dn; ea += 4; PIN(av); PIN(ea); }
        };
        auto hook7 = [&](int j) {
            if (j == 0)
                asm volatile("s_mov_b64 exec, 1\n\tds_add_u32 %0, %1 offset:%2\n\ts_mov_b64 exec, -1" ::"v"(ea), "v"(one), "i"((P_NONE - E_C1) * 4) : "memory");
            if (j == 1 && __builtin_expect((unsigned)__builtin_amdgcn_readfirstlane(ev) < 4u, 0)) cnt_spin(e0a, 4);
            if (j == 9) ev = *(volatile LAS unsigned*)(size_t)ea;
            if (j == 10 && __builtin_expect((unsigned)__builtin_amdgcn_readfirstlane(ev) < 4u, 0))
                cnt_wait(ea, 4);
        };
#pragma unroll 1
        for (int c = 0; c < 4; ++c) {
            const int dn = c == 2 ? -2 * CHUNK : CHUNK;
#pragma unroll
            for (int j = 0; j < 16; j += 2) {
                STEP(ka1, ka2, kb1, kb2, va0, va1, vb0, vb1, (j + 2) & 15, j + 1, hook0(j, dn), hook7(j));
                STEP(kb1, kb2, ka1, ka2, vb0, vb1, va0, va1, (j + 3) & 15, (j + 2) & 15, hook0(j + 1, dn), hook7(j + 1));
            }
        }
        pb1[3] = pk2(e2, e3);
        ob = mfma32(vb1, pb1, ob);
        u32x4 r0, r1, r2;
        auto finish = [&](const f32x16& pa, const f32x16& pb, float la, float lb) {
            const float ra = __builtin_amdgcn_rcpf(la), rb = __builtin_amdgcn_rcpf(lb);
            r0 = u32x4{keep(pk2(pa[0] * ra, pa[1] * ra)), pk2(pa[2] * ra, pa[3] * ra), keep(pk2(pa[4] * ra, pa[5] * ra)), pk2(pa[6] * ra, pa[7] * ra)};
            r1 = u32x4{keep(pk2(pb[0] * rb, pb[1] * rb)), pk2(pb[2] * rb, pb[3] * rb), keep(pk2(pb[4] * rb, pb[5] * rb)), pk2(pb[6] * rb, pb[7] * rb)};
            r2 = u32x4{keep(pk2(pa[8] * ra, pa[9] * ra)), pk2(pa[10] * ra, pa[11] * ra), keep(pk2(pb[8] * rb, pb[9] * rb)), pk2(pb[10] * rb, pb[11] * rb)};
        };
        {
            float am = 0.f;
#pragma unroll
            for (int i = 0; i < 12; i += 2) am = amax3(am, oa[i], oa[i + 1]);
            const float la = upper(oa[8]);
            asm volatile("s_nop 11" : "+v"(ob));
            const float lb = upper(ob[8]);
#pragma unroll
            for (int i = 0; i < 12; i += 2) am = amax3(am, ob[i], ob[i + 1]);
            finish(oa, ob, la, lb);
            const bool ok = (fminf(la, lb) >= 1e-18f) & (fmaxf(la, lb) <= 1e30f) & (am <= 1e37f);
            if (__builtin_expect(__ballot(!ok) != 0, 0)) {
                __builtin_amdgcn_fence(__ATOMIC_ACQUIRE, "agent");
                f32x16 xa, xb;
#pragma unroll 1
                for (int qt = 0; qt < 2; ++qt) {
                    xb = attn_exact(ws + WS_KV, qt ? qb1 : qa1, qt ? qb2[0] : qa2[0], lane, vrow);
                    if (qt == 0) xa = xb;
                }
                finish(xa, xb, upper(xa[8]), upper(xb[8]));
            }
        }
        bst16(rs, r0, vo0, so0);
        bst16(rs, r1, vo0 + 512, so0);
        bst16(rs, r2, vo1, so1);
    }
    u32x4 wd[12], ae[RE][2], we[PE];
    unsigned ge[9], e0, e1, ehv, eprobe, a0;
    const unsigned ehs = WS_F + (tb * 4 + (w & 2)) * 55296 + 16384;
    {
        LANE_VARS();
        const unsigned wl = L + lane * 16;
        f32x4 acc[5];
        u32x4 act[10][2], wf[PC];
        unsigned wa = wl + L_WO, wb = wa + 51200, gb[5];
        rot_bases(gb, WS_O + (tb * 4 + w) * 20480, rotc, 4096);
        unsigned v0 = lane * 16, v1 = v0 + 1024, hv = c16 * 256;
        unsigned sv = g == 0 ? c16 * 8 : 0x80000000u, h2v = (g & 1) ? 0x80000000u : (g >> 1) * 256 + c16 * 16;
        PIN(v0); PIN(v1); PIN(hv);
        auto sub = [](int n) { return n < 60 ? (n % 3 == 2 ? 4 : n % 3) : 2 + (n & 1); };
        auto kst = [](int n) { return n < 60 ? n / 3 : (n - 60) / 2; };
        auto wac = [&](int n) { return (n < 50 ? wa : wb) + (n % 50) * 1024; };
        kstart<RC, 2, 2048>(act, rs, ws, v0, v1, gb,
                           [&] { return __builtin_amdgcn_raw_buffer_load_b32(rs, hv, WS_O + (tb * 4 + w) * 20480 + 16384, SC1) == EMPTY; }, [&] {
            bar();
            PIN(wa); PIN(wb); PIN(sv); PIN(h2v);
#pragma unroll
            for (int n = 0; n < PC; ++n) { wf[n] = lds16(wac(n)); SBAR(); }
            res_init(acc, xr0, xr1, xr2);
            wait_vm<0>();
        }, [] {});
        const unsigned hb = WS_H + (tb * 4 + w) * 20480;
        unsigned t = 0;
        u64 bad = 0;
        static_for<0, 100>([&](auto nn) {
            constexpr int n = nn.v, j = n < 54 ? n / 6 : 0, i = n % 6, q0 = areq(j, RC, 10), q1 = areq(j + 1, RC, 10);
            if (n % 3 == 0) { lgkm<(PC - 3 < 15 ? PC - 3 : 15)>(); SBAR(); }
            mma16(acc[sub(n)], wf[n % PC], act[kst(n) >> 1][kst(n) & 1]);
            SBAR();
            if (n + PC < 100) wf[n % PC] = lds16(wac(n + PC));
            if (n == 59) cnt_add_now(CN2(M_P1));
            if (n < 54) {
                if (i < 4 && q0 + i / 2 < q1) act[(q0 + i / 2) % 10][i & 1] = aload<2, 2048>(rs, (i & 1) ? v1 : v0, gb, (q0 + i / 2) % 10);
                if (i == 3) { t = max(max(act[j + 1][1][0], act[j + 1][1][2]), max(act[j + 1][0][0], act[j + 1][0][2])); PIN(t); }
                if (i == 4) { bad = __ballot(t == EMPTY); asm volatile("" : "+s"(bad)); }
                if (i == 5 && __builtin_expect(bad != 0, 0))
                    refetch(act[j + 1][0], act[j + 1][1], ws + gb[(j + 1) / 2] + ((j + 1) & 1) * 2048, v0, v1);
            }
            if (n == 68) {
                xr0 = u32x4{keep(pk2(acc[0][0], acc[0][1])), pk2(acc[0][2], acc[0][3]), keep(pk2(acc[1][0], acc[1][1])), pk2(acc[1][2], acc[1][3])};
                bst16(rs, xr0, v0, hb + m * 1024);
            }
            if (n == 70) {
                xr2 = u32x2{keep(pk2(acc[4][0], acc[4][1])), pk2(acc[4][2], acc[4][3])};
                bst16(rs, rowpair(xr2, xr2), h2v, hb + 8192 + m * 512);
            }
            if (n == 74) t = *(volatile LAS unsigned*)(size_t)CN2(M_W0);
            if (n == 86) {
                if (__builtin_expect((unsigned)__builtin_amdgcn_readfirstlane(t) < 4u, 0)) cnt_spin(CN2(M_W0), 4);
                asm volatile("" ::: "memory");
            }
            if constexpr (n >= 88) {
                constexpr int k = n - 88;
                wd[k] = lds16(wl + (k % 7 < 5 ? 16384 + (k / 7) * 5120 + (k % 7) * 1024 : (k / 7) * 2048 + (k % 7 - 5) * 1024));
            }
            SBAR();
        });
        float sm = 0.f, sq = 0.f, sm2 = 0.f, sq2 = 0.f;
#pragma unroll
        for (int i = 0; i < 4; i += 2) { sm = dot2(xr0[i], ONES2, sm); sq = dot2(xr0[i], xr0[i], sq); sm2 = dot2(xr0[i + 1], ONES2, sm2); sq2 = dot2(xr0[i + 1], xr0[i + 1], sq2); }
        sm = dot2(xr2[0], ONES2, sm); sq = dot2(xr2[0], xr2[0], sq); sm2 = dot2(xr2[1], ONES2, sm2); sq2 = dot2(xr2[1], xr2[1], sq2);
        PIN(sm); PIN(sq); PIN(sm2); PIN(sq2);
        SBAR();
#pragma unroll
        for (int n = 0; n < 12; ++n) PIN(wd[n]);
        cnt_add(CN2(M_LDS));
        xr1 = u32x4{keep(pk2(acc[2][0], acc[2][1])), pk2(acc[2][2], acc[2][3]), keep(pk2(acc[3][0], acc[3][1])), pk2(acc[3][2], acc[3][3])};
        bst16(rs, xr1, v0, hb + (12 + m) * 1024);
#pragma unroll
        for (int i = 0; i < 4; i += 2) { sm = dot2(xr1[i], ONES2, sm); sq = dot2(xr1[i], xr1[i], sq); sm2 = dot2(xr1[i + 1], ONES2, sm2); sq2 = dot2(xr1[i + 1], xr1[i + 1], sq2); }
        sm = xsum16_32(sm + sm2); sq = xsum16_32(sq + sq2);
        bst8(rs, u32x2{keep(__float_as_uint(sm)), __float_as_uint(sq)}, sv, WS_ST + (tb * 4 + w) * 1024 + m * 128);
        SBAR();
    }
    {
        f32x4 acc[2][7], c4[7];
        u32x4 act[12][2];
        u32x2 st[4];
        float mu[2], rp[2];
        const int t16 = (tb >> 1) * 8 + 2 * w;
        unsigned v0 = lane_id() * 16, v1 = v0 + 20480, sv = v0 >> 1, wa = L + v0, wb = wa + 16384 + 46080, gb[5];
        PIN(sv); PIN(wa); PIN(wb); PIN(v0); PIN(v1);
        d_bases(gb, WS_H + t16 * 20480, rotd, 4096);
        unsigned fs[7], wc, fw, f2v, ca;
        auto late = [&] {
            zero_acc(acc[0]); zero_acc(acc[1]);
            e_bases(ge, WS_F + tb * 221184 + kh * 1024, rote, 6144);
            {
                const unsigned fb = WS_F + t16 * 55296 + ct * 1024;
                fs[0] = fb; fs[1] = fb + 55296; fs[2] = fb + 16384 - ct * 512; fs[3] = fb + 24576; fs[4] = fb + 24576 + 55296; fs[5] = fb + 39936; fs[6] = fb + 39936 + 55296;
#pragma unroll
                for (int i = 0; i < 7; ++i) asm volatile("" : "+s"(fs[i]));
                const int lane = v0 >> 4, g = lane >> 4, c16 = lane & 15;
                f2v = (g & 1) * 55296 + (g >> 1) * 256 + c16 * 16; ca = L + L_MISC + g * 16;
                ehv = (g & 1) * 55296 + c16 * 512;
                wc = wa + 118784;
                PIN(f2v); PIN(ca); PIN(ehv); PIN(wc);
            }
        };
        unsigned hv = ((v0 >> 8) & 1) * 20480 + ((v0 >> 4) & 7) * 512;
        PIN(hv);
        kstart<RD, 4, 1024>(act, rs, ws, v0, v1, gb, [&] { return __builtin_amdgcn_raw_buffer_load_b32(rs, hv, WS_H + t16 * 20480 + 8192, SC1) == EMPTY; },
                            [] {}, late);
        auto stload = [&] {
#pragma unroll
            for (int i = 0; i < 4; ++i) st[i] = __builtin_amdgcn_raw_buffer_load_b64(rs, sv + i * 512, WS_ST + t16 * 1024, SC1);
        };
        float sa[4];
        unsigned w1v = 0;
        auto dside = [&](int j, int i) {
            if ((j == 0 || j == 6) && i == 12) w1v = *(volatile LAS unsigned*)(size_t)(ca + 512 + (j ? M_W1 : M_W1A) * 4);
            if ((j == 1 || j == 7) && i == 12) {
                if (__builtin_expect((unsigned)__builtin_amdgcn_readfirstlane(w1v) < 4u, 0)) cnt_spin(CN2(j == 7 ? M_W1 : M_W1A), 4);
                asm volatile("" ::: "memory");
            }
            if (j == 5 && i == 4) stload();
            if (j == 9 && i == 6 && __builtin_expect(__any(max(max(st[0][0], st[1][0]), max(st[2][0], st[3][0])) == EMPTY), 0)) {
#pragma unroll 1
                for (int spin = 0; spin < (1 << 16); ++spin) {
                    asm volatile("" ::: "memory");
                    stload();
                    if (!__any(max(max(st[0][0], st[1][0]), max(st[2][0], st[3][0])) == EMPTY)) break;
                }
            }
            if ((j == 10 || j == 11) && (i == 4 || i == 8)) {
                const int k = (j - 10) * 2 + (i == 8);
                sa[k] = xsum16_32(__uint_as_float(st[k & 2][k & 1]) + __uint_as_float(st[(k & 2) + 1][k & 1])); PIN(sa[k]);
            }
            if (j == 12 && (i == 4 || i == 8)) {
                const int t = i == 8;
                mu[t] = sa[2 * t] * (-1.f / D);
                rp[t] = __builtin_amdgcn_rsqf(fmaxf(sa[2 * t + 1] * (1.f / D) - mu[t] * mu[t], 0.f) + LN_EPS) * LOG2E;
                PIN(mu[t]); PIN(rp[t]);
            }
        };
        auto wad = [&](int n) {
            const int j = n < 56 ? n / 7 : n < 86 ? 8 + (n - 56) / 5 : n < 98 ? 14 + (n - 86) / 2 : 14 + (n - 98) / 3;
            const int s = n < 56 ? n % 7 : n < 86 ? (n - 56) % 5 : n < 98 ? (n - 86) % 2 : 2 + (n - 98) % 3;
            return n >= 116 ? wc + (n - 116) * 1024 : s >= 5 ? wa + j * 2048 + (s - 5) * 1024 : (j < 9 ? wa + 16384 + j * 5120 : wb + (j - 9) * 5120) + s * 1024; };
        kloop<0, 8, 20, 0, 140, 7, 5, RD, 4, 1024>(acc, act, wd, wad, rs, ws, v0, v1, gb, dside);
        kloop<8, 1, 20, 56, 140, 5, 5, RD, 4, 1024>(acc, act, wd, wad, rs, ws, v0, v1, gb, dside);
        cnt_add_now(CN2(M_K8));
        kloop<9, 5, 20, 61, 140, 5, 2, RD, 4, 1024>(acc, act, wd, wad, rs, ws, v0, v1, gb, dside);
        c4[0] = ldsf4(ca); c4[1] = ldsf4(ca + 64);
        SBAR();
        kloop<14, 6, 20, 86, 140, 2, 4, RD, 4, 1024>(acc, act, wd, wad, rs, ws, v0, v1, gb, dside);
        u32x2 lo, hi;
        auto silu = [&](const f32x4& a, const f32x4& c, int t, u32x2& r, auto side) {
            f32x4 y = (a + mu[t] * c) * rp[t], e;
            PIN(y); SBAR();
#pragma unroll
            for (int i = 0; i < 4; ++i) { side(i); SBAR(); e[i] = EX2(-y[i]); PIN(e); SBAR(); }
            e = e * LOG2E + LOG2E; PIN(e); SBAR();
#pragma unroll
            for (int i = 0; i < 4; ++i) { side(4 + i); SBAR(); e[i] = __builtin_amdgcn_rcpf(e[i]); PIN(e); SBAR(); }
            e *= y;
            r = u32x2{keep(pk2(e[0], e[1])), pk2(e[2], e[3])};
            PIN(r); SBAR();
        };
        auto rest = [&](int q) {
            const int n = 98 + (q >> 1), j = n < 116 ? 14 + (n - 98) / 3 : 8 + (n - 116) / 2, s = n < 116 ? 2 + (n - 98) % 3 : 5 + (n - 116) % 2;
            mma16(acc[q & 1][s], wd[n % 12], act[j % 12][q & 1]);
            SBAR();
            if ((q & 1) && n + 12 < 140) wd[n % 12] = lds16(wad(n + 12));
            if (q >= 24 && q < 74 && q % 10 == 4) c4[q == 24 ? 6 : q / 10 - 1] = ldsf4(ca + (q == 24 ? 6 : q / 10 - 1) * 64);
        };
        auto early = [&](int q) {
            if (q == 0) eprobe = __builtin_amdgcn_raw_buffer_load_b32(rs, ehv, ehs, SC1);
            ae[q >> 1][q & 1] = aload<3, 2048>(rs, (q & 1) ? e1 : e0, ge, q >> 1);
        };
#pragma unroll
        for (int q = 0; q < 8; ++q) { rest(q); SBAR(); }
#pragma unroll
        for (int b = 0; b < 10; ++b) {
            const int ut = b < 4 ? b >> 1 : b < 6 ? b & 1 : (b - 6) >> 1, ui = b < 4 ? b & 1 : b < 6 ? 2 : 3 + (b & 1), us = ui < 2 ? ui : ui == 2 ? 6 : ui - 1;
            silu(acc[ut][ui], c4[us], ut, (b & 1) ? hi : lo, [&](int i) { if (8 + b * 8 + i < 84) rest(8 + b * 8 + i); });
            if (b == 5) bst16(rs, rowpair(lo, hi), f2v, fs[2]);
            else if (b & 1) bst16(rs, u32x4{lo[0], lo[1], hi[0], hi[1]}, b < 5 ? v0 : fw, fs[b >> 1]);
            if (b == 1) cnt_add(CN2(M_T4));
            if (b == 7) cnt_add(CN2(M_T5));
            if (b == 8) w1v = *(volatile LAS unsigned*)(size_t)(ca + 512 + (G_RDY + 1) * 4);
            if (b == 5) {
                PIN(v0);
                fw = ct < 15 ? v0 : 0x80000000u; e0 = v0 + w * 55296; e1 = v0 + (w ^ 1) * 55296; a0 = L + v0 + kh * 5120;
                PIN(fw); PIN(e0); PIN(e1); PIN(a0);
            }
            SBAR();
        }
        if (__builtin_expect((unsigned)__builtin_amdgcn_readfirstlane(w1v) < 4u, 0)) cnt_spin(CN2(G_RDY + 1), 4);
        asm volatile("" ::: "memory");
        mma_done();
#pragma unroll
        for (int n = 0; n < PE; ++n) we[n] = lds16(a0 + (n / 5) * 10240 + (n % 5) * 1024);
#pragma unroll
        for (int t = 0; t < 2; ++t) {
            u32x4 r;
#pragma unroll
            for (int u = 0; u < 2; ++u) {
                const f32x4 y = (acc[t][5 + u] + mu[t] * c4[4 + u]) * rp[t];
                f32x4 e = f32x4{EX2(-y[0]), EX2(-y[1]), EX2(-y[2]), EX2(-y[3])} * LOG2E + LOG2E;
                e = f32x4{__builtin_amdgcn_rcpf(e[0]), __builtin_amdgcn_rcpf(e[1]), __builtin_amdgcn_rcpf(e[2]), __builtin_amdgcn_rcpf(e[3])} * y;
                r[2 * u] = keep(pk2(e[0], e[1])); r[2 * u + 1] = pk2(e[2], e[3]);
            }
            bst16(rs, r, fw, fs[5 + t]);
        }
        SBAR();
#pragma unroll
        for (int q = 0; q < 2 * FIRSTFILL; ++q) { early(q); SBAR(); }
    }
    {
        f32x4 acc[2][5];
        unsigned a1 = a0 + 61440, a2 = a0 + 122880;
        PIN(a1); PIN(a2);
        res_init(acc[0], xr0, xr1, xr2); zero_acc(acc[1]);
        if (__any(eprobe == EMPTY))
            kstart<RE, 3, 2048>(ae, rs, ws, e0, e1, ge, [&] { return __builtin_amdgcn_raw_buffer_load_b32(rs, ehv, ehs, SC1) == EMPTY; }, [] {}, [] {});
        if (__builtin_expect(missing(ae[0][0], ae[0][1]), 0)) refetch(ae[0][0], ae[0][1], ws + ge[0], e0, e1);
        SBAR();
        auto wae = [&](int n) { const int q = n / 5 % 15; return (q < 6 ? a0 : q < 12 ? a1 : a2) + (q % 6) * 10240 + (n % 5) * 1024; };
        unsigned cg = CN2(0), gv = 0;
        PIN(cg);
        kloop<0, 27, 27, 0, 135, 5, 5, RE, 3, 2048>(acc, ae, we, wae, rs, ws, e0, e1, ge, [&](int j, int i) {
            if (j % 3 == 1 && i == 8 && j / 3 + 2 < 9) gv = *(volatile LAS unsigned*)(size_t)(cg + (G_RDY + j / 3 + 2) * 4);
            if (j % 3 == 2 && i == 8 && j / 3 + 2 < 9) {
                if (__builtin_expect((unsigned)__builtin_amdgcn_readfirstlane(gv) < 4u, 0)) cnt_spin(cg + (G_RDY + j / 3 + 2) * 4, 4);
                asm volatile("" ::: "memory");
            }
            if (j % 3 == 2 && i == 4 && j / 3 < 5)
                asm volatile("s_mov_b64 exec, 1\n\tds_add_u32 %0, %1 offset:%2\n\ts_mov_b64 exec, -1" ::"v"(cg), "v"(1u), "i"((G_USED + j / 3) * 4) : "memory");
            if (j == 24 && i == 8) gv = *(volatile LAS unsigned*)(size_t)(cg + (G_USED + 4) * 4);
        });
        if (__builtin_expect((unsigned)__builtin_amdgcn_readfirstlane(gv) < 4u, 0)) cnt_spin(cg + (G_USED + 4) * 4, 4);
        mma_done();
        LANE_VARS();
        const unsigned wl = L + lane * 16;
        send_half(acc[1], wl + 122880 + (w ^ 1) * 5120);
        bar();
        recv_half(acc[0], wl + 122880 + w * 5120);
        res_pack(xr0, xr1, xr2, acc[0]);
        char* ob = out + (size_t)(tb * 64 + w * 16 + c16) * 1280 + m * 160;
        asm volatile("global_store_dwordx4 %0, %1, off nt\n\tglobal_store_dwordx4 %0, %2, off offset:64 nt\n\t"
                     "global_store_dwordx2 %3, %4, off offset:128 nt\n\ts_nop 1" ::"v"(ob + g * 16), "v"(xr0), "v"(xr1), "v"(ob + g * 8), "v"(xr2) : "memory");
    }
    asm volatile(".Lpf_end1:");
    __builtin_amdgcn_endpgm();
}

extern "C" void kernel_launch(void* const* d_in, const int* in_sizes, int n_in, void* d_out, int out_size, void* d_ws, size_t ws_size,
                              hipStream_t stream) {
    (void)hipFuncSetAttribute((const void*)k_block, hipFuncAttributeMaxDynamicSharedMemorySize, LDS_BYTES);
    k_block<<<256, 512, LDS_BYTES, stream>>>((const char*)d_in[0], (const char*)d_in[1], (const char*)d_in[2], (const char*)d_in[3],
                                             (const char*)d_in[4], (const char*)d_in[6], (const char*)d_in[7], (char*)d_ws, (char*)d_out);
}
```

```cpp
#include <hip/hip_runtime.h>
#include <hip/hip_bf16.h>
#include <stdint.h>

typedef __attribute__((ext_vector_type(8))) __bf16 bf16x8;
typedef __attribute__((ext_vector_type(2))) __bf16 bf16x2;
typedef __attribute__((ext_vector_type(4))) short s16x4;
typedef __attribute__((ext_vector_type(4))) float f32x4;
typedef __attribute__((ext_vector_type(16))) float f32x16;
typedef __attribute__((ext_vector_type(4))) unsigned u32x4;
typedef __attribute__((ext_vector_type(2))) unsigned u32x2;
typedef unsigned long long u64;
#define LAS __attribute__((address_space(3)))
#define GAS __attribute__((address_space(1)))
#define DI __device__ __forceinline__

constexpr int D = 640;
constexpr float LN_EPS = 1e-5f;
constexpr float LOG2E = 1.4426950408889634f;
constexpr float QSCALE = 0.22360679774997896f * LOG2E;

constexpr int KV_TILE = 2688;
constexpr int VS = 704;
constexpr unsigned WS_FL = 0;
constexpr unsigned FL_KV = 32768, FL_PR = 2 * 32768, FL_MK = 3 * 32768;
constexpr unsigned WS_KV = 4 * 32768;
constexpr unsigned WS_WO = WS_KV + 64 * KV_TILE;
constexpr unsigned WS_W1 = WS_WO + 8 * 20 * 5 * 1024;
constexpr unsigned WS_W2 = WS_W1 + 16 * 20 * 7 * 1024;
constexpr unsigned WS_CS = WS_W2 + 8 * 54 * 5 * 1024;
constexpr unsigned WS_O  = WS_CS + 20 * 1792 * 4;
constexpr unsigned WS_H  = WS_O + 128 * 20 * 1024;
constexpr unsigned WS_F  = WS_H + 128 * 20 * 1024;
constexpr unsigned WS_ST = WS_F + 128 * 54 * 1024;
constexpr unsigned WS_END = WS_ST + 2048 * 64;
constexpr unsigned EMPTY = 0xffffffffu;
static_assert((WS_END - WS_O) == 256 * 48640, "fill share per workgroup");

constexpr int CHUNK = 16 * KV_TILE;
constexpr int L_X = 0, L_WQ2 = 43008, L_WKV = 83968, L_WQ1 = 122880,
               L_CSX = 111104, L_QX = 152576, L_MISC = 162816;
constexpr int L_WO = CHUNK, L_CNT = 110592, L_JUNK = 113664, L_JUNK2 = 153600, LDS_BYTES = 163840;

constexpr int FIRSTFILL = 4;
constexpr int RC = 6, RD = 8, RE = 7;
constexpr int PC = 24, PE = 30;
constexpr int THROT = 3, NEARLY = 12;

DI void glds(unsigned voff, const void* sbase, unsigned lds) {
    asm volatile("s_mov_b32 m0, %2\n\ts_nop 0\n\tglobal_load_lds_dwordx4 %0, %1" ::"v"(voff), "s"(sbase), "s"(lds) : "memory");
}
DI void glds_dev(unsigned voff, const void* sbase, unsigned lds) {
    asm volatile("s_mov_b32 m0, %2\n\ts_nop 0\n\tglobal_load_lds_dwordx4 %0, %1 sc1" ::"v"(voff), "s"(sbase), "s"(lds) : "memory");
}
DI void touch(unsigned voff, const void* sbase, unsigned lds, u64 mask) {
    asm volatile("s_mov_b32 m0, %2\n\ts_mov_b64 exec, %3\n\tglobal_load_lds_dword %0, %1\n\ts_mov_b64 exec, -1" ::"v"(voff), "s"(sbase), "s"(lds), "s"(mask) : "memory");
}
template <int N> DI void wait_vm() { asm volatile("s_waitcnt vmcnt(%0)" ::"i"(N) : "memory"); }
DI void bar() { asm volatile("s_waitcnt lgkmcnt(0)\n\ts_barrier" ::: "memory"); }
DI void bar_raw() { asm volatile("s_barrier" ::: "memory"); }
#define PIN(x) asm volatile("" : "+v"(x))
#define SBAR() __builtin_amdgcn_sched_barrier(0)
DI unsigned pk2(float a, float b) { bf16x2 v = {(__bf16)a, (__bf16)b}; return __builtin_bit_cast(unsigned, v); }
DI float bflo(unsigned u) { return __uint_as_float(u << 16); }
DI float bfhi(unsigned u) { return __uint_as_float(u & 0xffff0000u); }
DI f32x4 mfma16(u32x4 a, u32x4 b, f32x4 c) {
    return __builtin_amdgcn_mfma_f32_16x16x32_bf16(__builtin_bit_cast(bf16x8, a), __builtin_bit_cast(bf16x8, b), c, 0, 0, 0);
}
DI f32x16 mfma32(u32x4 a, u32x4 b, f32x16 c) {
    return __builtin_amdgcn_mfma_f32_32x32x16_bf16(__builtin_bit_cast(bf16x8, a), __builtin_bit_cast(bf16x8, b), c, 0, 0, 0);
}
DI f32x16 mfma32k8(u32x2 a, u32x2 b, f32x16 c) {
    return __builtin_amdgcn_mfma_f32_32x32x8bf16_1k(__builtin_bit_cast(s16x4, a), __builtin_bit_cast(s16x4, b), c, 0, 0, 0);
}
DI u32x4 lds16(unsigned a) { return *(const LAS u32x4*)(size_t)a; }
DI u32x2 lds8(unsigned a) { return *(const LAS u32x2*)(size_t)a; }
DI unsigned lds4(unsigned a) { return *(const LAS unsigned*)(size_t)a; }
DI f32x4 ldsf4(unsigned a) { return *(const LAS f32x4*)(size_t)a; }
DI u32x2 ldstr(unsigned a) { return __builtin_bit_cast(u32x2, __builtin_amdgcn_ds_read_tr16_b64_v4i16((LAS s16x4*)(size_t)a)); }
DI u32x4 ldstr2(unsigned a0, unsigned a1) { u32x2 lo = ldstr(a0), hi = ldstr(a1); return u32x4{lo[0], lo[1], hi[0], hi[1]}; }
DI float dot2(unsigned a, unsigned b, float c) {
    return __builtin_amdgcn_fdot2_f32_bf16(__builtin_bit_cast(bf16x2, a), __builtin_bit_cast(bf16x2, b), c, false);
}
DI float xsum16_32(float v) {
    const auto a = __builtin_amdgcn_permlane16_swap(__float_as_uint(v), __float_as_uint(v), false, false);
    v = __uint_as_float(a[0]) + __uint_as_float(a[1]);
    const auto b = __builtin_amdgcn_permlane32_swap(__float_as_uint(v), __float_as_uint(v), false, false);
    return __uint_as_float(b[0]) + __uint_as_float(b[1]);
}
constexpr unsigned ONES2 = 0x3f803f80u;
#define EX2(v) __builtin_amdgcn_exp2f(v)
typedef __amdgpu_buffer_rsrc_t rsrc_t;
constexpr int SC1 = 16;
DI u32x4 bld16(rsrc_t r, unsigned v, unsigned s) { return __builtin_amdgcn_raw_buffer_load_b128(r, v, s, SC1); }
DI void bst16(rsrc_t r, u32x4 d, unsigned v, unsigned s) {
    __builtin_amdgcn_raw_buffer_store_b128(d, r, v, s, SC1);
    asm volatile("s_nop 1" ::"v"(d) : "memory");
}
DI void bst8(rsrc_t r, u32x2 d, unsigned v, unsigned s) { __builtin_amdgcn_raw_buffer_store_b64(d, r, v, s, SC1); }

DI void set_flag(char* ws, unsigned off, u64 tag) { __hip_atomic_store((GAS u64*)(ws + off), tag, __ATOMIC_RELAXED, __HIP_MEMORY_SCOPE_AGENT); }
DI void poll(char* ws, unsigned arr, int index, u64 tag) {
    const GAS u64* p = (const GAS u64*)(ws + arr + index * 128);
    bool ok = index < 0;
#pragma unroll 1
    for (int spin = 0; spin < (1 << 18); ++spin) {
        if (!ok) ok = __hip_atomic_load(p, __ATOMIC_RELAXED, __HIP_MEMORY_SCOPE_AGENT) == tag;
        if (__all(ok)) break;
        __builtin_amdgcn_s_sleep(1);
    }
    asm volatile("" ::: "memory");
}
DI void poll_fast(const char* base, unsigned voff, u64 tag, u64& a, u64& b, u64& c) {
    asm volatile("global_load_dwordx2 %0, %3, %4 sc1\n\ts_sleep %5\n\tglobal_load_dwordx2 %1, %3, %4 sc1\n\ts_sleep %5\n\tglobal_load_dwordx2 %2, %3, %4 sc1"
                 : "=&v"(a), "=&v"(b), "=&v"(c) : "v"(voff), "s"(base), "i"(4) : "memory");
#define POLL_STEP(R) asm volatile("s_waitcnt vmcnt(2)" : "+v"(R) :: "memory"); if (__all(R == tag)) break; \
        asm volatile("global_load_dwordx2 %0, %1, %2 sc1" : "+v"(R) : "v"(voff), "s"(base) : "memory")
#pragma unroll 1
    for (int spin = 0; spin < (1 << 17); ++spin) { POLL_STEP(a); POLL_STEP(b); POLL_STEP(c); }
}
DI void copy_dev(const char* src, unsigned lds, int n, int hw, int lane) {
#pragma unroll 1
    for (int p = hw; p < n; p += 4) glds_dev(lane * 16, src + p * 1024, lds + p * 1024);
}

DI void cnt_add(unsigned a) {
    asm volatile("s_waitcnt lgkmcnt(0)\n\ts_mov_b64 exec, 1\n\tds_add_u32 %0, %1\n\ts_mov_b64 exec, -1" ::"v"(a), "v"(1u) : "memory");
}
DI void cnt_add_now(unsigned a) {
    asm volatile("s_mov_b64 exec, 1\n\tds_add_u32 %0, %1\n\ts_mov_b64 exec, -1" ::"v"(a), "v"(1u) : "memory");
}
DI unsigned cnt_arrive(unsigned a) {
    unsigned r;
    asm volatile("s_waitcnt lgkmcnt(0)\n\ts_mov_b64 exec, 1\n\tds_add_rtn_u32 %0, %1, %2\n\ts_mov_b64 exec, -1\n\ts_waitcnt lgkmcnt(0)"
                 : "=&v"(r) : "v"(a), "v"(1u) : "memory");
    return __builtin_amdgcn_readfirstlane(r);
}
DI void cnt_spin(unsigned a, unsigned n) {
#pragma unroll 1
    while ((unsigned)__builtin_amdgcn_readfirstlane(*(volatile LAS unsigned*)(size_t)a) < n) {}
    asm volatile("" ::: "memory");
}
template <int NAP = 1> DI void cnt_wait(unsigned a, unsigned n) {
#pragma unroll 1
    while ((unsigned)__builtin_amdgcn_readfirstlane(*(volatile LAS unsigned*)(size_t)a) < n) __builtin_amdgcn_s_sleep(NAP);
    asm volatile("" ::: "memory");
}
DI unsigned keep(unsigned v) { return min(v, EMPTY - 1); }
DI bool missing(const u32x4& a, const u32x4& b) { return __any(max(max(a[0], a[2]), max(b[0], b[2])) == EMPTY); }
DI void refetch(u32x4& a, u32x4& b, const char* base, unsigned v0, unsigned v1) {
#pragma unroll 1
    for (int spin = 0; spin < (1 << 16); ++spin) {
        asm volatile("global_load_dwordx4 %0, %2, %4 sc1\n\tglobal_load_dwordx4 %1, %3, %4 sc1\n\ts_waitcnt vmcnt(0)"
                     : "+v"(a), "+v"(b) : "v"(v0), "v"(v1), "s"(base) : "memory");
        if (!missing(a, b)) break;
    }
}
DI u32x4 rowpair(const u32x2& a, const u32x2& b) {
    const auto s0 = __builtin_amdgcn_permlane16_swap(a[0], b[0], false, false), s1 = __builtin_amdgcn_permlane16_swap(a[1], b[1], false, false);
    return u32x4{s0[0], s1[0], s0[1], s1[1]};
}
DI int lane_id() {
    int l;
    asm volatile("v_mbcnt_lo_u32_b32 %0, -1, 0\n\tv_mbcnt_hi_u32_b32 %0, -1, %0" : "=v"(l));
    return l;
}
#define LANE_VARS() const int lane = lane_id(), g = lane >> 4, c16 = lane & 15; (void)g; (void)c16
DI float halfmax(float m) {
    auto r = __builtin_amdgcn_permlane32_swap(__float_as_uint(m), __float_as_uint(m), false, false);
    return fmaxf(__uint_as_float(r[0]), __uint_as_float(r[1]));
}
DI float upper(float v) {
    auto r = __builtin_amdgcn_permlane32_swap(__float_as_uint(v), __float_as_uint(v), false, false);
    return __uint_as_float(r[1]);
}
DI float max16(const f32x16& s) {
    float a = fmaxf(fmaxf(s[0], s[1]), s[2]), b = fmaxf(fmaxf(s[3], s[4]), s[5]);
    a = fmaxf(fmaxf(a, s[6]), s[7]); b = fmaxf(fmaxf(b, s[8]), s[9]);
    a = fmaxf(fmaxf(a, s[10]), s[11]); b = fmaxf(fmaxf(b, s[12]), s[13]);
    a = fmaxf(fmaxf(a, s[14]), s[15]);
    return fmaxf(a, b);
}
DI float amax3(float m, float a, float b) {
    float r;
    asm("v_maximum3_f32 %0, %1, |%2|, |%3|" : "=v"(r) : "v"(m), "v"(a), "v"(b));
    return r;
}
DI f32x16 attn_exact(const char* kv, u32x4 q1, unsigned q2lo, int lane, int vrow) {
    const int r32 = lane & 31, h = lane >> 5;
    const f32x16 zero16 = {0, 0, 0, 0, 0, 0, 0, 0, 0, 0, 0, 0, 0, 0, 0, 0};
    const u32x2 q2 = {q2lo, 0u};
    float m = -INFINITY;
    f32x16 o = zero16;
#pragma unroll 1
    for (int t = 0; t < 64; ++t) {
        const GAS char* tp = (const GAS char*)kv + t * KV_TILE;
        const u32x4 fk1 = *(const GAS u32x4*)(tp + lane * 16);
        const u32x2 fk2 = {*(const GAS unsigned*)(tp + 1024 + r32 * 8 + h * 4), 0u};
        const u32x4 fv0 = *(const GAS u32x4*)(tp + 1280 + (h * 22 + vrow) * 16), fv1 = *(const GAS u32x4*)(tp + 1280 + VS + (h * 22 + vrow) * 16);
        f32x16 s = mfma32(fk1, q1, mfma32k8(fk2, q2, zero16));
        const float mn = fmaxf(m, halfmax(max16(s))), al = EX2(m - mn);
        m = mn;
        u32x4 p0, p1;
#pragma unroll
        for (int i = 0; i < 4; ++i) { p0[i] = pk2(EX2(s[2 * i] - m), EX2(s[2 * i + 1] - m)); p1[i] = pk2(EX2(s[8 + 2 * i] - m), EX2(s[9 + 2 * i] - m)); }
#pragma unroll
        for (int i = 0; i < 16; ++i) o[i] *= al;
        o = mfma32(fv1, p1, mfma32(fv0, p0, o));
    }
    return o;
}

DI void mma16(f32x4& c, const u32x4& a, const u32x4& b) {
    asm volatile("v_mfma_f32_16x16x32_bf16 %0, %1, %2, %0" : "+v"(c) : "v"(a), "v"(b));
}
DI void mma16z(f32x4& c, const u32x4& a, const u32x4& b) { asm volatile("v_mfma_f32_16x16x32_bf16 %0, %1, %2, 0" : "=&v"(c) : "v"(a), "v"(b)); }
DI void mma_done() { SBAR(); asm volatile("s_nop 7\n\ts_nop 1"); SBAR(); }
template <int NS> DI void zero_acc(f32x4 (&a)[NS]) {
#pragma unroll
    for (int s = 0; s < NS; ++s) { a[s] = f32x4{0, 0, 0, 0}; PIN(a[s]); }
}
template <int GS, int ASTR, int NG> DI u32x4 aload(rsrc_t rs, unsigned& v, const unsigned (&gb)[NG], int j) {
    const int o = (j % GS) * ASTR;
    PIN(v);
    return bld16(rs, v + (o & 4095), gb[j / GS] + (o & ~4095));
}
constexpr int areq(int j, int PD, int KT) {
    const int a = FIRSTFILL + 2 * j, b = PD + (j > 0 ? j - 1 : 0), c = a < b ? a : b;
    return c < KT ? c : KT;
}
template <int N> struct Int { static constexpr int v = N; };
template <int A, int B, class Fn> DI void static_for(Fn fn) { if constexpr (A < B) { fn(Int<A>{}); static_for<A + 1, B>(fn); } }
template <int N> DI void lgkm() { __builtin_amdgcn_s_waitcnt(0xC07F | (N << 8)); }
template <int K0, int NK, int KT, int F0, int FT, int NS, int NSN, int PD, int GS, int ASTR, int NA, int RING, int POOL, int NG, class WAddr, class Side>
DI void kloop(f32x4 (&acc)[2][NA], u32x4 (&act)[RING][2], u32x4 (&wf)[POOL], WAddr waddr, rsrc_t rs, const char* ws, unsigned& v0,
              unsigned& v1, const unsigned (&gb)[NG], Side side) {
    if constexpr (NK > 0) {
        constexpr int j = K0, q0 = areq(j, PD, KT), q1 = areq(j + 1, PD, KT), jc = j + 1 < KT ? j + 1 : 0, nn = NK > 1 ? NS : NSN;
        constexpr int issued = F0 + NS + POOL < FT ? F0 + NS + POOL : FT, need = F0 + NS + nn < FT ? F0 + NS + nn : FT;
        unsigned t = 0;
        u64 bad = 0;
        if (j == 0) { lgkm<(POOL > 15 ? NS : POOL - NS)>(); SBAR(); }
#pragma unroll
        for (int i = 0; i < 2 * NS; ++i) {
            const int n = F0 + (i >> 1);
            mma16(acc[i & 1][i >> 1], wf[n % POOL], act[j % RING][i & 1]);
            SBAR();
            if ((i & 1) && n + POOL < FT) wf[n % POOL] = lds16(waddr(n + POOL));
            if (!(i & 1) && q0 + i / 4 < q1) act[(q0 + i / 4) % RING][i / 2 & 1] = aload<GS, ASTR>(rs, (i & 2) ? v1 : v0, gb, q0 + i / 4);
            if (jc && !(j & 1)) {
                constexpr int jd = j + 2 < KT ? j + 2 : jc;
                if (i == (NS > 2 ? 2 * NS - 6 : 0)) {
                    t = max(max(act[jd % RING][1][0], act[jd % RING][1][2]), act[jd % RING][0][0]);
                    t = max(max(t, act[jd % RING][0][2]), act[jc % RING][1][0]); PIN(t);
                }
                if (i == (NS > 2 ? 2 * NS - 5 : 0)) {
                    t = max(max(t, act[jc % RING][1][2]), act[jc % RING][0][0]);
                    t = max(t, act[jc % RING][0][2]); PIN(t);
                }
                if (i == (NS > 2 ? 2 * NS - 4 : 1)) { bad = __ballot(t == EMPTY); asm volatile("" : "+s"(bad)); }
                if (i == 2 * NS - 2 && __builtin_expect(bad != 0, 0)) {
                    refetch(act[jc % RING][0], act[jc % RING][1], ws + gb[jc / GS] + (jc % GS) * ASTR, v0, v1);
                    refetch(act[jd % RING][0], act[jd % RING][1], ws + gb[jd / GS] + (jd % GS) * ASTR, v0, v1);
                }
            }
            side(j, i);
            if (i == 2 * NS - 1 && need > F0 + NS) { SBAR(); lgkm<(POOL > 15 && issued - need > NS ? NS : issued - need)>(); }
            SBAR();
        }
        kloop<K0 + 1, NK - 1, KT, F0 + NS, FT, NS, NSN, PD, GS, ASTR>(acc, act, wf, waddr, rs, ws, v0, v1, gb, side);
    }
}
template <int PD, int GS, int ASTR, int RING, int NG, class Probe, class Before, class After>
DI void kstart(u32x4 (&act)[RING][2], rsrc_t rs, const char* ws, unsigned& v0, unsigned& v1, const unsigned (&gb)[NG], Probe probe,
               Before before, After after) {
    before();
    SBAR();
#pragma unroll 1
    for (int spin = 0; spin < (1 << 16); ++spin) {
        const bool miss = probe();
        SBAR();
#pragma unroll
        for (int k = 0; k < FIRSTFILL; ++k) {
            act[k][0] = aload<GS, ASTR>(rs, v0, gb, k); SBAR();
            act[k][1] = aload<GS, ASTR>(rs, v1, gb, k); SBAR();
        }
        if (spin == 0) after();
        if (!__any(miss)) break;
        asm volatile("" ::: "memory");
    }
    SBAR();
    if (__builtin_expect(missing(act[0][0], act[0][1]), 0)) refetch(act[0][0], act[0][1], ws + gb[0], v0, v1);
    SBAR();
}
DI void e_bases(unsigned (&gb)[9], unsigned base, int m, int stride) {
    const int m3 = m % 3;
#pragma unroll
    for (int i = 0; i < 9; ++i) {
        const int q = i < 4 ? (i + m) & 3 : i < 6 ? 4 + ((i + (m >> 2)) & 1) : 6 + (i - 6 + m3 >= 3 ? i - 9 + m3 : i - 6 + m3);
        gb[i] = base + q * stride;
        asm volatile("" : "+s"(gb[i]));
    }
}
DI void d_bases(unsigned (&gb)[5], unsigned base, int ct, int stride) {
    const int c3 = ct % 3;
#pragma unroll
    for (int i = 0; i < 5; ++i) {
        gb[i] = base + (i < 3 ? (i + c3 >= 3 ? i + c3 - 3 : i + c3) : 3 + ((i + 1 + ct) & 1)) * stride;
        asm volatile("" : "+s"(gb[i]));
    }
}
template <int NG> DI void rot_bases(unsigned (&gb)[NG], unsigned base, int rot, int stride) {
#pragma unroll
    for (int i = 0; i < NG; ++i) { const int q = i + rot; gb[i] = base + (q >= NG ? q - NG : q) * stride; asm volatile("" : "+s"(gb[i])); }
}
DI void send_half(const f32x4 (&a)[5], unsigned dst) {
#pragma unroll
    for (int s = 0; s < 5; ++s) *(LAS f32x4*)(size_t)(dst + s * 1024) = a[s];
}
DI void recv_half(f32x4 (&a)[5], unsigned src) {
#pragma unroll
    for (int s = 0; s < 5; ++s) a[s] += ldsf4(src + s * 1024);
}
DI void ldsw16(unsigned a, const u32x4& v) { *(LAS u32x4*)(size_t)a = v; }
DI f32x4 unpk(unsigned r0, unsigned r1) { return f32x4{bflo(r0), bfhi(r0), bflo(r1), bfhi(r1)}; }
DI void res_init(f32x4 (&a)[5], const u32x4& x0, const u32x4& x1, const u32x2& x2) {
    a[0] = unpk(x0[0], x0[1]); a[1] = unpk(x0[2], x0[3]); a[2] = unpk(x1[0], x1[1]); a[3] = unpk(x1[2], x1[3]); a[4] = unpk(x2[0], x2[1]);
#pragma unroll
    for (int s = 0; s < 5; ++s) PIN(a[s]);
}
DI void res_pack(u32x4& x0, u32x4& x1, u32x2& x2, const f32x4 (&a)[5]) {
    x0 = u32x4{pk2(a[0][0], a[0][1]), pk2(a[0][2], a[0][3]), pk2(a[1][0], a[1][1]), pk2(a[1][2], a[1][3])};
    x1 = u32x4{pk2(a[2][0], a[2][1]), pk2(a[2][2], a[2][3]), pk2(a[3][0], a[3][1]), pk2(a[3][2], a[3][3])};
    x2 = u32x2{pk2(a[4][0], a[4][1]), pk2(a[4][2], a[4][3])};
}
enum { C_WKV, C_KFUSED, C_X, C_XUSED, C_WQ, C_CSP = C_WQ + 5, C_CS, C_KVST, C_KVOK, C_R1, C_XCH, C_NUM = C_XCH + 2 };
enum { E_C1, E_C2, E_C3, E_MK, P_NONE, P_C0, P_C1, P_C2, P_M0, P_LATE = P_M0 + 3, E_C0, H_PREP, H_FILL, M_LDS, M_P1, M_W0, M_W1A, M_W1, M_K8, M_T4, M_T5, G_RDY, G_USED = G_RDY + 9, M_NUM = G_USED + 5 };

__global__ __launch_bounds__(512, 2) void k_block(const char* __restrict__ x, const char* __restrict__ wq, const char* __restrict__ wk,
                                                  const char* __restrict__ wv, const char* __restrict__ wo, const char* __restrict__ w1,
                                                  const char* __restrict__ w2, char* ws, char* __restrict__ out) {
    extern __shared__ __attribute__((aligned(16))) char smem[];
    const unsigned L = (unsigned)(size_t)(LAS char*)smem;
    const int w = __builtin_amdgcn_readfirstlane(threadIdx.x >> 6);
    const int xg = blockIdx.x & 7, idx = blockIdx.x >> 3;
    const int tb = xg * 4 + (idx >> 3), m = idx & 7, cid = tb * 8 + m;
    const int ct = (m >> 2) * 8 + (tb & 1) * 4 + (m & 3);
    const rsrc_t rs = __builtin_amdgcn_make_buffer_rsrc(ws, 0, WS_END, 0x00020000);
    const u32x4 ones = {ONES2, ONES2, ONES2, ONES2};
    const f32x4 z4 = {0, 0, 0, 0};

#define LOAD_TAG() u64 tag; asm volatile("global_load_dwordx2 %0, %1, %2 sc1" : "=v"(tag) : "v"(0u), "s"(ws + WS_FL) : "memory")
#define MAKE_TAG() do { PIN(tag); \
        tag = ((u64)(unsigned)__builtin_amdgcn_readfirstlane(tag >> 32) << 32) | (unsigned)__builtin_amdgcn_readfirstlane((unsigned)tag); \
        tag = (tag ^ 0x9E3779B97F4A7C15ull) * 0xBF58476D1CE4E5B9ull + 0x632BE59BD9B4E019ull; asm volatile("" : "+s"(tag)); } while (0)

#define CNT(i) (L + L_CNT + (i) * 4)
#define CN2(i) (L + L_MISC + 512 + (i) * 4)
#define WQ_AT(r) ((r) < 2 ? L_WQ1 + (r) * 20480 : L_WQ2 + (r) % 3 * 20480)
#define WQ_ADDR() const unsigned wrow = L + (8 * g + (c16 >> 2)) * 160 + (lane & 3) * 8, wo0 = wrow + (g & 1) * 640, wo1 = wrow + ((g & 1) ^ 1) * 640
    const int rq = tb & 3;
    const int rotc = (m + tb) % 5, rotd = ct + (tb >> 1), rote = m + tb;

#define WQ_SRC() unsigned vw[5]; _Pragma("unroll") for (int i = 0; i < 5; ++i) { \
        const int ci = i * 64 + lane, pos = (ci * 6554) >> 16, ch = ci - pos * 10, k = pos ^ (((pos >> 3) & 1) << 2); \
        vw[i] = k * 1280 + ch * 16; PIN(vw[i]); } \
    auto wqload = [&](int r) {     \
        _Pragma("unroll") for (int i = 0; i < 5; ++i) glds(vw[i], wq + m * 160 + hw * 40960 + (r + rq >= 5 ? r + rq - 5 : r + rq) * 163840, L + WQ_AT(r) + hw * 5120 + i * 1024); }
    if (w < 4) {
        const int lane = lane_id();
        {
            const int wi = idx * 4 + w;
            touch(lane * 128, x + xg * 327680 + wi * 2560, L + L_JUNK, 0xfffffull);
            touch(lane * 128, ((wi & 1) ? wv : wk) + min(wi >> 1, 49) * 512, L + L_JUNK, 0xfull);
            const char* pc; int l1, l2;
            asm volatile("s_getpc_b64 %0\n.Lpf_pc:\n\ts_mov_b32 %1, .Lpf_end1-.Lpf_pc\n\ts_mov_b32 %2, .Lpf_end2-.Lpf_pc" : "=s"(pc), "=s"(l1), "=s"(l2));
            touch(lane * 128, pc + max(min(wi * 512, max(l1, l2) - 512), 0), L + L_JUNK, 0xfull);
        }
        if (w == 0 && lane < C_NUM) *(LAS unsigned*)(size_t)CNT(lane) = 0u;
        bar();
    } else {
        const int hw = w - 4;
        LANE_VARS();
        __builtin_amdgcn_s_setprio(1);
        if (m < 4) {
            const char* src = m < 2 ? wk : wv;
#pragma unroll 1
            for (int p = hw; p < 25; p += 4) glds(lane * 16, src + (p + 5 * rq >= 25 ? p + 5 * rq - 25 : p + 5 * rq) * 1024, L + L_WKV + p * 1024);
        }
        const unsigned vx0 = (lane >> 3) * 1280 + (((lane & 7) ^ g) << 4), vx1 = (8 + (lane >> 3)) * 1280 + (((lane & 7) ^ (4 + g)) << 4);
        const char* xb = x + (size_t)(tb * 64 + hw * 16) * 1280;
        int sg = m;
        auto xload = [&](int n) {
#pragma unroll 1
            for (int i = 0; i < n; ++i) {
                const int sp = sg - 2 * rq < 0 ? sg - 2 * rq + 10 : sg - 2 * rq;
                glds(vx0, xb + sg * 128, L + L_X + (hw * 20 + sp) * 1024);
                glds(vx1, xb + sg * 128, L + L_X + (hw * 20 + 10 + sp) * 1024);
                sg = sg == 9 ? 0 : sg + 1;
            }
        };
        xload(3);
        WQ_SRC();
        bar_raw();
        wait_vm<6>();
        cnt_add(CNT(C_WKV));
        xload(7);
        wqload(0);
        wait_vm<5>();
        cnt_add(CNT(C_X));
        wqload(1);
    }
    int role = w;
    asm volatile("" : "+s"(role));
    if (role >= 4) {
        const int hw = w - 4;
        LANE_VARS();
        u64 f0 = 0, tag;
        const int c0 = tb >> 3;
        {
            WQ_SRC();
            WQ_ADDR();
            f32x4 cs[5];
            u32x4 one = ones;
            auto wqsum = [&](int r) {
                u32x4 f[5];
#pragma unroll
                for (int s = 0; s < 5; ++s) f[s] = ldstr2(wo0 + WQ_AT(r) + hw * 5120 + s * 32, wo1 + WQ_AT(r) + hw * 5120 + s * 32);
#pragma unroll
                for (int s = 0; s < 5; ++s) mma16(cs[s], f[s], one);
            };
            PIN(one);
            zero_acc(cs);
            cnt_spin(CNT(C_KFUSED), 4);
            wqload(2); wait_vm<10>(); cnt_add(CNT(C_WQ));
            cnt_spin(CNT(C_XUSED), 4);
            wqload(3); wait_vm<10>(); cnt_add(CNT(C_WQ + 1));
            asm volatile("global_load_dwordx2 %0, %1, %2 sc1" : "=v"(tag) : "v"(0u), "s"(ws + WS_FL) : "memory");
            wqload(4);
            wqsum(0); wqsum(1);
            wait_vm<11>(); cnt_add(CNT(C_WQ + 2)); wqsum(2);
            wait_vm<6>(); cnt_add(CNT(C_WQ + 3)); wqsum(3);
            wait_vm<0>(); cnt_add(CNT(C_WQ + 4));
            MAKE_TAG();
            if (hw == 0) f0 = __hip_atomic_load((const GAS u64*)(ws + FL_KV + (c0 * 32 + (lane & 31)) * 128), __ATOMIC_RELAXED, __HIP_MEMORY_SCOPE_AGENT);
            wqsum(4);
            mma_done();
            if (c16 == 0) {
#pragma unroll
                for (int s = 0; s < 5; ++s) *(LAS f32x4*)(size_t)(L + L_CSX + hw * 320 + s * 64 + g * 16) = cs[s];
            }
            if (cnt_arrive(CNT(C_CSP)) == 3) {
                const unsigned ca = L + L_CSX + (lane < 20 ? lane : 0) * 16;
                const f32x4 c4 = (ldsf4(ca) + ldsf4(ca + 320)) + (ldsf4(ca + 640) + ldsf4(ca + 960));
                *(LAS f32x4*)(size_t)(ca + 1280) = c4;
                cnt_add(CNT(C_CS));
            }
        }
        const char* kv = ws + WS_KV;
        u64 p0 = 0, p1 = 0, p2 = 0;
        if (hw == 0) {
            if (!__all(f0 == tag)) poll_fast(ws + FL_KV, (c0 * 32 + (lane & 31)) * 128, tag, p0, p1, p2);
            cnt_add(CNT(C_KVOK));
        } else cnt_wait<1>(CNT(C_KVOK), 1);
        copy_dev(kv + c0 * CHUNK, L, 42, hw, lane);
        u64 f1;
        asm volatile("global_load_dwordx2 %0, %1, %2 sc1" : "=v"(f1) : "v"(((c0 * 32 + 32 + lane) & 127) * 128), "s"(ws + FL_KV) : "memory");
        wait_vm<8>();
        __builtin_amdgcn_s_setprio(2);
        bar_raw();
        wait_vm<0>();
        cnt_add(CN2(E_C0));
        PIN(f1); PIN(p0); PIN(p1); PIN(p2);
        poll(ws, FL_KV, f1 != tag ? (c0 * 32 + 32 + lane) & 127 : -1, tag);
        copy_dev(kv + ((c0 + 1) & 3) * CHUNK, L + CHUNK, 42, hw, lane);
        {
            int u = blockIdx.x * 4 + hw;
            const int pmat = u < 160 ? 0 : (u < 480 ? 1 : (u < 912 ? 2 : 3));
            u = pmat == 0 ? u : pmat == 1 ? u - 160 : pmat == 2 ? u - 480 : 0;
            const int pc = pmat < 2 ? u / 20 : u / 54, ks = u - pc * (pmat < 2 ? 20 : 54);
            int rb, a3, a2, a1;
            if (pmat == 0) { if (ks < 16) { rb = 40 * ks; a3 = 20; a2 = 4; a1 = 8; } else { rb = 160 * (ks - 16) + 16; a3 = 80; a2 = 40; a1 = 20; } }
            else if (pmat == 1) { rb = ks < 8 ? 80 * ks : ks < 12 ? 160 * (ks - 8) + 64 : 80 * (ks - 12) + 32; a3 = ks >= 8 && ks < 12 ? 80 : 16; a2 = 8; a1 = 4; }
            else if (ks >= 16 && ks < 24) { rb = 224 * (ks - 16) + 96; a3 = ks < 23 ? 112 : 48; a2 = 8; a1 = 4; }
            else { rb = ks < 16 ? 112 * ks : (ks < 39 ? 112 * (ks - 24) + 32 : 112 * (ks - 39) + 64); a3 = 8; a2 = 4; a1 = 16; }
            const int ld = pmat == 1 ? 3456 : 1280, chmax = pmat != 1 ? 9 : (pc == 15 ? 5 : 13);
            const char* psrc = pmat == 0 ? wo + pc * 160 : pmat == 1 ? w1 + pc * 224 : w2 + pc * 160;
            const unsigned pb = L + 3 * CHUNK + hw * 8192, pv = g * ld + min(c16, chmax) * 16;
            cnt_wait(CN2(P_NONE), 4);
#pragma unroll 1
            for (int i = 0; i < 8; ++i) glds(pv, psrc + (rb + a3 * (i >> 2) + a2 * ((i >> 1) & 1) + a1 * (i & 1)) * ld, pb + i * 1024);
            wait_vm<8>();
            cnt_add(CN2(E_C1));
            copy_dev(kv + ((c0 + 2) & 3) * CHUNK, L + 2 * CHUNK, 42, hw, lane);
            wait_vm<10>();
            const unsigned a0 = pb + (8 * g + (c16 >> 2)) * 256 + (lane & 3) * 8;
            if (pmat == 1) {
                const unsigned pdst = WS_W1 + pc * 143360 + ks * 5120, pd2 = WS_W1 + pc * 143360 + 102400 + ks * 2048;
                const unsigned cv = c16 == 0 ? g * 16 : 0x80000000u;
#pragma unroll
                for (int s = 0; s < 7; ++s) {
                    const unsigned a1 = a0 + (s == 6 ? (pc == 15 ? 64 : 192) : s * 32);
                    const u32x4 f = ldstr2(a1, a1 + 1024);
                    bst16(rs, f, lane * 16, s < 2 ? pdst + s * 1024 : s < 4 ? pdst + (s + 1) * 1024 : s < 6 ? pd2 + (s - 4) * 1024 : pdst + 2048);
                    bst16(rs, __builtin_bit_cast(u32x4, mfma16(f, ones, z4)), cv, WS_CS + (ks * 1792 + pc * 112) * 4 + s * 64);
                }
            } else if (pmat < 3) {
                const unsigned a1 = a0 + (lane & 3) * 8, pdst = pmat == 0 ? WS_WO + pc * 102400 + ks * 3072 : WS_W2 + u * 5120;
                const unsigned pd2 = pmat == 0 ? WS_WO + pc * 102400 + 61440 + ks * 2048 : pdst + 2048, pd4 = pdst + (pmat == 0 ? 2048 : 4096);
#pragma unroll
                for (int s = 0; s < 5; ++s) {
                    const unsigned a2 = s < 4 ? a1 + (s >> 1) * 64 + (s & 1) * 8 : a0 + 128;
                    bst16(rs, ldstr2(a2, a2 + 1024), lane * 16, s < 2 ? pdst + s * 1024 : s < 4 ? pd2 + (s - 2) * 1024 : pd4);
                }
            }
        }
        wait_vm<0>();
        cnt_add(CN2(E_C2));
        if (cnt_arrive(CN2(H_PREP)) == 3 && lane == 0) set_flag(ws, FL_PR + cid * 128, tag);
        cnt_wait<3>(CN2(H_PREP), 4);
        {
            const u32x4 e = {EMPTY, EMPTY, EMPTY, EMPTY};
#pragma unroll 1
            for (int p = hw; p < 44; p += 4) bst16(rs, e, lane * 16, WS_O + blockIdx.x * 48640 + p * 1024);
            bst16(rs, e, hw < 3 || lane < 32 ? lane * 16 : 0x80000000u, WS_O + blockIdx.x * 48640 + (44 + hw) * 1024);
        }
        poll(ws, FL_KV, (c0 * 32 + 96 + (lane & 31)) & 127, tag);
        wait_vm<0>();
        if (cnt_arrive(CN2(H_FILL)) == 3 && lane == 0) set_flag(ws, FL_MK + cid * 128, tag);
        cnt_wait<10>(CN2(P_C0), 4);
        copy_dev(kv + ((c0 + 3) & 3) * CHUNK, L, 42, hw, lane);
        poll(ws, FL_PR, hw * 64 + lane, tag);
        wait_vm<0>();
        cnt_add(CN2(E_C3));
        cnt_wait<3>(CN2(E_C3), 4);
        if (blockIdx.x == 0 && hw == 1 && lane == 0) set_flag(ws, WS_FL, tag);
        auto wocopy = [&](int p0, int p1) {
#pragma unroll 1
            for (int p = p0 + hw; p < p1; p += 4) {
                const int q = p + rotc * (p < 60 ? 12 : 8), r = p < 60 ? (q >= 60 ? q - 60 : q) : (q >= 100 ? q - 40 : q);
                glds_dev(lane * 16, ws + WS_WO + m * 102400 + r * 1024, L + L_WO + p * 1024);
            }
        };
        u32x4 r1[35], r2[9][8];
        unsigned g1[5], g1b[5], g2[9];
        d_bases(g1, WS_W1 + ct * 143360, rotd, 20480);
        d_bases(g1b, WS_W1 + ct * 143360 + 102400, rotd, 8192);
        e_bases(g2, WS_W2 + m * 276480, rote, 30720);
        const unsigned vp = lane * 16 + hw * 1024, vq = lane * 16 + min(28 + hw, 29) * 1024;
        unsigned lp0 = L + vp, lp1 = lp0 + 61440, lp2 = lp0 + 122880, lq0 = L + vq, lq1 = lq0 + 61440, lq2 = lq0 + 122880;
        PIN(lp0); PIN(lp1); PIN(lp2); PIN(lq0); PIN(lq1); PIN(lq2);
#define LD2(G, THR) do { _Pragma("unroll") for (int j_ = 0; j_ < 7; ++j_) { r2[G][j_] = bld16(rs, vp, g2[G] + j_ * 4096); wait_vm<THR>(); } \
        r2[G][7] = bld16(rs, vq, g2[G]); wait_vm<THR>(); } while (0)
#define ST2E(G, j) do { constexpr int sl_ = (G) % 5; \
        ldsw16((j) < 7 ? (sl_ < 2 ? lp0 : sl_ < 4 ? lp1 : lp2) + (sl_ & 1) * 30720 + (j) * 4096 : (sl_ < 2 ? lq0 : sl_ < 4 ? lq1 : lq2) + (sl_ & 1) * 30720, r2[G][j]); } while (0)
#pragma unroll
        for (int i = 0; i < 35; ++i) r1[i] = bld16(rs, vp, i < 25 ? g1[i / 5] + (i % 5) * 4096 : g1b[(i - 25) / 2] + ((i - 25) & 1) * 4096);
        SBAR();
        if (hw == 3) {
            u32x4 c[10];
            const unsigned cv = min(lane & 31, 27) * 16 + (lane >> 5) * 71680;
#pragma unroll
            for (int ks = 0; ks < 10; ++ks) c[ks] = bld16(rs, cv, WS_CS + (ks * 1792 + ct * 112) * 4);
            f32x4 c4 = z4;
#pragma unroll
            for (int ks = 0; ks < 10; ++ks) c4 += __builtin_bit_cast(f32x4, c[ks]);
#pragma unroll
            for (int i = 0; i < 4; ++i) c4[i] += upper(c4[i]);
            if (lane < 28) *(LAS f32x4*)(size_t)(L + L_MISC + lane * 16) = c4;
        }
        LD2(6, 63); LD2(7, 63); LD2(8, 63);
        cnt_wait<10>(CN2(P_C1), 4);
        wocopy(0, 42);
        poll(ws, FL_MK, hw * 64 + lane, tag);
        cnt_add(CN2(E_MK));
        cnt_wait<10>(CN2(P_C2), 4);
        wocopy(42, 100);
        wait_vm<0>();
#pragma unroll 1
        for (int i = 0; i < 12; i += 3) {
            if ((unsigned)__builtin_amdgcn_readfirstlane(*(volatile LAS unsigned*)(size_t)CN2(P_LATE)) != 0) break;
#pragma unroll
            for (int k = 0; k < 3; ++k) glds_dev(lane * 16, ws + WS_W2 + m * 276480 + (((tb & 3) * 4 + hw) * 12 + i + k) * 1024, L + L_JUNK2 + hw * 1024);
            wait_vm<3>();
        }
        __builtin_amdgcn_s_setprio(0);
        bar_raw();
#pragma unroll
        for (int i = 0; i < 10; ++i) ldsw16(lp0 + i * 4096, r1[i < 4 ? 25 + i : i - 4]);
        cnt_add(CN2(M_W0));
        cnt_wait(CN2(M_P1), 4);
#pragma unroll
        for (int i = 10; i < 35; ++i) {
            if (i == 25) cnt_wait(CN2(M_LDS), 4);
            ldsw16((i < 15 ? lp0 : i < 30 ? lp1 : lp2) + (i % 15) * 4096, r1[i < 29 ? i - 4 : i]);
            SBAR();
            if (i == 17) cnt_add(CN2(M_W1A));
            if (i >= 18 && i - 18 < NEARLY) {
                const int e = i - 18, G = 3 + (e >> 3), j = e & 7;
                r2[G][j] = bld16(rs, j < 7 ? vp : vq, g2[G] + (j < 7 ? j * 4096 : 0));
                SBAR();
            }
        }
        cnt_add(CN2(M_W1));
#pragma unroll
        for (int e = NEARLY; e < 24; ++e) {
            const int G = 3 + (e >> 3), j = e & 7;
            r2[G][j] = bld16(rs, j < 7 ? vp : vq, g2[G] + (j < 7 ? j * 4096 : 0));
            wait_vm<THROT>();
        }
        cnt_wait<3>(CN2(M_K8), 4);
#pragma unroll 1
        for (int p = hw; p < 60; p += 4) { glds_dev(lane * 16, ws + (p < 30 ? g2[0] + p * 1024 : g2[1] + (p - 30) * 1024), L + p * 1024); wait_vm<THROT>(); }
        wait_vm<0>();
        cnt_add(CN2(G_RDY + 1));
        cnt_wait<3>(CN2(M_T4), 4);
        copy_dev(ws + g2[2], L + 61440, 30, hw, lane);
        wait_vm<0>();
        cnt_add(CN2(G_RDY + 2));
        static_for<3, 9>([&](auto gg) {
            constexpr int G = gg.v;
            if (G == 3) cnt_wait<2>(CN2(M_T5), 4);
            if (G >= 5) cnt_wait<2>(CN2(G_USED + G - 5), 4);
#pragma unroll
            for (int j = 0; j < 8; ++j) { ST2E(G, j); SBAR(); }
            cnt_add(CN2(G_RDY + G));
        });
        asm volatile(".Lpf_end2:");
        __builtin_amdgcn_endpgm();
    }

    u32x4 xr0, xr1; u32x2 xr2;
    u32x4 qa1, qb1; u32x2 qa2, qb2;
    const int kh = w & 1;
    {
        LANE_VARS();
        LOAD_TAG();
        u32x4 xf[20], xo[10], kf[20];
        const unsigned xrow = L + L_X + w * 20480 + (c16 >> 3) * 10240 + (c16 & 7) * 128;
        const int sw = c16 >> 1, t32 = (w & 1) * 16 + c16;
        unsigned xa = xrow + (((kh * 4 + g) ^ sw) << 4), xb = xrow + ((((kh ^ 1) * 4 + g) ^ sw) << 4), xc = xa + ((w ^ 1) - w) * 20480, xra[3], qw[5], kvo;
        {
            auto rotx = [&](int c) { c -= 16 * rq; return c < 0 ? c + 80 : c; };
            const int c0 = rotx(m * 10 + g), c1 = rotx(m * 10 + 4 + g), c2 = rotx(m * 10 + 8 + (g >> 1));
            xra[0] = xrow + (c0 >> 3) * 1024 + (((c0 & 7) ^ sw) << 4);
            xra[1] = xrow + (c1 >> 3) * 1024 + (((c1 & 7) ^ sw) << 4);
            xra[2] = xrow + (c2 >> 3) * 1024 + (((c2 & 7) ^ sw) << 4) + (g & 1) * 8;
            const int d = (m & 1) * 16 + c16;
            kvo = m == 0 ? ((g >> 1) * 32 + t32) * 16 + (g & 1) * 8
                : m == 1 ? (g == 0 ? 1024 + t32 * 8 : 0x80000000u)
                         : (d <= 21 ? 1280 + (((w & 1) * 2 + (g & 1)) * 22 + d) * 16 + (g >> 1) * 8 : 0x80000000u);
            PIN(xa); PIN(xb); PIN(xc); PIN(xra[0]); PIN(xra[1]); PIN(xra[2]); PIN(kvo);
        }
#pragma unroll
        for (int s = 0; s < 5; ++s) {
            const int f0 = s * 16 + 4 * g, hh = f0 / 20, d0 = f0 - hh * 20;
            qw[s] = L + L_QX + (hh * 2 + (w >> 1)) * 1280 + (d0 < 16 ? ((d0 >> 3) * 32 + t32) * 16 + (d0 & 4) * 2 : 1024 + t32 * 8);
            PIN(qw[s]);
        }
        WQ_ADDR();
        unsigned b0 = wo0 + L_WQ1 + kh * 5120, b1 = wo1 + L_WQ1 + kh * 5120;
        PIN(b0); PIN(b1);
        wait_vm<0>();
        MAKE_TAG();
        f32x4 ka = z4, kc = z4, ssm = z4, ssq = z4, acc[2][5];
        u32x4 one = ones;
        PIN(ssm); PIN(ssq); PIN(one);
        if (m < 4) {
            cnt_wait(CNT(C_WKV), 4);
            const unsigned a0 = L + L_WKV + (8 * g + (c16 >> 2)) * 40 + (m & 1) * 32 + (lane & 3) * 8;
            const unsigned a1 = a0 + kh * 1280, a2 = a0 + (kh ^ 1) * 1280;
#pragma unroll
            for (int j = 0; j < 10; ++j) { kf[j] = ldstr2(a1 + j * 2560, a1 + j * 2560 + 160); kf[10 + j] = ldstr2(a2 + j * 2560, a2 + j * 2560 + 160); }
            if (m < 2) {
#pragma unroll
                for (int j = 0; j < 20; ++j) kc = mfma16(kf[j], ones, kc);
            } else {
#pragma unroll
                for (int j = 0; j < 20; ++j) kc = mfma16(ones, kf[j], kc);
            }
            PIN(kc);
        }
        cnt_add(CNT(C_KFUSED));
        cnt_spin(CNT(C_X), 4);
#pragma unroll
        for (int j = 0; j < 10; ++j) xf[j] = lds16(xa + j * 1024);
#pragma unroll
        for (int j = 0; j < 10; ++j) xf[10 + j] = lds16(xb + j * 1024);
#pragma unroll
        for (int j = 0; j < 10; ++j) xo[j] = lds16(xc + j * 1024);
        xr0 = lds16(xra[0]); xr1 = lds16(xra[1]); xr2 = lds8(xra[2]);
        SBAR();
        if (m < 2) {
#pragma unroll
            for (int j = 0; j < 20; ++j) ka = mfma16(kf[j], xf[j], ka);
        } else if (m < 4) {
#pragma unroll
            for (int j = 0; j < 20; ++j) ka = mfma16(xf[j], kf[j], ka);
        }
        PIN(ka);
#pragma unroll
        for (int j = 0; j < 20; ++j) {
            mma16(ssm, one, xf[j]); mma16(ssq, xf[j], xf[j]);
            if (j == 4) {
#pragma unroll
                for (int i = 0; i < 10; ++i) { PIN(xf[i]); PIN(xf[10 + i]); PIN(xo[i]); }
                PIN(xr0); PIN(xr1); PIN(xr2);
                cnt_add(CNT(C_XUSED));
            }
        }
        mma_done();
        const float sd = (c16 & 2) ? ((c16 & 1) ? ssq[3] : ssq[2]) : ((c16 & 1) ? ssq[1] : ssq[0]);
        const float mean = ssm[0] * (1.f / D), rstd = __builtin_amdgcn_rsqf(fmaxf(__shfl(sd, (c16 >> 2) * 16 + c16) * (1.f / D) - mean * mean, 0.f) + LN_EPS);
        if (m < 4) {
            u32x2 pv;
            if (m < 2) {
                pv = u32x2{pk2((ka[0] - mean * kc[0]) * rstd, (ka[1] - mean * kc[1]) * rstd), pk2((ka[2] - mean * kc[2]) * rstd, (ka[3] - mean * kc[3]) * rstd)};
            } else {
                float v[4];
#pragma unroll
                for (int r = 0; r < 4; ++r) v[r] = (ka[r] - __shfl(mean, 4 * g + r) * kc[0]) * __shfl(rstd, 4 * g + r);
                pv = u32x2{pk2(v[0], v[1]), pk2(v[2], v[3])};
                if (m == 3 && c16 >= 4) pv = c16 == 4 ? u32x2{ONES2, ONES2} : u32x2{0u, 0u};
            }
            bst8(rs, pv, kvo, WS_KV + (tb * 2 + (w >> 1)) * KV_TILE);
        }
        SBAR();
        u32x2 f[2][5][2];
        unsigned xs, xv;
        float sc = rstd * QSCALE, ms = -mean * sc;
        PIN(sc); PIN(ms);
        unsigned b2 = b0 + (L_WQ2 - L_WQ1), b3 = b1 + (L_WQ2 - L_WQ1), cq = CNT(0), cv = 0;
        PIN(b2); PIN(b3); PIN(cq);
        auto qhalf = [&](int j, int s, int e) { return ldstr((j < 4 ? (e ? b1 : b0) + j * 10240 : (e ? b3 : b2) + j % 6 * 10240) + s * 32); };
        auto kvflag = [&] {
            if (m < 4) {
                wait_vm<0>();
                if (cnt_arrive(CNT(C_KVST)) == 3 && lane == 0) set_flag(ws, FL_KV + (tb * 4 + m) * 128, tag);
            }
        };
        cnt_spin(CNT(C_WQ), 4);
#pragma unroll
        for (int s = 0; s < 5; ++s) { f[0][s][0] = qhalf(0, s, 0); f[0][s][1] = qhalf(0, s, 1); }
        SBAR();
#pragma unroll
        for (int j = 0; j < 10; ++j) {
            if (j < 8 && !(j & 1)) cv = *(volatile LAS unsigned*)(size_t)(cq + (C_WQ + j / 2 + 1) * 4);
            if (j < 9 && (j & 1) && __builtin_expect((unsigned)__builtin_amdgcn_readfirstlane(cv) < 4u, 0)) cnt_spin(CNT(C_WQ + j / 2 + 1), 4);
            asm volatile("" ::: "memory");
            SBAR();
#pragma unroll
            for (int s = 0; s < 5; ++s) {
                const u32x4 fr = {f[j & 1][s][0][0], f[j & 1][s][0][1], f[j & 1][s][1][0], f[j & 1][s][1][1]};
                if (j == 0) mma16z(acc[0][s], fr, xf[j]); else mma16(acc[0][s], fr, xf[j]);
                SBAR();
                if (j < 9) f[(j + 1) & 1][s][0] = qhalf(j + 1, s, 0);
                SBAR();
                if (j == 0) mma16z(acc[1][s], fr, xo[j]); else mma16(acc[1][s], fr, xo[j]);
                SBAR();
                if (j < 9) f[(j + 1) & 1][s][1] = qhalf(j + 1, s, 1);
                SBAR();
            }
            if (j == 0) kvflag();
            if (j == 3) asm volatile("s_mov_b64 exec, 1\n\tds_add_u32 %0, %1 offset:%2\n\ts_mov_b64 exec, -1" ::"v"(cq), "v"(1u), "i"(C_R1 * 4) : "memory");
            if (j == 5) { xs = L + L_WQ1 + (w ^ 1) * 5120 + lane * 16; xv = L + L_WQ1 + w * 5120 + lane * 16; PIN(xs); PIN(xv); }
            if (j == 7) cnt_spin(CNT(C_R1), 4);
        }
        mma_done();
        send_half(acc[1], xs);
        cnt_add(CNT(C_XCH + (w >> 1)));
        if (w == 0 && lane < M_NUM) *(LAS unsigned*)(size_t)CN2(lane) = 0u;
        f32x4 c5[5], p5[5];
#pragma unroll 1
        for (;;) {
            const unsigned n1 = *(volatile LAS unsigned*)(size_t)CNT(C_CS), n2 = *(volatile LAS unsigned*)(size_t)CNT(C_XCH + (w >> 1));
#pragma unroll
            for (int s = 0; s < 5; ++s) {
                c5[s] = *(volatile LAS f32x4*)(size_t)(L + L_CSX + 1280 + s * 64 + g * 16);
                p5[s] = *(volatile LAS f32x4*)(size_t)(xv + s * 1024);
            }
            if (__builtin_amdgcn_readfirstlane(n1 + n2) == 3) break;
        }
#pragma unroll
        for (int s = 0; s < 5; ++s) {
            const f32x4 v = (acc[0][s] + p5[s]) * sc + c5[s] * ms;
            *(LAS u32x2*)(size_t)qw[s] = u32x2{pk2(v[0], v[1]), pk2(v[2], v[3])};
        }
        bar();
        const int r32 = lane & 31, h = lane >> 5;
        const unsigned qb = L + L_QX + ((w & 1) * 4 + (w >> 1)) * 1280;
        qa1 = lds16(qb + lane * 16); qb1 = lds16(qb + 2560 + lane * 16);
        qa2 = u32x2{lds4(qb + 1024 + r32 * 8 + h * 4), 0u}; qb2 = u32x2{lds4(qb + 2560 + 1024 + r32 * 8 + h * 4), 0u};
    }
    {
        LANE_VARS();
        const int r32 = lane & 31, h = lane >> 5, vrow = min(r32, 21);
        const unsigned kconst = h == 0 ? 0x00003f80u : 0u;
        unsigned ak1 = L + lane * 16, ak2 = L + 1024 + r32 * 8 + h * 4, av = L + 1280 + (h * 22 + vrow) * 16;
        const f32x16 zero16 = {0, 0, 0, 0, 0, 0, 0, 0, 0, 0, 0, 0, 0, 0, 0, 0};
        f32x16 sa, sb, oa = zero16, ob = zero16;
        u32x4 ka1 = lds16(ak1), kb1 = lds16(ak1 + KV_TILE), va0 = lds16(av), va1 = lds16(av + VS), vb0 = va0, vb1 = va0;
        u32x2 ka2 = {lds4(ak2), kconst}, kb2 = {lds4(ak2 + KV_TILE), kconst};
        sa = mfma32(ka1, qa1, mfma32k8(ka2, qa2, zero16));
        sb = mfma32(ka1, qb1, mfma32k8(ka2, qb2, zero16));
        {
            const float ma = halfmax(max16(sa)), mb = halfmax(max16(sb));
            qa2[1] = h == 0 ? (pk2(-ma, 0.f) & 0xffffu) : 0u;
            qb2[1] = h == 0 ? (pk2(-mb, 0.f) & 0xffffu) : 0u;
        }
        sa = mfma32(ka1, qa1, mfma32k8(ka2, qa2, zero16));
        u32x4 pa0 = {0u, 0u, 0u, 0u}, pa1 = pa0, pb0 = pa0, pb1 = pa0;
        float e2 = 0.f, e3 = 0.f;
        unsigned vo0 = (r32 >> 4) * 20480 + h * 256 + (r32 & 15) * 16, vo1 = h ? 0x80000000u : vo0;
        PIN(pa0); PIN(pa1); PIN(pb0); PIN(pb1); PIN(ka2); PIN(kb2); PIN(vo0); PIN(vo1);
#define SLOT(R, MF, X, S, i, PP, jp, P, j) do { R = MF; PIN(R); SBAR(); X; SBAR(); \
        float e0 = EX2(S[i]), e1 = EX2(S[i + 1]); PIN(e0); PIN(e1); SBAR(); \
        PP[jp] = pk2(e2, e3); PIN(PP); SBAR(); \
        e2 = EX2(S[i + 2]); e3 = EX2(S[i + 3]); PIN(e2); PIN(e3); SBAR(); \
        P[j] = pk2(e0, e1); PIN(P); SBAR(); } while (0)
#define STEP(KC1, KC2, KN1, KN2, VC0, VC1, VP0, VP1, OK, OV, X0, X7) do { \
        SLOT(sb, mfma32k8(KC2, qb2, zero16), X0, sa, 0, pb1, 3, pa0, 0); \
        SLOT(sb, mfma32(KC1, qb1, sb), __builtin_amdgcn_s_waitcnt(0xC07F), sa, 4, pa0, 1, pa0, 2); \
        SLOT(ob, mfma32(VP1, pb1, ob), KC1 = lds16(ak1 + (OK) * KV_TILE), sa, 8, pa0, 3, pa1, 0); \
        SLOT(oa, mfma32(VC0, pa0, oa), KC2[0] = lds4(ak2 + (OK) * KV_TILE), sa, 12, pa1, 1, pa1, 2); \
        SLOT(sa, mfma32k8(KN2, qa2, zero16), (void)0, sb, 0, pa1, 3, pb0, 0); \
        SLOT(sa, mfma32(KN1, qa1, sa), VP0 = lds16(av + (OV) * KV_TILE), sb, 4, pb0, 1, pb0, 2); \
        SLOT(oa, mfma32(VC1, pa1, oa), VP1 = lds16(av + (OV) * KV_TILE + VS), sb, 8, pb0, 3, pb1, 0); \
        SLOT(ob, mfma32(VC0, pb0, ob), X7, sb, 12, pb1, 1, pb1, 2); } while (0)
        unsigned so1 = WS_O + (tb * 4 + (w & 2)) * 20480, so0 = so1 + (2 * m + (w & 1)) * 1024;
        so1 += 16384 + (2 * m + (w & 1)) * 256;
        asm volatile("" : "+s"(so0), "+s"(so1));
        unsigned ea = CN2(E_C1), e0a = CN2(E_C0), ev = 0, one = 1;
        PIN(ea); PIN(e0a); PIN(one); PIN(ak1); PIN(ak2); PIN(av);
        auto hook0 = [&](int j, int dn) {
            if (j == 1) ev = *(volatile LAS unsigned*)(size_t)e0a;
            if (j == 14) { ak1 += dn; ak2 += dn; PIN(ak1); PIN(ak2); }
            if (j == 15) { av += dn; ea += 4; PIN(av); PIN(ea); }
        };
        auto hook7 = [&](int j) {
            if (j == 0)
                asm volatile("s_mov_b64 exec, 1\n\tds_add_u32 %0, %1 offset:%2\n\ts_mov_b64 exec, -1" ::"v"(ea), "v"(one), "i"((P_NONE - E_C1) * 4) : "memory");
            if (j == 1 && __builtin_expect((unsigned)__builtin_amdgcn_readfirstlane(ev) < 4u, 0)) cnt_spin(e0a, 4);
            if (j == 11) asm volatile("s_mov_b64 exec, 1\n\tds_add_u32 %0, %1 offset:%2\n\ts_mov_b64 exec, -1" ::"v"(ea), "v"(one), "i"((P_M0 - E_C1) * 4) : "memory");
            if (j == 9) ev = *(volatile LAS unsigned*)(size_t)ea;
            if (j == 10 && __builtin_expect((unsigned)__builtin_amdgcn_readfirstlane(ev) < 4u, 0))
                cnt_wait(ea, 4);
        };
#pragma unroll 1
        for (int c = 0; c < 4; ++c) {
            const int dn = c == 2 ? -2 * CHUNK : CHUNK;
#pragma unroll
            for (int j = 0; j < 16; j += 2) {
                STEP(ka1, ka2, kb1, kb2, va0, va1, vb0, vb1, (j + 2) & 15, j + 1, hook0(j, dn), hook7(j));
                STEP(kb1, kb2, ka1, ka2, vb0, vb1, va0, va1, (j + 3) & 15, (j + 2) & 15, hook0(j + 1, dn), hook7(j + 1));
            }
        }
        pb1[3] = pk2(e2, e3);
        ob = mfma32(vb1, pb1, ob);
        u32x4 r0, r1, r2;
        auto finish = [&](const f32x16& pa, const f32x16& pb, float la, float lb) {
            const float ra = __builtin_amdgcn_rcpf(la), rb = __builtin_amdgcn_rcpf(lb);
            r0 = u32x4{keep(pk2(pa[0] * ra, pa[1] * ra)), pk2(pa[2] * ra, pa[3] * ra), keep(pk2(pa[4] * ra, pa[5] * ra)), pk2(pa[6] * ra, pa[7] * ra)};
            r1 = u32x4{keep(pk2(pb[0] * rb, pb[1] * rb)), pk2(pb[2] * rb, pb[3] * rb), keep(pk2(pb[4] * rb, pb[5] * rb)), pk2(pb[6] * rb, pb[7] * rb)};
            r2 = u32x4{keep(pk2(pa[8] * ra, pa[9] * ra)), pk2(pa[10] * ra, pa[11] * ra), keep(pk2(pb[8] * rb, pb[9] * rb)), pk2(pb[10] * rb, pb[11] * rb)};
        };
        {
            float am = 0.f;
#pragma unroll
            for (int i = 0; i < 12; i += 2) am = amax3(am, oa[i], oa[i + 1]);
            const float la = upper(oa[8]);
            asm volatile("s_nop 11" : "+v"(ob));
            const float lb = upper(ob[8]);
#pragma unroll
            for (int i = 0; i < 12; i += 2) am = amax3(am, ob[i], ob[i + 1]);
            finish(oa, ob, la, lb);
            const bool ok = (fminf(la, lb) >= 1e-18f) & (fmaxf(la, lb) <= 1e30f) & (am <= 1e37f);
            if (__builtin_expect(__ballot(!ok) != 0, 0)) {
                __builtin_amdgcn_fence(__ATOMIC_ACQUIRE, "agent");
                f32x16 xa, xb;
#pragma unroll 1
                for (int qt = 0; qt < 2; ++qt) {
                    xb = attn_exact(ws + WS_KV, qt ? qb1 : qa1, qt ? qb2[0] : qa2[0], lane, vrow);
                    if (qt == 0) xa = xb;
                }
                finish(xa, xb, upper(xa[8]), upper(xb[8]));
            }
        }
        bst16(rs, r0, vo0, so0);
        bst16(rs, r1, vo0 + 512, so0);
        bst16(rs, r2, vo1, so1);
    }
    u32x4 wd[12], ae[RE][2], we[PE];
    unsigned ge[9], e0, e1, ehv, eprobe, a0;
    const unsigned ehs = WS_F + (tb * 4 + (w & 2)) * 55296 + 16384;
    {
        LANE_VARS();
        const unsigned wl = L + lane * 16;
        f32x4 acc[5];
        u32x4 act[10][2], wf[PC];
        unsigned wa = wl + L_WO, wb = wa + 51200, gb[5];
        rot_bases(gb, WS_O + (tb * 4 + w) * 20480, rotc, 4096);
        unsigned v0 = lane * 16, v1 = v0 + 1024, hv = c16 * 256;
        unsigned sv = g == 0 ? c16 * 8 : 0x80000000u, h2v = (g & 1) ? 0x80000000u : (g >> 1) * 256 + c16 * 16;
        PIN(v0); PIN(v1); PIN(hv);
        auto sub = [](int n) { return n < 60 ? (n % 3 == 2 ? 4 : n % 3) : 2 + (n & 1); };
        auto kst = [](int n) { return n < 60 ? n / 3 : (n - 60) / 2; };
        auto wac = [&](int n) { return (n < 50 ? wa : wb) + (n % 50) * 1024; };
        kstart<RC, 2, 2048>(act, rs, ws, v0, v1, gb,
                           [&] { return __builtin_amdgcn_raw_buffer_load_b32(rs, hv, WS_O + (tb * 4 + w) * 20480 + 16384, SC1) == EMPTY; }, [&] {
            bar();
            PIN(wa); PIN(wb); PIN(sv); PIN(h2v);
#pragma unroll
            for (int n = 0; n < PC; ++n) { wf[n] = lds16(wac(n)); SBAR(); }
            res_init(acc, xr0, xr1, xr2);
            wait_vm<0>();
        }, [] {});
        const unsigned hb = WS_H + (tb * 4 + w) * 20480;
        unsigned t = 0;
        u64 bad = 0;
        static_for<0, 100>([&](auto nn) {
            constexpr int n = nn.v, j = n < 54 ? n / 6 : 0, i = n % 6, q0 = areq(j, RC, 10), q1 = areq(j + 1, RC, 10);
            if (n % 3 == 0) { lgkm<(PC - 3 < 15 ? PC - 3 : 15)>(); SBAR(); }
            mma16(acc[sub(n)], wf[n % PC], act[kst(n) >> 1][kst(n) & 1]);
            SBAR();
            if (n + PC < 100) wf[n % PC] = lds16(wac(n + PC));
            if (n == 59) cnt_add_now(CN2(M_P1));
            if (n < 54) {
                if (i < 4 && q0 + i / 2 < q1) act[(q0 + i / 2) % 10][i & 1] = aload<2, 2048>(rs, (i & 1) ? v1 : v0, gb, (q0 + i / 2) % 10);
                if (i == 3) { t = max(max(act[j + 1][1][0], act[j + 1][1][2]), max(act[j + 1][0][0], act[j + 1][0][2])); PIN(t); }
                if (i == 4) { bad = __ballot(t == EMPTY); asm volatile("" : "+s"(bad)); }
                if (i == 5 && __builtin_expect(bad != 0, 0))
                    refetch(act[j + 1][0], act[j + 1][1], ws + gb[(j + 1) / 2] + ((j + 1) & 1) * 2048, v0, v1);
            }
            if (n == 68) {
                xr0 = u32x4{keep(pk2(acc[0][0], acc[0][1])), pk2(acc[0][2], acc[0][3]), keep(pk2(acc[1][0], acc[1][1])), pk2(acc[1][2], acc[1][3])};
                bst16(rs, xr0, v0, hb + m * 1024);
            }
            if (n == 70) {
                xr2 = u32x2{keep(pk2(acc[4][0], acc[4][1])), pk2(acc[4][2], acc[4][3])};
                bst16(rs, rowpair(xr2, xr2), h2v, hb + 8192 + m * 512);
            }
            if (n == 74) t = *(volatile LAS unsigned*)(size_t)CN2(M_W0);
            if (n == 86) {
                if (__builtin_expect((unsigned)__builtin_amdgcn_readfirstlane(t) < 4u, 0)) cnt_spin(CN2(M_W0), 4);
                asm volatile("" ::: "memory");
            }
            if constexpr (n >= 88) {
                constexpr int k = n - 88;
                wd[k] = lds16(wl + (k % 7 < 5 ? 16384 + (k / 7) * 5120 + (k % 7) * 1024 : (k / 7) * 2048 + (k % 7 - 5) * 1024));
            }
            SBAR();
        });
        float sm = 0.f, sq = 0.f, sm2 = 0.f, sq2 = 0.f;
#pragma unroll
        for (int i = 0; i < 4; i += 2) { sm = dot2(xr0[i], ONES2, sm); sq = dot2(xr0[i], xr0[i], sq); sm2 = dot2(xr0[i + 1], ONES2, sm2); sq2 = dot2(xr0[i + 1], xr0[i + 1], sq2); }
        sm = dot2(xr2[0], ONES2, sm); sq = dot2(xr2[0], xr2[0], sq); sm2 = dot2(xr2[1], ONES2, sm2); sq2 = dot2(xr2[1], xr2[1], sq2);
        PIN(sm); PIN(sq); PIN(sm2); PIN(sq2);
        SBAR();
#pragma unroll
        for (int n = 0; n < 12; ++n) PIN(wd[n]);
        cnt_add(CN2(M_LDS));
        xr1 = u32x4{keep(pk2(acc[2][0], acc[2][1])), pk2(acc[2][2], acc[2][3]), keep(pk2(acc[3][0], acc[3][1])), pk2(acc[3][2], acc[3][3])};
        bst16(rs, xr1, v0, hb + (12 + m) * 1024);
#pragma unroll
        for (int i = 0; i < 4; i += 2) { sm = dot2(xr1[i], ONES2, sm); sq = dot2(xr1[i], xr1[i], sq); sm2 = dot2(xr1[i + 1], ONES2, sm2); sq2 = dot2(xr1[i + 1], xr1[i + 1], sq2); }
        sm = xsum16_32(sm + sm2); sq = xsum16_32(sq + sq2);
        bst8(rs, u32x2{keep(__float_as_uint(sm)), __float_as_uint(sq)}, sv, WS_ST + (tb * 4 + w) * 1024 + m * 128);
        SBAR();
    }
    {
        f32x4 acc[2][7], c4[7];
        u32x4 act[12][2];
        u32x2 st[4];
        float mu[2], rp[2];
        const int t16 = (tb >> 1) * 8 + 2 * w;
        unsigned v0 = lane_id() * 16, v1 = v0 + 20480, sv = v0 >> 1, wa = L + v0, wb = wa + 16384 + 46080, gb[5];
        PIN(sv); PIN(wa); PIN(wb); PIN(v0); PIN(v1);
        d_bases(gb, WS_H + t16 * 20480, rotd, 4096);
        unsigned fs[7], wc, fw, f2v, ca;
        auto late = [&] {
            zero_acc(acc[0]); zero_acc(acc[1]);
            e_bases(ge, WS_F + tb * 221184 + kh * 1024, rote, 6144);
            {
                const unsigned fb = WS_F + t16 * 55296 + ct * 1024;
                fs[0] = fb; fs[1] = fb + 55296; fs[2] = fb + 16384 - ct * 512; fs[3] = fb + 24576; fs[4] = fb + 24576 + 55296; fs[5] = fb + 39936; fs[6] = fb + 39936 + 55296;
#pragma unroll
                for (int i = 0; i < 7; ++i) asm volatile("" : "+s"(fs[i]));
                const int lane = v0 >> 4, g = lane >> 4, c16 = lane & 15;
                f2v = (g & 1) * 55296 + (g >> 1) * 256 + c16 * 16; ca = L + L_MISC + g * 16;
                ehv = (g & 1) * 55296 + c16 * 512;
                wc = wa + 118784;
                PIN(f2v); PIN(ca); PIN(ehv); PIN(wc);
            }
        };
        unsigned hv = ((v0 >> 8) & 1) * 20480 + ((v0 >> 4) & 7) * 512;
        PIN(hv);
        kstart<RD, 4, 1024>(act, rs, ws, v0, v1, gb, [&] { return __builtin_amdgcn_raw_buffer_load_b32(rs, hv, WS_H + t16 * 20480 + 8192, SC1) == EMPTY; },
                            [] {}, late);
        auto stload = [&] {
#pragma unroll
            for (int i = 0; i < 4; ++i) st[i] = __builtin_amdgcn_raw_buffer_load_b64(rs, sv + i * 512, WS_ST + t16 * 1024, SC1);
        };
        float sa[4];
        unsigned w1v = 0;
        auto dside = [&](int j, int i) {
            if ((j == 0 || j == 6) && i == 12) w1v = *(volatile LAS unsigned*)(size_t)(ca + 512 + (j ? M_W1 : M_W1A) * 4);
            if ((j == 1 || j == 7) && i == 12) {
                if (__builtin_expect((unsigned)__builtin_amdgcn_readfirstlane(w1v) < 4u, 0)) cnt_spin(CN2(j == 7 ? M_W1 : M_W1A), 4);
                asm volatile("" ::: "memory");
            }
            if (j == 5 && i == 4) stload();
            if (j == 9 && i == 6 && __builtin_expect(__any(max(max(st[0][0], st[1][0]), max(st[2][0], st[3][0])) == EMPTY), 0)) {
#pragma unroll 1
                for (int spin = 0; spin < (1 << 16); ++spin) {
                    asm volatile("" ::: "memory");
                    stload();
                    if (!__any(max(max(st[0][0], st[1][0]), max(st[2][0], st[3][0])) == EMPTY)) break;
                }
            }
            if ((j == 10 || j == 11) && (i == 4 || i == 8)) {
                const int k = (j - 10) * 2 + (i == 8);
                sa[k] = xsum16_32(__uint_as_float(st[k & 2][k & 1]) + __uint_as_float(st[(k & 2) + 1][k & 1])); PIN(sa[k]);
            }
            if (j == 12 && (i == 4 || i == 8)) {
                const int t = i == 8;
                mu[t] = sa[2 * t] * (-1.f / D);
                rp[t] = __builtin_amdgcn_rsqf(fmaxf(sa[2 * t + 1] * (1.f / D) - mu[t] * mu[t], 0.f) + LN_EPS) * LOG2E;
                PIN(mu[t]); PIN(rp[t]);
            }
        };
        auto wad = [&](int n) {
            const int j = n < 56 ? n / 7 : n < 86 ? 8 + (n - 56) / 5 : n < 98 ? 14 + (n - 86) / 2 : 14 + (n - 98) / 3;
            const int s = n < 56 ? n % 7 : n < 86 ? (n - 56) % 5 : n < 98 ? (n - 86) % 2 : 2 + (n - 98) % 3;
            return n >= 116 ? wc + (n - 116) * 1024 : s >= 5 ? wa + j * 2048 + (s - 5) * 1024 : (j < 9 ? wa + 16384 + j * 5120 : wb + (j - 9) * 5120) + s * 1024; };
        kloop<0, 8, 20, 0, 140, 7, 5, RD, 4, 1024>(acc, act, wd, wad, rs, ws, v0, v1, gb, dside);
        kloop<8, 1, 20, 56, 140, 5, 5, RD, 4, 1024>(acc, act, wd, wad, rs, ws, v0, v1, gb, dside);
        cnt_add_now(CN2(M_K8));
        kloop<9, 5, 20, 61, 140, 5, 2, RD, 4, 1024>(acc, act, wd, wad, rs, ws, v0, v1, gb, dside);
        c4[0] = ldsf4(ca); c4[1] = ldsf4(ca + 64);
        SBAR();
        kloop<14, 6, 20, 86, 140, 2, 4, RD, 4, 1024>(acc, act, wd, wad, rs, ws, v0, v1, gb, dside);
        u32x2 lo, hi;
        auto silu = [&](const f32x4& a, const f32x4& c, int t, u32x2& r, auto side) {
            f32x4 y = (a + mu[t] * c) * rp[t], e;
            PIN(y); SBAR();
#pragma unroll
            for (int i = 0; i < 4; ++i) { side(i); SBAR(); e[i] = EX2(-y[i]); PIN(e); SBAR(); }
            e = e * LOG2E + LOG2E; PIN(e); SBAR();
#pragma unroll
            for (int i = 0; i < 4; ++i) { side(4 + i); SBAR(); e[i] = __builtin_amdgcn_rcpf(e[i]); PIN(e); SBAR(); }
            e *= y;
            r = u32x2{keep(pk2(e[0], e[1])), pk2(e[2], e[3])};
            PIN(r); SBAR();
        };
        auto rest = [&](int q) {
            const int n = 98 + (q >> 1), j = n < 116 ? 14 + (n - 98) / 3 : 8 + (n - 116) / 2, s = n < 116 ? 2 + (n - 98) % 3 : 5 + (n - 116) % 2;
            mma16(acc[q & 1][s], wd[n % 12], act[j % 12][q & 1]);
            SBAR();
            if ((q & 1) && n + 12 < 140) wd[n % 12] = lds16(wad(n + 12));
            if (q >= 24 && q < 74 && q % 10 == 4) c4[q == 24 ? 6 : q / 10 - 1] = ldsf4(ca + (q == 24 ? 6 : q / 10 - 1) * 64);
        };
        auto early = [&](int q) {
            if (q == 0) eprobe = __builtin_amdgcn_raw_buffer_load_b32(rs, ehv, ehs, SC1);
            ae[q >> 1][q & 1] = aload<3, 2048>(rs, (q & 1) ? e1 : e0, ge, q >> 1);
        };
#pragma unroll
        for (int q = 0; q < 8; ++q) { rest(q); SBAR(); }
#pragma unroll
        for (int b = 0; b < 10; ++b) {
            const int ut = b < 4 ? b >> 1 : b < 6 ? b & 1 : (b - 6) >> 1, ui = b < 4 ? b & 1 : b < 6 ? 2 : 3 + (b & 1), us = ui < 2 ? ui : ui == 2 ? 6 : ui - 1;
            silu(acc[ut][ui], c4[us], ut, (b & 1) ? hi : lo, [&](int i) { if (8 + b * 8 + i < 84) rest(8 + b * 8 + i); });
            if (b == 5) bst16(rs, rowpair(lo, hi), f2v, fs[2]);
            else if (b & 1) bst16(rs, u32x4{lo[0], lo[1], hi[0], hi[1]}, b < 5 ? v0 : fw, fs[b >> 1]);
            if (b == 1) cnt_add(CN2(M_T4));
            if (b == 7) cnt_add(CN2(M_T5));
            if (b == 8) w1v = *(volatile LAS unsigned*)(size_t)(ca + 512 + (G_RDY + 1) * 4);
            if (b == 5) {
                PIN(v0);
                fw = ct < 15 ? v0 : 0x80000000u; e0 = v0 + w * 55296; e1 = v0 + (w ^ 1) * 55296; a0 = L + v0 + kh * 5120;
                PIN(fw); PIN(e0); PIN(e1); PIN(a0);
            }
            SBAR();
        }
        if (__builtin_expect((unsigned)__builtin_amdgcn_readfirstlane(w1v) < 4u, 0)) cnt_spin(CN2(G_RDY + 1), 4);
        asm volatile("" ::: "memory");
        mma_done();
#pragma unroll
        for (int n = 0; n < 15; ++n) we[n] = lds16(a0 + (n / 5) * 10240 + (n % 5) * 1024);
#pragma unroll
        for (int t = 0; t < 2; ++t) {
            u32x4 r;
#pragma unroll
            for (int u = 0; u < 2; ++u) {
                const f32x4 y = (acc[t][5 + u] + mu[t] * c4[4 + u]) * rp[t];
                f32x4 e = f32x4{EX2(-y[0]), EX2(-y[1]), EX2(-y[2]), EX2(-y[3])} * LOG2E + LOG2E;
                e = f32x4{__builtin_amdgcn_rcpf(e[0]), __builtin_amdgcn_rcpf(e[1]), __builtin_amdgcn_rcpf(e[2]), __builtin_amdgcn_rcpf(e[3])} * y;
                r[2 * u] = keep(pk2(e[0], e[1])); r[2 * u + 1] = pk2(e[2], e[3]);
            }
            bst16(rs, r, fw, fs[5 + t]);
        }
        SBAR();
#pragma unroll
        for (int q = 0; q < 2 * FIRSTFILL; ++q) { early(q); SBAR(); }
    }
    {
        f32x4 acc[2][5];
        unsigned a1 = a0 + 61440, a2 = a0 + 122880;
        PIN(a1); PIN(a2);
        cnt_add(CN2(G_USED));
#pragma unroll
        for (int n = 15; n < 30; ++n) we[n] = lds16(a0 + (n / 5) * 10240 + (n % 5) * 1024);
        res_init(acc[0], xr0, xr1, xr2); zero_acc(acc[1]);
#pragma unroll
        for (int n = 0; n < 30; ++n) PIN(we[n]);
        cnt_add(CN2(G_USED + 1));
        cnt_spin(CN2(G_RDY + 2), 4);
        if (__any(eprobe == EMPTY))
            kstart<RE, 3, 2048>(ae, rs, ws, e0, e1, ge, [&] { return __builtin_amdgcn_raw_buffer_load_b32(rs, ehv, ehs, SC1) == EMPTY; }, [] {}, [] {});
        if (__builtin_expect(missing(ae[0][0], ae[0][1]), 0)) refetch(ae[0][0], ae[0][1], ws + ge[0], e0, e1);
        SBAR();
        auto wae = [&](int n) { const int q = n / 5 % 15; return (q < 6 ? a0 : q < 12 ? a1 : a2) + (q % 6) * 10240 + (n % 5) * 1024; };
        unsigned cg = CN2(0), gv = 0;
        PIN(cg);
        kloop<0, 27, 27, 0, 135, 5, 5, RE, 3, 2048>(acc, ae, we, wae, rs, ws, e0, e1, ge, [&](int j, int i) {
            if (j % 3 == 1 && i == 8 && j / 3 + 3 < 9) gv = *(volatile LAS unsigned*)(size_t)(cg + (G_RDY + j / 3 + 3) * 4);
            if (j % 3 == 2 && i == 8 && j / 3 + 3 < 9) {
                if (__builtin_expect((unsigned)__builtin_amdgcn_readfirstlane(gv) < 4u, 0)) cnt_spin(cg + (G_RDY + j / 3 + 3) * 4, 4);
                asm volatile("" ::: "memory");
            }
            if (j % 3 == 1 && i == 4 && j / 3 >= 1 && j / 3 + 1 < 5)
                asm volatile("s_mov_b64 exec, 1\n\tds_add_u32 %0, %1 offset:%2\n\ts_mov_b64 exec, -1" ::"v"(cg), "v"(1u), "i"((G_USED + j / 3 + 1) * 4) : "memory");
            if (j == 24 && i == 8) gv = *(volatile LAS unsigned*)(size_t)(cg + (G_USED + 4) * 4);
        });
        if (__builtin_expect((unsigned)__builtin_amdgcn_readfirstlane(gv) < 4u, 0)) cnt_spin(cg + (G_USED + 4) * 4, 4);
        mma_done();
        LANE_VARS();
        const unsigned wl = L + lane * 16;
        send_half(acc[1], wl + 122880 + (w ^ 1) * 5120);
        bar();
        recv_half(acc[0], wl + 122880 + w * 5120);
        res_pack(xr0, xr1, xr2, acc[0]);
        char* ob = out + (size_t)(tb * 64 + w * 16 + c16) * 1280 + m * 160;
        asm volatile("global_store_dwordx4 %0, %1, off nt\n\tglobal_store_dwordx4 %0, %2, off offset:64 nt\n\t"
                     "global_store_dwordx2 %3, %4, off offset:128 nt\n\ts_nop 1" ::"v"(ob + g * 16), "v"(xr0), "v"(xr1), "v"(ob + g * 8), "v"(xr2) : "memory");
    }
    asm volatile(".Lpf_end1:");
    __builtin_amdgcn_endpgm();
}

extern "C" void kernel_launch(void* const* d_in, const int* in_sizes, int n_in, void* d_out, int out_size, void* d_ws, size_t ws_size,
                              hipStream_t stream) {
    (void)hipFuncSetAttribute((const void*)k_block, hipFuncAttributeMaxDynamicSharedMemorySize, LDS_BYTES);
    k_block<<<256, 512, LDS_BYTES, stream>>>((const char*)d_in[0], (const char*)d_in[1], (const char*)d_in[2], (const char*)d_in[3],
                                             (const char*)d_in[4], (const char*)d_in[6], (const char*)d_in[7], (char*)d_ws, (char*)d_out);
}
```
